```python
import torch

def _softmax(x, axis=-1):
    return torch.softmax(x, dim=axis)
def _gelu(x):
    return torch.nn.functional.gelu(x, approximate="tanh")
def _silu(x):
    return torch.nn.functional.silu(x)
def _sigmoid(x):
    return torch.sigmoid(x)
def _layernorm(x, axis=-1, eps=1e-5):
    m = x.mean(dim=axis, keepdim=True)
    v = x.var(dim=axis, keepdim=True, unbiased=False)
    return (x - m) / torch.sqrt(v + eps)
def _scatter_add(tgt, idx, src, axis=0):
    return tgt.clone().index_add_(axis, idx, src)
def _segment_sum(src, seg, n):
    out = torch.zeros((n,) + src.shape[1:], dtype=src.dtype)
    return out.index_add_(0, seg, src)
def _pad(x, pw):
    flat = tuple(v for pair in reversed(pw) for v in pair)
    return torch.nn.functional.pad(x, flat)

kernel_name = 'parallel_sum__compressed_kv_cla__glu_gated_glu__decode'

def setup_inputs():

    _w = (lambda a: (lambda t: t.to(torch.bfloat16) if t.is_floating_point() else t)(torch.as_tensor(a)))
    def _A_setup():
        B, H, Sq, Sk, D, M = 2, 32, 1, 4096, 128, 512
        Dc = 4 * D
        return {
            "x": _w((lambda *s: torch.randn(*s, dtype=torch.bfloat16) * 0.05)(B, Sq, M)),
            "c_kv": _w((lambda *s: torch.randn(*s, dtype=torch.bfloat16) * 1.6)(B, Sk, Dc)),
            "w_q": _w((lambda *s: torch.randn(*s, dtype=torch.bfloat16) * 1.6)(M, H, D) * (M ** -0.5)),
            "w_uk": _w((lambda *s: torch.randn(*s, dtype=torch.bfloat16) * 1.6)(Dc, H, D) * (Dc ** -0.5)),
            "w_uv": _w((lambda *s: torch.randn(*s, dtype=torch.bfloat16) * 1.6)(Dc, H, D) * (Dc ** -0.5)),
            "w_o": _w((lambda *s: torch.randn(*s, dtype=torch.bfloat16) * 1.6)(H, D, M) * ((H * D) ** -0.5)),
        }
    _A_in = _A_setup()

    def _B_setup():
        B, S, D, F = 2, 1, 512, 1376
        return {
            "x": _w((lambda *s: torch.randn(*s, dtype=torch.bfloat16) * 0.05)(B, S, D)),
            "w_gate": _w((lambda *s: torch.randn(*s, dtype=torch.bfloat16) * 0.1)(D, F)),
            "w_up": _w((lambda *s: torch.randn(*s, dtype=torch.bfloat16) * 0.05)(D, F)),
            "w_down": _w((lambda *s: torch.randn(*s, dtype=torch.bfloat16) * 0.05)(F, D)),
        }
    _B_in = _B_setup()

    return {
        "A_x": _A_in["x"],
        "A_c_kv": _A_in["c_kv"],
        "A_w_q": _A_in["w_q"],
        "A_w_uk": _A_in["w_uk"],
        "A_w_uv": _A_in["w_uv"],
        "A_w_o": _A_in["w_o"],
        "B_x": _B_in["x"],
        "B_w_gate": _B_in["w_gate"],
        "B_w_up": _B_in["w_up"],
        "B_w_down": _B_in["w_down"],
    }

def reference(A_x, A_c_kv, A_w_q, A_w_uk, A_w_uv, A_w_o, B_x, B_w_gate, B_w_up, B_w_down):
    def _A_ref(x, c_kv, w_q, w_uk, w_uv, w_o):
        B, Sq, M = x.shape[0], x.shape[1], x.shape[2]
        Sk = c_kv.shape[1]
        H, D = w_q.shape[1], w_q.shape[2]
        q = torch.einsum("bqm,mhd->bhqd", x, w_q)
        k = torch.einsum("bkc,chd->bhkd", c_kv, w_uk)
        v = torch.einsum("bkc,chd->bhkd", c_kv, w_uv)
        scores = torch.einsum("bhqd,bhkd->bhqk", q, k) * (D ** -0.5)
        off = Sk - Sq
        qi = torch.arange(Sq)[:, None]
        kj = torch.arange(Sk)[None, :]
        cmask = kj <= (qi + off)
        scores = torch.where(cmask[None, None], scores, -1e9)
        p = _softmax(scores, axis=-1)
        ctx = torch.einsum("bhqk,bhkd->bhqd", p, v)
        return torch.einsum("bhqd,hdm->bqm", ctx, w_o)

    def _B_ref(x, w_gate, w_up, w_down):
        g = torch.einsum("bsd,df->bsf", x, w_gate)
        u = torch.einsum("bsd,df->bsf", x, w_up)
        h = _sigmoid(g) * u
        return torch.einsum("bsf,fd->bsd", h, w_down)

    x = A_x
    xn = _layernorm(x, axis=-1)
    return x + _A_ref(xn, A_c_kv, A_w_q, A_w_uk, A_w_uv, A_w_o) + _B_ref(xn, B_w_gate, B_w_up, B_w_down)
```

```cpp
#include <hip/hip_runtime.h>
#include <hip/hip_bf16.h>
typedef unsigned short u16;
__device__ __forceinline__ float bf2f(u16 b){ return __uint_as_float(((unsigned)b)<<16); }
__device__ __forceinline__ u16 f2bf(float f){ __hip_bfloat16 h = __float2bfloat16(f); return *(u16*)&h; }
constexpr int B=2,H=32,SK=4096,D=128,M=512,DC=512,F=1376;

__global__ void k_ln(const u16* x, float* xn){
  int b=blockIdx.x, t=threadIdx.x;
  __shared__ float red[16];
  float v=bf2f(x[b*M+t]);
  float s=v; for(int o=32;o;o>>=1) s+=__shfl_xor(s,o);
  if((t&63)==0) red[t>>6]=s; __syncthreads();
  float m=0; for(int i=0;i<8;i++) m+=red[i]; m/=M; __syncthreads();
  float d=v-m; s=d*d; for(int o=32;o;o>>=1) s+=__shfl_xor(s,o);
  if((t&63)==0) red[t>>6]=s; __syncthreads();
  float var=0; for(int i=0;i<8;i++) var+=red[i]; var/=M;
  xn[b*M+t]=d/sqrtf(var+1e-5f);
}
__global__ void k_q(const float* xn,const u16* wq,float* q){
  int n=blockIdx.x*256+threadIdx.x;
  float a0=0,a1=0;
  for(int m=0;m<M;m++){ float w=bf2f(wq[m*4096+n]); a0+=xn[m]*w; a1+=xn[M+m]*w; }
  q[n]=a0; q[4096+n]=a1;
}
__global__ void k_glu1(const float* xn,const u16* wg,const u16* wu,float* hh){
  int n=blockIdx.x*64+threadIdx.x; if(n>=F) return;
  float g0=0,g1=0,u0=0,u1=0;
  for(int m=0;m<M;m++){ float a=bf2f(wg[m*F+n]),c=bf2f(wu[m*F+n]); g0+=xn[m]*a; g1+=xn[M+m]*a; u0+=xn[m]*c; u1+=xn[M+m]*c; }
  hh[n]=u0/(1.f+__expf(-g0)); hh[F+n]=u1/(1.f+__expf(-g1));
}
__global__ void k_qabs(const float* q,const u16* wuk,float* qa){
  int h=blockIdx.x, c=threadIdx.x;
  float a0=0,a1=0;
  for(int d=0;d<D;d++){ float w=bf2f(wuk[(c*H+h)*D+d]); a0+=q[h*D+d]*w; a1+=q[4096+h*D+d]*w; }
  const float sc=0.08838834764831845f;
  qa[(0*H+h)*DC+c]=a0*sc; qa[(1*H+h)*DC+c]=a1*sc;
}
__global__ void k_sc(const float* qa,const u16* ckv,float* s){
  int b=blockIdx.z,h=blockIdx.y,k=blockIdx.x*256+threadIdx.x;
  float a=0; const u16* r=ckv+((size_t)b*SK+k)*DC; const float* qq=qa+(b*H+h)*DC;
  for(int c=0;c<DC;c++) a+=qq[c]*bf2f(r[c]);
  s[(b*H+h)*SK+k]=a;
}
__global__ void k_pv(const float* s,const u16* ckv,float* cc){
  int b=blockIdx.y,h=blockIdx.x,t=threadIdx.x;
  __shared__ float p[SK]; __shared__ float red[16];
  const float* ss=s+(b*H+h)*SK;
  float m=-1e30f; for(int k=t;k<SK;k+=512) m=fmaxf(m,ss[k]);
  for(int o=32;o;o>>=1) m=fmaxf(m,__shfl_xor(m,o));
  if((t&63)==0) red[t>>6]=m; __syncthreads();
  for(int i=0;i<8;i++) m=fmaxf(m,red[i]); __syncthreads();
  float l=0; for(int k=t;k<SK;k+=512){ float e=__expf(ss[k]-m); p[k]=e; l+=e; }
  for(int o=32;o;o>>=1) l+=__shfl_xor(l,o);
  if((t&63)==0) red[t>>6]=l; __syncthreads();
  l=0; for(int i=0;i<8;i++) l+=red[i];
  float a=0; for(int k=0;k<SK;k++) a+=p[k]*bf2f(ckv[((size_t)b*SK+k)*DC+t]);
  cc[(b*H+h)*DC+t]=a/l;
}
__global__ void k_ctx(const float* cc,const u16* wuv,float* ctx){
  int h=blockIdx.x,d=threadIdx.x;
  float a0=0,a1=0;
  for(int c=0;c<DC;c++){ float w=bf2f(wuv[(c*H+h)*D+d]); a0+=cc[(0*H+h)*DC+c]*w; a1+=cc[(H+h)*DC+c]*w; }
  ctx[h*D+d]=a0; ctx[4096+h*D+d]=a1;
}
__global__ void k_out(const u16* x,const float* ctx,const float* hh,const u16* wo,const u16* wd,u16* out){
  int m=blockIdx.x*64+threadIdx.x;
  float a0=0,a1=0;
  for(int n=0;n<4096;n++){ float w=bf2f(wo[n*M+m]); a0+=ctx[n]*w; a1+=ctx[4096+n]*w; }
  for(int f=0;f<F;f++){ float w=bf2f(wd[f*M+m]); a0+=hh[f]*w; a1+=hh[F+f]*w; }
  out[m]=f2bf(a0+bf2f(x[m])); out[M+m]=f2bf(a1+bf2f(x[M+m]));
}
extern "C" void kernel_launch(void* const* d_in, const int* in_sizes, int n_in, void* d_out, int out_size, void* d_ws, size_t ws_size, hipStream_t stream){
  const u16* x=(const u16*)d_in[0]; const u16* ckv=(const u16*)d_in[1]; const u16* wq=(const u16*)d_in[2];
  const u16* wuk=(const u16*)d_in[3]; const u16* wuv=(const u16*)d_in[4]; const u16* wo=(const u16*)d_in[5];
  const u16* wg=(const u16*)d_in[7]; const u16* wu=(const u16*)d_in[8]; const u16* wd=(const u16*)d_in[9];
  float* ws=(float*)d_ws;
  float* xn=ws; float* q=xn+1024; float* hh=q+8192; float* qa=hh+4096; float* s=qa+32768; float* cc=s+2*32*4096; float* ctx=cc+32768;
  k_ln<<<2,512,0,stream>>>(x,xn);
  k_q<<<16,256,0,stream>>>(xn,wq,q);
  k_glu1<<<22,64,0,stream>>>(xn,wg,wu,hh);
  k_qabs<<<32,512,0,stream>>>(q,wuk,qa);
  k_sc<<<dim3(16,32,2),256,0,stream>>>(qa,ckv,s);
  k_pv<<<dim3(32,2),512,0,stream>>>(s,ckv,cc);
  k_ctx<<<32,128,0,stream>>>(cc,wuv,ctx);
  k_out<<<8,64,0,stream>>>(x,ctx,hh,wo,wd,(u16*)d_out);
}
```

```cpp
#include <hip/hip_runtime.h>
#include <stdint.h>

#define GAS __attribute__((address_space(1)))
#define LAS __attribute__((address_space(3)))
typedef unsigned short u16;
typedef unsigned v4u __attribute__((ext_vector_type(4)));
typedef unsigned v2u __attribute__((ext_vector_type(2)));
typedef float f4 __attribute__((ext_vector_type(4)));
typedef float f2 __attribute__((ext_vector_type(2)));
typedef __bf16 bf8 __attribute__((ext_vector_type(8)));
typedef __bf16 bf2 __attribute__((ext_vector_type(2)));
typedef short s4 __attribute__((ext_vector_type(4)));

namespace {
constexpr int NH = 32, SK = 4096, DH = 128, DM = 512, DC = 512, FF = 1376;
constexpr int NSL = FF / 16;
constexpr int WS_E = 0;
constexpr int WS_QP = 4096;
constexpr int WS_QA = WS_QP + NH * 4 * 1024;
constexpr int WS_LSE = WS_QA + 2 * NH * 2048;
constexpr int WS_OPT = WS_LSE + 2 * 2 * 32 * 128;
constexpr int WS_OPG = WS_OPT + 32 * 64 * 128;
constexpr int WS_PART = WS_OPG + 32 * 96 * 128;
constexpr int WS_BYTES = WS_PART + NH * 2 * 2 * 32 * 512;
constexpr int WS_STAMP = 8 << 20;
constexpr int CP = 1056;
constexpr int L_C = 0, L_Q = 128 * CP, L_P = L_Q + 16 * CP, L_MX = L_P + 4096, L_LS = L_MX + 256, L_RR = L_LS + 256,
              L_END = L_RR + 1024;
constexpr int L_GG = 0, L_GU = 16384, L_GD = 32768, L_GR = 65536;
constexpr int L_WUV = 0, L_WO = 65536, L_WQ = 65536, L_WUK = 98304, L_CC = 131072, CCP = 576, L_CT = L_CC + 2048, CTP = 320;
constexpr unsigned SPIN_CAP = 1u << 20;
constexpr int AUX_SC1 = 16;

#define STAMP(i)
#define STAMP_FLUSH()

__device__ __forceinline__ f4 mfma32(v4u a, v4u b, f4 c) {
  return __builtin_amdgcn_mfma_f32_16x16x32_bf16(__builtin_bit_cast(bf8, a), __builtin_bit_cast(bf8, b), c, 0, 0, 0);
}
__device__ __forceinline__ f4 mfma16(v2u a, v2u b, f4 c) {
  return __builtin_amdgcn_mfma_f32_16x16x16bf16_1k(__builtin_bit_cast(s4, a), __builtin_bit_cast(s4, b), c, 0, 0, 0);
}
__device__ __forceinline__ unsigned pk(float lo, float hi) {
  f2 x = {lo, hi};
  return __builtin_bit_cast(unsigned, __builtin_convertvector(x, bf2));
}
__device__ __forceinline__ float asf(unsigned u) { return __builtin_bit_cast(float, u); }
__device__ __forceinline__ float blo(unsigned u) { return __builtin_bit_cast(float, u << 16); }
__device__ __forceinline__ float bhi(unsigned u) { return __builtin_bit_cast(float, u & 0xffff0000u); }
template <int CTRL> __device__ __forceinline__ float dpp(float v) {
  return __builtin_bit_cast(float, __builtin_amdgcn_update_dpp(0, __builtin_bit_cast(int, v), CTRL, 0xf, 0xf, true));
}
__device__ __forceinline__ float fmax2(float a, float b) { return __builtin_amdgcn_fmed3f(a, b, __builtin_inff()); }
constexpr int ROR1 = 0x121, ROR2 = 0x122, ROR4 = 0x124, ROR8 = 0x128, QP_2301 = 0x4e, QP_2222 = 0xaa;
__device__ __forceinline__ float sw32_add(float a, float b) {
  auto r = __builtin_amdgcn_permlane32_swap(__builtin_bit_cast(unsigned, a), __builtin_bit_cast(unsigned, b), false, false);
  return asf(r[0]) + asf(r[1]);
}
__device__ __forceinline__ float sw16_add(float a, float b) {
  auto r = __builtin_amdgcn_permlane16_swap(__builtin_bit_cast(unsigned, a), __builtin_bit_cast(unsigned, b), false, false);
  return asf(r[0]) + asf(r[1]);
}
__device__ __forceinline__ float sw32_max(float a) {
  auto r = __builtin_amdgcn_permlane32_swap(__builtin_bit_cast(unsigned, a), __builtin_bit_cast(unsigned, a), false, false);
  return fmax2(asf(r[0]), asf(r[1]));
}
__device__ __forceinline__ float sw16_max(float a) {
  auto r = __builtin_amdgcn_permlane16_swap(__builtin_bit_cast(unsigned, a), __builtin_bit_cast(unsigned, a), false, false);
  return fmax2(asf(r[0]), asf(r[1]));
}
__device__ __forceinline__ float wave_sum(float v) {
  v += dpp<ROR8>(v);
  v += dpp<ROR4>(v);
  v += dpp<ROR2>(v);
  v += dpp<ROR1>(v);
  v = sw16_add(v, v);
  return sw32_add(v, v);
}
__device__ __forceinline__ v2u trrd(LAS char* p) {
  return __builtin_bit_cast(v2u, __builtin_amdgcn_ds_read_tr16_b64_v4i16((LAS s4*)p));
}
__device__ __forceinline__ v4u trrd2(LAS char* p0, LAS char* p1) {
  v2u a = trrd(p0), b = trrd(p1);
  return v4u{a.x, a.y, b.x, b.y};
}
#define BAR() asm volatile("s_waitcnt lgkmcnt(0)\n\ts_barrier" ::: "memory")
#define VMWAIT(n) asm volatile("s_waitcnt vmcnt(" #n ")" ::: "memory")
__device__ __forceinline__ void gld16(v4u& d, const void* base, unsigned off) {
  asm volatile("global_load_dwordx4 %0, %1, %2" : "=v"(d) : "v"(off), "s"(base) : "memory");
}
__device__ __forceinline__ void gld4_sc1(unsigned& d, const void* base, unsigned off) {
  asm volatile("global_load_dword %0, %1, %2 sc1" : "=v"(d) : "v"(off), "s"(base) : "memory");
}
__device__ __forceinline__ void dma16(const void* base, unsigned off, unsigned lds) {
  unsigned keep;
  asm volatile("s_mov_b32 %0, m0\n\ts_mov_b32 m0, %3\n\ts_nop 0\n\tglobal_load_lds_dwordx4 %1, %2\n\ts_mov_b32 m0, %0"
               : "=&s"(keep)
               : "v"(off), "s"(base), "s"(lds)
               : "memory");
}
__device__ __forceinline__ v4u ld_sc1(__amdgpu_buffer_rsrc_t rs, int off) {
  return __builtin_amdgcn_raw_buffer_load_b128(rs, off, 0, AUX_SC1);
}
__device__ __forceinline__ v2u ld8_sc1(__amdgpu_buffer_rsrc_t rs, int off) {
  return __builtin_amdgcn_raw_buffer_load_b64(rs, off, 0, AUX_SC1);
}
__device__ __forceinline__ void st_sc1(__amdgpu_buffer_rsrc_t rs, int off, v4u v) {
  __builtin_amdgcn_raw_buffer_store_b128(v, rs, off, 0, AUX_SC1);
}
__device__ __forceinline__ void ln_stats(v4u r, float& mean, float& rstd) {
  float e[8] = {blo(r.x), bhi(r.x), blo(r.y), bhi(r.y), blo(r.z), bhi(r.z), blo(r.w), bhi(r.w)};
  float s = ((e[0] + e[1]) + (e[2] + e[3])) + ((e[4] + e[5]) + (e[6] + e[7]));
  mean = wave_sum(s) * (1.f / DM);
  float q = 0.f;
#pragma unroll
  for (int i = 0; i < 8; ++i) {
    float d = e[i] - mean;
    q = fmaf(d, d, q);
  }
  rstd = __builtin_amdgcn_rsqf(wave_sum(q) * (1.f / DM) + 1e-5f);
}
}

__global__ void __launch_bounds__(512, 2)
mla_glu_decode(const u16* __restrict__ x, const u16* __restrict__ ckv, const u16* __restrict__ wq, const u16* __restrict__ wuk,
               const u16* __restrict__ wuv, const u16* __restrict__ wo, const u16* __restrict__ wg, const u16* __restrict__ wu,
               const u16* __restrict__ wd, u16* __restrict__ out, char* __restrict__ ws) {
  __shared__ __attribute__((aligned(16))) char smem_[L_END];
  LAS char* sm = (LAS char*)smem_;
  const unsigned smb = (unsigned)(size_t)sm;
  const int tid = threadIdx.x, l = tid & 63, w = __builtin_amdgcn_readfirstlane(tid >> 6);
  const int col = l & 15, g4 = l >> 4, tq = (l & 15) >> 2, tp = l & 3;
  const int xcd = blockIdx.x & 7, r = blockIdx.x >> 3;
  const __amdgpu_buffer_rsrc_t rs = __builtin_amdgcn_make_buffer_rsrc(ws, 0, WS_BYTES, 0x00020000);
  asm volatile("" ::"s"(x), "s"(ckv), "s"(wq), "s"(wuk), "s"(wuv), "s"(wo), "s"(wg), "s"(wu), "s"(wd), "s"(out), "s"(ws));
  STAMP(0);

  if (__builtin_expect(r < 16, 1)) {
    const int h = 4 * xcd + (r >> 2), j = r & 3, ca = j >> 1, mb = j & 1;
    v4u xr0, xr1, xf[4];
    unsigned ep;
    gld4_sc1(ep, ws, 0u);
    gld16(xr0, x, l * 16u);
    gld16(xr1, x, 1024u + l * 16u);
#pragma unroll
    for (int s = 0; s < 4; ++s) gld16(xf[s], x, (unsigned)(((col & 1) * DM + 128 * j + 32 * s + 8 * g4) * 2));
#pragma unroll
    for (int i = 0; i < 4; ++i) {
      const int k = 4 * w + i, row = 4 * k + g4, f = (row & 3) | ((row >> 1) & 4);
      dma16(wq, (unsigned)(((128 * j + row) * 4096 + h * DH + 8 * (col ^ (f << 1))) * 2), smb + L_WQ + k * 1024);
    }
    if (w < 4) {
#pragma unroll
      for (int i = 0; i < 8; ++i) {
        const int row = 32 * w + 4 * i + g4;
        dma16(wuk, (unsigned)((((128 * j + row) * NH + h) * DH + 8 * (col ^ (row & 15))) * 2), smb + L_WUK + (8 * w + i) * 1024);
      }
      VMWAIT(12);
    } else {
      const v4u ones = {~0u, ~0u, ~0u, ~0u};
      const int cb = WS_PART + ((h * 2 + ca) * 2 + mb) * 16384 + (tid - 256) * 16;
#pragma unroll
      for (int i = 0; i < 4; ++i) st_sc1(rs, cb + 4096 * i, ones);
      VMWAIT(8);
    }
    asm volatile("" : "+v"(ep), "+v"(xr0), "+v"(xr1), "+v"(xf[0]), "+v"(xf[1]), "+v"(xf[2]), "+v"(xf[3])::"memory");
    const unsigned T = ep + 1u;
    float m0, r0, m1, r1;
    ln_stats(xr0, m0, r0);
    ln_stats(xr1, m1, r1);
    const float mean = (col & 1) ? m1 : m0, rstd = (col & 1) ? r1 : r0;
    if (col & 2) {
#pragma unroll
      for (int s = 0; s < 4; ++s) xf[s] = v4u{0x3f803f80u, 0x3f803f80u, 0x3f803f80u, 0x3f803f80u};
    }
    STAMP(1);
    if (w < 4) VMWAIT(8);
    else VMWAIT(0);
    BAR();
    STAMP(2);
    const int fl = tq | ((g4 & 1) << 2);
    {
      LAS char* p = sm + L_WQ + 256 * (8 * g4 + tq) + 16 * ((2 * w + (tp >> 1)) ^ (fl << 1)) + 8 * (tp & 1);
      v4u af[4];
#pragma unroll
      for (int s = 0; s < 4; ++s) af[s] = trrd2(p + 256 * 32 * s, p + 256 * (32 * s + 4));
      __builtin_amdgcn_sched_barrier(0);
      f4 acc = {0.f, 0.f, 0.f, 0.f};
#pragma unroll
      for (int s = 0; s < 4; ++s) acc = mfma32(af[s], xf[s], acc);
      float q[4];
#pragma unroll
      for (int e = 0; e < 4; ++e) q[e] = rstd * (acc[e] - mean * dpp<QP_2222>(acc[e]));
      if (col < 2) st_sc1(rs, WS_QP + ((h * 4 + j) * 8 + w) * 128 + col * 64 + g4 * 16, v4u{pk(q[0], q[1]), T, pk(q[2], q[3]), T});
    }
    STAMP(3);
    if (w < 4) {
      VMWAIT(1);
      v4u uk[8];
#pragma unroll
      for (int t = 0; t < 2; ++t)
#pragma unroll
        for (int s = 0; s < 4; ++s) uk[4 * t + s] = *(LAS v4u*)(sm + L_WUK + 256 * (32 * w + 16 * t + col) + 16 * ((4 * s + g4) ^ col));
      const int o = WS_QP + ((h * 4 + ((col >> 1) & 3)) * 8 + (g4 >> 1)) * 128 + (col & 1) * 64 + (g4 & 1) * 32;
      v4u a[8];
      for (unsigned spin = 0;; ++spin) {
        asm volatile("" ::: "memory");
#pragma unroll
        for (int i = 0; i < 8; ++i) a[i] = ld_sc1(rs, o + 256 * (i >> 1) + 16 * (i & 1));
        bool ok = true;
#pragma unroll
        for (int i = 0; i < 8; ++i) ok &= (a[i].y == T) & (a[i].w == T);
        if (__all(ok) || spin > SPIN_CAP) break;
      }
      STAMP(4);
      f4 a0 = {0.f, 0.f, 0.f, 0.f}, a1 = a0;
#pragma unroll
      for (int s = 0; s < 4; ++s) {
        const v4u qf = {a[2 * s].x, a[2 * s].z, a[2 * s + 1].x, a[2 * s + 1].z};
        a0 = mfma32(uk[s], qf, a0);
        a1 = mfma32(uk[4 + s], qf, a1);
      }
      float q[4];
#pragma unroll
      for (int e = 0; e < 4; ++e) {
        float v0 = a0[e] + dpp<QP_2301>(a0[e]), v1 = a1[e] + dpp<QP_2301>(a1[e]);
        float v = (col & 2) ? v1 : v0;
        v += dpp<ROR4>(v);
        q[e] = v * (0.08838834764831845f * 1.4426950408889634f);
      }
      if (col < 4)
        st_sc1(rs, WS_QA + ((col & 1) * NH + h) * 2048 + (64 * j + 16 * w + 8 * (col >> 1) + 2 * g4) * 8,
               v4u{pk(q[0], q[1]), T, pk(q[2], q[3]), T});
    }
    STAMP(5);
    BAR();
#pragma unroll
    for (int i = 0; i < 8; ++i) {
      const int k = 8 * w + ((i + 4 * mb) & 7), row = 4 * k + g4, f = (row & 3) | ((row >> 1) & 4);
      dma16(wuv, (unsigned)((((256 * ca + row) * NH + h) * DH + 8 * (col ^ (f << 1))) * 2), smb + L_WUV + k * 1024);
    }
#pragma unroll
    for (int i = 0; i < 8; ++i) {
      const int k = 8 * w + ((i + 4 * ca) & 7), row = 2 * k + (l >> 5), f = (row & 3) | ((row >> 1) & 4);
      dma16(wo, (unsigned)(((h * DH + row) * DM + 256 * mb + 8 * ((l & 31) ^ (f << 1))) * 2), smb + L_WO + k * 1024);
    }
    VMWAIT(0);
    BAR();
    STAMP(6);
    v4u av[8], ao[8];
    {
      LAS char* p = sm + L_WUV + 256 * (8 * g4 + tq) + 16 * ((2 * w + (tp >> 1)) ^ (fl << 1)) + 8 * (tp & 1);
#pragma unroll
      for (int s = 0; s < 8; ++s) av[s] = trrd2(p + 256 * 32 * s, p + 256 * (32 * s + 4));
#pragma unroll
      for (int t = 0; t < 2; ++t) {
        LAS char* po = sm + L_WO + 512 * (8 * g4 + tq) + 16 * ((4 * w + 2 * t + (tp >> 1)) ^ (fl << 1)) + 8 * (tp & 1);
#pragma unroll
        for (int s = 0; s < 4; ++s) ao[4 * t + s] = trrd2(po + 512 * 32 * s, po + 512 * (32 * s + 4));
      }
    }
    STAMP(7);
    {
      const int b = w >> 2, cw = w & 3, nr = l >> 3;
      const int o = WS_PART + (((h * 2 + ca) * 2 + b) * 32 + nr) * 512 + 128 * cw + 16 * (l & 7);
      const int ol = WS_LSE + ((b * 2 + (h >> 4)) * 32 + nr) * 128 + (h & 15) * 8;
      v4u a[4];
      v2u ls[4];
      for (unsigned spin = 0;; ++spin) {
        asm volatile("" ::: "memory");
#pragma unroll
        for (int i = 0; i < 4; ++i) ls[i] = ld8_sc1(rs, ol + 1024 * i);
#pragma unroll
        for (int i = 0; i < 4; ++i) a[i] = ld_sc1(rs, o + 4096 * i);
        bool ok = true;
        unsigned mx = 0u;
#pragma unroll
        for (int i = 0; i < 4; ++i) {
          ok &= ls[i].y == T;
          mx = max(max(mx, a[i].x), max(a[i].y, max(a[i].z, a[i].w)));
        }
        ok &= mx != ~0u;
        if (__all(ok) || spin > SPIN_CAP) break;
      }
      STAMP(8);
      float e[4];
#pragma unroll
      for (int i = 0; i < 4; ++i) e[i] = asf(ls[i].x);
      float M = fmax2(fmax2(e[0], e[1]), fmax2(e[2], e[3]));
      M = fmax2(M, dpp<ROR8>(M));
      M = sw32_max(sw16_max(M));
#pragma unroll
      for (int i = 0; i < 4; ++i) e[i] = __builtin_amdgcn_exp2f(e[i] - M);
      float L = (e[0] + e[1]) + (e[2] + e[3]);
      L += dpp<ROR8>(L);
      L = sw16_add(L, L);
      L = sw32_add(L, L);
      float acc[8];
#pragma unroll
      for (int c = 0; c < 4; ++c) {
        acc[2 * c] = e[0] * blo(a[0][c]);
        acc[2 * c + 1] = e[0] * bhi(a[0][c]);
      }
#pragma unroll
      for (int i = 1; i < 4; ++i)
#pragma unroll
        for (int c = 0; c < 4; ++c) {
          acc[2 * c] = fmaf(e[i], blo(a[i][c]), acc[2 * c]);
          acc[2 * c + 1] = fmaf(e[i], bhi(a[i][c]), acc[2 * c + 1]);
        }
      float rr[4];
#pragma unroll
      for (int c = 0; c < 4; ++c) rr[c] = sw32_add(acc[c], acc[c + 4]);
      float s0 = sw16_add(rr[0], rr[2]), s1 = sw16_add(rr[1], rr[3]);
      s0 += dpp<ROR8>(s0);
      s1 += dpp<ROR8>(s1);
      const float il = __builtin_amdgcn_rcpf(L);
      if (!(l & 8)) *(LAS unsigned*)(sm + L_CC + b * CCP + (64 * cw + 8 * (l & 7) + 2 * g4) * 2) = pk(s0 * il, s1 * il);
    }
    BAR();
    {
      f4 acc = {0.f, 0.f, 0.f, 0.f};
      LAS char* p = sm + L_CC + (col & 1) * CCP + 16 * g4;
#pragma unroll
      for (int s = 0; s < 8; ++s) acc = mfma32(av[s], *(LAS v4u*)(p + 64 * s), acc);
      if (col < 2) *(LAS v2u*)(sm + L_CT + col * CTP + (16 * w + 4 * g4) * 2) = v2u{pk(acc[0], acc[1]), pk(acc[2], acc[3])};
    }
    BAR();
    {
      f4 a0 = {0.f, 0.f, 0.f, 0.f}, a1 = a0;
      LAS char* p = sm + L_CT + (col & 1) * CTP + 16 * g4;
#pragma unroll
      for (int s = 0; s < 4; ++s) {
        const v4u bfr = *(LAS v4u*)(p + 64 * s);
        a0 = mfma32(ao[s], bfr, a0);
        a1 = mfma32(ao[4 + s], bfr, a1);
      }
      if (col & 2) a0 = a1;
      if (col < 4)
        st_sc1(rs, WS_OPT + ((16 * mb + 2 * w + (col >> 1)) * 64 + 2 * h + ca) * 128 + ((col & 1) * 4 + g4) * 16,
               v4u{pk(a0[0], a0[1]), T, pk(a0[2], a0[3]), T});
    }
    STAMP(9);
  } else {
    const int a = r - 16, b = xcd >> 2, n = 8 * (xcd & 3) + (a >> 1), g = a & 1;
    unsigned ep;
    gld4_sc1(ep, ws, 0u);
#pragma unroll
    for (int i = 0; i < 16; ++i) {
      const int row = 16 * w + ((i + 8 * g) & 15);
      dma16(ckv, (unsigned)(((b * SK + 128 * n + row) * DC + 8 * l) * 2), smb + L_C + row * CP);
    }
    asm volatile("s_waitcnt vmcnt(0)" : "+v"(ep)::"memory");
    const unsigned T = ep + 1u;
    BAR();
    STAMP(1);
    v4u fr[32];
    if (w < 4) {
      LAS char* p = sm + L_C + (32 * w + col) * CP + 16 * g4;
#pragma unroll
      for (int kt = 0; kt < 2; ++kt)
#pragma unroll
        for (int s = 0; s < 16; ++s) fr[16 * kt + s] = *(LAS v4u*)(p + 16 * kt * CP + 64 * s);
    } else {
      LAS char* p = sm + L_C + (4 * g4 + tq) * CP + 256 * (w - 4) + 8 * tp;
#pragma unroll
      for (int ct = 0; ct < 8; ++ct)
#pragma unroll
        for (int u = 0; u < 4; ++u) fr[4 * ct + u] = trrd2(p + 32 * u * CP + 32 * ct, p + (32 * u + 16) * CP + 32 * ct);
    }
    STAMP(2);
    {
      const int o = WS_QA + (b * NH + 16 * g) * 2048 + tid * 16;
      v4u q[4];
      for (unsigned spin = 0;; ++spin) {
        asm volatile("" ::: "memory");
#pragma unroll
        for (int i = 0; i < 4; ++i) q[i] = ld_sc1(rs, o + 8192 * i);
        bool ok = true;
#pragma unroll
        for (int i = 0; i < 4; ++i) ok &= (q[i].y == T) & (q[i].w == T);
        if (__all(ok) || spin > SPIN_CAP) break;
      }
      STAMP(3);
#pragma unroll
      for (int i = 0; i < 4; ++i) *(LAS v2u*)(sm + L_Q + (4 * i + (tid >> 7)) * CP + (tid & 127) * 8) = v2u{q[i].x, q[i].z};
    }
    BAR();
    STAMP(4);
    if (w < 4) {
      f4 s0 = {0.f, 0.f, 0.f, 0.f}, s1 = s0;
      LAS char* p = sm + L_Q + col * CP + 16 * g4;
      v4u qf[16];
#pragma unroll
      for (int s = 0; s < 16; ++s) qf[s] = *(LAS v4u*)(p + 64 * s);
      __builtin_amdgcn_sched_barrier(0);
#pragma unroll
      for (int s = 0; s < 16; ++s) {
        s0 = mfma32(fr[s], qf[s], s0);
        s1 = mfma32(fr[16 + s], qf[s], s1);
      }
      float mx = fmax2(fmax2(fmax2(s0[0], s0[1]), fmax2(s0[2], s0[3])), fmax2(fmax2(s1[0], s1[1]), fmax2(s1[2], s1[3])));
      mx = sw32_max(sw16_max(mx));
      if (g4 == 0) *(LAS float*)(sm + L_MX + col * 16 + w * 4) = mx;
      STAMP(5);
      BAR();
      const f4 m4 = *(LAS f4*)(sm + L_MX + col * 16);
      const float M = fmax2(fmax2(m4[0], m4[1]), fmax2(m4[2], m4[3]));
      float ps = 0.f;
#pragma unroll
      for (int e = 0; e < 4; ++e) {
        s0[e] = __builtin_amdgcn_exp2f(s0[e] - M);
        s1[e] = __builtin_amdgcn_exp2f(s1[e] - M);
        ps += s0[e] + s1[e];
      }
      *(LAS v4u*)(sm + L_P + w * 1024 + l * 16) = v4u{pk(s0[0], s0[1]), pk(s0[2], s0[3]), pk(s1[0], s1[1]), pk(s1[2], s1[3])};
      ps = sw16_add(ps, ps);
      ps = sw32_add(ps, ps);
      if (g4 == 0) *(LAS float*)(sm + L_LS + col * 16 + w * 4) = ps;
      BAR();
      STAMP(6);
      if (w == 0) {
        const f4 l4 = *(LAS f4*)(sm + L_LS + col * 16);
        const float lse = M + __builtin_amdgcn_logf((l4[0] + l4[1]) + (l4[2] + l4[3]));
        if (g4 == 0)
          __builtin_amdgcn_raw_buffer_store_b64(v2u{__builtin_bit_cast(unsigned, lse), T}, rs,
                                                WS_LSE + ((b * 2 + g) * 32 + n) * 128 + col * 8, 0, AUX_SC1);
      }
    } else {
      BAR();
      BAR();
      const int v = w - 4;
      v4u pf[4];
#pragma unroll
      for (int u = 0; u < 4; ++u) pf[u] = *(LAS v4u*)(sm + L_P + u * 1024 + l * 16);
      const f4 l4 = *(LAS f4*)(sm + L_LS + col * 16);
      const float il = __builtin_amdgcn_rcpf((l4[0] + l4[1]) + (l4[2] + l4[3]));
      const int o = WS_PART + ((((16 * g + (col & 7)) * 2 + (v >> 1)) * 2 + b) * 32 + n) * 512 + 256 * (v & 1) + (col >> 3) * 64 +
                    (g4 & 1) * 32 + (g4 >> 1) * 16;
#pragma unroll
      for (int pp = 0; pp < 2; ++pp) {
        v4u R[2];
#pragma unroll
        for (int k = 0; k < 2; ++k) {
          const int pr = 2 * pp + k;
          f4 c0 = {0.f, 0.f, 0.f, 0.f}, c1 = c0;
#pragma unroll
          for (int u = 0; u < 4; ++u) {
            c0 = mfma32(fr[8 * pr + u], pf[u], c0);
            c1 = mfma32(fr[8 * pr + 4 + u], pf[u], c1);
          }
          const unsigned x0 = min(pk(c0[0] * il, c0[1] * il), 0xfffffffeu), x1 = min(pk(c0[2] * il, c0[3] * il), 0xfffffffeu);
          const unsigned y0 = min(pk(c1[0] * il, c1[1] * il), 0xfffffffeu), y1 = min(pk(c1[2] * il, c1[3] * il), 0xfffffffeu);
          auto r0 = __builtin_amdgcn_permlane16_swap(x0, y0, false, false);
          auto r1 = __builtin_amdgcn_permlane16_swap(x1, y1, false, false);
          R[k] = v4u{r0[0], r1[0], r0[1], r1[1]};
        }
        v4u A, B;
#pragma unroll
        for (int e = 0; e < 4; ++e) {
          A[e] = (unsigned)__builtin_amdgcn_update_dpp((int)R[0][e], (int)R[1][e], ROR8, 0xf, 0xc, false);
          B[e] = (unsigned)__builtin_amdgcn_update_dpp((int)R[1][e], (int)R[0][e], ROR8, 0xf, 0x3, false);
        }
        st_sc1(rs, o + 128 * pp, A);
        st_sc1(rs, o + 128 * pp + 8 * 65536, B);
      }
      STAMP(7);
    }
    const int sl = 12 * xcd + a;
    if (a < 12) {
      if (sl < NSL) {
        BAR();
        v4u xr0, xr1, xf[8];
#pragma unroll
        for (int i = 0; i < 2; ++i) {
          const int k = 2 * w + i;
          const unsigned o = (unsigned)(((32 * k + (l >> 1)) * FF + 16 * sl + 8 * (l & 1)) * 2);
          dma16(wg, o, smb + L_GG + k * 1024);
          dma16(wu, o, smb + L_GU + k * 1024);
          dma16(wd, (unsigned)(((16 * sl + k) * DM + 8 * l) * 2), smb + L_GD + k * CP);
        }
        gld16(xr0, x, l * 16u);
        gld16(xr1, x, 1024u + l * 16u);
#pragma unroll
        for (int s = 0; s < 8; ++s) gld16(xf[s], x, (unsigned)(((col & 1) * DM + 256 * (w & 1) + 32 * s + 8 * g4) * 2));
        asm volatile("s_waitcnt vmcnt(0)"
                     : "+v"(xr0), "+v"(xr1), "+v"(xf[0]), "+v"(xf[1]), "+v"(xf[2]), "+v"(xf[3]), "+v"(xf[4]), "+v"(xf[5]), "+v"(xf[6]),
                       "+v"(xf[7])::"memory");
        BAR();
        STAMP(8);
        if (w < 4) {
          if (col & 2) {
#pragma unroll
            for (int s = 0; s < 8; ++s) xf[s] = v4u{0x3f803f80u, 0x3f803f80u, 0x3f803f80u, 0x3f803f80u};
          }
          LAS char* p = sm + L_GG + (w >> 1) * 16384 + 32 * (256 * (w & 1) + 8 * g4 + tq) + 8 * tp;
          f4 acc = {0.f, 0.f, 0.f, 0.f};
#pragma unroll
          for (int s = 0; s < 8; ++s) acc = mfma32(trrd2(p + 32 * 32 * s, p + 32 * (32 * s + 4)), xf[s], acc);
          *(LAS f4*)(sm + L_GR + w * 1024 + l * 16) = acc;
        }
        float m0, r0, m1, r1;
        ln_stats(xr0, m0, r0);
        ln_stats(xr1, m1, r1);
        const float mean = (col & 1) ? m1 : m0, rstd = (col & 1) ? r1 : r0;
        v2u dn[4];
#pragma unroll
        for (int t = 0; t < 4; ++t) dn[t] = trrd(sm + L_GD + (4 * g4 + tq) * CP + 32 * (4 * w + t) + 8 * tp);
        BAR();
        LAS char* pr = sm + L_GR + (16 * g4 + (col & 1)) * 16;
        LAS char* po = sm + L_GR + (16 * g4 + 2) * 16;
        const f4 gx = *(LAS f4*)(pr) + *(LAS f4*)(pr + 1024), go = *(LAS f4*)(po) + *(LAS f4*)(po + 1024);
        const f4 ux = *(LAS f4*)(pr + 2048) + *(LAS f4*)(pr + 3072), uo = *(LAS f4*)(po + 2048) + *(LAS f4*)(po + 3072);
        float hh[4];
#pragma unroll
        for (int e = 0; e < 4; ++e) {
          const float gv = rstd * (gx[e] - mean * go[e]), uv = rstd * (ux[e] - mean * uo[e]);
          hh[e] = uv * __builtin_amdgcn_rcpf(1.f + __builtin_amdgcn_exp2f(-1.4426950408889634f * gv));
        }
        const v2u hb = {pk(hh[0], hh[1]), pk(hh[2], hh[3])};
        const f4 z = {0.f, 0.f, 0.f, 0.f};
        f4 d0 = mfma16(dn[0], hb, z), d1 = mfma16(dn[1], hb, z), d2 = mfma16(dn[2], hb, z), d3 = mfma16(dn[3], hb, z);
        if (col & 2) {
          d0 = d1;
          d2 = d3;
        }
        if (col & 4) d0 = d2;
        if (col < 8)
          st_sc1(rs, WS_OPG + ((4 * w + (col >> 1)) * 96 + sl) * 128 + ((col & 1) * 4 + g4) * 16,
                 v4u{pk(d0[0], d0[1]), T, pk(d0[2], d0[3]), T});
        STAMP(9);
      }
    } else {
      const int rd = 4 * xcd + (a - 12);
      const int ln = tid >> 3;
      const int ot = WS_OPT + rd * 8192 + tid * 16, og = WS_OPG + rd * 96 * 128 + tid * 16;
      const bool two = ln < NSL - 64;
      const int og2 = two ? og + 8192 : og;
      float xres = 0.f;
      if (tid < 32) xres = blo(x[(tid >> 4) * DM + 16 * rd + (tid & 15)]);
      v4u p0, p1, p2;
      for (unsigned spin = 0;; ++spin) {
        asm volatile("" ::: "memory");
        p0 = ld_sc1(rs, ot);
        p1 = ld_sc1(rs, og);
        p2 = ld_sc1(rs, og2);
        const bool ok = (p0.y == T) & (p0.w == T) & (p1.y == T) & (p1.w == T) & (p2.y == T) & (p2.w == T);
        if (__all(ok) || spin > SPIN_CAP) break;
      }
      STAMP(8);
      if (!two) p2 = v4u{0u, 0u, 0u, 0u};
      const float v0 = blo(p0.x) + (blo(p1.x) + blo(p2.x)), v1 = bhi(p0.x) + (bhi(p1.x) + bhi(p2.x));
      const float v2 = blo(p0.z) + (blo(p1.z) + blo(p2.z)), v3 = bhi(p0.z) + (bhi(p1.z) + bhi(p2.z));
      float s = sw16_add(sw32_add(v0, v2), sw32_add(v1, v3));
      s += dpp<ROR8>(s);
      if (!(l & 8)) *(LAS float*)(sm + L_RR + w * 128 + (4 * (l & 7) + g4) * 4) = s;
      BAR();
      if (tid < 32) {
        float t = xres;
#pragma unroll
        for (int i = 0; i < 8; ++i) t += *(LAS float*)(sm + L_RR + i * 128 + tid * 4);
        out[(tid >> 4) * DM + 16 * rd + (tid & 15)] = (u16)(pk(t, t) & 0xffffu);
        if (rd == 0 && tid == 0) __builtin_amdgcn_raw_buffer_store_b32(T, rs, WS_E, 0, AUX_SC1);
      }
      STAMP(9);
    }
  }
  STAMP_FLUSH();
}

extern "C" void kernel_launch(void* const* d_in, const int* in_sizes, int n_in, void* d_out, int out_size, void* d_ws, size_t ws_size,
                              hipStream_t stream) {
  mla_glu_decode<<<256, 512, 0, stream>>>((const u16*)d_in[0], (const u16*)d_in[1], (const u16*)d_in[2], (const u16*)d_in[3],
                                          (const u16*)d_in[4], (const u16*)d_in[5], (const u16*)d_in[7], (const u16*)d_in[8],
                                          (const u16*)d_in[9], (u16*)d_out, (char*)d_ws);
}
```

```cpp
#define PROBE_CLASS 0
#define PROBE_IDX 0
#define PROBE_WAVE 0
#define PROBE_K 0
#include <hip/hip_runtime.h>
#include <stdint.h>

#define GAS __attribute__((address_space(1)))
#define LAS __attribute__((address_space(3)))
typedef unsigned short u16;
typedef unsigned v4u __attribute__((ext_vector_type(4)));
typedef unsigned v2u __attribute__((ext_vector_type(2)));
typedef float f4 __attribute__((ext_vector_type(4)));
typedef float f2 __attribute__((ext_vector_type(2)));
typedef __bf16 bf8 __attribute__((ext_vector_type(8)));
typedef __bf16 bf2 __attribute__((ext_vector_type(2)));
typedef short s4 __attribute__((ext_vector_type(4)));

namespace {
constexpr int NH = 32, SK = 4096, DH = 128, DM = 512, DC = 512, FF = 1376;
constexpr int NSL = FF / 16;
constexpr int WS_E = 0;
constexpr int WS_QP = 4096;
constexpr int WS_QA = WS_QP + NH * 4 * 1024;
constexpr int WS_LSE = WS_QA + 2 * NH * 2048;
constexpr int WS_OPT = WS_LSE + 2 * 2 * 32 * 128;
constexpr int WS_OPG = WS_OPT + 32 * 64 * 128;
constexpr int WS_PART = WS_OPG + 32 * 96 * 128;
constexpr int WS_BYTES = WS_PART + NH * 2 * 2 * 32 * 512;
constexpr int WS_STAMP = 8 << 20;
constexpr int CP = 1056;
constexpr int L_C = 0, L_Q = 128 * CP, L_P = L_Q + 16 * CP, L_MX = L_P + 4096, L_LS = L_MX + 256, L_RR = L_LS + 256,
              L_END = L_RR + 1024;
constexpr int L_GG = 0, L_GU = 16384, L_GD = 32768, L_GR = 65536;
constexpr int L_WUV = 0, L_WO = 65536, L_WQ = 65536, L_WUK = 98304, L_CC = 131072, CCP = 576, L_CT = L_CC + 2048, CTP = 320;
constexpr unsigned SPIN_CAP = 1u << 20;
constexpr int AUX_SC1 = 16;

#define STAMPS
#define STAMP(i) asm volatile("s_memrealtime %0\n\ts_waitcnt lgkmcnt(0)" : "=s"(ts[i])::"memory")
#define STAMP_FLUSH()                                                                      \
  do {                                                                                     \
    const int cls_ = r < 16 ? 0 : (r < 28 ? 1 : 2);                                        \
    if (cls_ == PROBE_CLASS && w == PROBE_WAVE && ts[PROBE_IDX] != 0) {                    \
      const unsigned long long until_ = tstart + 4000ull + PROBE_K * (ts[PROBE_IDX] - tstart);  \
      while (__builtin_amdgcn_s_memrealtime() < until_) __builtin_amdgcn_s_sleep(1);       \
    }                                                                                      \
  } while (0)

__device__ __forceinline__ f4 mfma32(v4u a, v4u b, f4 c) {
  return __builtin_amdgcn_mfma_f32_16x16x32_bf16(__builtin_bit_cast(bf8, a), __builtin_bit_cast(bf8, b), c, 0, 0, 0);
}
__device__ __forceinline__ f4 mfma16(v2u a, v2u b, f4 c) {
  return __builtin_amdgcn_mfma_f32_16x16x16bf16_1k(__builtin_bit_cast(s4, a), __builtin_bit_cast(s4, b), c, 0, 0, 0);
}
__device__ __forceinline__ unsigned pk(float lo, float hi) {
  f2 x = {lo, hi};
  return __builtin_bit_cast(unsigned, __builtin_convertvector(x, bf2));
}
__device__ __forceinline__ float asf(unsigned u) { return __builtin_bit_cast(float, u); }
__device__ __forceinline__ float blo(unsigned u) { return __builtin_bit_cast(float, u << 16); }
__device__ __forceinline__ float bhi(unsigned u) { return __builtin_bit_cast(float, u & 0xffff0000u); }
template <int CTRL> __device__ __forceinline__ float dpp(float v) {
  return __builtin_bit_cast(float, __builtin_amdgcn_update_dpp(0, __builtin_bit_cast(int, v), CTRL, 0xf, 0xf, true));
}
__device__ __forceinline__ float fmax2(float a, float b) { return __builtin_amdgcn_fmed3f(a, b, __builtin_inff()); }
constexpr int ROR1 = 0x121, ROR2 = 0x122, ROR4 = 0x124, ROR8 = 0x128, QP_2301 = 0x4e, QP_2222 = 0xaa;
__device__ __forceinline__ float sw32_add(float a, float b) {
  auto r = __builtin_amdgcn_permlane32_swap(__builtin_bit_cast(unsigned, a), __builtin_bit_cast(unsigned, b), false, false);
  return asf(r[0]) + asf(r[1]);
}
__device__ __forceinline__ float sw16_add(float a, float b) {
  auto r = __builtin_amdgcn_permlane16_swap(__builtin_bit_cast(unsigned, a), __builtin_bit_cast(unsigned, b), false, false);
  return asf(r[0]) + asf(r[1]);
}
__device__ __forceinline__ float sw32_max(float a) {
  auto r = __builtin_amdgcn_permlane32_swap(__builtin_bit_cast(unsigned, a), __builtin_bit_cast(unsigned, a), false, false);
  return fmax2(asf(r[0]), asf(r[1]));
}
__device__ __forceinline__ float sw16_max(float a) {
  auto r = __builtin_amdgcn_permlane16_swap(__builtin_bit_cast(unsigned, a), __builtin_bit_cast(unsigned, a), false, false);
  return fmax2(asf(r[0]), asf(r[1]));
}
__device__ __forceinline__ float wave_sum(float v) {
  v += dpp<ROR8>(v);
  v += dpp<ROR4>(v);
  v += dpp<ROR2>(v);
  v += dpp<ROR1>(v);
  v = sw16_add(v, v);
  return sw32_add(v, v);
}
__device__ __forceinline__ v2u trrd(LAS char* p) {
  return __builtin_bit_cast(v2u, __builtin_amdgcn_ds_read_tr16_b64_v4i16((LAS s4*)p));
}
__device__ __forceinline__ v4u trrd2(LAS char* p0, LAS char* p1) {
  v2u a = trrd(p0), b = trrd(p1);
  return v4u{a.x, a.y, b.x, b.y};
}
#define BAR() asm volatile("s_waitcnt lgkmcnt(0)\n\ts_barrier" ::: "memory")
#define VMWAIT(n) asm volatile("s_waitcnt vmcnt(" #n ")" ::: "memory")
__device__ __forceinline__ void gld16(v4u& d, const void* base, unsigned off) {
  asm volatile("global_load_dwordx4 %0, %1, %2" : "=v"(d) : "v"(off), "s"(base) : "memory");
}
__device__ __forceinline__ void gld4_sc1(unsigned& d, const void* base, unsigned off) {
  asm volatile("global_load_dword %0, %1, %2 sc1" : "=v"(d) : "v"(off), "s"(base) : "memory");
}
__device__ __forceinline__ void dma16(const void* base, unsigned off, unsigned lds) {
  unsigned keep;
  asm volatile("s_mov_b32 %0, m0\n\ts_mov_b32 m0, %3\n\ts_nop 0\n\tglobal_load_lds_dwordx4 %1, %2\n\ts_mov_b32 m0, %0"
               : "=&s"(keep)
               : "v"(off), "s"(base), "s"(lds)
               : "memory");
}
__device__ __forceinline__ v4u ld_sc1(__amdgpu_buffer_rsrc_t rs, int off) {
  return __builtin_amdgcn_raw_buffer_load_b128(rs, off, 0, AUX_SC1);
}
__device__ __forceinline__ v2u ld8_sc1(__amdgpu_buffer_rsrc_t rs, int off) {
  return __builtin_amdgcn_raw_buffer_load_b64(rs, off, 0, AUX_SC1);
}
__device__ __forceinline__ void st_sc1(__amdgpu_buffer_rsrc_t rs, int off, v4u v) {
  __builtin_amdgcn_raw_buffer_store_b128(v, rs, off, 0, AUX_SC1);
}
__device__ __forceinline__ void ln_stats(v4u r, float& mean, float& rstd) {
  float e[8] = {blo(r.x), bhi(r.x), blo(r.y), bhi(r.y), blo(r.z), bhi(r.z), blo(r.w), bhi(r.w)};
  float s = ((e[0] + e[1]) + (e[2] + e[3])) + ((e[4] + e[5]) + (e[6] + e[7]));
  mean = wave_sum(s) * (1.f / DM);
  float q = 0.f;
#pragma unroll
  for (int i = 0; i < 8; ++i) {
    float d = e[i] - mean;
    q = fmaf(d, d, q);
  }
  rstd = __builtin_amdgcn_rsqf(wave_sum(q) * (1.f / DM) + 1e-5f);
}
}

__global__ void __launch_bounds__(512, 2)
mla_glu_decode(const u16* __restrict__ x, const u16* __restrict__ ckv, const u16* __restrict__ wq, const u16* __restrict__ wuk,
               const u16* __restrict__ wuv, const u16* __restrict__ wo, const u16* __restrict__ wg, const u16* __restrict__ wu,
               const u16* __restrict__ wd, u16* __restrict__ out, char* __restrict__ ws) {
  const unsigned long long tstart = __builtin_amdgcn_s_memrealtime();
  __builtin_amdgcn_sched_barrier(0);
  __shared__ __attribute__((aligned(16))) char smem_[L_END];
  LAS char* sm = (LAS char*)smem_;
  const unsigned smb = (unsigned)(size_t)sm;
  const int tid = threadIdx.x, l = tid & 63, w = __builtin_amdgcn_readfirstlane(tid >> 6);
  const int col = l & 15, g4 = l >> 4, tq = (l & 15) >> 2, tp = l & 3;
  const int xcd = blockIdx.x & 7, r = blockIdx.x >> 3;
  const __amdgpu_buffer_rsrc_t rs = __builtin_amdgcn_make_buffer_rsrc(ws, 0, WS_BYTES, 0x00020000);
  asm volatile("" ::"s"(x), "s"(ckv), "s"(wq), "s"(wuk), "s"(wuv), "s"(wo), "s"(wg), "s"(wu), "s"(wd), "s"(out), "s"(ws));
  unsigned long long ts[10] = {0, 0, 0, 0, 0, 0, 0, 0, 0, 0};
  STAMP(0);

  if (__builtin_expect(r < 16, 1)) {
    const int h = 4 * xcd + (r >> 2), j = r & 3, ca = j >> 1, mb = j & 1;
    v4u xr0, xr1, xf[4];
    unsigned ep;
    gld4_sc1(ep, ws, 0u);
    gld16(xr0, x, l * 16u);
    gld16(xr1, x, 1024u + l * 16u);
#pragma unroll
    for (int s = 0; s < 4; ++s) gld16(xf[s], x, (unsigned)(((col & 1) * DM + 128 * j + 32 * s + 8 * g4) * 2));
#pragma unroll
    for (int i = 0; i < 4; ++i) {
      const int k = 4 * w + i, row = 4 * k + g4, f = (row & 3) | ((row >> 1) & 4);
      dma16(wq, (unsigned)(((128 * j + row) * 4096 + h * DH + 8 * (col ^ (f << 1))) * 2), smb + L_WQ + k * 1024);
    }
    if (w < 4) {
#pragma unroll
      for (int i = 0; i < 8; ++i) {
        const int row = 32 * w + 4 * i + g4;
        dma16(wuk, (unsigned)((((128 * j + row) * NH + h) * DH + 8 * (col ^ (row & 15))) * 2), smb + L_WUK + (8 * w + i) * 1024);
      }
      VMWAIT(12);
    } else {
      const v4u ones = {~0u, ~0u, ~0u, ~0u};
      const int cb = WS_PART + ((h * 2 + ca) * 2 + mb) * 16384 + (tid - 256) * 16;
#pragma unroll
      for (int i = 0; i < 4; ++i) st_sc1(rs, cb + 4096 * i, ones);
      VMWAIT(8);
    }
    asm volatile("" : "+v"(ep), "+v"(xr0), "+v"(xr1), "+v"(xf[0]), "+v"(xf[1]), "+v"(xf[2]), "+v"(xf[3])::"memory");
    const unsigned T = ep + 1u;
    float m0, r0, m1, r1;
    ln_stats(xr0, m0, r0);
    ln_stats(xr1, m1, r1);
    const float mean = (col & 1) ? m1 : m0, rstd = (col & 1) ? r1 : r0;
    if (col & 2) {
#pragma unroll
      for (int s = 0; s < 4; ++s) xf[s] = v4u{0x3f803f80u, 0x3f803f80u, 0x3f803f80u, 0x3f803f80u};
    }
    STAMP(1);
    if (w < 4) VMWAIT(8);
    else VMWAIT(0);
    BAR();
    STAMP(2);
    const int fl = tq | ((g4 & 1) << 2);
    {
      LAS char* p = sm + L_WQ + 256 * (8 * g4 + tq) + 16 * ((2 * w + (tp >> 1)) ^ (fl << 1)) + 8 * (tp & 1);
      v4u af[4];
#pragma unroll
      for (int s = 0; s < 4; ++s) af[s] = trrd2(p + 256 * 32 * s, p + 256 * (32 * s + 4));
      __builtin_amdgcn_sched_barrier(0);
      f4 acc = {0.f, 0.f, 0.f, 0.f};
#pragma unroll
      for (int s = 0; s < 4; ++s) acc = mfma32(af[s], xf[s], acc);
      float q[4];
#pragma unroll
      for (int e = 0; e < 4; ++e) q[e] = rstd * (acc[e] - mean * dpp<QP_2222>(acc[e]));
      if (col < 2) st_sc1(rs, WS_QP + ((h * 4 + j) * 8 + w) * 128 + col * 64 + g4 * 16, v4u{pk(q[0], q[1]), T, pk(q[2], q[3]), T});
    }
    STAMP(3);
    if (w < 4) {
      VMWAIT(1);
      v4u uk[8];
#pragma unroll
      for (int t = 0; t < 2; ++t)
#pragma unroll
        for (int s = 0; s < 4; ++s) uk[4 * t + s] = *(LAS v4u*)(sm + L_WUK + 256 * (32 * w + 16 * t + col) + 16 * ((4 * s + g4) ^ col));
      const int o = WS_QP + ((h * 4 + ((col >> 1) & 3)) * 8 + (g4 >> 1)) * 128 + (col & 1) * 64 + (g4 & 1) * 32;
      v4u a[8];
      for (unsigned spin = 0;; ++spin) {
        asm volatile("" ::: "memory");
#pragma unroll
        for (int i = 0; i < 8; ++i) a[i] = ld_sc1(rs, o + 256 * (i >> 1) + 16 * (i & 1));
        bool ok = true;
#pragma unroll
        for (int i = 0; i < 8; ++i) ok &= (a[i].y == T) & (a[i].w == T);
        if (__all(ok) || spin > SPIN_CAP) break;
      }
      STAMP(4);
      f4 a0 = {0.f, 0.f, 0.f, 0.f}, a1 = a0;
#pragma unroll
      for (int s = 0; s < 4; ++s) {
        const v4u qf = {a[2 * s].x, a[2 * s].z, a[2 * s + 1].x, a[2 * s + 1].z};
        a0 = mfma32(uk[s], qf, a0);
        a1 = mfma32(uk[4 + s], qf, a1);
      }
      float q[4];
#pragma unroll
      for (int e = 0; e < 4; ++e) {
        float v0 = a0[e] + dpp<QP_2301>(a0[e]), v1 = a1[e] + dpp<QP_2301>(a1[e]);
        float v = (col & 2) ? v1 : v0;
        v += dpp<ROR4>(v);
        q[e] = v * (0.08838834764831845f * 1.4426950408889634f);
      }
      if (col < 4)
        st_sc1(rs, WS_QA + ((col & 1) * NH + h) * 2048 + (64 * j + 16 * w + 8 * (col >> 1) + 2 * g4) * 8,
               v4u{pk(q[0], q[1]), T, pk(q[2], q[3]), T});
    }
    STAMP(5);
    BAR();
#pragma unroll
    for (int i = 0; i < 8; ++i) {
      const int k = 8 * w + ((i + 4 * mb) & 7), row = 4 * k + g4, f = (row & 3) | ((row >> 1) & 4);
      dma16(wuv, (unsigned)((((256 * ca + row) * NH + h) * DH + 8 * (col ^ (f << 1))) * 2), smb + L_WUV + k * 1024);
    }
#pragma unroll
    for (int i = 0; i < 8; ++i) {
      const int k = 8 * w + ((i + 4 * ca) & 7), row = 2 * k + (l >> 5), f = (row & 3) | ((row >> 1) & 4);
      dma16(wo, (unsigned)(((h * DH + row) * DM + 256 * mb + 8 * ((l & 31) ^ (f << 1))) * 2), smb + L_WO + k * 1024);
    }
    VMWAIT(0);
    BAR();
    STAMP(6);
    v4u av[8], ao[8];
    {
      LAS char* p = sm + L_WUV + 256 * (8 * g4 + tq) + 16 * ((2 * w + (tp >> 1)) ^ (fl << 1)) + 8 * (tp & 1);
#pragma unroll
      for (int s = 0; s < 8; ++s) av[s] = trrd2(p + 256 * 32 * s, p + 256 * (32 * s + 4));
#pragma unroll
      for (int t = 0; t < 2; ++t) {
        LAS char* po = sm + L_WO + 512 * (8 * g4 + tq) + 16 * ((4 * w + 2 * t + (tp >> 1)) ^ (fl << 1)) + 8 * (tp & 1);
#pragma unroll
        for (int s = 0; s < 4; ++s) ao[4 * t + s] = trrd2(po + 512 * 32 * s, po + 512 * (32 * s + 4));
      }
    }
    STAMP(7);
    {
      const int b = w >> 2, cw = w & 3, nr = l >> 3;
      const int o = WS_PART + (((h * 2 + ca) * 2 + b) * 32 + nr) * 512 + 128 * cw + 16 * (l & 7);
      const int ol = WS_LSE + ((b * 2 + (h >> 4)) * 32 + nr) * 128 + (h & 15) * 8;
      v4u a[4];
      v2u ls[4];
      for (unsigned spin = 0;; ++spin) {
        asm volatile("" ::: "memory");
#pragma unroll
        for (int i = 0; i < 4; ++i) ls[i] = ld8_sc1(rs, ol + 1024 * i);
#pragma unroll
        for (int i = 0; i < 4; ++i) a[i] = ld_sc1(rs, o + 4096 * i);
        bool ok = true;
        unsigned mx = 0u;
#pragma unroll
        for (int i = 0; i < 4; ++i) {
          ok &= ls[i].y == T;
          mx = max(max(mx, a[i].x), max(a[i].y, max(a[i].z, a[i].w)));
        }
        ok &= mx != ~0u;
        if (__all(ok) || spin > SPIN_CAP) break;
      }
      STAMP(8);
      float e[4];
#pragma unroll
      for (int i = 0; i < 4; ++i) e[i] = asf(ls[i].x);
      float M = fmax2(fmax2(e[0], e[1]), fmax2(e[2], e[3]));
      M = fmax2(M, dpp<ROR8>(M));
      M = sw32_max(sw16_max(M));
#pragma unroll
      for (int i = 0; i < 4; ++i) e[i] = __builtin_amdgcn_exp2f(e[i] - M);
      float L = (e[0] + e[1]) + (e[2] + e[3]);
      L += dpp<ROR8>(L);
      L = sw16_add(L, L);
      L = sw32_add(L, L);
      float acc[8];
#pragma unroll
      for (int c = 0; c < 4; ++c) {
        acc[2 * c] = e[0] * blo(a[0][c]);
        acc[2 * c + 1] = e[0] * bhi(a[0][c]);
      }
#pragma unroll
      for (int i = 1; i < 4; ++i)
#pragma unroll
        for (int c = 0; c < 4; ++c) {
          acc[2 * c] = fmaf(e[i], blo(a[i][c]), acc[2 * c]);
          acc[2 * c + 1] = fmaf(e[i], bhi(a[i][c]), acc[2 * c + 1]);
        }
      float rr[4];
#pragma unroll
      for (int c = 0; c < 4; ++c) rr[c] = sw32_add(acc[c], acc[c + 4]);
      float s0 = sw16_add(rr[0], rr[2]), s1 = sw16_add(rr[1], rr[3]);
      s0 += dpp<ROR8>(s0);
      s1 += dpp<ROR8>(s1);
      const float il = __builtin_amdgcn_rcpf(L);
      if (!(l & 8)) *(LAS unsigned*)(sm + L_CC + b * CCP + (64 * cw + 8 * (l & 7) + 2 * g4) * 2) = pk(s0 * il, s1 * il);
    }
    BAR();
    {
      f4 acc = {0.f, 0.f, 0.f, 0.f};
      LAS char* p = sm + L_CC + (col & 1) * CCP + 16 * g4;
#pragma unroll
      for (int s = 0; s < 8; ++s) acc = mfma32(av[s], *(LAS v4u*)(p + 64 * s), acc);
      if (col < 2) *(LAS v2u*)(sm + L_CT + col * CTP + (16 * w + 4 * g4) * 2) = v2u{pk(acc[0], acc[1]), pk(acc[2], acc[3])};
    }
    BAR();
    {
      f4 a0 = {0.f, 0.f, 0.f, 0.f}, a1 = a0;
      LAS char* p = sm + L_CT + (col & 1) * CTP + 16 * g4;
#pragma unroll
      for (int s = 0; s < 4; ++s) {
        const v4u bfr = *(LAS v4u*)(p + 64 * s);
        a0 = mfma32(ao[s], bfr, a0);
        a1 = mfma32(ao[4 + s], bfr, a1);
      }
      if (col & 2) a0 = a1;
      if (col < 4)
        st_sc1(rs, WS_OPT + ((16 * mb + 2 * w + (col >> 1)) * 64 + 2 * h + ca) * 128 + ((col & 1) * 4 + g4) * 16,
               v4u{pk(a0[0], a0[1]), T, pk(a0[2], a0[3]), T});
    }
    STAMP(9);
  } else {
    const int a = r - 16, b = xcd >> 2, n = 8 * (xcd & 3) + (a >> 1), g = a & 1;
    unsigned ep;
    gld4_sc1(ep, ws, 0u);
#pragma unroll
    for (int i = 0; i < 16; ++i) {
      const int row = 16 * w + ((i + 8 * g) & 15);
      dma16(ckv, (unsigned)(((b * SK + 128 * n + row) * DC + 8 * l) * 2), smb + L_C + row * CP);
    }
    asm volatile("s_waitcnt vmcnt(0)" : "+v"(ep)::"memory");
    const unsigned T = ep + 1u;
    BAR();
    STAMP(1);
    v4u fr[32];
    if (w < 4) {
      LAS char* p = sm + L_C + (32 * w + col) * CP + 16 * g4;
#pragma unroll
      for (int kt = 0; kt < 2; ++kt)
#pragma unroll
        for (int s = 0; s < 16; ++s) fr[16 * kt + s] = *(LAS v4u*)(p + 16 * kt * CP + 64 * s);
    } else {
      LAS char* p = sm + L_C + (4 * g4 + tq) * CP + 256 * (w - 4) + 8 * tp;
#pragma unroll
      for (int ct = 0; ct < 8; ++ct)
#pragma unroll
        for (int u = 0; u < 4; ++u) fr[4 * ct + u] = trrd2(p + 32 * u * CP + 32 * ct, p + (32 * u + 16) * CP + 32 * ct);
    }
    STAMP(2);
    {
      const int o = WS_QA + (b * NH + 16 * g) * 2048 + tid * 16;
      v4u q[4];
      for (unsigned spin = 0;; ++spin) {
        asm volatile("" ::: "memory");
#pragma unroll
        for (int i = 0; i < 4; ++i) q[i] = ld_sc1(rs, o + 8192 * i);
        bool ok = true;
#pragma unroll
        for (int i = 0; i < 4; ++i) ok &= (q[i].y == T) & (q[i].w == T);
        if (__all(ok) || spin > SPIN_CAP) break;
      }
      STAMP(3);
#pragma unroll
      for (int i = 0; i < 4; ++i) *(LAS v2u*)(sm + L_Q + (4 * i + (tid >> 7)) * CP + (tid & 127) * 8) = v2u{q[i].x, q[i].z};
    }
    BAR();
    STAMP(4);
    if (w < 4) {
      f4 s0 = {0.f, 0.f, 0.f, 0.f}, s1 = s0;
      LAS char* p = sm + L_Q + col * CP + 16 * g4;
      v4u qf[16];
#pragma unroll
      for (int s = 0; s < 16; ++s) qf[s] = *(LAS v4u*)(p + 64 * s);
      __builtin_amdgcn_sched_barrier(0);
#pragma unroll
      for (int s = 0; s < 16; ++s) {
        s0 = mfma32(fr[s], qf[s], s0);
        s1 = mfma32(fr[16 + s], qf[s], s1);
      }
      float mx = fmax2(fmax2(fmax2(s0[0], s0[1]), fmax2(s0[2], s0[3])), fmax2(fmax2(s1[0], s1[1]), fmax2(s1[2], s1[3])));
      mx = sw32_max(sw16_max(mx));
      if (g4 == 0) *(LAS float*)(sm + L_MX + col * 16 + w * 4) = mx;
      STAMP(5);
      BAR();
      const f4 m4 = *(LAS f4*)(sm + L_MX + col * 16);
      const float M = fmax2(fmax2(m4[0], m4[1]), fmax2(m4[2], m4[3]));
      float ps = 0.f;
#pragma unroll
      for (int e = 0; e < 4; ++e) {
        s0[e] = __builtin_amdgcn_exp2f(s0[e] - M);
        s1[e] = __builtin_amdgcn_exp2f(s1[e] - M);
        ps += s0[e] + s1[e];
      }
      *(LAS v4u*)(sm + L_P + w * 1024 + l * 16) = v4u{pk(s0[0], s0[1]), pk(s0[2], s0[3]), pk(s1[0], s1[1]), pk(s1[2], s1[3])};
      ps = sw16_add(ps, ps);
      ps = sw32_add(ps, ps);
      if (g4 == 0) *(LAS float*)(sm + L_LS + col * 16 + w * 4) = ps;
      BAR();
      STAMP(6);
      if (w == 0) {
        const f4 l4 = *(LAS f4*)(sm + L_LS + col * 16);
        const float lse = M + __builtin_amdgcn_logf((l4[0] + l4[1]) + (l4[2] + l4[3]));
        if (g4 == 0)
          __builtin_amdgcn_raw_buffer_store_b64(v2u{__builtin_bit_cast(unsigned, lse), T}, rs,
                                                WS_LSE + ((b * 2 + g) * 32 + n) * 128 + col * 8, 0, AUX_SC1);
      }
    } else {
      BAR();
      BAR();
      const int v = w - 4;
      v4u pf[4];
#pragma unroll
      for (int u = 0; u < 4; ++u) pf[u] = *(LAS v4u*)(sm + L_P + u * 1024 + l * 16);
      const f4 l4 = *(LAS f4*)(sm + L_LS + col * 16);
      const float il = __builtin_amdgcn_rcpf((l4[0] + l4[1]) + (l4[2] + l4[3]));
      const int o = WS_PART + ((((16 * g + (col & 7)) * 2 + (v >> 1)) * 2 + b) * 32 + n) * 512 + 256 * (v & 1) + (col >> 3) * 64 +
                    (g4 & 1) * 32 + (g4 >> 1) * 16;
#pragma unroll
      for (int pp = 0; pp < 2; ++pp) {
        v4u R[2];
#pragma unroll
        for (int k = 0; k < 2; ++k) {
          const int pr = 2 * pp + k;
          f4 c0 = {0.f, 0.f, 0.f, 0.f}, c1 = c0;
#pragma unroll
          for (int u = 0; u < 4; ++u) {
            c0 = mfma32(fr[8 * pr + u], pf[u], c0);
            c1 = mfma32(fr[8 * pr + 4 + u], pf[u], c1);
          }
          const unsigned x0 = min(pk(c0[0] * il, c0[1] * il), 0xfffffffeu), x1 = min(pk(c0[2] * il, c0[3] * il), 0xfffffffeu);
          const unsigned y0 = min(pk(c1[0] * il, c1[1] * il), 0xfffffffeu), y1 = min(pk(c1[2] * il, c1[3] * il), 0xfffffffeu);
          auto r0 = __builtin_amdgcn_permlane16_swap(x0, y0, false, false);
          auto r1 = __builtin_amdgcn_permlane16_swap(x1, y1, false, false);
          R[k] = v4u{r0[0], r1[0], r0[1], r1[1]};
        }
        v4u A, B;
#pragma unroll
        for (int e = 0; e < 4; ++e) {
          A[e] = (unsigned)__builtin_amdgcn_update_dpp((int)R[0][e], (int)R[1][e], ROR8, 0xf, 0xc, false);
          B[e] = (unsigned)__builtin_amdgcn_update_dpp((int)R[1][e], (int)R[0][e], ROR8, 0xf, 0x3, false);
        }
        st_sc1(rs, o + 128 * pp, A);
        st_sc1(rs, o + 128 * pp + 8 * 65536, B);
      }
      STAMP(7);
    }
    const int sl = 12 * xcd + a;
    if (a < 12) {
      if (sl < NSL) {
        BAR();
        v4u xr0, xr1, xf[8];
#pragma unroll
        for (int i = 0; i < 2; ++i) {
          const int k = 2 * w + i;
          const unsigned o = (unsigned)(((32 * k + (l >> 1)) * FF + 16 * sl + 8 * (l & 1)) * 2);
          dma16(wg, o, smb + L_GG + k * 1024);
          dma16(wu, o, smb + L_GU + k * 1024);
          dma16(wd, (unsigned)(((16 * sl + k) * DM + 8 * l) * 2), smb + L_GD + k * CP);
        }
        gld16(xr0, x, l * 16u);
        gld16(xr1, x, 1024u + l * 16u);
#pragma unroll
        for (int s = 0; s < 8; ++s) gld16(xf[s], x, (unsigned)(((col & 1) * DM + 256 * (w & 1) + 32 * s + 8 * g4) * 2));
        asm volatile("s_waitcnt vmcnt(0)"
                     : "+v"(xr0), "+v"(xr1), "+v"(xf[0]), "+v"(xf[1]), "+v"(xf[2]), "+v"(xf[3]), "+v"(xf[4]), "+v"(xf[5]), "+v"(xf[6]),
                       "+v"(xf[7])::"memory");
        BAR();
        STAMP(8);
        if (w < 4) {
          if (col & 2) {
#pragma unroll
            for (int s = 0; s < 8; ++s) xf[s] = v4u{0x3f803f80u, 0x3f803f80u, 0x3f803f80u, 0x3f803f80u};
          }
          LAS char* p = sm + L_GG + (w >> 1) * 16384 + 32 * (256 * (w & 1) + 8 * g4 + tq) + 8 * tp;
          f4 acc = {0.f, 0.f, 0.f, 0.f};
#pragma unroll
          for (int s = 0; s < 8; ++s) acc = mfma32(trrd2(p + 32 * 32 * s, p + 32 * (32 * s + 4)), xf[s], acc);
          *(LAS f4*)(sm + L_GR + w * 1024 + l * 16) = acc;
        }
        float m0, r0, m1, r1;
        ln_stats(xr0, m0, r0);
        ln_stats(xr1, m1, r1);
        const float mean = (col & 1) ? m1 : m0, rstd = (col & 1) ? r1 : r0;
        v2u dn[4];
#pragma unroll
        for (int t = 0; t < 4; ++t) dn[t] = trrd(sm + L_GD + (4 * g4 + tq) * CP + 32 * (4 * w + t) + 8 * tp);
        BAR();
        LAS char* pr = sm + L_GR + (16 * g4 + (col & 1)) * 16;
        LAS char* po = sm + L_GR + (16 * g4 + 2) * 16;
        const f4 gx = *(LAS f4*)(pr) + *(LAS f4*)(pr + 1024), go = *(LAS f4*)(po) + *(LAS f4*)(po + 1024);
        const f4 ux = *(LAS f4*)(pr + 2048) + *(LAS f4*)(pr + 3072), uo = *(LAS f4*)(po + 2048) + *(LAS f4*)(po + 3072);
        float hh[4];
#pragma unroll
        for (int e = 0; e < 4; ++e) {
          const float gv = rstd * (gx[e] - mean * go[e]), uv = rstd * (ux[e] - mean * uo[e]);
          hh[e] = uv * __builtin_amdgcn_rcpf(1.f + __builtin_amdgcn_exp2f(-1.4426950408889634f * gv));
        }
        const v2u hb = {pk(hh[0], hh[1]), pk(hh[2], hh[3])};
        const f4 z = {0.f, 0.f, 0.f, 0.f};
        f4 d0 = mfma16(dn[0], hb, z), d1 = mfma16(dn[1], hb, z), d2 = mfma16(dn[2], hb, z), d3 = mfma16(dn[3], hb, z);
        if (col & 2) {
          d0 = d1;
          d2 = d3;
        }
        if (col & 4) d0 = d2;
        if (col < 8)
          st_sc1(rs, WS_OPG + ((4 * w + (col >> 1)) * 96 + sl) * 128 + ((col & 1) * 4 + g4) * 16,
                 v4u{pk(d0[0], d0[1]), T, pk(d0[2], d0[3]), T});
        STAMP(9);
      }
    } else {
      const int rd = 4 * xcd + (a - 12);
      const int ln = tid >> 3;
      const int ot = WS_OPT + rd * 8192 + tid * 16, og = WS_OPG + rd * 96 * 128 + tid * 16;
      const bool two = ln < NSL - 64;
      const int og2 = two ? og + 8192 : og;
      float xres = 0.f;
      if (tid < 32) xres = blo(x[(tid >> 4) * DM + 16 * rd + (tid & 15)]);
      v4u p0, p1, p2;
      for (unsigned spin = 0;; ++spin) {
        asm volatile("" ::: "memory");
        p0 = ld_sc1(rs, ot);
        p1 = ld_sc1(rs, og);
        p2 = ld_sc1(rs, og2);
        const bool ok = (p0.y == T) & (p0.w == T) & (p1.y == T) & (p1.w == T) & (p2.y == T) & (p2.w == T);
        if (__all(ok) || spin > SPIN_CAP) break;
      }
      STAMP(8);
      if (!two) p2 = v4u{0u, 0u, 0u, 0u};
      const float v0 = blo(p0.x) + (blo(p1.x) + blo(p2.x)), v1 = bhi(p0.x) + (bhi(p1.x) + bhi(p2.x));
      const float v2 = blo(p0.z) + (blo(p1.z) + blo(p2.z)), v3 = bhi(p0.z) + (bhi(p1.z) + bhi(p2.z));
      float s = sw16_add(sw32_add(v0, v2), sw32_add(v1, v3));
      s += dpp<ROR8>(s);
      if (!(l & 8)) *(LAS float*)(sm + L_RR + w * 128 + (4 * (l & 7) + g4) * 4) = s;
      BAR();
      if (tid < 32) {
        float t = xres;
#pragma unroll
        for (int i = 0; i < 8; ++i) t += *(LAS float*)(sm + L_RR + i * 128 + tid * 4);
        out[(tid >> 4) * DM + 16 * rd + (tid & 15)] = (u16)(pk(t, t) & 0xffffu);
        if (rd == 0 && tid == 0) __builtin_amdgcn_raw_buffer_store_b32(T, rs, WS_E, 0, AUX_SC1);
      }
      STAMP(9);
    }
  }
  STAMP_FLUSH();
}

extern "C" void kernel_launch(void* const* d_in, const int* in_sizes, int n_in, void* d_out, int out_size, void* d_ws, size_t ws_size,
                              hipStream_t stream) {
  mla_glu_decode<<<256, 512, 0, stream>>>((const u16*)d_in[0], (const u16*)d_in[1], (const u16*)d_in[2], (const u16*)d_in[3],
                                          (const u16*)d_in[4], (const u16*)d_in[5], (const u16*)d_in[7], (const u16*)d_in[8],
                                          (const u16*)d_in[9], (u16*)d_out, (char*)d_ws);
}
```

```cpp
#include <hip/hip_runtime.h>
#include <stdint.h>

#define GAS __attribute__((address_space(1)))
#define LAS __attribute__((address_space(3)))
typedef unsigned short u16;
typedef unsigned v4u __attribute__((ext_vector_type(4)));
typedef unsigned v2u __attribute__((ext_vector_type(2)));
typedef float f4 __attribute__((ext_vector_type(4)));
typedef float f2 __attribute__((ext_vector_type(2)));
typedef __bf16 bf8 __attribute__((ext_vector_type(8)));
typedef __bf16 bf2 __attribute__((ext_vector_type(2)));
typedef short s4 __attribute__((ext_vector_type(4)));

namespace {
constexpr int NH = 32, SK = 4096, DH = 128, DM = 512, DC = 512, FF = 1376;
constexpr int NSL = FF / 16;
constexpr int WS_E = 0;
constexpr int WS_QP = 4096;
constexpr int WS_QA = WS_QP + NH * 4 * 1024;
constexpr int WS_LSE = WS_QA + 2 * NH * 2048;
constexpr int WS_OPT = WS_LSE + 2 * 2 * 32 * 128;
constexpr int WS_OPG = WS_OPT + 32 * 64 * 128;
constexpr int WS_PART = WS_OPG + 32 * 96 * 128;
constexpr int WS_BYTES = WS_PART + NH * 2 * 2 * 32 * 512;
constexpr int WS_STAMP = 8 << 20;
constexpr int CP = 1056;
constexpr int L_C = 0, L_Q = 128 * CP, L_P = L_Q + 16 * CP, L_MX = L_P + 4096, L_LS = L_MX + 256, L_RR = L_LS + 256,
              L_END = L_RR + 1024;
constexpr int L_GG = 0, L_GU = 16384, L_GD = 32768, L_GR = 65536;
constexpr int L_WUV = 0, L_WO = 65536, L_WQ = 65536, L_WUK = 98304, L_CC = 131072, CCP = 576, L_CT = L_CC + 2048, CTP = 320;
constexpr unsigned SPIN_CAP = 1u << 20;
constexpr int AUX_SC1 = 16;

#define STAMP(i)
#define STAMP_FLUSH()

__device__ __forceinline__ f4 mfma32(v4u a, v4u b, f4 c) {
  return __builtin_amdgcn_mfma_f32_16x16x32_bf16(__builtin_bit_cast(bf8, a), __builtin_bit_cast(bf8, b), c, 0, 0, 0);
}
__device__ __forceinline__ f4 mfma16(v2u a, v2u b, f4 c) {
  return __builtin_amdgcn_mfma_f32_16x16x16bf16_1k(__builtin_bit_cast(s4, a), __builtin_bit_cast(s4, b), c, 0, 0, 0);
}
__device__ __forceinline__ unsigned pk(float lo, float hi) {
  f2 x = {lo, hi};
  return __builtin_bit_cast(unsigned, __builtin_convertvector(x, bf2));
}
__device__ __forceinline__ float asf(unsigned u) { return __builtin_bit_cast(float, u); }
__device__ __forceinline__ float blo(unsigned u) { return __builtin_bit_cast(float, u << 16); }
__device__ __forceinline__ float bhi(unsigned u) { return __builtin_bit_cast(float, u & 0xffff0000u); }
template <int CTRL> __device__ __forceinline__ float dpp(float v) {
  return __builtin_bit_cast(float, __builtin_amdgcn_update_dpp(0, __builtin_bit_cast(int, v), CTRL, 0xf, 0xf, true));
}
__device__ __forceinline__ float fmax2(float a, float b) { return __builtin_amdgcn_fmed3f(a, b, __builtin_inff()); }
constexpr int ROR1 = 0x121, ROR2 = 0x122, ROR4 = 0x124, ROR8 = 0x128, QP_2301 = 0x4e, QP_2222 = 0xaa;
__device__ __forceinline__ float sw32_add(float a, float b) {
  auto r = __builtin_amdgcn_permlane32_swap(__builtin_bit_cast(unsigned, a), __builtin_bit_cast(unsigned, b), false, false);
  return asf(r[0]) + asf(r[1]);
}
__device__ __forceinline__ float sw16_add(float a, float b) {
  auto r = __builtin_amdgcn_permlane16_swap(__builtin_bit_cast(unsigned, a), __builtin_bit_cast(unsigned, b), false, false);
  return asf(r[0]) + asf(r[1]);
}
__device__ __forceinline__ float sw32_max(float a) {
  auto r = __builtin_amdgcn_permlane32_swap(__builtin_bit_cast(unsigned, a), __builtin_bit_cast(unsigned, a), false, false);
  return fmax2(asf(r[0]), asf(r[1]));
}
__device__ __forceinline__ float sw16_max(float a) {
  auto r = __builtin_amdgcn_permlane16_swap(__builtin_bit_cast(unsigned, a), __builtin_bit_cast(unsigned, a), false, false);
  return fmax2(asf(r[0]), asf(r[1]));
}
__device__ __forceinline__ float wave_sum(float v) {
  v += dpp<ROR8>(v);
  v += dpp<ROR4>(v);
  v += dpp<ROR2>(v);
  v += dpp<ROR1>(v);
  v = sw16_add(v, v);
  return sw32_add(v, v);
}
__device__ __forceinline__ v2u trrd(LAS char* p) {
  return __builtin_bit_cast(v2u, __builtin_amdgcn_ds_read_tr16_b64_v4i16((LAS s4*)p));
}
__device__ __forceinline__ v4u trrd2(LAS char* p0, LAS char* p1) {
  v2u a = trrd(p0), b = trrd(p1);
  return v4u{a.x, a.y, b.x, b.y};
}
#define BAR() asm volatile("s_waitcnt lgkmcnt(0)\n\ts_barrier" ::: "memory")
#define VMWAIT(n) asm volatile("s_waitcnt vmcnt(" #n ")" ::: "memory")
__device__ __forceinline__ void gld16(v4u& d, const void* base, unsigned off) {
  asm volatile("global_load_dwordx4 %0, %1, %2" : "=v"(d) : "v"(off), "s"(base) : "memory");
}
__device__ __forceinline__ void gld4_sc1(unsigned& d, const void* base, unsigned off) {
  asm volatile("global_load_dword %0, %1, %2 sc1" : "=v"(d) : "v"(off), "s"(base) : "memory");
}
__device__ __forceinline__ void dma16(const void* base, unsigned off, unsigned lds) {
  unsigned keep;
  asm volatile("s_mov_b32 %0, m0\n\ts_mov_b32 m0, %3\n\ts_nop 0\n\tglobal_load_lds_dwordx4 %1, %2\n\ts_mov_b32 m0, %0"
               : "=&s"(keep)
               : "v"(off), "s"(base), "s"(lds)
               : "memory");
}
__device__ __forceinline__ v4u ld_sc1(__amdgpu_buffer_rsrc_t rs, int off) {
  return __builtin_amdgcn_raw_buffer_load_b128(rs, off, 0, AUX_SC1);
}
__device__ __forceinline__ v2u ld8_sc1(__amdgpu_buffer_rsrc_t rs, int off) {
  return __builtin_amdgcn_raw_buffer_load_b64(rs, off, 0, AUX_SC1);
}
__device__ __forceinline__ void st_sc1(__amdgpu_buffer_rsrc_t rs, int off, v4u v) {
  __builtin_amdgcn_raw_buffer_store_b128(v, rs, off, 0, AUX_SC1);
}
__device__ __forceinline__ void ln_stats(v4u r, float& mean, float& rstd) {
  float e[8] = {blo(r.x), bhi(r.x), blo(r.y), bhi(r.y), blo(r.z), bhi(r.z), blo(r.w), bhi(r.w)};
  float s = ((e[0] + e[1]) + (e[2] + e[3])) + ((e[4] + e[5]) + (e[6] + e[7]));
  mean = wave_sum(s) * (1.f / DM);
  float q = 0.f;
#pragma unroll
  for (int i = 0; i < 8; ++i) {
    float d = e[i] - mean;
    q = fmaf(d, d, q);
  }
  rstd = __builtin_amdgcn_rsqf(wave_sum(q) * (1.f / DM) + 1e-5f);
}
}

__global__ void __launch_bounds__(512, 2)
mla_glu_decode(const u16* __restrict__ x, const u16* __restrict__ ckv, const u16* __restrict__ wq, const u16* __restrict__ wuk,
               const u16* __restrict__ wuv, const u16* __restrict__ wo, const u16* __restrict__ wg, const u16* __restrict__ wu,
               const u16* __restrict__ wd, u16* __restrict__ out, char* __restrict__ ws) {
  __shared__ __attribute__((aligned(16))) char smem_[L_END];
  LAS char* sm = (LAS char*)smem_;
  const unsigned smb = (unsigned)(size_t)sm;
  const int tid = threadIdx.x, l = tid & 63, w = __builtin_amdgcn_readfirstlane(tid >> 6);
  const int col = l & 15, g4 = l >> 4, tq = (l & 15) >> 2, tp = l & 3;
  const int xcd = blockIdx.x & 7, r = blockIdx.x >> 3;
  const __amdgpu_buffer_rsrc_t rs = __builtin_amdgcn_make_buffer_rsrc(ws, 0, WS_BYTES, 0x00020000);
  asm volatile("" ::"s"(x), "s"(ckv), "s"(wq), "s"(wuk), "s"(wuv), "s"(wo), "s"(wg), "s"(wu), "s"(wd), "s"(out), "s"(ws));
  STAMP(0);

  if (__builtin_expect(r < 16, 1)) {
    const int h = 4 * xcd + (r >> 2), j = r & 3, ca = j >> 1, mb = j & 1;
    v4u xr0, xr1, xf[4];
    unsigned ep;
    gld4_sc1(ep, ws, 0u);
    gld16(xr0, x, l * 16u);
    gld16(xr1, x, 1024u + l * 16u);
#pragma unroll
    for (int s = 0; s < 4; ++s) gld16(xf[s], x, (unsigned)(((col & 1) * DM + 128 * j + 32 * s + 8 * g4) * 2));
#pragma unroll
    for (int i = 0; i < 4; ++i) {
      const int k = 4 * w + i, row = 4 * k + g4, f = (row & 3) | ((row >> 1) & 4);
      dma16(wq, (unsigned)(((128 * j + row) * 4096 + h * DH + 8 * (col ^ (f << 1))) * 2), smb + L_WQ + k * 1024);
    }
    if (w < 4) {
#pragma unroll
      for (int i = 0; i < 8; ++i) {
        const int row = 32 * w + 4 * i + g4;
        dma16(wuk, (unsigned)((((128 * j + row) * NH + h) * DH + 8 * (col ^ (row & 15))) * 2), smb + L_WUK + (8 * w + i) * 1024);
      }
      VMWAIT(12);
    } else {
      const v4u ones = {~0u, ~0u, ~0u, ~0u};
      const int cb = WS_PART + ((h * 2 + ca) * 2 + mb) * 16384 + (tid - 256) * 16;
#pragma unroll
      for (int i = 0; i < 4; ++i) st_sc1(rs, cb + 4096 * i, ones);
      VMWAIT(8);
    }
    asm volatile("" : "+v"(ep), "+v"(xr0), "+v"(xr1), "+v"(xf[0]), "+v"(xf[1]), "+v"(xf[2]), "+v"(xf[3])::"memory");
    const unsigned T = ep + 1u;
    float m0, r0, m1, r1;
    ln_stats(xr0, m0, r0);
    ln_stats(xr1, m1, r1);
    const float mean = (col & 1) ? m1 : m0, rstd = (col & 1) ? r1 : r0;
    if (col & 2) {
#pragma unroll
      for (int s = 0; s < 4; ++s) xf[s] = v4u{0x3f803f80u, 0x3f803f80u, 0x3f803f80u, 0x3f803f80u};
    }
    STAMP(1);
    if (w < 4) VMWAIT(8);
    else VMWAIT(0);
    BAR();
    STAMP(2);
    const int fl = tq | ((g4 & 1) << 2);
    {
      LAS char* p = sm + L_WQ + 256 * (8 * g4 + tq) + 16 * ((2 * w + (tp >> 1)) ^ (fl << 1)) + 8 * (tp & 1);
      v4u af[4];
#pragma unroll
      for (int s = 0; s < 4; ++s) af[s] = trrd2(p + 256 * 32 * s, p + 256 * (32 * s + 4));
      __builtin_amdgcn_sched_barrier(0);
      f4 acc = {0.f, 0.f, 0.f, 0.f};
#pragma unroll
      for (int s = 0; s < 4; ++s) acc = mfma32(af[s], xf[s], acc);
      float q[4];
#pragma unroll
      for (int e = 0; e < 4; ++e) q[e] = rstd * (acc[e] - mean * dpp<QP_2222>(acc[e]));
      if (col < 2) st_sc1(rs, WS_QP + ((h * 4 + j) * 8 + w) * 128 + col * 64 + g4 * 16, v4u{pk(q[0], q[1]), T, pk(q[2], q[3]), T});
    }
    STAMP(3);
    if (w < 4) {
      VMWAIT(1);
      v4u uk[8];
#pragma unroll
      for (int t = 0; t < 2; ++t)
#pragma unroll
        for (int s = 0; s < 4; ++s) uk[4 * t + s] = *(LAS v4u*)(sm + L_WUK + 256 * (32 * w + 16 * t + col) + 16 * ((4 * s + g4) ^ col));
      const int o = WS_QP + ((h * 4 + ((col >> 1) & 3)) * 8 + (g4 >> 1)) * 128 + (col & 1) * 64 + (g4 & 1) * 32;
      v4u a[8];
      for (unsigned spin = 0;; ++spin) {
        asm volatile("" ::: "memory");
#pragma unroll
        for (int i = 0; i < 8; ++i) a[i] = ld_sc1(rs, o + 256 * (i >> 1) + 16 * (i & 1));
        bool ok = true;
#pragma unroll
        for (int i = 0; i < 8; ++i) ok &= (a[i].y == T) & (a[i].w == T);
        if (__all(ok) || spin > SPIN_CAP) break;
      }
      STAMP(4);
      f4 a0 = {0.f, 0.f, 0.f, 0.f}, a1 = a0;
#pragma unroll
      for (int s = 0; s < 4; ++s) {
        const v4u qf = {a[2 * s].x, a[2 * s].z, a[2 * s + 1].x, a[2 * s + 1].z};
        a0 = mfma32(uk[s], qf, a0);
        a1 = mfma32(uk[4 + s], qf, a1);
      }
      float q[4];
#pragma unroll
      for (int e = 0; e < 4; ++e) {
        float v0 = a0[e] + dpp<QP_2301>(a0[e]), v1 = a1[e] + dpp<QP_2301>(a1[e]);
        float v = (col & 2) ? v1 : v0;
        v += dpp<ROR4>(v);
        q[e] = v * (0.08838834764831845f * 1.4426950408889634f);
      }
      if (col < 4)
        st_sc1(rs, WS_QA + ((col & 1) * NH + h) * 2048 + (64 * j + 16 * w + 8 * (col >> 1) + 2 * g4) * 8,
               v4u{pk(q[0], q[1]), T, pk(q[2], q[3]), T});
    }
    STAMP(5);
    BAR();
#pragma unroll
    for (int i = 0; i < 8; ++i) {
      const int k = 8 * w + ((i + 4 * mb) & 7), row = 4 * k + g4, f = (row & 3) | ((row >> 1) & 4);
      dma16(wuv, (unsigned)((((256 * ca + row) * NH + h) * DH + 8 * (col ^ (f << 1))) * 2), smb + L_WUV + k * 1024);
    }
#pragma unroll
    for (int i = 0; i < 8; ++i) {
      const int k = 8 * w + ((i + 4 * ca) & 7), row = 2 * k + (l >> 5), f = (row & 3) | ((row >> 1) & 4);
      dma16(wo, (unsigned)(((h * DH + row) * DM + 256 * mb + 8 * ((l & 31) ^ (f << 1))) * 2), smb + L_WO + k * 1024);
    }
    VMWAIT(0);
    BAR();
    STAMP(6);
    v4u av[8], ao[8];
    {
      LAS char* p = sm + L_WUV + 256 * (8 * g4 + tq) + 16 * ((2 * w + (tp >> 1)) ^ (fl << 1)) + 8 * (tp & 1);
#pragma unroll
      for (int s = 0; s < 8; ++s) av[s] = trrd2(p + 256 * 32 * s, p + 256 * (32 * s + 4));
#pragma unroll
      for (int t = 0; t < 2; ++t) {
        LAS char* po = sm + L_WO + 512 * (8 * g4 + tq) + 16 * ((4 * w + 2 * t + (tp >> 1)) ^ (fl << 1)) + 8 * (tp & 1);
#pragma unroll
        for (int s = 0; s < 4; ++s) ao[4 * t + s] = trrd2(po + 512 * 32 * s, po + 512 * (32 * s + 4));
      }
    }
    STAMP(7);
    {
      const int b = w >> 2, cw = w & 3, nr = l >> 3;
      const int o = WS_PART + (((h * 2 + ca) * 2 + b) * 32 + nr) * 512 + 128 * cw + 16 * (l & 7);
      const int ol = WS_LSE + ((b * 2 + (h >> 4)) * 32 + nr) * 128 + (h & 15) * 8;
      v4u a[4];
      v2u ls[4];
      for (unsigned spin = 0;; ++spin) {
        asm volatile("" ::: "memory");
#pragma unroll
        for (int i = 0; i < 4; ++i) ls[i] = ld8_sc1(rs, ol + 1024 * i);
#pragma unroll
        for (int i = 0; i < 4; ++i) a[i] = ld_sc1(rs, o + 4096 * i);
        bool ok = true;
        unsigned mx = 0u;
#pragma unroll
        for (int i = 0; i < 4; ++i) {
          ok &= ls[i].y == T;
          mx = max(max(mx, a[i].x), max(a[i].y, max(a[i].z, a[i].w)));
        }
        ok &= mx != ~0u;
        if (__all(ok) || spin > SPIN_CAP) break;
      }
      STAMP(8);
      float e[4];
#pragma unroll
      for (int i = 0; i < 4; ++i) e[i] = asf(ls[i].x);
      float M = fmax2(fmax2(e[0], e[1]), fmax2(e[2], e[3]));
      M = fmax2(M, dpp<ROR8>(M));
      M = sw32_max(sw16_max(M));
#pragma unroll
      for (int i = 0; i < 4; ++i) e[i] = __builtin_amdgcn_exp2f(e[i] - M);
      float L = (e[0] + e[1]) + (e[2] + e[3]);
      L += dpp<ROR8>(L);
      L = sw16_add(L, L);
      L = sw32_add(L, L);
      float acc[8];
#pragma unroll
      for (int c = 0; c < 4; ++c) {
        acc[2 * c] = e[0] * blo(a[0][c]);
        acc[2 * c + 1] = e[0] * bhi(a[0][c]);
      }
#pragma unroll
      for (int i = 1; i < 4; ++i)
#pragma unroll
        for (int c = 0; c < 4; ++c) {
          acc[2 * c] = fmaf(e[i], blo(a[i][c]), acc[2 * c]);
          acc[2 * c + 1] = fmaf(e[i], bhi(a[i][c]), acc[2 * c + 1]);
        }
      float rr[4];
#pragma unroll
      for (int c = 0; c < 4; ++c) rr[c] = sw32_add(acc[c], acc[c + 4]);
      float s0 = sw16_add(rr[0], rr[2]), s1 = sw16_add(rr[1], rr[3]);
      s0 += dpp<ROR8>(s0);
      s1 += dpp<ROR8>(s1);
      const float il = __builtin_amdgcn_rcpf(L);
      if (!(l & 8)) *(LAS unsigned*)(sm + L_CC + b * CCP + (64 * cw + 8 * (l & 7) + 2 * g4) * 2) = pk(s0 * il, s1 * il);
    }
    BAR();
    {
      f4 acc = {0.f, 0.f, 0.f, 0.f};
      LAS char* p = sm + L_CC + (col & 1) * CCP + 16 * g4;
#pragma unroll
      for (int s = 0; s < 8; ++s) acc = mfma32(av[s], *(LAS v4u*)(p + 64 * s), acc);
      if (col < 2) *(LAS v2u*)(sm + L_CT + col * CTP + (16 * w + 4 * g4) * 2) = v2u{pk(acc[0], acc[1]), pk(acc[2], acc[3])};
    }
    BAR();
    {
      f4 a0 = {0.f, 0.f, 0.f, 0.f}, a1 = a0;
      LAS char* p = sm + L_CT + (col & 1) * CTP + 16 * g4;
#pragma unroll
      for (int s = 0; s < 4; ++s) {
        const v4u bfr = *(LAS v4u*)(p + 64 * s);
        a0 = mfma32(ao[s], bfr, a0);
        a1 = mfma32(ao[4 + s], bfr, a1);
      }
      if (col & 2) a0 = a1;
      if (col < 4)
        st_sc1(rs, WS_OPT + ((16 * mb + 2 * w + (col >> 1)) * 64 + 2 * h + ca) * 128 + ((col & 1) * 4 + g4) * 16,
               v4u{pk(a0[0], a0[1]), T, pk(a0[2], a0[3]), T});
    }
    STAMP(9);
  } else {
    const int a = r - 16, b = xcd >> 2, n = 8 * (xcd & 3) + (a >> 1), g = a & 1;
    unsigned ep;
    gld4_sc1(ep, ws, 0u);
#pragma unroll
    for (int i = 0; i < 16; ++i) {
      const int row = 16 * w + ((i + 8 * g) & 15);
      dma16(ckv, (unsigned)(((b * SK + 128 * n + row) * DC + 8 * l) * 2), smb + L_C + row * CP);
    }
    asm volatile("s_waitcnt vmcnt(0)" : "+v"(ep)::"memory");
    const unsigned T = ep + 1u;
    BAR();
    STAMP(1);
    v4u fr[32];
    if (w < 4) {
      LAS char* p = sm + L_C + (32 * w + col) * CP + 16 * g4;
#pragma unroll
      for (int kt = 0; kt < 2; ++kt)
#pragma unroll
        for (int s = 0; s < 16; ++s) fr[16 * kt + s] = *(LAS v4u*)(p + 16 * kt * CP + 64 * s);
    } else {
      LAS char* p = sm + L_C + (4 * g4 + tq) * CP + 256 * (w - 4) + 8 * tp;
#pragma unroll
      for (int ct = 0; ct < 8; ++ct)
#pragma unroll
        for (int u = 0; u < 4; ++u) fr[4 * ct + u] = trrd2(p + 32 * u * CP + 32 * ct, p + (32 * u + 16) * CP + 32 * ct);
    }
    STAMP(2);
    {
      const int o = WS_QA + (b * NH + 16 * g) * 2048 + tid * 16;
      v4u q[4];
      for (unsigned spin = 0;; ++spin) {
        asm volatile("" ::: "memory");
#pragma unroll
        for (int i = 0; i < 4; ++i) q[i] = ld_sc1(rs, o + 8192 * i);
        bool ok = true;
#pragma unroll
        for (int i = 0; i < 4; ++i) ok &= (q[i].y == T) & (q[i].w == T);
        if (__all(ok) || spin > SPIN_CAP) break;
      }
      STAMP(3);
#pragma unroll
      for (int i = 0; i < 4; ++i) *(LAS v2u*)(sm + L_Q + (4 * i + (tid >> 7)) * CP + (tid & 127) * 8) = v2u{q[i].x, q[i].z};
    }
    BAR();
    STAMP(4);
    if (w < 4) {
      f4 s0 = {0.f, 0.f, 0.f, 0.f}, s1 = s0;
      LAS char* p = sm + L_Q + col * CP + 16 * g4;
      v4u qf[16];
#pragma unroll
      for (int s = 0; s < 16; ++s) qf[s] = *(LAS v4u*)(p + 64 * s);
      __builtin_amdgcn_sched_barrier(0);
#pragma unroll
      for (int s = 0; s < 16; ++s) {
        s0 = mfma32(fr[s], qf[s], s0);
        s1 = mfma32(fr[16 + s], qf[s], s1);
      }
      float mx = fmax2(fmax2(fmax2(s0[0], s0[1]), fmax2(s0[2], s0[3])), fmax2(fmax2(s1[0], s1[1]), fmax2(s1[2], s1[3])));
      mx = sw32_max(sw16_max(mx));
      if (g4 == 0) *(LAS float*)(sm + L_MX + col * 16 + w * 4) = mx;
      STAMP(5);
      BAR();
      const f4 m4 = *(LAS f4*)(sm + L_MX + col * 16);
      const float M = fmax2(fmax2(m4[0], m4[1]), fmax2(m4[2], m4[3]));
      float ps = 0.f;
#pragma unroll
      for (int e = 0; e < 4; ++e) {
        s0[e] = __builtin_amdgcn_exp2f(s0[e] - M);
        s1[e] = __builtin_amdgcn_exp2f(s1[e] - M);
        ps += s0[e] + s1[e];
      }
      *(LAS v4u*)(sm + L_P + w * 1024 + l * 16) = v4u{pk(s0[0], s0[1]), pk(s0[2], s0[3]), pk(s1[0], s1[1]), pk(s1[2], s1[3])};
      ps = sw16_add(ps, ps);
      ps = sw32_add(ps, ps);
      if (g4 == 0) *(LAS float*)(sm + L_LS + col * 16 + w * 4) = ps;
      BAR();
      STAMP(6);
      if (w == 0) {
        const f4 l4 = *(LAS f4*)(sm + L_LS + col * 16);
        const float lse = M + __builtin_amdgcn_logf((l4[0] + l4[1]) + (l4[2] + l4[3]));
        if (g4 == 0)
          __builtin_amdgcn_raw_buffer_store_b64(v2u{__builtin_bit_cast(unsigned, lse), T}, rs,
                                                WS_LSE + ((b * 2 + g) * 32 + n) * 128 + col * 8, 0, AUX_SC1);
      }
    } else {
      BAR();
      BAR();
      const int v = w - 4;
      v4u pf[4];
#pragma unroll
      for (int u = 0; u < 4; ++u) pf[u] = *(LAS v4u*)(sm + L_P + u * 1024 + l * 16);
      const f4 l4 = *(LAS f4*)(sm + L_LS + col * 16);
      const float il = __builtin_amdgcn_rcpf((l4[0] + l4[1]) + (l4[2] + l4[3]));
      const int o = WS_PART + ((((16 * g + (col & 7)) * 2 + (v >> 1)) * 2 + b) * 32 + n) * 512 + 256 * (v & 1) + (col >> 3) * 64 +
                    (g4 & 1) * 32 + (g4 >> 1) * 16;
#pragma unroll
      for (int pp = 0; pp < 2; ++pp) {
        v4u R[2];
#pragma unroll
        for (int k = 0; k < 2; ++k) {
          const int pr = 2 * pp + k;
          f4 c0 = {0.f, 0.f, 0.f, 0.f}, c1 = c0;
#pragma unroll
          for (int u = 0; u < 4; ++u) {
            c0 = mfma32(fr[8 * pr + u], pf[u], c0);
            c1 = mfma32(fr[8 * pr + 4 + u], pf[u], c1);
          }
          const unsigned x0 = min(pk(c0[0] * il, c0[1] * il), 0xfffffffeu), x1 = min(pk(c0[2] * il, c0[3] * il), 0xfffffffeu);
          const unsigned y0 = min(pk(c1[0] * il, c1[1] * il), 0xfffffffeu), y1 = min(pk(c1[2] * il, c1[3] * il), 0xfffffffeu);
          auto r0 = __builtin_amdgcn_permlane16_swap(x0, y0, false, false);
          auto r1 = __builtin_amdgcn_permlane16_swap(x1, y1, false, false);
          R[k] = v4u{r0[0], r1[0], r0[1], r1[1]};
        }
        v4u A, B;
#pragma unroll
        for (int e = 0; e < 4; ++e) {
          A[e] = (unsigned)__builtin_amdgcn_update_dpp((int)R[0][e], (int)R[1][e], ROR8, 0xf, 0xc, false);
          B[e] = (unsigned)__builtin_amdgcn_update_dpp((int)R[1][e], (int)R[0][e], ROR8, 0xf, 0x3, false);
        }
        st_sc1(rs, o + 128 * pp, A);
        st_sc1(rs, o + 128 * pp + 8 * 65536, B);
      }
      STAMP(7);
    }
    const int sl = 12 * xcd + a;
    if (a < 12) {
      if (sl < NSL) {
        BAR();
        v4u xr0, xr1, xf[8];
#pragma unroll
        for (int i = 0; i < 2; ++i) {
          const int k = 2 * w + i;
          const unsigned o = (unsigned)(((32 * k + (l >> 1)) * FF + 16 * sl + 8 * (l & 1)) * 2);
          dma16(wg, o, smb + L_GG + k * 1024);
          dma16(wu, o, smb + L_GU + k * 1024);
          dma16(wd, (unsigned)(((16 * sl + k) * DM + 8 * l) * 2), smb + L_GD + k * CP);
        }
        gld16(xr0, x, l * 16u);
        gld16(xr1, x, 1024u + l * 16u);
#pragma unroll
        for (int s = 0; s < 8; ++s) gld16(xf[s], x, (unsigned)(((col & 1) * DM + 256 * (w & 1) + 32 * s + 8 * g4) * 2));
        asm volatile("s_waitcnt vmcnt(0)"
                     : "+v"(xr0), "+v"(xr1), "+v"(xf[0]), "+v"(xf[1]), "+v"(xf[2]), "+v"(xf[3]), "+v"(xf[4]), "+v"(xf[5]), "+v"(xf[6]),
                       "+v"(xf[7])::"memory");
        BAR();
        STAMP(8);
        if (w < 4) {
          if (col & 2) {
#pragma unroll
            for (int s = 0; s < 8; ++s) xf[s] = v4u{0x3f803f80u, 0x3f803f80u, 0x3f803f80u, 0x3f803f80u};
          }
          LAS char* p = sm + L_GG + (w >> 1) * 16384 + 32 * (256 * (w & 1) + 8 * g4 + tq) + 8 * tp;
          f4 acc = {0.f, 0.f, 0.f, 0.f};
#pragma unroll
          for (int s = 0; s < 8; ++s) acc = mfma32(trrd2(p + 32 * 32 * s, p + 32 * (32 * s + 4)), xf[s], acc);
          *(LAS f4*)(sm + L_GR + w * 1024 + l * 16) = acc;
        }
        float m0, r0, m1, r1;
        ln_stats(xr0, m0, r0);
        ln_stats(xr1, m1, r1);
        const float mean = (col & 1) ? m1 : m0, rstd = (col & 1) ? r1 : r0;
        v2u dn[4];
#pragma unroll
        for (int t = 0; t < 4; ++t) dn[t] = trrd(sm + L_GD + (4 * g4 + tq) * CP + 32 * (4 * w + t) + 8 * tp);
        BAR();
        LAS char* pr = sm + L_GR + (16 * g4 + (col & 1)) * 16;
        LAS char* po = sm + L_GR + (16 * g4 + 2) * 16;
        const f4 gx = *(LAS f4*)(pr) + *(LAS f4*)(pr + 1024), go = *(LAS f4*)(po) + *(LAS f4*)(po + 1024);
        const f4 ux = *(LAS f4*)(pr + 2048) + *(LAS f4*)(pr + 3072), uo = *(LAS f4*)(po + 2048) + *(LAS f4*)(po + 3072);
        float hh[4];
#pragma unroll
        for (int e = 0; e < 4; ++e) {
          const float gv = rstd * (gx[e] - mean * go[e]), uv = rstd * (ux[e] - mean * uo[e]);
          hh[e] = uv * __builtin_amdgcn_rcpf(1.f + __builtin_amdgcn_exp2f(-1.4426950408889634f * gv));
        }
        const v2u hb = {pk(hh[0], hh[1]), pk(hh[2], hh[3])};
        const f4 z = {0.f, 0.f, 0.f, 0.f};
        f4 d0 = mfma16(dn[0], hb, z), d1 = mfma16(dn[1], hb, z), d2 = mfma16(dn[2], hb, z), d3 = mfma16(dn[3], hb, z);
        if (col & 2) {
          d0 = d1;
          d2 = d3;
        }
        if (col & 4) d0 = d2;
        if (col < 8)
          st_sc1(rs, WS_OPG + ((4 * w + (col >> 1)) * 96 + sl) * 128 + ((col & 1) * 4 + g4) * 16,
                 v4u{pk(d0[0], d0[1]), T, pk(d0[2], d0[3]), T});
        STAMP(9);
      }
    } else {
      const int rd = 4 * xcd + (a - 12);
      const int ln = tid >> 3;
      const int ot = WS_OPT + rd * 8192 + tid * 16, og = WS_OPG + rd * 96 * 128 + tid * 16;
      const bool two = ln < NSL - 64;
      const int og2 = two ? og + 8192 : og;
      float xres = 0.f;
      if (tid < 32) xres = blo(x[(tid >> 4) * DM + 16 * rd + (tid & 15)]);
      v4u p0, p1, p2;
      for (unsigned spin = 0;; ++spin) {
        asm volatile("" ::: "memory");
        p0 = ld_sc1(rs, ot);
        p1 = ld_sc1(rs, og);
        p2 = ld_sc1(rs, og2);
        const bool ok = (p0.y == T) & (p0.w == T) & (p1.y == T) & (p1.w == T) & (p2.y == T) & (p2.w == T);
        if (__all(ok) || spin > SPIN_CAP) break;
      }
      STAMP(8);
      if (!two) p2 = v4u{0u, 0u, 0u, 0u};
      const float v0 = blo(p0.x) + (blo(p1.x) + blo(p2.x)), v1 = bhi(p0.x) + (bhi(p1.x) + bhi(p2.x));
      const float v2 = blo(p0.z) + (blo(p1.z) + blo(p2.z)), v3 = bhi(p0.z) + (bhi(p1.z) + bhi(p2.z));
      float s = sw16_add(sw32_add(v0, v2), sw32_add(v1, v3));
      s += dpp<ROR8>(s);
      if (!(l & 8)) *(LAS float*)(sm + L_RR + w * 128 + (4 * (l & 7) + g4) * 4) = s;
      BAR();
      if (tid < 32) {
        float t = xres;
#pragma unroll
        for (int i = 0; i < 8; ++i) t += *(LAS float*)(sm + L_RR + i * 128 + tid * 4);
        out[(tid >> 4) * DM + 16 * rd + (tid & 15)] = (u16)(pk(t, t) & 0xffffu);
        if (rd == 0 && tid == 0) __builtin_amdgcn_raw_buffer_store_b32(T, rs, WS_E, 0, AUX_SC1);
      }
      STAMP(9);
    }
  }
  STAMP_FLUSH();
}

template <int LDSB, int NV> __global__ void __launch_bounds__(512, 2) k_extra(char* ws) {
  __shared__ char sm[LDSB];
  if (NV > 0) asm volatile("v_mov_b32 v200, 0" ::: "v200");
  if (ws == nullptr) { sm[threadIdx.x] = 1; ((volatile char*)16)[0] = sm[threadIdx.x ^ 1]; }
}
extern "C" void kernel_launch(void* const* d_in, const int* in_sizes, int n_in, void* d_out, int out_size, void* d_ws, size_t ws_size,
                              hipStream_t stream) {
  mla_glu_decode<<<256, 512, 0, stream>>>((const u16*)d_in[0], (const u16*)d_in[1], (const u16*)d_in[2], (const u16*)d_in[3],
                                          (const u16*)d_in[4], (const u16*)d_in[5], (const u16*)d_in[7], (const u16*)d_in[8],
                                          (const u16*)d_in[9], (u16*)d_out, (char*)d_ws);
}
```

```cpp
#define PROBE_CLASS 3
#define PROBE_IDX 0
#define PROBE_WAVE 0
#define PROBE_K 0
#include <hip/hip_runtime.h>
#include <stdint.h>

#define GAS __attribute__((address_space(1)))
#define LAS __attribute__((address_space(3)))
typedef unsigned short u16;
typedef unsigned v4u __attribute__((ext_vector_type(4)));
typedef unsigned v2u __attribute__((ext_vector_type(2)));
typedef float f4 __attribute__((ext_vector_type(4)));
typedef float f2 __attribute__((ext_vector_type(2)));
typedef __bf16 bf8 __attribute__((ext_vector_type(8)));
typedef __bf16 bf2 __attribute__((ext_vector_type(2)));
typedef short s4 __attribute__((ext_vector_type(4)));

namespace {
constexpr int NH = 32, SK = 4096, DH = 128, DM = 512, DC = 512, FF = 1376;
constexpr int NSL = FF / 16;
constexpr int WS_E = 0;
constexpr int WS_QP = 4096;
constexpr int WS_QA = WS_QP + NH * 4 * 1024;
constexpr int WS_LSE = WS_QA + 2 * NH * 2048;
constexpr int WS_OPT = WS_LSE + 2 * 2 * 32 * 128;
constexpr int WS_OPG = WS_OPT + 32 * 64 * 128;
constexpr int WS_PART = WS_OPG + 32 * 96 * 128;
constexpr int WS_BYTES = WS_PART + NH * 2 * 2 * 32 * 512;
constexpr int WS_STAMP = 8 << 20;
constexpr int CP = 1056;
constexpr int L_C = 0, L_Q = 128 * CP, L_P = L_Q + 16 * CP, L_MX = L_P + 4096, L_LS = L_MX + 256, L_RR = L_LS + 256,
              L_END = L_RR + 1024;
constexpr int L_GG = 0, L_GU = 16384, L_GD = 32768, L_GR = 65536;
constexpr int L_WUV = 0, L_WO = 65536, L_WQ = 65536, L_WUK = 98304, L_CC = 131072, CCP = 576, L_CT = L_CC + 2048, CTP = 320;
constexpr unsigned SPIN_CAP = 1u << 20;
constexpr int AUX_SC1 = 16;

#define STAMPS
#define STAMP(i) asm volatile("s_memrealtime %0\n\ts_waitcnt lgkmcnt(0)" : "=s"(ts[i])::"memory")
#define STAMP_FLUSH()                                                                      \
  do {                                                                                     \
    const int cls_ = r < 16 ? 0 : (r < 28 ? 1 : 2);                                        \
    if ((cls_ == PROBE_CLASS || PROBE_CLASS == 3) && w == PROBE_WAVE && ts[PROBE_IDX] != 0) {   \
      v4u g_;                                                                              \
      for (unsigned sp_ = 0; sp_ < SPIN_CAP; ++sp_) { asm volatile("" ::: "memory"); g_ = ld_sc1(rs, 2048); if (g_.z == Tg) break; } \
      const unsigned long long t0_ = ((unsigned long long)__builtin_amdgcn_readfirstlane(g_.y) << 32) | (unsigned)__builtin_amdgcn_readfirstlane(g_.x); \
      const long long d_ = (long long)(ts[PROBE_IDX] - t0_);                               \
      const unsigned long long until_ = t0_ + 4000ull + PROBE_K * d_;                      \
      while (__builtin_amdgcn_s_memrealtime() < until_) __builtin_amdgcn_s_sleep(1);       \
    }                                                                                      \
  } while (0)
#define PROBE_T(T_) do { Tg = T_; if (blockIdx.x == 0 && tid == 0) st_sc1(rs, 2048, v4u{(unsigned)tstart, (unsigned)(tstart >> 32), T_, 0u}); } while (0)

__device__ __forceinline__ f4 mfma32(v4u a, v4u b, f4 c) {
  return __builtin_amdgcn_mfma_f32_16x16x32_bf16(__builtin_bit_cast(bf8, a), __builtin_bit_cast(bf8, b), c, 0, 0, 0);
}
__device__ __forceinline__ f4 mfma16(v2u a, v2u b, f4 c) {
  return __builtin_amdgcn_mfma_f32_16x16x16bf16_1k(__builtin_bit_cast(s4, a), __builtin_bit_cast(s4, b), c, 0, 0, 0);
}
__device__ __forceinline__ unsigned pk(float lo, float hi) {
  f2 x = {lo, hi};
  return __builtin_bit_cast(unsigned, __builtin_convertvector(x, bf2));
}
__device__ __forceinline__ float asf(unsigned u) { return __builtin_bit_cast(float, u); }
__device__ __forceinline__ float blo(unsigned u) { return __builtin_bit_cast(float, u << 16); }
__device__ __forceinline__ float bhi(unsigned u) { return __builtin_bit_cast(float, u & 0xffff0000u); }
template <int CTRL> __device__ __forceinline__ float dpp(float v) {
  return __builtin_bit_cast(float, __builtin_amdgcn_update_dpp(0, __builtin_bit_cast(int, v), CTRL, 0xf, 0xf, true));
}
__device__ __forceinline__ float fmax2(float a, float b) { return __builtin_amdgcn_fmed3f(a, b, __builtin_inff()); }
constexpr int ROR1 = 0x121, ROR2 = 0x122, ROR4 = 0x124, ROR8 = 0x128, QP_2301 = 0x4e, QP_2222 = 0xaa;
__device__ __forceinline__ float sw32_add(float a, float b) {
  auto r = __builtin_amdgcn_permlane32_swap(__builtin_bit_cast(unsigned, a), __builtin_bit_cast(unsigned, b), false, false);
  return asf(r[0]) + asf(r[1]);
}
__device__ __forceinline__ float sw16_add(float a, float b) {
  auto r = __builtin_amdgcn_permlane16_swap(__builtin_bit_cast(unsigned, a), __builtin_bit_cast(unsigned, b), false, false);
  return asf(r[0]) + asf(r[1]);
}
__device__ __forceinline__ float sw32_max(float a) {
  auto r = __builtin_amdgcn_permlane32_swap(__builtin_bit_cast(unsigned, a), __builtin_bit_cast(unsigned, a), false, false);
  return fmax2(asf(r[0]), asf(r[1]));
}
__device__ __forceinline__ float sw16_max(float a) {
  auto r = __builtin_amdgcn_permlane16_swap(__builtin_bit_cast(unsigned, a), __builtin_bit_cast(unsigned, a), false, false);
  return fmax2(asf(r[0]), asf(r[1]));
}
__device__ __forceinline__ float wave_sum(float v) {
  v += dpp<ROR8>(v);
  v += dpp<ROR4>(v);
  v += dpp<ROR2>(v);
  v += dpp<ROR1>(v);
  v = sw16_add(v, v);
  return sw32_add(v, v);
}
__device__ __forceinline__ v2u trrd(LAS char* p) {
  return __builtin_bit_cast(v2u, __builtin_amdgcn_ds_read_tr16_b64_v4i16((LAS s4*)p));
}
__device__ __forceinline__ v4u trrd2(LAS char* p0, LAS char* p1) {
  v2u a = trrd(p0), b = trrd(p1);
  return v4u{a.x, a.y, b.x, b.y};
}
#define BAR() asm volatile("s_waitcnt lgkmcnt(0)\n\ts_barrier" ::: "memory")
#define VMWAIT(n) asm volatile("s_waitcnt vmcnt(" #n ")" ::: "memory")
__device__ __forceinline__ void gld16(v4u& d, const void* base, unsigned off) {
  asm volatile("global_load_dwordx4 %0, %1, %2" : "=v"(d) : "v"(off), "s"(base) : "memory");
}
__device__ __forceinline__ void gld4_sc1(unsigned& d, const void* base, unsigned off) {
  asm volatile("global_load_dword %0, %1, %2 sc1" : "=v"(d) : "v"(off), "s"(base) : "memory");
}
__device__ __forceinline__ void dma16(const void* base, unsigned off, unsigned lds) {
  unsigned keep;
  asm volatile("s_mov_b32 %0, m0\n\ts_mov_b32 m0, %3\n\ts_nop 0\n\tglobal_load_lds_dwordx4 %1, %2\n\ts_mov_b32 m0, %0"
               : "=&s"(keep)
               : "v"(off), "s"(base), "s"(lds)
               : "memory");
}
__device__ __forceinline__ v4u ld_sc1(__amdgpu_buffer_rsrc_t rs, int off) {
  return __builtin_amdgcn_raw_buffer_load_b128(rs, off, 0, AUX_SC1);
}
__device__ __forceinline__ v2u ld8_sc1(__amdgpu_buffer_rsrc_t rs, int off) {
  return __builtin_amdgcn_raw_buffer_load_b64(rs, off, 0, AUX_SC1);
}
__device__ __forceinline__ void st_sc1(__amdgpu_buffer_rsrc_t rs, int off, v4u v) {
  __builtin_amdgcn_raw_buffer_store_b128(v, rs, off, 0, AUX_SC1);
}
__device__ __forceinline__ void ln_stats(v4u r, float& mean, float& rstd) {
  float e[8] = {blo(r.x), bhi(r.x), blo(r.y), bhi(r.y), blo(r.z), bhi(r.z), blo(r.w), bhi(r.w)};
  float s = ((e[0] + e[1]) + (e[2] + e[3])) + ((e[4] + e[5]) + (e[6] + e[7]));
  mean = wave_sum(s) * (1.f / DM);
  float q = 0.f;
#pragma unroll
  for (int i = 0; i < 8; ++i) {
    float d = e[i] - mean;
    q = fmaf(d, d, q);
  }
  rstd = __builtin_amdgcn_rsqf(wave_sum(q) * (1.f / DM) + 1e-5f);
}
}

__global__ void __launch_bounds__(512, 2)
mla_glu_decode(const u16* __restrict__ x, const u16* __restrict__ ckv, const u16* __restrict__ wq, const u16* __restrict__ wuk,
               const u16* __restrict__ wuv, const u16* __restrict__ wo, const u16* __restrict__ wg, const u16* __restrict__ wu,
               const u16* __restrict__ wd, u16* __restrict__ out, char* __restrict__ ws) {
  const unsigned long long tstart = __builtin_amdgcn_s_memrealtime();
  __builtin_amdgcn_sched_barrier(0);
  __shared__ __attribute__((aligned(16))) char smem_[L_END];
  LAS char* sm = (LAS char*)smem_;
  const unsigned smb = (unsigned)(size_t)sm;
  const int tid = threadIdx.x, l = tid & 63, w = __builtin_amdgcn_readfirstlane(tid >> 6);
  const int col = l & 15, g4 = l >> 4, tq = (l & 15) >> 2, tp = l & 3;
  const int xcd = blockIdx.x & 7, r = blockIdx.x >> 3;
  const __amdgpu_buffer_rsrc_t rs = __builtin_amdgcn_make_buffer_rsrc(ws, 0, WS_BYTES, 0x00020000);
  asm volatile("" ::"s"(x), "s"(ckv), "s"(wq), "s"(wuk), "s"(wuv), "s"(wo), "s"(wg), "s"(wu), "s"(wd), "s"(out), "s"(ws));
  unsigned long long ts[10] = {0, 0, 0, 0, 0, 0, 0, 0, 0, 0};
  STAMP(0);
  unsigned Tg = 0;
  ts[0] = tstart;

  if (__builtin_expect(r < 16, 1)) {
    const int h = 4 * xcd + (r >> 2), j = r & 3, ca = j >> 1, mb = j & 1;
    v4u xr0, xr1, xf[4];
    unsigned ep;
    gld4_sc1(ep, ws, 0u);
    gld16(xr0, x, l * 16u);
    gld16(xr1, x, 1024u + l * 16u);
#pragma unroll
    for (int s = 0; s < 4; ++s) gld16(xf[s], x, (unsigned)(((col & 1) * DM + 128 * j + 32 * s + 8 * g4) * 2));
#pragma unroll
    for (int i = 0; i < 4; ++i) {
      const int k = 4 * w + i, row = 4 * k + g4, f = (row & 3) | ((row >> 1) & 4);
      dma16(wq, (unsigned)(((128 * j + row) * 4096 + h * DH + 8 * (col ^ (f << 1))) * 2), smb + L_WQ + k * 1024);
    }
    if (w < 4) {
#pragma unroll
      for (int i = 0; i < 8; ++i) {
        const int row = 32 * w + 4 * i + g4;
        dma16(wuk, (unsigned)((((128 * j + row) * NH + h) * DH + 8 * (col ^ (row & 15))) * 2), smb + L_WUK + (8 * w + i) * 1024);
      }
      VMWAIT(12);
    } else {
      const v4u ones = {~0u, ~0u, ~0u, ~0u};
      const int cb = WS_PART + ((h * 2 + ca) * 2 + mb) * 16384 + (tid - 256) * 16;
#pragma unroll
      for (int i = 0; i < 4; ++i) st_sc1(rs, cb + 4096 * i, ones);
      VMWAIT(8);
    }
    asm volatile("" : "+v"(ep), "+v"(xr0), "+v"(xr1), "+v"(xf[0]), "+v"(xf[1]), "+v"(xf[2]), "+v"(xf[3])::"memory");
    const unsigned T = ep + 1u;
    float m0, r0, m1, r1;
    ln_stats(xr0, m0, r0);
    ln_stats(xr1, m1, r1);
    const float mean = (col & 1) ? m1 : m0, rstd = (col & 1) ? r1 : r0;
    if (col & 2) {
#pragma unroll
      for (int s = 0; s < 4; ++s) xf[s] = v4u{0x3f803f80u, 0x3f803f80u, 0x3f803f80u, 0x3f803f80u};
    }
    STAMP(1);
    if (w < 4) VMWAIT(8);
    else VMWAIT(0);
    BAR();
    STAMP(2);
    const int fl = tq | ((g4 & 1) << 2);
    {
      LAS char* p = sm + L_WQ + 256 * (8 * g4 + tq) + 16 * ((2 * w + (tp >> 1)) ^ (fl << 1)) + 8 * (tp & 1);
      v4u af[4];
#pragma unroll
      for (int s = 0; s < 4; ++s) af[s] = trrd2(p + 256 * 32 * s, p + 256 * (32 * s + 4));
      __builtin_amdgcn_sched_barrier(0);
      f4 acc = {0.f, 0.f, 0.f, 0.f};
#pragma unroll
      for (int s = 0; s < 4; ++s) acc = mfma32(af[s], xf[s], acc);
      float q[4];
#pragma unroll
      for (int e = 0; e < 4; ++e) q[e] = rstd * (acc[e] - mean * dpp<QP_2222>(acc[e]));
      if (col < 2) st_sc1(rs, WS_QP + ((h * 4 + j) * 8 + w) * 128 + col * 64 + g4 * 16, v4u{pk(q[0], q[1]), T, pk(q[2], q[3]), T});
    }
    STAMP(3);
    if (w < 4) {
      VMWAIT(1);
      v4u uk[8];
#pragma unroll
      for (int t = 0; t < 2; ++t)
#pragma unroll
        for (int s = 0; s < 4; ++s) uk[4 * t + s] = *(LAS v4u*)(sm + L_WUK + 256 * (32 * w + 16 * t + col) + 16 * ((4 * s + g4) ^ col));
      const int o = WS_QP + ((h * 4 + ((col >> 1) & 3)) * 8 + (g4 >> 1)) * 128 + (col & 1) * 64 + (g4 & 1) * 32;
      v4u a[8];
      for (unsigned spin = 0;; ++spin) {
        asm volatile("" ::: "memory");
#pragma unroll
        for (int i = 0; i < 8; ++i) a[i] = ld_sc1(rs, o + 256 * (i >> 1) + 16 * (i & 1));
        bool ok = true;
#pragma unroll
        for (int i = 0; i < 8; ++i) ok &= (a[i].y == T) & (a[i].w == T);
        if (__all(ok) || spin > SPIN_CAP) break;
      }
      STAMP(4);
      f4 a0 = {0.f, 0.f, 0.f, 0.f}, a1 = a0;
#pragma unroll
      for (int s = 0; s < 4; ++s) {
        const v4u qf = {a[2 * s].x, a[2 * s].z, a[2 * s + 1].x, a[2 * s + 1].z};
        a0 = mfma32(uk[s], qf, a0);
        a1 = mfma32(uk[4 + s], qf, a1);
      }
      float q[4];
#pragma unroll
      for (int e = 0; e < 4; ++e) {
        float v0 = a0[e] + dpp<QP_2301>(a0[e]), v1 = a1[e] + dpp<QP_2301>(a1[e]);
        float v = (col & 2) ? v1 : v0;
        v += dpp<ROR4>(v);
        q[e] = v * (0.08838834764831845f * 1.4426950408889634f);
      }
      if (col < 4)
        st_sc1(rs, WS_QA + ((col & 1) * NH + h) * 2048 + (64 * j + 16 * w + 8 * (col >> 1) + 2 * g4) * 8,
               v4u{pk(q[0], q[1]), T, pk(q[2], q[3]), T});
    }
    STAMP(5);
    BAR();
#pragma unroll
    for (int i = 0; i < 8; ++i) {
      const int k = 8 * w + ((i + 4 * mb) & 7), row = 4 * k + g4, f = (row & 3) | ((row >> 1) & 4);
      dma16(wuv, (unsigned)((((256 * ca + row) * NH + h) * DH + 8 * (col ^ (f << 1))) * 2), smb + L_WUV + k * 1024);
    }
#pragma unroll
    for (int i = 0; i < 8; ++i) {
      const int k = 8 * w + ((i + 4 * ca) & 7), row = 2 * k + (l >> 5), f = (row & 3) | ((row >> 1) & 4);
      dma16(wo, (unsigned)(((h * DH + row) * DM + 256 * mb + 8 * ((l & 31) ^ (f << 1))) * 2), smb + L_WO + k * 1024);
    }
    VMWAIT(0);
    BAR();
    STAMP(6);
    v4u av[8], ao[8];
    {
      LAS char* p = sm + L_WUV + 256 * (8 * g4 + tq) + 16 * ((2 * w + (tp >> 1)) ^ (fl << 1)) + 8 * (tp & 1);
#pragma unroll
      for (int s = 0; s < 8; ++s) av[s] = trrd2(p + 256 * 32 * s, p + 256 * (32 * s + 4));
#pragma unroll
      for (int t = 0; t < 2; ++t) {
        LAS char* po = sm + L_WO + 512 * (8 * g4 + tq) + 16 * ((4 * w + 2 * t + (tp >> 1)) ^ (fl << 1)) + 8 * (tp & 1);
#pragma unroll
        for (int s = 0; s < 4; ++s) ao[4 * t + s] = trrd2(po + 512 * 32 * s, po + 512 * (32 * s + 4));
      }
    }
    STAMP(7);
    {
      const int b = w >> 2, cw = w & 3, nr = l >> 3;
      const int o = WS_PART + (((h * 2 + ca) * 2 + b) * 32 + nr) * 512 + 128 * cw + 16 * (l & 7);
      const int ol = WS_LSE + ((b * 2 + (h >> 4)) * 32 + nr) * 128 + (h & 15) * 8;
      v4u a[4];
      v2u ls[4];
      for (unsigned spin = 0;; ++spin) {
        asm volatile("" ::: "memory");
#pragma unroll
        for (int i = 0; i < 4; ++i) ls[i] = ld8_sc1(rs, ol + 1024 * i);
#pragma unroll
        for (int i = 0; i < 4; ++i) a[i] = ld_sc1(rs, o + 4096 * i);
        bool ok = true;
        unsigned mx = 0u;
#pragma unroll
        for (int i = 0; i < 4; ++i) {
          ok &= ls[i].y == T;
          mx = max(max(mx, a[i].x), max(a[i].y, max(a[i].z, a[i].w)));
        }
        ok &= mx != ~0u;
        if (__all(ok) || spin > SPIN_CAP) break;
      }
      STAMP(8);
      float e[4];
#pragma unroll
      for (int i = 0; i < 4; ++i) e[i] = asf(ls[i].x);
      float M = fmax2(fmax2(e[0], e[1]), fmax2(e[2], e[3]));
      M = fmax2(M, dpp<ROR8>(M));
      M = sw32_max(sw16_max(M));
#pragma unroll
      for (int i = 0; i < 4; ++i) e[i] = __builtin_amdgcn_exp2f(e[i] - M);
      float L = (e[0] + e[1]) + (e[2] + e[3]);
      L += dpp<ROR8>(L);
      L = sw16_add(L, L);
      L = sw32_add(L, L);
      float acc[8];
#pragma unroll
      for (int c = 0; c < 4; ++c) {
        acc[2 * c] = e[0] * blo(a[0][c]);
        acc[2 * c + 1] = e[0] * bhi(a[0][c]);
      }
#pragma unroll
      for (int i = 1; i < 4; ++i)
#pragma unroll
        for (int c = 0; c < 4; ++c) {
          acc[2 * c] = fmaf(e[i], blo(a[i][c]), acc[2 * c]);
          acc[2 * c + 1] = fmaf(e[i], bhi(a[i][c]), acc[2 * c + 1]);
        }
      float rr[4];
#pragma unroll
      for (int c = 0; c < 4; ++c) rr[c] = sw32_add(acc[c], acc[c + 4]);
      float s0 = sw16_add(rr[0], rr[2]), s1 = sw16_add(rr[1], rr[3]);
      s0 += dpp<ROR8>(s0);
      s1 += dpp<ROR8>(s1);
      const float il = __builtin_amdgcn_rcpf(L);
      if (!(l & 8)) *(LAS unsigned*)(sm + L_CC + b * CCP + (64 * cw + 8 * (l & 7) + 2 * g4) * 2) = pk(s0 * il, s1 * il);
    }
    BAR();
    {
      f4 acc = {0.f, 0.f, 0.f, 0.f};
      LAS char* p = sm + L_CC + (col & 1) * CCP + 16 * g4;
#pragma unroll
      for (int s = 0; s < 8; ++s) acc = mfma32(av[s], *(LAS v4u*)(p + 64 * s), acc);
      if (col < 2) *(LAS v2u*)(sm + L_CT + col * CTP + (16 * w + 4 * g4) * 2) = v2u{pk(acc[0], acc[1]), pk(acc[2], acc[3])};
    }
    BAR();
    {
      f4 a0 = {0.f, 0.f, 0.f, 0.f}, a1 = a0;
      LAS char* p = sm + L_CT + (col & 1) * CTP + 16 * g4;
#pragma unroll
      for (int s = 0; s < 4; ++s) {
        const v4u bfr = *(LAS v4u*)(p + 64 * s);
        a0 = mfma32(ao[s], bfr, a0);
        a1 = mfma32(ao[4 + s], bfr, a1);
      }
      if (col & 2) a0 = a1;
      if (col < 4)
        st_sc1(rs, WS_OPT + ((16 * mb + 2 * w + (col >> 1)) * 64 + 2 * h + ca) * 128 + ((col & 1) * 4 + g4) * 16,
               v4u{pk(a0[0], a0[1]), T, pk(a0[2], a0[3]), T});
    }
    STAMP(9);
    PROBE_T(T);
  } else {
    const int a = r - 16, b = xcd >> 2, n = 8 * (xcd & 3) + (a >> 1), g = a & 1;
    unsigned ep;
    gld4_sc1(ep, ws, 0u);
#pragma unroll
    for (int i = 0; i < 16; ++i) {
      const int row = 16 * w + ((i + 8 * g) & 15);
      dma16(ckv, (unsigned)(((b * SK + 128 * n + row) * DC + 8 * l) * 2), smb + L_C + row * CP);
    }
    asm volatile("s_waitcnt vmcnt(0)" : "+v"(ep)::"memory");
    const unsigned T = ep + 1u;
    PROBE_T(T);
    BAR();
    STAMP(1);
    v4u fr[32];
    if (w < 4) {
      LAS char* p = sm + L_C + (32 * w + col) * CP + 16 * g4;
#pragma unroll
      for (int kt = 0; kt < 2; ++kt)
#pragma unroll
        for (int s = 0; s < 16; ++s) fr[16 * kt + s] = *(LAS v4u*)(p + 16 * kt * CP + 64 * s);
    } else {
      LAS char* p = sm + L_C + (4 * g4 + tq) * CP + 256 * (w - 4) + 8 * tp;
#pragma unroll
      for (int ct = 0; ct < 8; ++ct)
#pragma unroll
        for (int u = 0; u < 4; ++u) fr[4 * ct + u] = trrd2(p + 32 * u * CP + 32 * ct, p + (32 * u + 16) * CP + 32 * ct);
    }
    STAMP(2);
    {
      const int o = WS_QA + (b * NH + 16 * g) * 2048 + tid * 16;
      v4u q[4];
      for (unsigned spin = 0;; ++spin) {
        asm volatile("" ::: "memory");
#pragma unroll
        for (int i = 0; i < 4; ++i) q[i] = ld_sc1(rs, o + 8192 * i);
        bool ok = true;
#pragma unroll
        for (int i = 0; i < 4; ++i) ok &= (q[i].y == T) & (q[i].w == T);
        if (__all(ok) || spin > SPIN_CAP) break;
      }
      STAMP(3);
#pragma unroll
      for (int i = 0; i < 4; ++i) *(LAS v2u*)(sm + L_Q + (4 * i + (tid >> 7)) * CP + (tid & 127) * 8) = v2u{q[i].x, q[i].z};
    }
    BAR();
    STAMP(4);
    if (w < 4) {
      f4 s0 = {0.f, 0.f, 0.f, 0.f}, s1 = s0;
      LAS char* p = sm + L_Q + col * CP + 16 * g4;
      v4u qf[16];
#pragma unroll
      for (int s = 0; s < 16; ++s) qf[s] = *(LAS v4u*)(p + 64 * s);
      __builtin_amdgcn_sched_barrier(0);
#pragma unroll
      for (int s = 0; s < 16; ++s) {
        s0 = mfma32(fr[s], qf[s], s0);
        s1 = mfma32(fr[16 + s], qf[s], s1);
      }
      float mx = fmax2(fmax2(fmax2(s0[0], s0[1]), fmax2(s0[2], s0[3])), fmax2(fmax2(s1[0], s1[1]), fmax2(s1[2], s1[3])));
      mx = sw32_max(sw16_max(mx));
      if (g4 == 0) *(LAS float*)(sm + L_MX + col * 16 + w * 4) = mx;
      STAMP(5);
      BAR();
      const f4 m4 = *(LAS f4*)(sm + L_MX + col * 16);
      const float M = fmax2(fmax2(m4[0], m4[1]), fmax2(m4[2], m4[3]));
      float ps = 0.f;
#pragma unroll
      for (int e = 0; e < 4; ++e) {
        s0[e] = __builtin_amdgcn_exp2f(s0[e] - M);
        s1[e] = __builtin_amdgcn_exp2f(s1[e] - M);
        ps += s0[e] + s1[e];
      }
      *(LAS v4u*)(sm + L_P + w * 1024 + l * 16) = v4u{pk(s0[0], s0[1]), pk(s0[2], s0[3]), pk(s1[0], s1[1]), pk(s1[2], s1[3])};
      ps = sw16_add(ps, ps);
      ps = sw32_add(ps, ps);
      if (g4 == 0) *(LAS float*)(sm + L_LS + col * 16 + w * 4) = ps;
      BAR();
      STAMP(6);
      if (w == 0) {
        const f4 l4 = *(LAS f4*)(sm + L_LS + col * 16);
        const float lse = M + __builtin_amdgcn_logf((l4[0] + l4[1]) + (l4[2] + l4[3]));
        if (g4 == 0)
          __builtin_amdgcn_raw_buffer_store_b64(v2u{__builtin_bit_cast(unsigned, lse), T}, rs,
                                                WS_LSE + ((b * 2 + g) * 32 + n) * 128 + col * 8, 0, AUX_SC1);
      }
    } else {
      BAR();
      BAR();
      const int v = w - 4;
      v4u pf[4];
#pragma unroll
      for (int u = 0; u < 4; ++u) pf[u] = *(LAS v4u*)(sm + L_P + u * 1024 + l * 16);
      const f4 l4 = *(LAS f4*)(sm + L_LS + col * 16);
      const float il = __builtin_amdgcn_rcpf((l4[0] + l4[1]) + (l4[2] + l4[3]));
      const int o = WS_PART + ((((16 * g + (col & 7)) * 2 + (v >> 1)) * 2 + b) * 32 + n) * 512 + 256 * (v & 1) + (col >> 3) * 64 +
                    (g4 & 1) * 32 + (g4 >> 1) * 16;
#pragma unroll
      for (int pp = 0; pp < 2; ++pp) {
        v4u R[2];
#pragma unroll
        for (int k = 0; k < 2; ++k) {
          const int pr = 2 * pp + k;
          f4 c0 = {0.f, 0.f, 0.f, 0.f}, c1 = c0;
#pragma unroll
          for (int u = 0; u < 4; ++u) {
            c0 = mfma32(fr[8 * pr + u], pf[u], c0);
            c1 = mfma32(fr[8 * pr + 4 + u], pf[u], c1);
          }
          const unsigned x0 = min(pk(c0[0] * il, c0[1] * il), 0xfffffffeu), x1 = min(pk(c0[2] * il, c0[3] * il), 0xfffffffeu);
          const unsigned y0 = min(pk(c1[0] * il, c1[1] * il), 0xfffffffeu), y1 = min(pk(c1[2] * il, c1[3] * il), 0xfffffffeu);
          auto r0 = __builtin_amdgcn_permlane16_swap(x0, y0, false, false);
          auto r1 = __builtin_amdgcn_permlane16_swap(x1, y1, false, false);
          R[k] = v4u{r0[0], r1[0], r0[1], r1[1]};
        }
        v4u A, B;
#pragma unroll
        for (int e = 0; e < 4; ++e) {
          A[e] = (unsigned)__builtin_amdgcn_update_dpp((int)R[0][e], (int)R[1][e], ROR8, 0xf, 0xc, false);
          B[e] = (unsigned)__builtin_amdgcn_update_dpp((int)R[1][e], (int)R[0][e], ROR8, 0xf, 0x3, false);
        }
        st_sc1(rs, o + 128 * pp, A);
        st_sc1(rs, o + 128 * pp + 8 * 65536, B);
      }
      STAMP(7);
    }
    const int sl = 12 * xcd + a;
    if (a < 12) {
      if (sl < NSL) {
        BAR();
        v4u xr0, xr1, xf[8];
#pragma unroll
        for (int i = 0; i < 2; ++i) {
          const int k = 2 * w + i;
          const unsigned o = (unsigned)(((32 * k + (l >> 1)) * FF + 16 * sl + 8 * (l & 1)) * 2);
          dma16(wg, o, smb + L_GG + k * 1024);
          dma16(wu, o, smb + L_GU + k * 1024);
          dma16(wd, (unsigned)(((16 * sl + k) * DM + 8 * l) * 2), smb + L_GD + k * CP);
        }
        gld16(xr0, x, l * 16u);
        gld16(xr1, x, 1024u + l * 16u);
#pragma unroll
        for (int s = 0; s < 8; ++s) gld16(xf[s], x, (unsigned)(((col & 1) * DM + 256 * (w & 1) + 32 * s + 8 * g4) * 2));
        asm volatile("s_waitcnt vmcnt(0)"
                     : "+v"(xr0), "+v"(xr1), "+v"(xf[0]), "+v"(xf[1]), "+v"(xf[2]), "+v"(xf[3]), "+v"(xf[4]), "+v"(xf[5]), "+v"(xf[6]),
                       "+v"(xf[7])::"memory");
        BAR();
        STAMP(8);
        if (w < 4) {
          if (col & 2) {
#pragma unroll
            for (int s = 0; s < 8; ++s) xf[s] = v4u{0x3f803f80u, 0x3f803f80u, 0x3f803f80u, 0x3f803f80u};
          }
          LAS char* p = sm + L_GG + (w >> 1) * 16384 + 32 * (256 * (w & 1) + 8 * g4 + tq) + 8 * tp;
          f4 acc = {0.f, 0.f, 0.f, 0.f};
#pragma unroll
          for (int s = 0; s < 8; ++s) acc = mfma32(trrd2(p + 32 * 32 * s, p + 32 * (32 * s + 4)), xf[s], acc);
          *(LAS f4*)(sm + L_GR + w * 1024 + l * 16) = acc;
        }
        float m0, r0, m1, r1;
        ln_stats(xr0, m0, r0);
        ln_stats(xr1, m1, r1);
        const float mean = (col & 1) ? m1 : m0, rstd = (col & 1) ? r1 : r0;
        v2u dn[4];
#pragma unroll
        for (int t = 0; t < 4; ++t) dn[t] = trrd(sm + L_GD + (4 * g4 + tq) * CP + 32 * (4 * w + t) + 8 * tp);
        BAR();
        LAS char* pr = sm + L_GR + (16 * g4 + (col & 1)) * 16;
        LAS char* po = sm + L_GR + (16 * g4 + 2) * 16;
        const f4 gx = *(LAS f4*)(pr) + *(LAS f4*)(pr + 1024), go = *(LAS f4*)(po) + *(LAS f4*)(po + 1024);
        const f4 ux = *(LAS f4*)(pr + 2048) + *(LAS f4*)(pr + 3072), uo = *(LAS f4*)(po + 2048) + *(LAS f4*)(po + 3072);
        float hh[4];
#pragma unroll
        for (int e = 0; e < 4; ++e) {
          const float gv = rstd * (gx[e] - mean * go[e]), uv = rstd * (ux[e] - mean * uo[e]);
          hh[e] = uv * __builtin_amdgcn_rcpf(1.f + __builtin_amdgcn_exp2f(-1.4426950408889634f * gv));
        }
        const v2u hb = {pk(hh[0], hh[1]), pk(hh[2], hh[3])};
        const f4 z = {0.f, 0.f, 0.f, 0.f};
        f4 d0 = mfma16(dn[0], hb, z), d1 = mfma16(dn[1], hb, z), d2 = mfma16(dn[2], hb, z), d3 = mfma16(dn[3], hb, z);
        if (col & 2) {
          d0 = d1;
          d2 = d3;
        }
        if (col & 4) d0 = d2;
        if (col < 8)
          st_sc1(rs, WS_OPG + ((4 * w + (col >> 1)) * 96 + sl) * 128 + ((col & 1) * 4 + g4) * 16,
                 v4u{pk(d0[0], d0[1]), T, pk(d0[2], d0[3]), T});
        STAMP(9);
      }
    } else {
      const int rd = 4 * xcd + (a - 12);
      const int ln = tid >> 3;
      const int ot = WS_OPT + rd * 8192 + tid * 16, og = WS_OPG + rd * 96 * 128 + tid * 16;
      const bool two = ln < NSL - 64;
      const int og2 = two ? og + 8192 : og;
      float xres = 0.f;
      if (tid < 32) xres = blo(x[(tid >> 4) * DM + 16 * rd + (tid & 15)]);
      v4u p0, p1, p2;
      for (unsigned spin = 0;; ++spin) {
        asm volatile("" ::: "memory");
        p0 = ld_sc1(rs, ot);
        p1 = ld_sc1(rs, og);
        p2 = ld_sc1(rs, og2);
        const bool ok = (p0.y == T) & (p0.w == T) & (p1.y == T) & (p1.w == T) & (p2.y == T) & (p2.w == T);
        if (__all(ok) || spin > SPIN_CAP) break;
      }
      STAMP(8);
      if (!two) p2 = v4u{0u, 0u, 0u, 0u};
      const float v0 = blo(p0.x) + (blo(p1.x) + blo(p2.x)), v1 = bhi(p0.x) + (bhi(p1.x) + bhi(p2.x));
      const float v2 = blo(p0.z) + (blo(p1.z) + blo(p2.z)), v3 = bhi(p0.z) + (bhi(p1.z) + bhi(p2.z));
      float s = sw16_add(sw32_add(v0, v2), sw32_add(v1, v3));
      s += dpp<ROR8>(s);
      if (!(l & 8)) *(LAS float*)(sm + L_RR + w * 128 + (4 * (l & 7) + g4) * 4) = s;
      BAR();
      if (tid < 32) {
        float t = xres;
#pragma unroll
        for (int i = 0; i < 8; ++i) t += *(LAS float*)(sm + L_RR + i * 128 + tid * 4);
        out[(tid >> 4) * DM + 16 * rd + (tid & 15)] = (u16)(pk(t, t) & 0xffffu);
        if (rd == 0 && tid == 0) __builtin_amdgcn_raw_buffer_store_b32(T, rs, WS_E, 0, AUX_SC1);
      }
      STAMP(9);
    }
  }
  STAMP_FLUSH();
}

extern "C" void kernel_launch(void* const* d_in, const int* in_sizes, int n_in, void* d_out, int out_size, void* d_ws, size_t ws_size,
                              hipStream_t stream) {
  mla_glu_decode<<<256, 512, 0, stream>>>((const u16*)d_in[0], (const u16*)d_in[1], (const u16*)d_in[2], (const u16*)d_in[3],
                                          (const u16*)d_in[4], (const u16*)d_in[5], (const u16*)d_in[7], (const u16*)d_in[8],
                                          (const u16*)d_in[9], (u16*)d_out, (char*)d_ws);
}
```

```cpp
#include <hip/hip_runtime.h>
#include <stdint.h>

#define GAS __attribute__((address_space(1)))
#define LAS __attribute__((address_space(3)))
typedef unsigned short u16;
typedef unsigned v4u __attribute__((ext_vector_type(4)));
typedef unsigned v2u __attribute__((ext_vector_type(2)));
typedef float f4 __attribute__((ext_vector_type(4)));
typedef float f2 __attribute__((ext_vector_type(2)));
typedef __bf16 bf8 __attribute__((ext_vector_type(8)));
typedef __bf16 bf2 __attribute__((ext_vector_type(2)));
typedef short s4 __attribute__((ext_vector_type(4)));

namespace {
constexpr int NH = 32, SK = 4096, DH = 128, DM = 512, DC = 512, FF = 1376;
constexpr int NSL = FF / 16;
constexpr int WS_E = 0;
constexpr int WS_QP = 4096;
constexpr int WS_QA = WS_QP + NH * 4 * 1024;
constexpr int WS_LSE = WS_QA + 2 * NH * 2048;
constexpr int WS_OPT = WS_LSE + 2 * 2 * 32 * 128;
constexpr int WS_OPG = WS_OPT + 32 * 64 * 128;
constexpr int WS_PART = WS_OPG + 32 * 96 * 128;
constexpr int WS_BYTES = WS_PART + NH * 2 * 2 * 32 * 512;
constexpr int WS_STAMP = 8 << 20;
constexpr int CP = 1056;
constexpr int L_C = 0, L_Q = 128 * CP, L_P = L_Q + 16 * CP, L_MX = L_P + 4096, L_LS = L_MX + 256, L_RR = L_LS + 256,
              L_END = L_RR + 1024;
constexpr int L_GG = 0, L_GU = 16384, L_GD = 32768, L_GR = 65536;
constexpr int L_WUV = 0, L_WO = 65536, L_WQ = 65536, L_WUK = 98304, L_CC = 131072, CCP = 576, L_CT = L_CC + 2048, CTP = 320;
constexpr unsigned SPIN_CAP = 1u << 20;
constexpr int AUX_SC1 = 16;

#define STAMP(i)
#define STAMP_FLUSH()
#define PROBE_T(T_)

__device__ __forceinline__ f4 mfma32(v4u a, v4u b, f4 c) {
  return __builtin_amdgcn_mfma_f32_16x16x32_bf16(__builtin_bit_cast(bf8, a), __builtin_bit_cast(bf8, b), c, 0, 0, 0);
}
__device__ __forceinline__ f4 mfma16(v2u a, v2u b, f4 c) {
  return __builtin_amdgcn_mfma_f32_16x16x16bf16_1k(__builtin_bit_cast(s4, a), __builtin_bit_cast(s4, b), c, 0, 0, 0);
}
__device__ __forceinline__ unsigned pk(float lo, float hi) {
  f2 x = {lo, hi};
  return __builtin_bit_cast(unsigned, __builtin_convertvector(x, bf2));
}
__device__ __forceinline__ float asf(unsigned u) { return __builtin_bit_cast(float, u); }
__device__ __forceinline__ float blo(unsigned u) { return __builtin_bit_cast(float, u << 16); }
__device__ __forceinline__ float bhi(unsigned u) { return __builtin_bit_cast(float, u & 0xffff0000u); }
template <int CTRL> __device__ __forceinline__ float dpp(float v) {
  return __builtin_bit_cast(float, __builtin_amdgcn_update_dpp(0, __builtin_bit_cast(int, v), CTRL, 0xf, 0xf, true));
}
__device__ __forceinline__ float fmax2(float a, float b) { return __builtin_amdgcn_fmed3f(a, b, __builtin_inff()); }
constexpr int ROR1 = 0x121, ROR2 = 0x122, ROR4 = 0x124, ROR8 = 0x128, QP_2301 = 0x4e, QP_2222 = 0xaa;
__device__ __forceinline__ float sw32_add(float a, float b) {
  auto r = __builtin_amdgcn_permlane32_swap(__builtin_bit_cast(unsigned, a), __builtin_bit_cast(unsigned, b), false, false);
  return asf(r[0]) + asf(r[1]);
}
__device__ __forceinline__ float sw16_add(float a, float b) {
  auto r = __builtin_amdgcn_permlane16_swap(__builtin_bit_cast(unsigned, a), __builtin_bit_cast(unsigned, b), false, false);
  return asf(r[0]) + asf(r[1]);
}
__device__ __forceinline__ float sw32_max(float a) {
  auto r = __builtin_amdgcn_permlane32_swap(__builtin_bit_cast(unsigned, a), __builtin_bit_cast(unsigned, a), false, false);
  return fmax2(asf(r[0]), asf(r[1]));
}
__device__ __forceinline__ float sw16_max(float a) {
  auto r = __builtin_amdgcn_permlane16_swap(__builtin_bit_cast(unsigned, a), __builtin_bit_cast(unsigned, a), false, false);
  return fmax2(asf(r[0]), asf(r[1]));
}
__device__ __forceinline__ float wave_sum(float v) {
  v += dpp<ROR8>(v);
  v += dpp<ROR4>(v);
  v += dpp<ROR2>(v);
  v += dpp<ROR1>(v);
  v = sw16_add(v, v);
  return sw32_add(v, v);
}
__device__ __forceinline__ v2u trrd(LAS char* p) {
  return __builtin_bit_cast(v2u, __builtin_amdgcn_ds_read_tr16_b64_v4i16((LAS s4*)p));
}
__device__ __forceinline__ v4u trrd2(LAS char* p0, LAS char* p1) {
  v2u a = trrd(p0), b = trrd(p1);
  return v4u{a.x, a.y, b.x, b.y};
}
#define BAR() asm volatile("s_waitcnt lgkmcnt(0)\n\ts_barrier" ::: "memory")
#define VMWAIT(n) asm volatile("s_waitcnt vmcnt(" #n ")" ::: "memory")
__device__ __forceinline__ void gld16(v4u& d, const void* base, unsigned off) {
  asm volatile("global_load_dwordx4 %0, %1, %2" : "=v"(d) : "v"(off), "s"(base) : "memory");
}
__device__ __forceinline__ void gld4_sc1(unsigned& d, const void* base, unsigned off) {
  asm volatile("global_load_dword %0, %1, %2 sc1" : "=v"(d) : "v"(off), "s"(base) : "memory");
}
__device__ __forceinline__ void dma16(const void* base, unsigned off, unsigned lds) {
  unsigned keep;
  asm volatile("s_mov_b32 %0, m0\n\ts_mov_b32 m0, %3\n\ts_nop 0\n\tglobal_load_lds_dwordx4 %1, %2\n\ts_mov_b32 m0, %0"
               : "=&s"(keep)
               : "v"(off), "s"(base), "s"(lds)
               : "memory");
}
__device__ __forceinline__ v4u ld_sc1(__amdgpu_buffer_rsrc_t rs, int off) {
  return __builtin_amdgcn_raw_buffer_load_b128(rs, off, 0, AUX_SC1);
}
__device__ __forceinline__ v2u ld8_sc1(__amdgpu_buffer_rsrc_t rs, int off) {
  return __builtin_amdgcn_raw_buffer_load_b64(rs, off, 0, AUX_SC1);
}
__device__ __forceinline__ void st_sc1(__amdgpu_buffer_rsrc_t rs, int off, v4u v) {
  __builtin_amdgcn_raw_buffer_store_b128(v, rs, off, 0, AUX_SC1);
}
__device__ __forceinline__ void ln_stats(v4u r, float& mean, float& rstd) {
  float e[8] = {blo(r.x), bhi(r.x), blo(r.y), bhi(r.y), blo(r.z), bhi(r.z), blo(r.w), bhi(r.w)};
  float s = ((e[0] + e[1]) + (e[2] + e[3])) + ((e[4] + e[5]) + (e[6] + e[7]));
  mean = wave_sum(s) * (1.f / DM);
  float q = 0.f;
#pragma unroll
  for (int i = 0; i < 8; ++i) {
    float d = e[i] - mean;
    q = fmaf(d, d, q);
  }
  rstd = __builtin_amdgcn_rsqf(wave_sum(q) * (1.f / DM) + 1e-5f);
}
}

__global__ void __launch_bounds__(512, 2)
mla_glu_decode(const u16* __restrict__ x, const u16* __restrict__ ckv, const u16* __restrict__ wq, const u16* __restrict__ wuk,
               const u16* __restrict__ wuv, const u16* __restrict__ wo, const u16* __restrict__ wg, const u16* __restrict__ wu,
               const u16* __restrict__ wd, u16* __restrict__ out, char* __restrict__ ws) {
  const unsigned long long xstart = __builtin_amdgcn_s_memrealtime();
  __builtin_amdgcn_sched_barrier(0);
  __shared__ __attribute__((aligned(16))) char smem_[L_END];
  LAS char* sm = (LAS char*)smem_;
  const unsigned smb = (unsigned)(size_t)sm;
  const int tid = threadIdx.x, l = tid & 63, w = __builtin_amdgcn_readfirstlane(tid >> 6);
  const int col = l & 15, g4 = l >> 4, tq = (l & 15) >> 2, tp = l & 3;
  const int xcd = blockIdx.x & 7, r = blockIdx.x >> 3;
  const __amdgpu_buffer_rsrc_t rs = __builtin_amdgcn_make_buffer_rsrc(ws, 0, WS_BYTES, 0x00020000);
  asm volatile("" ::"s"(x), "s"(ckv), "s"(wq), "s"(wuk), "s"(wuv), "s"(wo), "s"(wg), "s"(wu), "s"(wd), "s"(out), "s"(ws));
  STAMP(0);

  if (__builtin_expect(r < 16, 1)) {
    const int h = 4 * xcd + (r >> 2), j = r & 3, ca = j >> 1, mb = j & 1;
    v4u xr0, xr1, xf[4];
    unsigned ep;
    gld4_sc1(ep, ws, 0u);
    gld16(xr0, x, l * 16u);
    gld16(xr1, x, 1024u + l * 16u);
#pragma unroll
    for (int s = 0; s < 4; ++s) gld16(xf[s], x, (unsigned)(((col & 1) * DM + 128 * j + 32 * s + 8 * g4) * 2));
#pragma unroll
    for (int i = 0; i < 4; ++i) {
      const int k = 4 * w + i, row = 4 * k + g4, f = (row & 3) | ((row >> 1) & 4);
      dma16(wq, (unsigned)(((128 * j + row) * 4096 + h * DH + 8 * (col ^ (f << 1))) * 2), smb + L_WQ + k * 1024);
    }
    if (w < 4) {
#pragma unroll
      for (int i = 0; i < 8; ++i) {
        const int row = 32 * w + 4 * i + g4;
        dma16(wuk, (unsigned)((((128 * j + row) * NH + h) * DH + 8 * (col ^ (row & 15))) * 2), smb + L_WUK + (8 * w + i) * 1024);
      }
      VMWAIT(12);
    } else {
      const v4u ones = {~0u, ~0u, ~0u, ~0u};
      const int cb = WS_PART + ((h * 2 + ca) * 2 + mb) * 16384 + (tid - 256) * 16;
#pragma unroll
      for (int i = 0; i < 4; ++i) st_sc1(rs, cb + 4096 * i, ones);
      VMWAIT(8);
    }
    asm volatile("" : "+v"(ep), "+v"(xr0), "+v"(xr1), "+v"(xf[0]), "+v"(xf[1]), "+v"(xf[2]), "+v"(xf[3])::"memory");
    const unsigned T = ep + 1u;
    float m0, r0, m1, r1;
    ln_stats(xr0, m0, r0);
    ln_stats(xr1, m1, r1);
    const float mean = (col & 1) ? m1 : m0, rstd = (col & 1) ? r1 : r0;
    if (col & 2) {
#pragma unroll
      for (int s = 0; s < 4; ++s) xf[s] = v4u{0x3f803f80u, 0x3f803f80u, 0x3f803f80u, 0x3f803f80u};
    }
    STAMP(1);
    if (w < 4) VMWAIT(8);
    else VMWAIT(0);
    BAR();
    STAMP(2);
    const int fl = tq | ((g4 & 1) << 2);
    {
      LAS char* p = sm + L_WQ + 256 * (8 * g4 + tq) + 16 * ((2 * w + (tp >> 1)) ^ (fl << 1)) + 8 * (tp & 1);
      v4u af[4];
#pragma unroll
      for (int s = 0; s < 4; ++s) af[s] = trrd2(p + 256 * 32 * s, p + 256 * (32 * s + 4));
      __builtin_amdgcn_sched_barrier(0);
      f4 acc = {0.f, 0.f, 0.f, 0.f};
#pragma unroll
      for (int s = 0; s < 4; ++s) acc = mfma32(af[s], xf[s], acc);
      float q[4];
#pragma unroll
      for (int e = 0; e < 4; ++e) q[e] = rstd * (acc[e] - mean * dpp<QP_2222>(acc[e]));
      if (col < 2) st_sc1(rs, WS_QP + ((h * 4 + j) * 8 + w) * 128 + col * 64 + g4 * 16, v4u{pk(q[0], q[1]), T, pk(q[2], q[3]), T});
    }
    STAMP(3);
    if (w < 4) {
      VMWAIT(1);
      v4u uk[8];
#pragma unroll
      for (int t = 0; t < 2; ++t)
#pragma unroll
        for (int s = 0; s < 4; ++s) uk[4 * t + s] = *(LAS v4u*)(sm + L_WUK + 256 * (32 * w + 16 * t + col) + 16 * ((4 * s + g4) ^ col));
      const int o = WS_QP + ((h * 4 + ((col >> 1) & 3)) * 8 + (g4 >> 1)) * 128 + (col & 1) * 64 + (g4 & 1) * 32;
      v4u a[8];
      for (unsigned spin = 0;; ++spin) {
        asm volatile("" ::: "memory");
#pragma unroll
        for (int i = 0; i < 8; ++i) a[i] = ld_sc1(rs, o + 256 * (i >> 1) + 16 * (i & 1));
        bool ok = true;
#pragma unroll
        for (int i = 0; i < 8; ++i) ok &= (a[i].y == T) & (a[i].w == T);
        if (__all(ok) || spin > SPIN_CAP) break;
      }
      STAMP(4);
      f4 a0 = {0.f, 0.f, 0.f, 0.f}, a1 = a0;
#pragma unroll
      for (int s = 0; s < 4; ++s) {
        const v4u qf = {a[2 * s].x, a[2 * s].z, a[2 * s + 1].x, a[2 * s + 1].z};
        a0 = mfma32(uk[s], qf, a0);
        a1 = mfma32(uk[4 + s], qf, a1);
      }
      float q[4];
#pragma unroll
      for (int e = 0; e < 4; ++e) {
        float v0 = a0[e] + dpp<QP_2301>(a0[e]), v1 = a1[e] + dpp<QP_2301>(a1[e]);
        float v = (col & 2) ? v1 : v0;
        v += dpp<ROR4>(v);
        q[e] = v * (0.08838834764831845f * 1.4426950408889634f);
      }
      if (col < 4)
        st_sc1(rs, WS_QA + ((col & 1) * NH + h) * 2048 + (64 * j + 16 * w + 8 * (col >> 1) + 2 * g4) * 8,
               v4u{pk(q[0], q[1]), T, pk(q[2], q[3]), T});
    }
    STAMP(5);
    BAR();
#pragma unroll
    for (int i = 0; i < 8; ++i) {
      const int k = 8 * w + ((i + 4 * mb) & 7), row = 4 * k + g4, f = (row & 3) | ((row >> 1) & 4);
      dma16(wuv, (unsigned)((((256 * ca + row) * NH + h) * DH + 8 * (col ^ (f << 1))) * 2), smb + L_WUV + k * 1024);
    }
#pragma unroll
    for (int i = 0; i < 8; ++i) {
      const int k = 8 * w + ((i + 4 * ca) & 7), row = 2 * k + (l >> 5), f = (row & 3) | ((row >> 1) & 4);
      dma16(wo, (unsigned)(((h * DH + row) * DM + 256 * mb + 8 * ((l & 31) ^ (f << 1))) * 2), smb + L_WO + k * 1024);
    }
    VMWAIT(0);
    BAR();
    STAMP(6);
    v4u av[8], ao[8];
    {
      LAS char* p = sm + L_WUV + 256 * (8 * g4 + tq) + 16 * ((2 * w + (tp >> 1)) ^ (fl << 1)) + 8 * (tp & 1);
#pragma unroll
      for (int s = 0; s < 8; ++s) av[s] = trrd2(p + 256 * 32 * s, p + 256 * (32 * s + 4));
#pragma unroll
      for (int t = 0; t < 2; ++t) {
        LAS char* po = sm + L_WO + 512 * (8 * g4 + tq) + 16 * ((4 * w + 2 * t + (tp >> 1)) ^ (fl << 1)) + 8 * (tp & 1);
#pragma unroll
        for (int s = 0; s < 4; ++s) ao[4 * t + s] = trrd2(po + 512 * 32 * s, po + 512 * (32 * s + 4));
      }
    }
    STAMP(7);
    {
      const int b = w >> 2, cw = w & 3, nr = l >> 3;
      const int o = WS_PART + (((h * 2 + ca) * 2 + b) * 32 + nr) * 512 + 128 * cw + 16 * (l & 7);
      const int ol = WS_LSE + ((b * 2 + (h >> 4)) * 32 + nr) * 128 + (h & 15) * 8;
      v4u a[4];
      v2u ls[4];
      for (unsigned spin = 0;; ++spin) {
        asm volatile("" ::: "memory");
#pragma unroll
        for (int i = 0; i < 4; ++i) ls[i] = ld8_sc1(rs, ol + 1024 * i);
#pragma unroll
        for (int i = 0; i < 4; ++i) a[i] = ld_sc1(rs, o + 4096 * i);
        bool ok = true;
        unsigned mx = 0u;
#pragma unroll
        for (int i = 0; i < 4; ++i) {
          ok &= ls[i].y == T;
          mx = max(max(mx, a[i].x), max(a[i].y, max(a[i].z, a[i].w)));
        }
        ok &= mx != ~0u;
        if (__all(ok) || spin > SPIN_CAP) break;
      }
      STAMP(8);
      float e[4];
#pragma unroll
      for (int i = 0; i < 4; ++i) e[i] = asf(ls[i].x);
      float M = fmax2(fmax2(e[0], e[1]), fmax2(e[2], e[3]));
      M = fmax2(M, dpp<ROR8>(M));
      M = sw32_max(sw16_max(M));
#pragma unroll
      for (int i = 0; i < 4; ++i) e[i] = __builtin_amdgcn_exp2f(e[i] - M);
      float L = (e[0] + e[1]) + (e[2] + e[3]);
      L += dpp<ROR8>(L);
      L = sw16_add(L, L);
      L = sw32_add(L, L);
      float acc[8];
#pragma unroll
      for (int c = 0; c < 4; ++c) {
        acc[2 * c] = e[0] * blo(a[0][c]);
        acc[2 * c + 1] = e[0] * bhi(a[0][c]);
      }
#pragma unroll
      for (int i = 1; i < 4; ++i)
#pragma unroll
        for (int c = 0; c < 4; ++c) {
          acc[2 * c] = fmaf(e[i], blo(a[i][c]), acc[2 * c]);
          acc[2 * c + 1] = fmaf(e[i], bhi(a[i][c]), acc[2 * c + 1]);
        }
      float rr[4];
#pragma unroll
      for (int c = 0; c < 4; ++c) rr[c] = sw32_add(acc[c], acc[c + 4]);
      float s0 = sw16_add(rr[0], rr[2]), s1 = sw16_add(rr[1], rr[3]);
      s0 += dpp<ROR8>(s0);
      s1 += dpp<ROR8>(s1);
      const float il = __builtin_amdgcn_rcpf(L);
      if (!(l & 8)) *(LAS unsigned*)(sm + L_CC + b * CCP + (64 * cw + 8 * (l & 7) + 2 * g4) * 2) = pk(s0 * il, s1 * il);
    }
    BAR();
    {
      f4 acc = {0.f, 0.f, 0.f, 0.f};
      LAS char* p = sm + L_CC + (col & 1) * CCP + 16 * g4;
#pragma unroll
      for (int s = 0; s < 8; ++s) acc = mfma32(av[s], *(LAS v4u*)(p + 64 * s), acc);
      if (col < 2) *(LAS v2u*)(sm + L_CT + col * CTP + (16 * w + 4 * g4) * 2) = v2u{pk(acc[0], acc[1]), pk(acc[2], acc[3])};
    }
    BAR();
    {
      f4 a0 = {0.f, 0.f, 0.f, 0.f}, a1 = a0;
      LAS char* p = sm + L_CT + (col & 1) * CTP + 16 * g4;
#pragma unroll
      for (int s = 0; s < 4; ++s) {
        const v4u bfr = *(LAS v4u*)(p + 64 * s);
        a0 = mfma32(ao[s], bfr, a0);
        a1 = mfma32(ao[4 + s], bfr, a1);
      }
      if (col & 2) a0 = a1;
      if (col < 4)
        st_sc1(rs, WS_OPT + ((16 * mb + 2 * w + (col >> 1)) * 64 + 2 * h + ca) * 128 + ((col & 1) * 4 + g4) * 16,
               v4u{pk(a0[0], a0[1]), T, pk(a0[2], a0[3]), T});
    }
    STAMP(9);
    PROBE_T(T);
  } else {
    const int a = r - 16, b = xcd >> 2, n = 8 * (xcd & 3) + (a >> 1), g = a & 1;
    unsigned ep;
    gld4_sc1(ep, ws, 0u);
#pragma unroll
    for (int i = 0; i < 16; ++i) {
      const int row = 16 * w + ((i + 8 * g) & 15);
      dma16(ckv, (unsigned)(((b * SK + 128 * n + row) * DC + 8 * l) * 2), smb + L_C + row * CP);
    }
    asm volatile("s_waitcnt vmcnt(0)" : "+v"(ep)::"memory");
    const unsigned T = ep + 1u;
    PROBE_T(T);
    BAR();
    STAMP(1);
    v4u fr[32];
    if (w < 4) {
      LAS char* p = sm + L_C + (32 * w + col) * CP + 16 * g4;
#pragma unroll
      for (int kt = 0; kt < 2; ++kt)
#pragma unroll
        for (int s = 0; s < 16; ++s) fr[16 * kt + s] = *(LAS v4u*)(p + 16 * kt * CP + 64 * s);
    } else {
      LAS char* p = sm + L_C + (4 * g4 + tq) * CP + 256 * (w - 4) + 8 * tp;
#pragma unroll
      for (int ct = 0; ct < 8; ++ct)
#pragma unroll
        for (int u = 0; u < 4; ++u) fr[4 * ct + u] = trrd2(p + 32 * u * CP + 32 * ct, p + (32 * u + 16) * CP + 32 * ct);
    }
    STAMP(2);
    {
      const int o = WS_QA + (b * NH + 16 * g) * 2048 + tid * 16;
      v4u q[4];
      for (unsigned spin = 0;; ++spin) {
        asm volatile("" ::: "memory");
#pragma unroll
        for (int i = 0; i < 4; ++i) q[i] = ld_sc1(rs, o + 8192 * i);
        bool ok = true;
#pragma unroll
        for (int i = 0; i < 4; ++i) ok &= (q[i].y == T) & (q[i].w == T);
        if (__all(ok) || spin > SPIN_CAP) break;
      }
      STAMP(3);
#pragma unroll
      for (int i = 0; i < 4; ++i) *(LAS v2u*)(sm + L_Q + (4 * i + (tid >> 7)) * CP + (tid & 127) * 8) = v2u{q[i].x, q[i].z};
    }
    BAR();
    STAMP(4);
    if (w < 4) {
      f4 s0 = {0.f, 0.f, 0.f, 0.f}, s1 = s0;
      LAS char* p = sm + L_Q + col * CP + 16 * g4;
      v4u qf[16];
#pragma unroll
      for (int s = 0; s < 16; ++s) qf[s] = *(LAS v4u*)(p + 64 * s);
      __builtin_amdgcn_sched_barrier(0);
#pragma unroll
      for (int s = 0; s < 16; ++s) {
        s0 = mfma32(fr[s], qf[s], s0);
        s1 = mfma32(fr[16 + s], qf[s], s1);
      }
      float mx = fmax2(fmax2(fmax2(s0[0], s0[1]), fmax2(s0[2], s0[3])), fmax2(fmax2(s1[0], s1[1]), fmax2(s1[2], s1[3])));
      mx = sw32_max(sw16_max(mx));
      if (g4 == 0) *(LAS float*)(sm + L_MX + col * 16 + w * 4) = mx;
      STAMP(5);
      BAR();
      const f4 m4 = *(LAS f4*)(sm + L_MX + col * 16);
      const float M = fmax2(fmax2(m4[0], m4[1]), fmax2(m4[2], m4[3]));
      float ps = 0.f;
#pragma unroll
      for (int e = 0; e < 4; ++e) {
        s0[e] = __builtin_amdgcn_exp2f(s0[e] - M);
        s1[e] = __builtin_amdgcn_exp2f(s1[e] - M);
        ps += s0[e] + s1[e];
      }
      *(LAS v4u*)(sm + L_P + w * 1024 + l * 16) = v4u{pk(s0[0], s0[1]), pk(s0[2], s0[3]), pk(s1[0], s1[1]), pk(s1[2], s1[3])};
      ps = sw16_add(ps, ps);
      ps = sw32_add(ps, ps);
      if (g4 == 0) *(LAS float*)(sm + L_LS + col * 16 + w * 4) = ps;
      BAR();
      STAMP(6);
      if (w == 0) {
        const f4 l4 = *(LAS f4*)(sm + L_LS + col * 16);
        const float lse = M + __builtin_amdgcn_logf((l4[0] + l4[1]) + (l4[2] + l4[3]));
        if (g4 == 0)
          __builtin_amdgcn_raw_buffer_store_b64(v2u{__builtin_bit_cast(unsigned, lse), T}, rs,
                                                WS_LSE + ((b * 2 + g) * 32 + n) * 128 + col * 8, 0, AUX_SC1);
      }
    } else {
      BAR();
      BAR();
      const int v = w - 4;
      v4u pf[4];
#pragma unroll
      for (int u = 0; u < 4; ++u) pf[u] = *(LAS v4u*)(sm + L_P + u * 1024 + l * 16);
      const f4 l4 = *(LAS f4*)(sm + L_LS + col * 16);
      const float il = __builtin_amdgcn_rcpf((l4[0] + l4[1]) + (l4[2] + l4[3]));
      const int o = WS_PART + ((((16 * g + (col & 7)) * 2 + (v >> 1)) * 2 + b) * 32 + n) * 512 + 256 * (v & 1) + (col >> 3) * 64 +
                    (g4 & 1) * 32 + (g4 >> 1) * 16;
#pragma unroll
      for (int pp = 0; pp < 2; ++pp) {
        v4u R[2];
#pragma unroll
        for (int k = 0; k < 2; ++k) {
          const int pr = 2 * pp + k;
          f4 c0 = {0.f, 0.f, 0.f, 0.f}, c1 = c0;
#pragma unroll
          for (int u = 0; u < 4; ++u) {
            c0 = mfma32(fr[8 * pr + u], pf[u], c0);
            c1 = mfma32(fr[8 * pr + 4 + u], pf[u], c1);
          }
          const unsigned x0 = min(pk(c0[0] * il, c0[1] * il), 0xfffffffeu), x1 = min(pk(c0[2] * il, c0[3] * il), 0xfffffffeu);
          const unsigned y0 = min(pk(c1[0] * il, c1[1] * il), 0xfffffffeu), y1 = min(pk(c1[2] * il, c1[3] * il), 0xfffffffeu);
          auto r0 = __builtin_amdgcn_permlane16_swap(x0, y0, false, false);
          auto r1 = __builtin_amdgcn_permlane16_swap(x1, y1, false, false);
          R[k] = v4u{r0[0], r1[0], r0[1], r1[1]};
        }
        v4u A, B;
#pragma unroll
        for (int e = 0; e < 4; ++e) {
          A[e] = (unsigned)__builtin_amdgcn_update_dpp((int)R[0][e], (int)R[1][e], ROR8, 0xf, 0xc, false);
          B[e] = (unsigned)__builtin_amdgcn_update_dpp((int)R[1][e], (int)R[0][e], ROR8, 0xf, 0x3, false);
        }
        st_sc1(rs, o + 128 * pp, A);
        st_sc1(rs, o + 128 * pp + 8 * 65536, B);
      }
      STAMP(7);
    }
    const int sl = 12 * xcd + a;
    if (a < 12) {
      if (sl < NSL) {
        BAR();
        v4u xr0, xr1, xf[8];
#pragma unroll
        for (int i = 0; i < 2; ++i) {
          const int k = 2 * w + i;
          const unsigned o = (unsigned)(((32 * k + (l >> 1)) * FF + 16 * sl + 8 * (l & 1)) * 2);
          dma16(wg, o, smb + L_GG + k * 1024);
          dma16(wu, o, smb + L_GU + k * 1024);
          dma16(wd, (unsigned)(((16 * sl + k) * DM + 8 * l) * 2), smb + L_GD + k * CP);
        }
        gld16(xr0, x, l * 16u);
        gld16(xr1, x, 1024u + l * 16u);
#pragma unroll
        for (int s = 0; s < 8; ++s) gld16(xf[s], x, (unsigned)(((col & 1) * DM + 256 * (w & 1) + 32 * s + 8 * g4) * 2));
        asm volatile("s_waitcnt vmcnt(0)"
                     : "+v"(xr0), "+v"(xr1), "+v"(xf[0]), "+v"(xf[1]), "+v"(xf[2]), "+v"(xf[3]), "+v"(xf[4]), "+v"(xf[5]), "+v"(xf[6]),
                       "+v"(xf[7])::"memory");
        BAR();
        STAMP(8);
        if (w < 4) {
          if (col & 2) {
#pragma unroll
            for (int s = 0; s < 8; ++s) xf[s] = v4u{0x3f803f80u, 0x3f803f80u, 0x3f803f80u, 0x3f803f80u};
          }
          LAS char* p = sm + L_GG + (w >> 1) * 16384 + 32 * (256 * (w & 1) + 8 * g4 + tq) + 8 * tp;
          f4 acc = {0.f, 0.f, 0.f, 0.f};
#pragma unroll
          for (int s = 0; s < 8; ++s) acc = mfma32(trrd2(p + 32 * 32 * s, p + 32 * (32 * s + 4)), xf[s], acc);
          *(LAS f4*)(sm + L_GR + w * 1024 + l * 16) = acc;
        }
        float m0, r0, m1, r1;
        ln_stats(xr0, m0, r0);
        ln_stats(xr1, m1, r1);
        const float mean = (col & 1) ? m1 : m0, rstd = (col & 1) ? r1 : r0;
        v2u dn[4];
#pragma unroll
        for (int t = 0; t < 4; ++t) dn[t] = trrd(sm + L_GD + (4 * g4 + tq) * CP + 32 * (4 * w + t) + 8 * tp);
        BAR();
        LAS char* pr = sm + L_GR + (16 * g4 + (col & 1)) * 16;
        LAS char* po = sm + L_GR + (16 * g4 + 2) * 16;
        const f4 gx = *(LAS f4*)(pr) + *(LAS f4*)(pr + 1024), go = *(LAS f4*)(po) + *(LAS f4*)(po + 1024);
        const f4 ux = *(LAS f4*)(pr + 2048) + *(LAS f4*)(pr + 3072), uo = *(LAS f4*)(po + 2048) + *(LAS f4*)(po + 3072);
        float hh[4];
#pragma unroll
        for (int e = 0; e < 4; ++e) {
          const float gv = rstd * (gx[e] - mean * go[e]), uv = rstd * (ux[e] - mean * uo[e]);
          hh[e] = uv * __builtin_amdgcn_rcpf(1.f + __builtin_amdgcn_exp2f(-1.4426950408889634f * gv));
        }
        const v2u hb = {pk(hh[0], hh[1]), pk(hh[2], hh[3])};
        const f4 z = {0.f, 0.f, 0.f, 0.f};
        f4 d0 = mfma16(dn[0], hb, z), d1 = mfma16(dn[1], hb, z), d2 = mfma16(dn[2], hb, z), d3 = mfma16(dn[3], hb, z);
        if (col & 2) {
          d0 = d1;
          d2 = d3;
        }
        if (col & 4) d0 = d2;
        if (col < 8)
          st_sc1(rs, WS_OPG + ((4 * w + (col >> 1)) * 96 + sl) * 128 + ((col & 1) * 4 + g4) * 16,
                 v4u{pk(d0[0], d0[1]), T, pk(d0[2], d0[3]), T});
        STAMP(9);
      }
    } else {
      const int rd = 4 * xcd + (a - 12);
      const int ln = tid >> 3;
      const int ot = WS_OPT + rd * 8192 + tid * 16, og = WS_OPG + rd * 96 * 128 + tid * 16;
      const bool two = ln < NSL - 64;
      const int og2 = two ? og + 8192 : og;
      float xres = 0.f;
      if (tid < 32) xres = blo(x[(tid >> 4) * DM + 16 * rd + (tid & 15)]);
      v4u p0, p1, p2;
      for (unsigned spin = 0;; ++spin) {
        asm volatile("" ::: "memory");
        p0 = ld_sc1(rs, ot);
        p1 = ld_sc1(rs, og);
        p2 = ld_sc1(rs, og2);
        const bool ok = (p0.y == T) & (p0.w == T) & (p1.y == T) & (p1.w == T) & (p2.y == T) & (p2.w == T);
        if (__all(ok) || spin > SPIN_CAP) break;
      }
      STAMP(8);
      if (!two) p2 = v4u{0u, 0u, 0u, 0u};
      const float v0 = blo(p0.x) + (blo(p1.x) + blo(p2.x)), v1 = bhi(p0.x) + (bhi(p1.x) + bhi(p2.x));
      const float v2 = blo(p0.z) + (blo(p1.z) + blo(p2.z)), v3 = bhi(p0.z) + (bhi(p1.z) + bhi(p2.z));
      float s = sw16_add(sw32_add(v0, v2), sw32_add(v1, v3));
      s += dpp<ROR8>(s);
      if (!(l & 8)) *(LAS float*)(sm + L_RR + w * 128 + (4 * (l & 7) + g4) * 4) = s;
      BAR();
      if (tid < 32) {
        float t = xres;
#pragma unroll
        for (int i = 0; i < 8; ++i) t += *(LAS float*)(sm + L_RR + i * 128 + tid * 4);
        out[(tid >> 4) * DM + 16 * rd + (tid & 15)] = (u16)(pk(t, t) & 0xffffu);
        if (rd == 0 && tid == 0) __builtin_amdgcn_raw_buffer_store_b32(T, rs, WS_E, 0, AUX_SC1);
      }
      STAMP(9);
    }
  }
  if (blockIdx.x == 0 && w == 0) { const unsigned long long te = *(volatile unsigned long long*)(ws + (9 << 20));
    const unsigned long long until = te + 6000ull + 0ull * (xstart - te);
    while (__builtin_amdgcn_s_memrealtime() < until) __builtin_amdgcn_s_sleep(1); }
}

__global__ void k_pre(char* ws) { if (threadIdx.x == 0) *(unsigned long long*)(ws + (9 << 20)) = __builtin_amdgcn_s_memrealtime(); }
__global__ void k_post(char* ws, int K) {
  const unsigned long long t0 = __builtin_amdgcn_s_memrealtime();
  unsigned long long m = 0;
  for (int i = 0; i < 4; ++i) { unsigned long long v = ((unsigned long long*)(ws + (10 << 20)))[threadIdx.x + 64 * i]; m = v > m ? v : m; }
  for (int o = 32; o; o >>= 1) { unsigned long long v = __shfl_xor(m, o); m = v > m ? v : m; }
  const unsigned long long st = ((unsigned long long*)(ws + (10 << 20)))[256];
  const unsigned long long until = st + 6000ull + (unsigned long long)K * (t0 - m);
  while (__builtin_amdgcn_s_memrealtime() < until) __builtin_amdgcn_s_sleep(1);
}
extern "C" void kernel_launch(void* const* d_in, const int* in_sizes, int n_in, void* d_out, int out_size, void* d_ws, size_t ws_size,
                              hipStream_t stream) {
  k_pre<<<1,64,0,stream>>>((char*)d_ws);
  mla_glu_decode<<<256, 512, 0, stream>>>((const u16*)d_in[0], (const u16*)d_in[1], (const u16*)d_in[2], (const u16*)d_in[3],
                                          (const u16*)d_in[4], (const u16*)d_in[5], (const u16*)d_in[7], (const u16*)d_in[8],
                                          (const u16*)d_in[9], (u16*)d_out, (char*)d_ws);
}
```

```cpp
#define PROBE_CLASS 3
#define PROBE_IDX 0
#define PROBE_WAVE 0
#define PROBE_K 10
#define PRO_MODE 2
#define PRO_SET 1
#include <hip/hip_runtime.h>
#include <stdint.h>

#define GAS __attribute__((address_space(1)))
#define LAS __attribute__((address_space(3)))
typedef unsigned short u16;
typedef unsigned v4u __attribute__((ext_vector_type(4)));
typedef unsigned v2u __attribute__((ext_vector_type(2)));
typedef float f4 __attribute__((ext_vector_type(4)));
typedef float f2 __attribute__((ext_vector_type(2)));
typedef __bf16 bf8 __attribute__((ext_vector_type(8)));
typedef __bf16 bf2 __attribute__((ext_vector_type(2)));
typedef short s4 __attribute__((ext_vector_type(4)));

namespace {
constexpr int NH = 32, SK = 4096, DH = 128, DM = 512, DC = 512, FF = 1376;
constexpr int NSL = FF / 16;
constexpr int WS_E = 0;
constexpr int WS_QP = 4096;
constexpr int WS_QA = WS_QP + NH * 4 * 1024;
constexpr int WS_LSE = WS_QA + 2 * NH * 2048;
constexpr int WS_OPT = WS_LSE + 2 * 2 * 32 * 128;
constexpr int WS_OPG = WS_OPT + 32 * 64 * 128;
constexpr int WS_PART = WS_OPG + 32 * 96 * 128;
constexpr int WS_BYTES = WS_PART + NH * 2 * 2 * 32 * 512;
constexpr int WS_STAMP = 8 << 20;
constexpr int CP = 1056;
constexpr int L_C = 0, L_Q = 128 * CP, L_P = L_Q + 16 * CP, L_MX = L_P + 4096, L_LS = L_MX + 256, L_RR = L_LS + 256,
              L_END = L_RR + 1024;
constexpr int L_GG = 0, L_GU = 16384, L_GD = 32768, L_GR = 65536;
constexpr int L_WUV = 0, L_WO = 65536, L_WQ = 65536, L_WUK = 98304, L_CC = 131072, CCP = 576, L_CT = L_CC + 2048, CTP = 320;
constexpr unsigned SPIN_CAP = 1u << 20;
constexpr int AUX_SC1 = 16;

#define STAMPS
#define STAMP(i) asm volatile("s_memrealtime %0\n\ts_waitcnt lgkmcnt(0)" : "=s"(ts[i])::"memory")
#define STAMP_FLUSH()                                                                      \
  do {                                                                                     \
    const int cls_ = r < 16 ? 0 : (r < 28 ? 1 : 2);                                        \
    if ((cls_ == PROBE_CLASS || PROBE_CLASS == 3) && w == PROBE_WAVE && ts[PROBE_IDX] != 0) {   \
      v4u g_;                                                                              \
      for (unsigned sp_ = 0; sp_ < SPIN_CAP; ++sp_) { asm volatile("" ::: "memory"); g_ = ld_sc1(rs, 2048); if (g_.z == Tg) break; } \
      const unsigned long long t0_ = ((unsigned long long)__builtin_amdgcn_readfirstlane(g_.y) << 32) | (unsigned)__builtin_amdgcn_readfirstlane(g_.x); \
      const long long d_ = (long long)(ts[PROBE_IDX] - t0_);                               \
      const unsigned long long until_ = t0_ + 4000ull + PROBE_K * d_;                      \
      while (__builtin_amdgcn_s_memrealtime() < until_) __builtin_amdgcn_s_sleep(1);       \
    }                                                                                      \
  } while (0)
#define PROBE_T(T_) do { Tg = T_; if (blockIdx.x == 0 && tid == 0) st_sc1(rs, 2048, v4u{(unsigned)tstart, (unsigned)(tstart >> 32), T_, 0u}); } while (0)

__device__ __forceinline__ f4 mfma32(v4u a, v4u b, f4 c) {
  return __builtin_amdgcn_mfma_f32_16x16x32_bf16(__builtin_bit_cast(bf8, a), __builtin_bit_cast(bf8, b), c, 0, 0, 0);
}
__device__ __forceinline__ f4 mfma16(v2u a, v2u b, f4 c) {
  return __builtin_amdgcn_mfma_f32_16x16x16bf16_1k(__builtin_bit_cast(s4, a), __builtin_bit_cast(s4, b), c, 0, 0, 0);
}
__device__ __forceinline__ unsigned pk(float lo, float hi) {
  f2 x = {lo, hi};
  return __builtin_bit_cast(unsigned, __builtin_convertvector(x, bf2));
}
__device__ __forceinline__ float asf(unsigned u) { return __builtin_bit_cast(float, u); }
__device__ __forceinline__ float blo(unsigned u) { return __builtin_bit_cast(float, u << 16); }
__device__ __forceinline__ float bhi(unsigned u) { return __builtin_bit_cast(float, u & 0xffff0000u); }
template <int CTRL> __device__ __forceinline__ float dpp(float v) {
  return __builtin_bit_cast(float, __builtin_amdgcn_update_dpp(0, __builtin_bit_cast(int, v), CTRL, 0xf, 0xf, true));
}
__device__ __forceinline__ float fmax2(float a, float b) { return __builtin_amdgcn_fmed3f(a, b, __builtin_inff()); }
constexpr int ROR1 = 0x121, ROR2 = 0x122, ROR4 = 0x124, ROR8 = 0x128, QP_2301 = 0x4e, QP_2222 = 0xaa;
__device__ __forceinline__ float sw32_add(float a, float b) {
  auto r = __builtin_amdgcn_permlane32_swap(__builtin_bit_cast(unsigned, a), __builtin_bit_cast(unsigned, b), false, false);
  return asf(r[0]) + asf(r[1]);
}
__device__ __forceinline__ float sw16_add(float a, float b) {
  auto r = __builtin_amdgcn_permlane16_swap(__builtin_bit_cast(unsigned, a), __builtin_bit_cast(unsigned, b), false, false);
  return asf(r[0]) + asf(r[1]);
}
__device__ __forceinline__ float sw32_max(float a) {
  auto r = __builtin_amdgcn_permlane32_swap(__builtin_bit_cast(unsigned, a), __builtin_bit_cast(unsigned, a), false, false);
  return fmax2(asf(r[0]), asf(r[1]));
}
__device__ __forceinline__ float sw16_max(float a) {
  auto r = __builtin_amdgcn_permlane16_swap(__builtin_bit_cast(unsigned, a), __builtin_bit_cast(unsigned, a), false, false);
  return fmax2(asf(r[0]), asf(r[1]));
}
__device__ __forceinline__ float wave_sum(float v) {
  v += dpp<ROR8>(v);
  v += dpp<ROR4>(v);
  v += dpp<ROR2>(v);
  v += dpp<ROR1>(v);
  v = sw16_add(v, v);
  return sw32_add(v, v);
}
__device__ __forceinline__ v2u trrd(LAS char* p) {
  return __builtin_bit_cast(v2u, __builtin_amdgcn_ds_read_tr16_b64_v4i16((LAS s4*)p));
}
__device__ __forceinline__ v4u trrd2(LAS char* p0, LAS char* p1) {
  v2u a = trrd(p0), b = trrd(p1);
  return v4u{a.x, a.y, b.x, b.y};
}
#define BAR() asm volatile("s_waitcnt lgkmcnt(0)\n\ts_barrier" ::: "memory")
#define VMWAIT(n) asm volatile("s_waitcnt vmcnt(" #n ")" ::: "memory")
__device__ __forceinline__ void gld16(v4u& d, const void* base, unsigned off) {
  asm volatile("global_load_dwordx4 %0, %1, %2" : "=v"(d) : "v"(off), "s"(base) : "memory");
}
__device__ __forceinline__ void gld4_sc1(unsigned& d, const void* base, unsigned off) {
  asm volatile("global_load_dword %0, %1, %2 sc1" : "=v"(d) : "v"(off), "s"(base) : "memory");
}
__device__ __forceinline__ void dma16(const void* base, unsigned off, unsigned lds) {
  unsigned keep;
  asm volatile("s_mov_b32 %0, m0\n\ts_mov_b32 m0, %3\n\ts_nop 0\n\tglobal_load_lds_dwordx4 %1, %2\n\ts_mov_b32 m0, %0"
               : "=&s"(keep)
               : "v"(off), "s"(base), "s"(lds)
               : "memory");
}
__device__ __forceinline__ v4u ld_sc1(__amdgpu_buffer_rsrc_t rs, int off) {
  return __builtin_amdgcn_raw_buffer_load_b128(rs, off, 0, AUX_SC1);
}
__device__ __forceinline__ v2u ld8_sc1(__amdgpu_buffer_rsrc_t rs, int off) {
  return __builtin_amdgcn_raw_buffer_load_b64(rs, off, 0, AUX_SC1);
}
__device__ __forceinline__ void st_sc1(__amdgpu_buffer_rsrc_t rs, int off, v4u v) {
  __builtin_amdgcn_raw_buffer_store_b128(v, rs, off, 0, AUX_SC1);
}
__device__ __forceinline__ void ln_stats(v4u r, float& mean, float& rstd) {
  float e[8] = {blo(r.x), bhi(r.x), blo(r.y), bhi(r.y), blo(r.z), bhi(r.z), blo(r.w), bhi(r.w)};
  float s = ((e[0] + e[1]) + (e[2] + e[3])) + ((e[4] + e[5]) + (e[6] + e[7]));
  mean = wave_sum(s) * (1.f / DM);
  float q = 0.f;
#pragma unroll
  for (int i = 0; i < 8; ++i) {
    float d = e[i] - mean;
    q = fmaf(d, d, q);
  }
  rstd = __builtin_amdgcn_rsqf(wave_sum(q) * (1.f / DM) + 1e-5f);
}
}

__global__ void __launch_bounds__(512, 2)
mla_glu_decode(const u16* __restrict__ x, const u16* __restrict__ ckv, const u16* __restrict__ wq, const u16* __restrict__ wuk,
               const u16* __restrict__ wuv, const u16* __restrict__ wo, const u16* __restrict__ wg, const u16* __restrict__ wu,
               const u16* __restrict__ wd, u16* __restrict__ out, char* __restrict__ ws) {
  const unsigned long long tstart = __builtin_amdgcn_s_memrealtime();
  __builtin_amdgcn_sched_barrier(0);
  __shared__ __attribute__((aligned(16))) char smem_[L_END];
  LAS char* sm = (LAS char*)smem_;
  const unsigned smb = (unsigned)(size_t)sm;
  const int tid = threadIdx.x, l = tid & 63, w = __builtin_amdgcn_readfirstlane(tid >> 6);
  const int col = l & 15, g4 = l >> 4, tq = (l & 15) >> 2, tp = l & 3;
  const int xcd = blockIdx.x & 7, r = blockIdx.x >> 3;
  const __amdgpu_buffer_rsrc_t rs = __builtin_amdgcn_make_buffer_rsrc(ws, 0, WS_BYTES, 0x00020000);
  asm volatile("" ::"s"(x), "s"(ckv), "s"(wq), "s"(wuk), "s"(wuv), "s"(wo), "s"(wg), "s"(wu), "s"(wd), "s"(out), "s"(ws));
  unsigned long long ts[10] = {0, 0, 0, 0, 0, 0, 0, 0, 0, 0};
  STAMP(0);
  unsigned Tg = 0;
  ts[0] = tstart;

  {
    const unsigned bo = blockIdx.x;
    v4u a[14];
#define PLD(i, base, off)                                                                                              \
    if (PRO_MODE == 0) asm volatile("global_load_dwordx4 %0, %1, %2" : "=v"(a[i]) : "v"(off), "s"(base) : "memory");    \
    else if (PRO_MODE == 1) asm volatile("global_load_dwordx4 %0, %1, %2 nt" : "=v"(a[i]) : "v"(off), "s"(base) : "memory"); \
    else if (PRO_MODE == 2) asm volatile("s_mov_b32 m0, %2\n\ts_nop 0\n\tglobal_load_lds_dwordx4 %0, %1" :: "v"(off), "s"(base), "s"(smb + (i * 8 + w) * 1024) : "memory"); \
    else asm volatile("s_mov_b32 m0, %2\n\ts_nop 0\n\tglobal_load_lds_dwordx4 %0, %1 nt" :: "v"(off), "s"(base), "s"(smb + (i * 8 + w) * 1024) : "memory");
    PLD(0, wq, bo * 16384u + tid * 16u) PLD(1, wq, bo * 16384u + 8192u + tid * 16u)
    PLD(2, wuk, bo * 16384u + tid * 16u) PLD(3, wuk, bo * 16384u + 8192u + tid * 16u)
    if (PRO_SET != 1) {
    PLD(4, ckv, bo * 32768u + tid * 16u) PLD(5, ckv, bo * 32768u + 8192u + tid * 16u)
    PLD(6, ckv, bo * 32768u + 16384u + tid * 16u) PLD(7, ckv, bo * 32768u + 24576u + tid * 16u)
    }
    if (PRO_SET == 0) {
    PLD(8, wuv, bo * 16384u + tid * 16u) PLD(9, wuv, bo * 16384u + 8192u + tid * 16u)
    PLD(10, wo, bo * 16384u + tid * 16u) PLD(11, wo, bo * 16384u + 8192u + tid * 16u)
    const unsigned to = bo * 5504u + (tid < 344 ? tid : 343) * 16u;
    PLD(12, wg, to) PLD(13, wu, to) PLD(11, wd, to)
    }
    VMWAIT(0);
    BAR();
    ts[0] = __builtin_amdgcn_s_memrealtime();
    asm volatile("s_waitcnt lgkmcnt(0)" : "+s"(ts[0]));
  }

  if (__builtin_expect(r < 16, 1)) {
    const int h = 4 * xcd + (r >> 2), j = r & 3, ca = j >> 1, mb = j & 1;
    v4u xr0, xr1, xf[4];
    unsigned ep;
    gld4_sc1(ep, ws, 0u);
    gld16(xr0, x, l * 16u);
    gld16(xr1, x, 1024u + l * 16u);
#pragma unroll
    for (int s = 0; s < 4; ++s) gld16(xf[s], x, (unsigned)(((col & 1) * DM + 128 * j + 32 * s + 8 * g4) * 2));
#pragma unroll
    for (int i = 0; i < 4; ++i) {
      const int k = 4 * w + i, row = 4 * k + g4, f = (row & 3) | ((row >> 1) & 4);
      dma16(wq, (unsigned)(((128 * j + row) * 4096 + h * DH + 8 * (col ^ (f << 1))) * 2), smb + L_WQ + k * 1024);
    }
    if (w < 4) {
#pragma unroll
      for (int i = 0; i < 8; ++i) {
        const int row = 32 * w + 4 * i + g4;
        dma16(wuk, (unsigned)((((128 * j + row) * NH + h) * DH + 8 * (col ^ (row & 15))) * 2), smb + L_WUK + (8 * w + i) * 1024);
      }
      VMWAIT(12);
    } else {
      const v4u ones = {~0u, ~0u, ~0u, ~0u};
      const int cb = WS_PART + ((h * 2 + ca) * 2 + mb) * 16384 + (tid - 256) * 16;
#pragma unroll
      for (int i = 0; i < 4; ++i) st_sc1(rs, cb + 4096 * i, ones);
      VMWAIT(8);
    }
    asm volatile("" : "+v"(ep), "+v"(xr0), "+v"(xr1), "+v"(xf[0]), "+v"(xf[1]), "+v"(xf[2]), "+v"(xf[3])::"memory");
    const unsigned T = ep + 1u;
    float m0, r0, m1, r1;
    ln_stats(xr0, m0, r0);
    ln_stats(xr1, m1, r1);
    const float mean = (col & 1) ? m1 : m0, rstd = (col & 1) ? r1 : r0;
    if (col & 2) {
#pragma unroll
      for (int s = 0; s < 4; ++s) xf[s] = v4u{0x3f803f80u, 0x3f803f80u, 0x3f803f80u, 0x3f803f80u};
    }
    STAMP(1);
    if (w < 4) VMWAIT(8);
    else VMWAIT(0);
    BAR();
    STAMP(2);
    const int fl = tq | ((g4 & 1) << 2);
    {
      LAS char* p = sm + L_WQ + 256 * (8 * g4 + tq) + 16 * ((2 * w + (tp >> 1)) ^ (fl << 1)) + 8 * (tp & 1);
      v4u af[4];
#pragma unroll
      for (int s = 0; s < 4; ++s) af[s] = trrd2(p + 256 * 32 * s, p + 256 * (32 * s + 4));
      __builtin_amdgcn_sched_barrier(0);
      f4 acc = {0.f, 0.f, 0.f, 0.f};
#pragma unroll
      for (int s = 0; s < 4; ++s) acc = mfma32(af[s], xf[s], acc);
      float q[4];
#pragma unroll
      for (int e = 0; e < 4; ++e) q[e] = rstd * (acc[e] - mean * dpp<QP_2222>(acc[e]));
      if (col < 2) st_sc1(rs, WS_QP + ((h * 4 + j) * 8 + w) * 128 + col * 64 + g4 * 16, v4u{pk(q[0], q[1]), T, pk(q[2], q[3]), T});
    }
    STAMP(3);
    if (w < 4) {
      VMWAIT(1);
      v4u uk[8];
#pragma unroll
      for (int t = 0; t < 2; ++t)
#pragma unroll
        for (int s = 0; s < 4; ++s) uk[4 * t + s] = *(LAS v4u*)(sm + L_WUK + 256 * (32 * w + 16 * t + col) + 16 * ((4 * s + g4) ^ col));
      const int o = WS_QP + ((h * 4 + ((col >> 1) & 3)) * 8 + (g4 >> 1)) * 128 + (col & 1) * 64 + (g4 & 1) * 32;
      v4u a[8];
      for (unsigned spin = 0;; ++spin) {
        asm volatile("" ::: "memory");
#pragma unroll
        for (int i = 0; i < 8; ++i) a[i] = ld_sc1(rs, o + 256 * (i >> 1) + 16 * (i & 1));
        bool ok = true;
#pragma unroll
        for (int i = 0; i < 8; ++i) ok &= (a[i].y == T) & (a[i].w == T);
        if (__all(ok) || spin > SPIN_CAP) break;
      }
      STAMP(4);
      f4 a0 = {0.f, 0.f, 0.f, 0.f}, a1 = a0;
#pragma unroll
      for (int s = 0; s < 4; ++s) {
        const v4u qf = {a[2 * s].x, a[2 * s].z, a[2 * s + 1].x, a[2 * s + 1].z};
        a0 = mfma32(uk[s], qf, a0);
        a1 = mfma32(uk[4 + s], qf, a1);
      }
      float q[4];
#pragma unroll
      for (int e = 0; e < 4; ++e) {
        float v0 = a0[e] + dpp<QP_2301>(a0[e]), v1 = a1[e] + dpp<QP_2301>(a1[e]);
        float v = (col & 2) ? v1 : v0;
        v += dpp<ROR4>(v);
        q[e] = v * (0.08838834764831845f * 1.4426950408889634f);
      }
      if (col < 4)
        st_sc1(rs, WS_QA + ((col & 1) * NH + h) * 2048 + (64 * j + 16 * w + 8 * (col >> 1) + 2 * g4) * 8,
               v4u{pk(q[0], q[1]), T, pk(q[2], q[3]), T});
    }
    STAMP(5);
    BAR();
#pragma unroll
    for (int i = 0; i < 8; ++i) {
      const int k = 8 * w + ((i + 4 * mb) & 7), row = 4 * k + g4, f = (row & 3) | ((row >> 1) & 4);
      dma16(wuv, (unsigned)((((256 * ca + row) * NH + h) * DH + 8 * (col ^ (f << 1))) * 2), smb + L_WUV + k * 1024);
    }
#pragma unroll
    for (int i = 0; i < 8; ++i) {
      const int k = 8 * w + ((i + 4 * ca) & 7), row = 2 * k + (l >> 5), f = (row & 3) | ((row >> 1) & 4);
      dma16(wo, (unsigned)(((h * DH + row) * DM + 256 * mb + 8 * ((l & 31) ^ (f << 1))) * 2), smb + L_WO + k * 1024);
    }
    VMWAIT(0);
    BAR();
    STAMP(6);
    v4u av[8], ao[8];
    {
      LAS char* p = sm + L_WUV + 256 * (8 * g4 + tq) + 16 * ((2 * w + (tp >> 1)) ^ (fl << 1)) + 8 * (tp & 1);
#pragma unroll
      for (int s = 0; s < 8; ++s) av[s] = trrd2(p + 256 * 32 * s, p + 256 * (32 * s + 4));
#pragma unroll
      for (int t = 0; t < 2; ++t) {
        LAS char* po = sm + L_WO + 512 * (8 * g4 + tq) + 16 * ((4 * w + 2 * t + (tp >> 1)) ^ (fl << 1)) + 8 * (tp & 1);
#pragma unroll
        for (int s = 0; s < 4; ++s) ao[4 * t + s] = trrd2(po + 512 * 32 * s, po + 512 * (32 * s + 4));
      }
    }
    STAMP(7);
    {
      const int b = w >> 2, cw = w & 3, nr = l >> 3;
      const int o = WS_PART + (((h * 2 + ca) * 2 + b) * 32 + nr) * 512 + 128 * cw + 16 * (l & 7);
      const int ol = WS_LSE + ((b * 2 + (h >> 4)) * 32 + nr) * 128 + (h & 15) * 8;
      v4u a[4];
      v2u ls[4];
      for (unsigned spin = 0;; ++spin) {
        asm volatile("" ::: "memory");
#pragma unroll
        for (int i = 0; i < 4; ++i) ls[i] = ld8_sc1(rs, ol + 1024 * i);
#pragma unroll
        for (int i = 0; i < 4; ++i) a[i] = ld_sc1(rs, o + 4096 * i);
        bool ok = true;
        unsigned mx = 0u;
#pragma unroll
        for (int i = 0; i < 4; ++i) {
          ok &= ls[i].y == T;
          mx = max(max(mx, a[i].x), max(a[i].y, max(a[i].z, a[i].w)));
        }
        ok &= mx != ~0u;
        if (__all(ok) || spin > SPIN_CAP) break;
      }
      STAMP(8);
      float e[4];
#pragma unroll
      for (int i = 0; i < 4; ++i) e[i] = asf(ls[i].x);
      float M = fmax2(fmax2(e[0], e[1]), fmax2(e[2], e[3]));
      M = fmax2(M, dpp<ROR8>(M));
      M = sw32_max(sw16_max(M));
#pragma unroll
      for (int i = 0; i < 4; ++i) e[i] = __builtin_amdgcn_exp2f(e[i] - M);
      float L = (e[0] + e[1]) + (e[2] + e[3]);
      L += dpp<ROR8>(L);
      L = sw16_add(L, L);
      L = sw32_add(L, L);
      float acc[8];
#pragma unroll
      for (int c = 0; c < 4; ++c) {
        acc[2 * c] = e[0] * blo(a[0][c]);
        acc[2 * c + 1] = e[0] * bhi(a[0][c]);
      }
#pragma unroll
      for (int i = 1; i < 4; ++i)
#pragma unroll
        for (int c = 0; c < 4; ++c) {
          acc[2 * c] = fmaf(e[i], blo(a[i][c]), acc[2 * c]);
          acc[2 * c + 1] = fmaf(e[i], bhi(a[i][c]), acc[2 * c + 1]);
        }
      float rr[4];
#pragma unroll
      for (int c = 0; c < 4; ++c) rr[c] = sw32_add(acc[c], acc[c + 4]);
      float s0 = sw16_add(rr[0], rr[2]), s1 = sw16_add(rr[1], rr[3]);
      s0 += dpp<ROR8>(s0);
      s1 += dpp<ROR8>(s1);
      const float il = __builtin_amdgcn_rcpf(L);
      if (!(l & 8)) *(LAS unsigned*)(sm + L_CC + b * CCP + (64 * cw + 8 * (l & 7) + 2 * g4) * 2) = pk(s0 * il, s1 * il);
    }
    BAR();
    {
      f4 acc = {0.f, 0.f, 0.f, 0.f};
      LAS char* p = sm + L_CC + (col & 1) * CCP + 16 * g4;
#pragma unroll
      for (int s = 0; s < 8; ++s) acc = mfma32(av[s], *(LAS v4u*)(p + 64 * s), acc);
      if (col < 2) *(LAS v2u*)(sm + L_CT + col * CTP + (16 * w + 4 * g4) * 2) = v2u{pk(acc[0], acc[1]), pk(acc[2], acc[3])};
    }
    BAR();
    {
      f4 a0 = {0.f, 0.f, 0.f, 0.f}, a1 = a0;
      LAS char* p = sm + L_CT + (col & 1) * CTP + 16 * g4;
#pragma unroll
      for (int s = 0; s < 4; ++s) {
        const v4u bfr = *(LAS v4u*)(p + 64 * s);
        a0 = mfma32(ao[s], bfr, a0);
        a1 = mfma32(ao[4 + s], bfr, a1);
      }
      if (col & 2) a0 = a1;
      if (col < 4)
        st_sc1(rs, WS_OPT + ((16 * mb + 2 * w + (col >> 1)) * 64 + 2 * h + ca) * 128 + ((col & 1) * 4 + g4) * 16,
               v4u{pk(a0[0], a0[1]), T, pk(a0[2], a0[3]), T});
    }
    STAMP(9);
    PROBE_T(T);
  } else {
    const int a = r - 16, b = xcd >> 2, n = 8 * (xcd & 3) + (a >> 1), g = a & 1;
    unsigned ep;
    gld4_sc1(ep, ws, 0u);
#pragma unroll
    for (int i = 0; i < 16; ++i) {
      const int row = 16 * w + ((i + 8 * g) & 15);
      dma16(ckv, (unsigned)(((b * SK + 128 * n + row) * DC + 8 * l) * 2), smb + L_C + row * CP);
    }
    asm volatile("s_waitcnt vmcnt(0)" : "+v"(ep)::"memory");
    const unsigned T = ep + 1u;
    PROBE_T(T);
    BAR();
    STAMP(1);
    v4u fr[32];
    if (w < 4) {
      LAS char* p = sm + L_C + (32 * w + col) * CP + 16 * g4;
#pragma unroll
      for (int kt = 0; kt < 2; ++kt)
#pragma unroll
        for (int s = 0; s < 16; ++s) fr[16 * kt + s] = *(LAS v4u*)(p + 16 * kt * CP + 64 * s);
    } else {
      LAS char* p = sm + L_C + (4 * g4 + tq) * CP + 256 * (w - 4) + 8 * tp;
#pragma unroll
      for (int ct = 0; ct < 8; ++ct)
#pragma unroll
        for (int u = 0; u < 4; ++u) fr[4 * ct + u] = trrd2(p + 32 * u * CP + 32 * ct, p + (32 * u + 16) * CP + 32 * ct);
    }
    STAMP(2);
    {
      const int o = WS_QA + (b * NH + 16 * g) * 2048 + tid * 16;
      v4u q[4];
      for (unsigned spin = 0;; ++spin) {
        asm volatile("" ::: "memory");
#pragma unroll
        for (int i = 0; i < 4; ++i) q[i] = ld_sc1(rs, o + 8192 * i);
        bool ok = true;
#pragma unroll
        for (int i = 0; i < 4; ++i) ok &= (q[i].y == T) & (q[i].w == T);
        if (__all(ok) || spin > SPIN_CAP) break;
      }
      STAMP(3);
#pragma unroll
      for (int i = 0; i < 4; ++i) *(LAS v2u*)(sm + L_Q + (4 * i + (tid >> 7)) * CP + (tid & 127) * 8) = v2u{q[i].x, q[i].z};
    }
    BAR();
    STAMP(4);
    if (w < 4) {
      f4 s0 = {0.f, 0.f, 0.f, 0.f}, s1 = s0;
      LAS char* p = sm + L_Q + col * CP + 16 * g4;
      v4u qf[16];
#pragma unroll
      for (int s = 0; s < 16; ++s) qf[s] = *(LAS v4u*)(p + 64 * s);
      __builtin_amdgcn_sched_barrier(0);
#pragma unroll
      for (int s = 0; s < 16; ++s) {
        s0 = mfma32(fr[s], qf[s], s0);
        s1 = mfma32(fr[16 + s], qf[s], s1);
      }
      float mx = fmax2(fmax2(fmax2(s0[0], s0[1]), fmax2(s0[2], s0[3])), fmax2(fmax2(s1[0], s1[1]), fmax2(s1[2], s1[3])));
      mx = sw32_max(sw16_max(mx));
      if (g4 == 0) *(LAS float*)(sm + L_MX + col * 16 + w * 4) = mx;
      STAMP(5);
      BAR();
      const f4 m4 = *(LAS f4*)(sm + L_MX + col * 16);
      const float M = fmax2(fmax2(m4[0], m4[1]), fmax2(m4[2], m4[3]));
      float ps = 0.f;
#pragma unroll
      for (int e = 0; e < 4; ++e) {
        s0[e] = __builtin_amdgcn_exp2f(s0[e] - M);
        s1[e] = __builtin_amdgcn_exp2f(s1[e] - M);
        ps += s0[e] + s1[e];
      }
      *(LAS v4u*)(sm + L_P + w * 1024 + l * 16) = v4u{pk(s0[0], s0[1]), pk(s0[2], s0[3]), pk(s1[0], s1[1]), pk(s1[2], s1[3])};
      ps = sw16_add(ps, ps);
      ps = sw32_add(ps, ps);
      if (g4 == 0) *(LAS float*)(sm + L_LS + col * 16 + w * 4) = ps;
      BAR();
      STAMP(6);
      if (w == 0) {
        const f4 l4 = *(LAS f4*)(sm + L_LS + col * 16);
        const float lse = M + __builtin_amdgcn_logf((l4[0] + l4[1]) + (l4[2] + l4[3]));
        if (g4 == 0)
          __builtin_amdgcn_raw_buffer_store_b64(v2u{__builtin_bit_cast(unsigned, lse), T}, rs,
                                                WS_LSE + ((b * 2 + g) * 32 + n) * 128 + col * 8, 0, AUX_SC1);
      }
    } else {
      BAR();
      BAR();
      const int v = w - 4;
      v4u pf[4];
#pragma unroll
      for (int u = 0; u < 4; ++u) pf[u] = *(LAS v4u*)(sm + L_P + u * 1024 + l * 16);
      const f4 l4 = *(LAS f4*)(sm + L_LS + col * 16);
      const float il = __builtin_amdgcn_rcpf((l4[0] + l4[1]) + (l4[2] + l4[3]));
      const int o = WS_PART + ((((16 * g + (col & 7)) * 2 + (v >> 1)) * 2 + b) * 32 + n) * 512 + 256 * (v & 1) + (col >> 3) * 64 +
                    (g4 & 1) * 32 + (g4 >> 1) * 16;
#pragma unroll
      for (int pp = 0; pp < 2; ++pp) {
        v4u R[2];
#pragma unroll
        for (int k = 0; k < 2; ++k) {
          const int pr = 2 * pp + k;
          f4 c0 = {0.f, 0.f, 0.f, 0.f}, c1 = c0;
#pragma unroll
          for (int u = 0; u < 4; ++u) {
            c0 = mfma32(fr[8 * pr + u], pf[u], c0);
            c1 = mfma32(fr[8 * pr + 4 + u], pf[u], c1);
          }
          const unsigned x0 = min(pk(c0[0] * il, c0[1] * il), 0xfffffffeu), x1 = min(pk(c0[2] * il, c0[3] * il), 0xfffffffeu);
          const unsigned y0 = min(pk(c1[0] * il, c1[1] * il), 0xfffffffeu), y1 = min(pk(c1[2] * il, c1[3] * il), 0xfffffffeu);
          auto r0 = __builtin_amdgcn_permlane16_swap(x0, y0, false, false);
          auto r1 = __builtin_amdgcn_permlane16_swap(x1, y1, false, false);
          R[k] = v4u{r0[0], r1[0], r0[1], r1[1]};
        }
        v4u A, B;
#pragma unroll
        for (int e = 0; e < 4; ++e) {
          A[e] = (unsigned)__builtin_amdgcn_update_dpp((int)R[0][e], (int)R[1][e], ROR8, 0xf, 0xc, false);
          B[e] = (unsigned)__builtin_amdgcn_update_dpp((int)R[1][e], (int)R[0][e], ROR8, 0xf, 0x3, false);
        }
        st_sc1(rs, o + 128 * pp, A);
        st_sc1(rs, o + 128 * pp + 8 * 65536, B);
      }
      STAMP(7);
    }
    const int sl = 12 * xcd + a;
    if (a < 12) {
      if (sl < NSL) {
        BAR();
        v4u xr0, xr1, xf[8];
#pragma unroll
        for (int i = 0; i < 2; ++i) {
          const int k = 2 * w + i;
          const unsigned o = (unsigned)(((32 * k + (l >> 1)) * FF + 16 * sl + 8 * (l & 1)) * 2);
          dma16(wg, o, smb + L_GG + k * 1024);
          dma16(wu, o, smb + L_GU + k * 1024);
          dma16(wd, (unsigned)(((16 * sl + k) * DM + 8 * l) * 2), smb + L_GD + k * CP);
        }
        gld16(xr0, x, l * 16u);
        gld16(xr1, x, 1024u + l * 16u);
#pragma unroll
        for (int s = 0; s < 8; ++s) gld16(xf[s], x, (unsigned)(((col & 1) * DM + 256 * (w & 1) + 32 * s + 8 * g4) * 2));
        asm volatile("s_waitcnt vmcnt(0)"
                     : "+v"(xr0), "+v"(xr1), "+v"(xf[0]), "+v"(xf[1]), "+v"(xf[2]), "+v"(xf[3]), "+v"(xf[4]), "+v"(xf[5]), "+v"(xf[6]),
                       "+v"(xf[7])::"memory");
        BAR();
        STAMP(8);
        if (w < 4) {
          if (col & 2) {
#pragma unroll
            for (int s = 0; s < 8; ++s) xf[s] = v4u{0x3f803f80u, 0x3f803f80u, 0x3f803f80u, 0x3f803f80u};
          }
          LAS char* p = sm + L_GG + (w >> 1) * 16384 + 32 * (256 * (w & 1) + 8 * g4 + tq) + 8 * tp;
          f4 acc = {0.f, 0.f, 0.f, 0.f};
#pragma unroll
          for (int s = 0; s < 8; ++s) acc = mfma32(trrd2(p + 32 * 32 * s, p + 32 * (32 * s + 4)), xf[s], acc);
          *(LAS f4*)(sm + L_GR + w * 1024 + l * 16) = acc;
        }
        float m0, r0, m1, r1;
        ln_stats(xr0, m0, r0);
        ln_stats(xr1, m1, r1);
        const float mean = (col & 1) ? m1 : m0, rstd = (col & 1) ? r1 : r0;
        v2u dn[4];
#pragma unroll
        for (int t = 0; t < 4; ++t) dn[t] = trrd(sm + L_GD + (4 * g4 + tq) * CP + 32 * (4 * w + t) + 8 * tp);
        BAR();
        LAS char* pr = sm + L_GR + (16 * g4 + (col & 1)) * 16;
        LAS char* po = sm + L_GR + (16 * g4 + 2) * 16;
        const f4 gx = *(LAS f4*)(pr) + *(LAS f4*)(pr + 1024), go = *(LAS f4*)(po) + *(LAS f4*)(po + 1024);
        const f4 ux = *(LAS f4*)(pr + 2048) + *(LAS f4*)(pr + 3072), uo = *(LAS f4*)(po + 2048) + *(LAS f4*)(po + 3072);
        float hh[4];
#pragma unroll
        for (int e = 0; e < 4; ++e) {
          const float gv = rstd * (gx[e] - mean * go[e]), uv = rstd * (ux[e] - mean * uo[e]);
          hh[e] = uv * __builtin_amdgcn_rcpf(1.f + __builtin_amdgcn_exp2f(-1.4426950408889634f * gv));
        }
        const v2u hb = {pk(hh[0], hh[1]), pk(hh[2], hh[3])};
        const f4 z = {0.f, 0.f, 0.f, 0.f};
        f4 d0 = mfma16(dn[0], hb, z), d1 = mfma16(dn[1], hb, z), d2 = mfma16(dn[2], hb, z), d3 = mfma16(dn[3], hb, z);
        if (col & 2) {
          d0 = d1;
          d2 = d3;
        }
        if (col & 4) d0 = d2;
        if (col < 8)
          st_sc1(rs, WS_OPG + ((4 * w + (col >> 1)) * 96 + sl) * 128 + ((col & 1) * 4 + g4) * 16,
                 v4u{pk(d0[0], d0[1]), T, pk(d0[2], d0[3]), T});
        STAMP(9);
      }
    } else {
      const int rd = 4 * xcd + (a - 12);
      const int ln = tid >> 3;
      const int ot = WS_OPT + rd * 8192 + tid * 16, og = WS_OPG + rd * 96 * 128 + tid * 16;
      const bool two = ln < NSL - 64;
      const int og2 = two ? og + 8192 : og;
      float xres = 0.f;
      if (tid < 32) xres = blo(x[(tid >> 4) * DM + 16 * rd + (tid & 15)]);
      v4u p0, p1, p2;
      for (unsigned spin = 0;; ++spin) {
        asm volatile("" ::: "memory");
        p0 = ld_sc1(rs, ot);
        p1 = ld_sc1(rs, og);
        p2 = ld_sc1(rs, og2);
        const bool ok = (p0.y == T) & (p0.w == T) & (p1.y == T) & (p1.w == T) & (p2.y == T) & (p2.w == T);
        if (__all(ok) || spin > SPIN_CAP) break;
      }
      STAMP(8);
      if (!two) p2 = v4u{0u, 0u, 0u, 0u};
      const float v0 = blo(p0.x) + (blo(p1.x) + blo(p2.x)), v1 = bhi(p0.x) + (bhi(p1.x) + bhi(p2.x));
      const float v2 = blo(p0.z) + (blo(p1.z) + blo(p2.z)), v3 = bhi(p0.z) + (bhi(p1.z) + bhi(p2.z));
      float s = sw16_add(sw32_add(v0, v2), sw32_add(v1, v3));
      s += dpp<ROR8>(s);
      if (!(l & 8)) *(LAS float*)(sm + L_RR + w * 128 + (4 * (l & 7) + g4) * 4) = s;
      BAR();
      if (tid < 32) {
        float t = xres;
#pragma unroll
        for (int i = 0; i < 8; ++i) t += *(LAS float*)(sm + L_RR + i * 128 + tid * 4);
        out[(tid >> 4) * DM + 16 * rd + (tid & 15)] = (u16)(pk(t, t) & 0xffffu);
        if (rd == 0 && tid == 0) __builtin_amdgcn_raw_buffer_store_b32(T, rs, WS_E, 0, AUX_SC1);
      }
      STAMP(9);
    }
  }
  STAMP_FLUSH();
}

extern "C" void kernel_launch(void* const* d_in, const int* in_sizes, int n_in, void* d_out, int out_size, void* d_ws, size_t ws_size,
                              hipStream_t stream) {
  mla_glu_decode<<<256, 512, 0, stream>>>((const u16*)d_in[0], (const u16*)d_in[1], (const u16*)d_in[2], (const u16*)d_in[3],
                                          (const u16*)d_in[4], (const u16*)d_in[5], (const u16*)d_in[7], (const u16*)d_in[8],
                                          (const u16*)d_in[9], (u16*)d_out, (char*)d_ws);
}
```

```cpp
#define A_DELAY 24
#define UK_DELAY 16
#define T_DELAY 20
#include <hip/hip_runtime.h>
#include <stdint.h>

#define GAS __attribute__((address_space(1)))
#define LAS __attribute__((address_space(3)))
typedef unsigned short u16;
typedef unsigned v4u __attribute__((ext_vector_type(4)));
typedef unsigned v2u __attribute__((ext_vector_type(2)));
typedef float f4 __attribute__((ext_vector_type(4)));
typedef float f2 __attribute__((ext_vector_type(2)));
typedef __bf16 bf8 __attribute__((ext_vector_type(8)));
typedef __bf16 bf2 __attribute__((ext_vector_type(2)));
typedef short s4 __attribute__((ext_vector_type(4)));

namespace {
constexpr int NH = 32, SK = 4096, DH = 128, DM = 512, DC = 512, FF = 1376;
constexpr int NSL = FF / 16;
constexpr int WS_E = 0;
constexpr int WS_QP = 4096;
constexpr int WS_QA = WS_QP + NH * 4 * 1024;
constexpr int WS_LSE = WS_QA + 2 * NH * 2048;
constexpr int WS_OPT = WS_LSE + 2 * 2 * 32 * 128;
constexpr int WS_OPG = WS_OPT + 32 * 64 * 128;
constexpr int WS_PART = WS_OPG + 32 * 128 * 128;
constexpr int WS_BYTES = WS_PART + NH * 2 * 2 * 32 * 512;
constexpr int WS_STAMP = 7 << 19;
constexpr int CP = 1056;
constexpr int XP = 1088, SP = 288, SP2 = 544;
constexpr int L_C = 0, L_Q = 128 * CP, L_P = L_Q + 16 * CP, L_X = L_P + 4096, L_MX = L_X + 2 * XP, L_RR = L_MX + 256,
              L_JUNK = L_RR + 1024, L_FL = L_JUNK + 256, L_END = L_FL + 128;
static_assert(L_END <= 163840, "LDS");
constexpr int L_GG = 0, L_GD = 32768, L_GR = 65536;
constexpr int L_WUV = 0, L_WO = 65536, L_WQ = 0, L_WUK = 32768, L_CC = 131072, L_CT = L_CC + 8 * SP2, L_W = L_CT + 2 * SP;
#define H1_DELAY 8
#define GLU_FLAG_Q 1
#define NOCLEAR 0
#define OPT_W 0
#define QA_WARM 0
#define LSE_DELAY 40
#define PF_GLU 1
constexpr unsigned SPIN_CAP = 1u << 20;
constexpr int AUX_SC1 = 16;

#define STAMP_W 0
#define STAMP(i)
#define STAMP_FLUSH()
#define PROBE_T(T_)

__device__ __forceinline__ f4 mfma32(v4u a, v4u b, f4 c) {
  return __builtin_amdgcn_mfma_f32_16x16x32_bf16(__builtin_bit_cast(bf8, a), __builtin_bit_cast(bf8, b), c, 0, 0, 0);
}
__device__ __forceinline__ f4 mfma16(v2u a, v2u b, f4 c) {
  return __builtin_amdgcn_mfma_f32_16x16x16bf16_1k(__builtin_bit_cast(s4, a), __builtin_bit_cast(s4, b), c, 0, 0, 0);
}
__device__ __forceinline__ unsigned pk(float lo, float hi) {
  f2 x = {lo, hi};
  return __builtin_bit_cast(unsigned, __builtin_convertvector(x, bf2));
}
__device__ __forceinline__ float asf(unsigned u) { return __builtin_bit_cast(float, u); }
__device__ __forceinline__ float blo(unsigned u) { return __builtin_bit_cast(float, u << 16); }
__device__ __forceinline__ float bhi(unsigned u) { return __builtin_bit_cast(float, u & 0xffff0000u); }
template <int CTRL> __device__ __forceinline__ float dpp(float v) {
  return __builtin_bit_cast(float, __builtin_amdgcn_update_dpp(0, __builtin_bit_cast(int, v), CTRL, 0xf, 0xf, true));
}
__device__ __forceinline__ float fmax2(float a, float b) {
#pragma float_control(precise, off)
  return __builtin_fmaxf(a, b);
}
typedef unsigned short h2 __attribute__((ext_vector_type(2)));
__device__ __forceinline__ unsigned scale_dn(unsigned p, unsigned kk) {
  return __builtin_bit_cast(unsigned, __builtin_elementwise_sub_sat(__builtin_bit_cast(h2, p), __builtin_bit_cast(h2, kk)));
}
#define PIN(x) asm volatile("" : "+v"(x))
constexpr int ROR1 = 0x121, ROR2 = 0x122, ROR4 = 0x124, ROR8 = 0x128, QP_2301 = 0x4e, QP_2222 = 0xaa;
__device__ __forceinline__ float sw32_add(float a, float b) {
  auto r = __builtin_amdgcn_permlane32_swap(__builtin_bit_cast(unsigned, a), __builtin_bit_cast(unsigned, b), false, false);
  return asf(r[0]) + asf(r[1]);
}
__device__ __forceinline__ float sw16_add(float a, float b) {
  auto r = __builtin_amdgcn_permlane16_swap(__builtin_bit_cast(unsigned, a), __builtin_bit_cast(unsigned, b), false, false);
  return asf(r[0]) + asf(r[1]);
}
__device__ __forceinline__ float vmax(unsigned a, unsigned b) {
  float r;
  asm("v_max_f32 %0, %1, %2" : "=v"(r) : "v"(a), "v"(b));
  return r;
}
__device__ __forceinline__ float sw32_max(float a) {
  auto r = __builtin_amdgcn_permlane32_swap(__builtin_bit_cast(unsigned, a), __builtin_bit_cast(unsigned, a), false, false);
  return vmax(r[0], r[1]);
}
__device__ __forceinline__ float sw16_max(float a) {
  auto r = __builtin_amdgcn_permlane16_swap(__builtin_bit_cast(unsigned, a), __builtin_bit_cast(unsigned, a), false, false);
  return vmax(r[0], r[1]);
}
__device__ __forceinline__ float wave_sum(float v) {
  v += dpp<ROR8>(v);
  v += dpp<ROR4>(v);
  v += dpp<ROR2>(v);
  v += dpp<ROR1>(v);
  v = sw16_add(v, v);
  return sw32_add(v, v);
}
__device__ __forceinline__ v2u trrd(LAS char* p) {
  return __builtin_bit_cast(v2u, __builtin_amdgcn_ds_read_tr16_b64_v4i16((LAS s4*)p));
}
__device__ __forceinline__ v4u trrd2(LAS char* p0, LAS char* p1) {
  v2u a = trrd(p0), b = trrd(p1);
  return v4u{a.x, a.y, b.x, b.y};
}
#define BAR() asm volatile("s_waitcnt lgkmcnt(0)\n\ts_barrier" ::: "memory")
#define VMWAIT(k) asm volatile("s_waitcnt vmcnt(%0)" ::"n"(k) : "memory")
__device__ __forceinline__ void gld16(v4u& d, const void* base, unsigned off) {
  asm volatile("global_load_dwordx4 %0, %1, %2" : "=v"(d) : "v"(off), "s"(base) : "memory");
}
__device__ __forceinline__ void gld4_sc1(unsigned& d, const void* base, unsigned off) {
  asm volatile("global_load_dword %0, %1, %2 sc1" : "=v"(d) : "v"(off), "s"(base) : "memory");
}
__device__ __forceinline__ void touch(const void* base, unsigned off, unsigned lds) {
  asm volatile("s_mov_b32 m0, %2\n\ts_nop 0\n\tglobal_load_lds_dword %0, %1" ::"v"(off), "s"(base), "s"(lds) : "memory");
}
__device__ __forceinline__ void touch_sc1(const void* base, unsigned off, unsigned lds) {
  asm volatile("s_mov_b32 m0, %2\n\ts_nop 0\n\tglobal_load_lds_dword %0, %1 sc1" ::"v"(off), "s"(base), "s"(lds) : "memory");
}
#define LD16(i, a, o) "global_load_dwordx4 %" #i ", %" #a ", %" #o
#define SWEEP_4(q, o, o0, o1, o2, o3, W)                                                                                     \
  asm volatile(LD16(0, 4, 5) " offset:" #o0 " sc1\n\t" LD16(1, 4, 5) " offset:" #o1 " sc1\n\t" LD16(2, 4, 5) " offset:" #o2         \
               " sc1\n\t" LD16(3, 4, 5) " offset:" #o3 " sc1" W                                                               \
               : "=&v"((q)[0]), "=&v"((q)[1]), "=&v"((q)[2]), "=&v"((q)[3])                                                  \
               : "v"(o), "s"(ws)                                                                                             \
               : "memory")
#define SWEEP_L(W)                                                                                                          \
  asm volatile("global_load_dwordx2 %0, %3, %5 sc1\n\tglobal_load_dwordx2 %1, %4, %5 sc1\n\t"                              \
               "global_load_dwordx2 %2, %4, %5 offset:512 sc1" W                                                            \
               : "=&v"(la), "=&v"(l0), "=&v"(l1)                                                                            \
               : "v"(ola), "v"(olo), "s"(ws)                                                                                \
               : "memory")
#define SWEEP_2(p, q, oa, ob, W) \
  asm volatile(LD16(0, 2, 4) " sc1\n\t" LD16(1, 3, 4) " sc1" W : "=&v"(p), "=&v"(q) : "v"(oa), "v"(ob), "s"(ws) : "memory")
#define TAGS_BAD4(q) \
  __builtin_amdgcn_ballot_w64((T & (q)[0].y & (q)[0].w & (q)[1].y & (q)[1].w & (q)[2].y & (q)[2].w & (q)[3].y & (q)[3].w) != \
                              (T | (q)[0].y | (q)[0].w | (q)[1].y | (q)[1].w | (q)[2].y | (q)[2].w | (q)[3].y | (q)[3].w))
#define FLAG_SET(i) asm volatile("ds_write_b32 %0, %1" ::"v"(smb + L_FL + 4 * (i)), "v"(1u) : "memory")
#define FLAG_WAIT4(i)                                                                                     \
  for (unsigned spin_ = 0; spin_ < SPIN_CAP; ++spin_) {                                                   \
    v4u f_;                                                                                               \
    asm volatile("ds_read_b128 %0, %1\n\ts_waitcnt lgkmcnt(0)" : "=v"(f_) : "v"(smb + L_FL + 16 * (i)) : "memory"); \
    if (__builtin_amdgcn_readfirstlane(f_.x & f_.y & f_.z & f_.w)) break;                                 \
  }
#define WAIT0 "\n\ts_waitcnt vmcnt(0)"
__device__ __forceinline__ void dma16(const void* base, unsigned off, unsigned lds) {
  asm volatile("s_mov_b32 m0, %2\n\ts_nop 0\n\tglobal_load_lds_dwordx4 %0, %1" ::"v"(off), "s"(base), "s"(lds) : "memory");
}
__device__ __forceinline__ v4u ld_sc1(__amdgpu_buffer_rsrc_t rs, int off) {
  return __builtin_amdgcn_raw_buffer_load_b128(rs, off, 0, AUX_SC1);
}
__device__ __forceinline__ v2u ld8_sc1(__amdgpu_buffer_rsrc_t rs, int off) {
  return __builtin_amdgcn_raw_buffer_load_b64(rs, off, 0, AUX_SC1);
}
__device__ __forceinline__ void st_sc1(__amdgpu_buffer_rsrc_t rs, int off, v4u v) {
  __builtin_amdgcn_raw_buffer_store_b128(v, rs, off, 0, AUX_SC1);
}
__device__ __forceinline__ void ln_stats(v4u r, float& mean, float& rstd) {
  float e[8] = {blo(r.x), bhi(r.x), blo(r.y), bhi(r.y), blo(r.z), bhi(r.z), blo(r.w), bhi(r.w)};
  float s = ((e[0] + e[1]) + (e[2] + e[3])) + ((e[4] + e[5]) + (e[6] + e[7]));
  mean = wave_sum(s) * (1.f / DM);
  float q = 0.f;
#pragma unroll
  for (int i = 0; i < 8; ++i) {
    float d = e[i] - mean;
    q = fmaf(d, d, q);
  }
  rstd = __builtin_amdgcn_rsqf(wave_sum(q) * (1.f / DM) + 1e-5f);
}
}

__global__ void __launch_bounds__(512, 2)
mla_glu_decode(const u16* __restrict__ x, const u16* __restrict__ ckv, const u16* __restrict__ wq, const u16* __restrict__ wuk,
               const u16* __restrict__ wuv, const u16* __restrict__ wo, const u16* __restrict__ wg, const u16* __restrict__ wu,
               const u16* __restrict__ wd, u16* __restrict__ out, char* __restrict__ ws) {
  __shared__ __attribute__((aligned(16))) char smem_[L_END];
  LAS char* sm = (LAS char*)smem_;
  const unsigned smb = (unsigned)(size_t)sm;
  const int tid = threadIdx.x, l = tid & 63, w = __builtin_amdgcn_readfirstlane(tid >> 6);
  const int col = l & 15, g4 = l >> 4, tq = (l & 15) >> 2, tp = l & 3;
  const int xcd = blockIdx.x & 7, r = blockIdx.x >> 3;
  const __amdgpu_buffer_rsrc_t rs = __builtin_amdgcn_make_buffer_rsrc(ws, 0, WS_BYTES, 0x00020000);
  asm volatile("" ::"s"(x), "s"(ckv), "s"(wq), "s"(wuk), "s"(wuv), "s"(wo), "s"(wg), "s"(wu), "s"(wd), "s"(out), "s"(ws));
  STAMP(0);

  unsigned ep, T;
  LAS char* const px = sm + L_X + (col & 1) * XP + 16 * g4;
  if ((w >> 1) == 2) dma16(x, (unsigned)((w & 1) * 1024 + l * 16), smb + L_X + (w & 1) * XP);
  const f4 z = {0.f, 0.f, 0.f, 0.f};
#define LAYERNORM()                                                                                                              \
  {                                                                                                                              \
    const v4u xr = *(LAS v4u*)(sm + L_X + (w & 1) * XP + l * 16);                                                                \
    float e[8] = {blo(xr.x), bhi(xr.x), blo(xr.y), bhi(xr.y), blo(xr.z), bhi(xr.z), blo(xr.w), bhi(xr.w)};                       \
    const float mean = wave_sum(((e[0] + e[1]) + (e[2] + e[3])) + ((e[4] + e[5]) + (e[6] + e[7]))) * (1.f / DM);                 \
    float q = 0.f;                                                                                                               \
    _Pragma("unroll") for (int i = 0; i < 8; ++i) {                                                                              \
      e[i] -= mean;                                                                                                              \
      q = fmaf(e[i], e[i], q);                                                                                                   \
    }                                                                                                                            \
    const float rstd = __builtin_amdgcn_rsqf(wave_sum(q) * (1.f / DM) + 1e-5f);                                                  \
    *(LAS v4u*)(sm + L_X + (w & 1) * XP + l * 16) =                                                                              \
        v4u{pk(e[0] * rstd, e[1] * rstd), pk(e[2] * rstd, e[3] * rstd), pk(e[4] * rstd, e[5] * rstd), pk(e[6] * rstd, e[7] * rstd)}; \
  }

  if (__builtin_expect(r < 16, 1)) {
    const int h = 4 * xcd + (r >> 2), j = r & 3, ca = j >> 1, mb = j & 1;
    const int fl = tq | ((g4 & 1) << 2);
    gld4_sc1(ep, ws, 0u);
    if (__builtin_expect(w < 4, 1)) {
      const unsigned vq = (unsigned)(((128 * j + g4) * 4096 + h * DH + 8 * (col ^ ((g4 | ((w & 2) << 1)) << 1))) * 2);
#pragma unroll 1
      for (int k = w; k < 32; k += 4) dma16(wq, vq + (unsigned)k * 32768u, smb + L_WQ + k * 1024);
    } else if (w < 6) {
      VMWAIT(1);
      LAYERNORM();
    } else {
      __builtin_amdgcn_s_sleep(UK_DELAY);
      const unsigned vu = (unsigned)((((128 * j + g4) * NH + h) * DH) * 2);
#pragma unroll 1
      for (int k = w - 6; k < 32; k += 2) dma16(wuk, vu + (unsigned)k * 32768u + 16u * (col ^ ((4 * k + g4) & 15)), smb + L_WUK + k * 1024);
    }
    unsigned p = L_WQ + 256 * (8 * g4 + tq) + 16 * ((2 * w + (tp >> 1)) ^ (fl << 1)) + 8 * (tp & 1);
    unsigned oqp = WS_QP + ((h * 4 + j) * 8 + w) * 128 + col * 64 + g4 * 16;
    unsigned pxj = L_X + (col & 1) * XP + 16 * g4 + 256 * j;
    unsigned o = WS_QP + ((h * 4 + ((col >> 1) & 3)) * 8 + (g4 >> 1)) * 128 + (col & 1) * 64 + (g4 & 1) * 32;
    unsigned oqa = WS_QA + ((col & 1) * NH + h) * 2048 + (64 * j + 16 * w + 8 * (col >> 1) + 2 * g4) * 8;
    unsigned pu = L_WUK + 256 * (32 * w + col) + 16 * (g4 ^ col);
    PIN(p); PIN(oqp); PIN(pxj); PIN(o); PIN(oqa); PIN(pu);
    if (w < 6) VMWAIT(0);
    else VMWAIT(16);
    PIN(ep);
    T = ep + 1u;
    PIN(T);
    STAMP(1);
    BAR();
    STAMP(2);
    {
      v4u af[4], xf[4];
#pragma unroll
      for (int s = 0; s < 4; ++s) {
        af[s] = trrd2(sm + p + 256 * 32 * s, sm + p + 256 * (32 * s + 4));
        xf[s] = *(LAS v4u*)(sm + pxj + 64 * s);
      }
      __builtin_amdgcn_sched_barrier(0);
      f4 acc = z;
#pragma unroll
      for (int s = 0; s < 4; ++s) acc = mfma32(af[s], xf[s], acc);
      if (col < 2) st_sc1(rs, oqp, v4u{pk(acc[0], acc[1]), T, pk(acc[2], acc[3]), T});
    }
    STAMP(3);
    if (w >= 6) VMWAIT(1);
    if (w >= 4) BAR();
    if (w < 4) {
      v4u a[8], uk[8];
      __builtin_amdgcn_s_sleep(H1_DELAY);
      SWEEP_4(a, o, 0, 16, 256, 272, "");
      SWEEP_4(a + 4, o, 512, 528, 768, 784, "");
      BAR();
#pragma unroll
      for (int t = 0; t < 2; ++t)
#pragma unroll
        for (int s = 0; s < 4; ++s) uk[4 * t + s] = *(LAS v4u*)(sm + (pu ^ (64 * s)) + 4096 * t);
      asm volatile("s_waitcnt vmcnt(%8)"
                   : "+v"(a[0]), "+v"(a[1]), "+v"(a[2]), "+v"(a[3]), "+v"(a[4]), "+v"(a[5]), "+v"(a[6]), "+v"(a[7])
                   : "n"(OPT_W)
                   : "memory");
      for (unsigned spin = 0;; ++spin) {
        if (!(TAGS_BAD4(a) | TAGS_BAD4(a + 4)) || spin > SPIN_CAP) break;
        SWEEP_4(a, o, 0, 16, 256, 272, "");
        SWEEP_4(a + 4, o, 512, 528, 768, 784, WAIT0);
        asm volatile("" : "+v"(a[0]), "+v"(a[1]), "+v"(a[2]), "+v"(a[3]));
      }
      STAMP(4);
      f4 a0 = z, a1 = z;
#pragma unroll
      for (int s = 0; s < 4; ++s) {
        const v4u qf = {a[2 * s].x, a[2 * s].z, a[2 * s + 1].x, a[2 * s + 1].z};
        a0 = mfma32(uk[s], qf, a0);
        a1 = mfma32(uk[4 + s], qf, a1);
      }
      float q[4];
#pragma unroll
      for (int e = 0; e < 4; ++e) {
        const float mine = (col & 2) ? a1[e] : a0[e], other = (col & 2) ? a0[e] : a1[e];
        q[e] = mine + dpp<QP_2301>(other);
        q[e] += dpp<ROR4>(q[e]);
      }
      if (col < 4) st_sc1(rs, oqa, v4u{pk(q[0], q[1]), T, pk(q[2], q[3]), T});
    }
    STAMP(5);
    PROBE_T(T);
    BAR();
    __builtin_amdgcn_s_sleep(T_DELAY);
    {
      const unsigned vv = (unsigned)((((256 * ca + g4) * NH + h) * DH + 8 * (col ^ ((g4 | ((w & 2) << 1)) << 1))) * 2);
      const unsigned vo = (unsigned)(((h * DH + (l >> 5)) * DM + 256 * mb) * 2) + 16u * ((l & 31) ^ (((l >> 5) | ((w & 1) << 1) | (w & 4)) << 1));
      int kv = w + 32 * mb, ko = w + 32 * ca;
#pragma unroll 1
      for (int hf = 0; hf < 2; ++hf) {
#pragma unroll 1
        for (int i = 0; i < 4; ++i, kv = (kv + 8) & 63) dma16(wuv, vv + (unsigned)kv * 32768u, smb + L_WUV + kv * 1024);
#pragma unroll 1
        for (int i = 0; i < 4; ++i, ko = (ko + 8) & 63) dma16(wo, vo + (unsigned)ko * 2048u, smb + L_WO + ko * 1024);
        if (hf == 0) asm volatile("s_barrier" ::: "memory");
      }
    }
    const int tb = w >> 2, nq = w & 3;
    o = WS_PART + (((h * 2 + ca) * 2 + tb) * 32 + 8 * nq + g4) * 512 + 16 * col;
    unsigned pw = L_W + (tb * 32 + 8 * nq + g4) * 4;
    unsigned pv = L_WUV + 256 * (8 * g4 + tq) + 16 * ((2 * w + (tp >> 1)) ^ (fl << 1)) + 8 * (tp & 1);
    unsigned po = L_WO + 512 * (8 * g4 + tq) + 16 * ((4 * w + (tp >> 1)) ^ (fl << 1)) + 8 * (tp & 1);
    unsigned pcw = L_CC + (nq * 2 + tb) * SP2 + 16 * col + 8 * g4, pcr = L_CC + (col & 7) * SP2 + 16 * g4;
    unsigned ptw = L_CT + col * SP + (16 * w + 4 * g4) * 2, ptr = L_CT + (col & 1) * SP + 16 * g4;
    unsigned oo = WS_OPT + ((16 * mb + 2 * w + (col >> 1)) * 64 + 2 * h + ca) * 128 + ((col & 1) * 4 + g4) * 16;
    v4u mk;
#pragma unroll
    for (int d = 0; d < 4; ++d) mk[d] = (col >> 1) == d ? ((col & 1) ? 0xffff0000u : 0xffffu) : 0u;
    PIN(o); PIN(pw); PIN(pv); PIN(po); PIN(pcw); PIN(pcr); PIN(ptw); PIN(ptr); PIN(oo); PIN(mk);
    if (w == 7) {
      const unsigned ol = WS_LSE + (((l >> 5) * 2 + (h >> 4)) * 32 + (l & 31)) * 128 + (h & 15) * 8;
      v2u la;
      for (unsigned spin = 0;; ++spin) {
        asm volatile("global_load_dwordx2 %0, %1, %2 sc1" WAIT0 : "=&v"(la) : "v"(ol), "s"(ws) : "memory");
        if (!__builtin_amdgcn_ballot_w64(la.y != T) || spin > SPIN_CAP) break;
      }
      float M = asf(la.x);
      M = fmax2(M, dpp<ROR8>(M));
      M = fmax2(M, dpp<ROR4>(M));
      M = fmax2(M, dpp<ROR2>(M));
      M = fmax2(M, dpp<ROR1>(M));
      M = sw16_max(M);
      const float e = __builtin_amdgcn_exp2f(asf(la.x) - M);
      float L = e + dpp<ROR8>(e);
      L += dpp<ROR4>(L);
      L += dpp<ROR2>(L);
      L += dpp<ROR1>(L);
      L = sw16_add(L, L);
      const float wt = e * __builtin_amdgcn_rcpf(L);
      *(LAS unsigned*)(sm + L_W + 4 * l) = pk(wt, wt);
      STAMP(7);
    }
    VMWAIT(0);
    STAMP(6);
    BAR();
    v4u a[4];
    SWEEP_4(a, o, 0, 256, 2048, 2304, "");
    v4u A0 = mk & *(LAS unsigned*)(sm + pw), A1 = mk & *(LAS unsigned*)(sm + pw + 16);
    v4u av[8], ao[8];
#pragma unroll
    for (int s = 0; s < 8; ++s) av[s] = trrd2(sm + pv + 256 * 32 * s, sm + pv + 256 * (32 * s + 4));
#pragma unroll
    for (int t = 0; t < 2; ++t)
#pragma unroll
      for (int s = 0; s < 4; ++s) ao[4 * t + s] = trrd2(sm + (po ^ (32 * t)) + 512 * 32 * s, sm + (po ^ (32 * t)) + 512 * (32 * s + 4));
    asm volatile("s_waitcnt vmcnt(0)" : "+v"(a[0]), "+v"(a[1]), "+v"(a[2]), "+v"(a[3]), "+v"(A0), "+v"(A1)::"memory");
    for (unsigned spin = 0;; ++spin) {
      unsigned mx = 0u;
#pragma unroll
      for (int i = 0; i < 4; ++i) mx = max(max(mx, max(a[i].x, a[i].y)), max(a[i].z, a[i].w));
      if (!__builtin_amdgcn_ballot_w64(mx == ~0u) || spin > SPIN_CAP) break;
      SWEEP_4(a, o, 0, 256, 2048, 2304, WAIT0);
    }
    STAMP(8);
    {
      f4 c0 = mfma32(A0, a[0], z), c1 = mfma32(A0, a[1], z);
      c0 = mfma32(A1, a[2], c0);
      c1 = mfma32(A1, a[3], c1);
      if (g4 < 2) {
        *(LAS v2u*)(sm + pcw) = v2u{pk(c0[0], c0[1]), pk(c0[2], c0[3])};
        *(LAS v2u*)(sm + pcw + 256) = v2u{pk(c1[0], c1[1]), pk(c1[2], c1[3])};
      }
    }
    BAR();
    {
      f4 acc = z;
      v4u bf[8];
#pragma unroll
      for (int s = 0; s < 8; ++s) bf[s] = *(LAS v4u*)(sm + pcr + 64 * s);
      __builtin_amdgcn_sched_barrier(0);
#pragma unroll
      for (int s = 0; s < 8; ++s) acc = mfma32(av[s], bf[s], acc);
#pragma unroll
      for (int e = 0; e < 4; ++e) {
        acc[e] += dpp<QP_2301>(acc[e]);
        acc[e] += dpp<ROR4>(acc[e]);
      }
      if (col < 2) *(LAS v2u*)(sm + ptw) = v2u{pk(acc[0], acc[1]), pk(acc[2], acc[3])};
    }
    BAR();
    {
      f4 c0 = z, c1 = z;
      v4u bf[4];
#pragma unroll
      for (int s = 0; s < 4; ++s) bf[s] = *(LAS v4u*)(sm + ptr + 64 * s);
      __builtin_amdgcn_sched_barrier(0);
#pragma unroll
      for (int s = 0; s < 4; ++s) {
        c0 = mfma32(ao[s], bf[s], c0);
        c1 = mfma32(ao[4 + s], bf[s], c1);
      }
      if (col & 2) c0 = c1;
      if (col < 4) st_sc1(rs, oo, v4u{pk(c0[0], c0[1]), T, pk(c0[2], c0[3]), T});
    }
    STAMP(9);
  } else {
    const int a = r - 16, b = xcd >> 2, n = 8 * (xcd & 3) + (a >> 1), g = a & 1;
    const int sl = 12 * xcd + a, rd = (4 * xcd + a) & 31;
    const bool glu = a < 12 && sl < NSL;
    const int v = w & 3;
    unsigned op = WS_PART + ((((16 * g + (col & 7)) * 2 + (v >> 1)) * 2 + b) * 32 + n) * 512 + 256 * (v & 1) + (col >> 3) * 64 +
                  (g4 & 1) * 32 + (g4 >> 1) * 16;
    unsigned xres;
    gld4_sc1(ep, ws, 0u);
    asm volatile("global_load_ushort %0, %1, %2" : "=v"(xres) : "v"((unsigned)(((l >> 4) & 1) * 1024 + rd * 32 + col * 2)), "s"(x) : "memory");
    {
      const unsigned vc = (unsigned)(((b * SK + 128 * n) * DC + 8 * l) * 2);
      int row = w + 64 * g;
      if (w >= 4) {
#pragma unroll
        for (int q = 0; q < 4; ++q)
          if (!NOCLEAR) st_sc1(rs, op + 128 * (q >> 1) + (q & 1) * 8 * 65536, v4u{~0u, ~0u, ~0u, ~0u});
        if (w == 7 && l < 32) *(LAS unsigned*)(sm + L_FL + 4 * l) = 0u;
      }
      __builtin_amdgcn_s_sleep(A_DELAY);
#pragma unroll 1
      for (int q = 0; q < 4; ++q) {
        row = w + ((64 * g + (OPT_W ? 64 * (q & 1) + 32 * (q >> 1) : 32 * q)) & 127);
#pragma unroll 1
        for (int i = 0; i < 4; ++i, row += 8) dma16(ckv, vc + row * 1024u, smb + L_C + row * CP);
        if (q < 3) asm volatile("s_barrier" ::: "memory");
      }
      if ((w >> 1) == 2) {
        VMWAIT(22);
        LAYERNORM();
      }
    }
    unsigned o = WS_QA + (b * NH + 16 * g) * 2048 + w * 4096 + l * 16, pqs = smb + L_Q + 2 * w * CP + l * 8;
    PIN(o); PIN(pqs);
    asm volatile("s_waitcnt vmcnt(0)" : "+v"(ep), "+v"(xres)::"memory");
    T = ep + 1u;
    PROBE_T(T);
    BAR();
    STAMP(1);
    v4u fr[32], qg[4];
    SWEEP_4(qg, o, 0, 1024, 2048, 3072, "");
#define QGATHER()                                                                                                           \
  asm volatile("s_waitcnt vmcnt(0)" : "+v"(qg[0]), "+v"(qg[1]), "+v"(qg[2]), "+v"(qg[3])::"memory");                        \
  for (unsigned spin = 0;; ++spin) {                                                                                        \
    if (!TAGS_BAD4(qg) || spin > SPIN_CAP) break;                                                                           \
    SWEEP_4(qg, o, 0, 1024, 2048, 3072, WAIT0);                                                                             \
  }                                                                                                                         \
  STAMP(3);                                                                                                                 \
  asm volatile("ds_write2_b32 %0, %2, %3 offset0:0 offset1:1\n\tds_write2_b32 %0, %4, %5 offset0:128 offset1:129\n\t"        \
               "ds_write2_b32 %1, %6, %7 offset0:0 offset1:1\n\tds_write2_b32 %1, %8, %9 offset0:128 offset1:129" ::"v"(pqs), \
               "v"(pqs + CP), "v"(qg[0].x), "v"(qg[0].z), "v"(qg[1].x), "v"(qg[1].z), "v"(qg[2].x), "v"(qg[2].z), "v"(qg[3].x), \
               "v"(qg[3].z)                                                                                                 \
               : "memory")
    if (__builtin_expect(w < 4, 1)) {
      LAS char* pc = sm + L_C + (32 * w + col) * CP + 16 * g4;
#pragma unroll
      for (int kt = 0; kt < 2; ++kt)
#pragma unroll
        for (int s = 0; s < 16; ++s) fr[16 * kt + s] = *(LAS v4u*)(pc + 16 * kt * CP + 64 * s);
      unsigned p = L_Q + col * CP + 16 * g4, pp = L_P + w * 1024 + l * 16, pm = L_MX + col * 16 + w * 4;
      PIN(p); PIN(pp); PIN(pm);
      STAMP(2);
      QGATHER();
      BAR();
      STAMP(4);
      f4 s0 = z, s1 = z;
      v4u qf[16];
#pragma unroll
      for (int s = 0; s < 6; ++s) qf[s] = *(LAS v4u*)(sm + p + 64 * s);
      __builtin_amdgcn_sched_barrier(0);
#pragma unroll
      for (int s = 0; s < 16; ++s) {
        if (s + 6 < 16) qf[s + 6] = *(LAS v4u*)(sm + p + 64 * (s + 6));
        s0 = mfma32(fr[s], qf[s], s0);
        s1 = mfma32(fr[16 + s], qf[s], s1);
        __builtin_amdgcn_sched_barrier(0);
      }
      float mx = fmax2(fmax2(fmax2(s0[0], s0[1]), fmax2(s0[2], s0[3])), fmax2(fmax2(s1[0], s1[1]), fmax2(s1[2], s1[3])));
      mx = sw32_max(sw16_max(mx));
      constexpr float SC = 0.08838834764831845f * 1.4426950408889634f;
      const float mi = __builtin_ceilf(mx * SC);
#pragma unroll
      for (int e = 0; e < 4; ++e) {
        s0[e] = __builtin_amdgcn_exp2f(fmaf(s0[e], SC, -mi));
        s1[e] = __builtin_amdgcn_exp2f(fmaf(s1[e], SC, -mi));
      }
      *(LAS float*)(sm + pm) = mi;
      *(LAS v4u*)(sm + pp) = v4u{pk(s0[0], s0[1]), pk(s0[2], s0[3]), pk(s1[0], s1[1]), pk(s1[2], s1[3])};
      BAR();
      STAMP(5);
      if (glu) {
        v4u af[8], xf[8];
#pragma unroll
        for (int s = 0; s < 8; ++s) xf[s] = *(LAS v4u*)(px + 512 * (w >> 1) + 64 * s);
        FLAG_WAIT4(0);
        unsigned og = (unsigned)(((128 * w + (l >> 1)) * FF + 16 * sl + 8 * (l & 1)) * 2);
#pragma unroll 1
        for (int i = 0; i < 4; ++i, og += 32 * FF * 2) {
          dma16(wg, og, smb + L_GG + (4 * w + i) * 1024);
          dma16(wu, og, smb + L_GG + 16384 + (4 * w + i) * 1024);
        }
#pragma unroll 1
        for (int i = 0; i < 4; ++i) dma16(wd, (unsigned)(((16 * sl + 4 * w + i) * DM + 8 * l) * 2), smb + L_GD + (4 * w + i) * CP);
        VMWAIT(4);
        FLAG_SET(4 + w);
        FLAG_WAIT4(1);
        STAMP(7);
        LAS char* pg = sm + L_GG + (w & 1) * 16384 + 32 * (256 * (w >> 1) + 8 * g4 + tq) + 8 * tp;
        f4 acc = z;
#pragma unroll
        for (int s = 0; s < 8; ++s) af[s] = trrd2(pg + 32 * 32 * s, pg + 32 * (32 * s + 4));
        __builtin_amdgcn_sched_barrier(0);
#pragma unroll
        for (int s = 0; s < 8; ++s) acc = mfma32(af[s], xf[s], acc);
        *(LAS f4*)(sm + L_GR + ((w & 1) * 2 + (w >> 1)) * 1024 + l * 16) = acc;
        VMWAIT(0);
        FLAG_SET(8 + w);
        FLAG_WAIT4(2);
        v2u dn[8];
#pragma unroll
        for (int t = 0; t < 8; ++t) dn[t] = trrd(sm + L_GD + (4 * g4 + tq) * CP + 32 * (8 * w + t) + 8 * tp);
        LAS char* pr = sm + L_GR + l * 16;
        const f4 gx = *(LAS f4*)(pr) + *(LAS f4*)(pr + 1024), ux = *(LAS f4*)(pr + 2048) + *(LAS f4*)(pr + 3072);
        float hh[4];
#pragma unroll
        for (int e = 0; e < 4; ++e) hh[e] = ux[e] * __builtin_amdgcn_rcpf(1.f + __builtin_amdgcn_exp2f(-1.4426950408889634f * gx[e]));
        const v2u hb = {pk(hh[0], hh[1]), pk(hh[2], hh[3])};
        f4 d = z;
#pragma unroll
        for (int t = 0; t < 8; ++t) d = mfma16(dn[t], (col >> 1) == t ? hb : v2u{0u, 0u}, d);
        st_sc1(rs, WS_OPG + ((8 * w + (col >> 1)) * 128 + sl) * 128 + ((col & 1) * 4 + g4) * 16, v4u{pk(d[0], d[1]), T, pk(d[2], d[3]), T});
        STAMP(9);
      }
    } else {
      LAS char* p0 = sm + L_C + (4 * g4 + tq) * CP + 256 * v + 8 * tp;
      unsigned p1 = L_C + (64 + 4 * g4 + tq) * CP + 256 * v + 8 * tp;
      PIN(p1);
#pragma unroll
      for (int ct = 0; ct < 8; ++ct)
#pragma unroll
        for (int u = 0; u < 4; ++u) {
          LAS char* p = (u < 2 ? p0 : sm + p1) + 32 * (u & 1) * CP + 32 * ct;
          fr[4 * ct + u] = trrd2(p, p + 16 * CP);
        }
      unsigned ol = WS_LSE + ((b * 2 + g) * 32 + n) * 128 + col * 8, pp = L_P + l * 16, pm = L_MX + col * 16;
      v4u one = {0x3f803f80u, 0x3f803f80u, 0x3f803f80u, 0x3f803f80u};
      PIN(ol); PIN(pp); PIN(pm); PIN(one); PIN(op);
      STAMP(2);
      QGATHER();
      BAR();
      STAMP(4);
      {
        const int ln = 128 * a + 64 * (v >> 1) + l;
        touch((v & 1) ? wu : wg, (unsigned)((ln >> 2) * (FF * 2)) + (unsigned)min(384 * xcd + 128 * (ln & 3), FF * 2 - 4), smb + L_JUNK);
      }
      BAR();
      const f4 m4 = *(LAS f4*)(sm + pm);
      v4u pf[4];
#pragma unroll
      for (int u = 0; u < 4; ++u) pf[u] = *(LAS v4u*)(sm + pp + u * 1024);
      const float M = fmax2(fmax2(m4[0], m4[1]), fmax2(m4[2], m4[3]));
#pragma unroll
      for (int u = 0; u < 4; ++u) {
        const unsigned kk = min((unsigned)(M - m4[u]), 255u) * 0x00800080u;
#pragma unroll
        for (int e = 0; e < 4; ++e) pf[u][e] = scale_dn(pf[u][e], kk);
      }
      f4 ls = z;
#pragma unroll
      for (int u = 0; u < 4; ++u) ls = mfma32(one, pf[u], ls);
      const float il = __builtin_amdgcn_rcpf(ls[0]);
      if (v == 0 && g4 == 0)
        __builtin_amdgcn_raw_buffer_store_b64(v2u{__builtin_bit_cast(unsigned, M + __builtin_amdgcn_logf(ls[0])), T}, rs, ol, 0, AUX_SC1);
#pragma unroll
      for (int q = 0; q < 2; ++q) {
        f4 c[4];
#pragma unroll
        for (int k = 0; k < 4; ++k) c[k] = z;
#pragma unroll
        for (int u = 0; u < 4; ++u)
#pragma unroll
          for (int k = 0; k < 4; ++k) c[k] = mfma32(fr[16 * q + 4 * k + u], pf[u], c[k]);
        v4u R[2];
#pragma unroll
        for (int k = 0; k < 2; ++k) {
          const f4 c0 = c[2 * k] * il, c1 = c[2 * k + 1] * il;
          const unsigned x0 = min(pk(c0[0], c0[1]), 0xfffffffeu), x1 = min(pk(c0[2], c0[3]), 0xfffffffeu);
          const unsigned y0 = min(pk(c1[0], c1[1]), 0xfffffffeu), y1 = min(pk(c1[2], c1[3]), 0xfffffffeu);
          auto r0 = __builtin_amdgcn_permlane16_swap(x0, y0, false, false);
          auto r1 = __builtin_amdgcn_permlane16_swap(x1, y1, false, false);
          R[k] = v4u{r0[0], r1[0], r0[1], r1[1]};
        }
        v4u A, B;
#pragma unroll
        for (int e = 0; e < 4; ++e) {
          A[e] = (unsigned)__builtin_amdgcn_update_dpp((int)R[0][e], (int)R[1][e], ROR8, 0xf, 0xc, false);
          B[e] = (unsigned)__builtin_amdgcn_update_dpp((int)R[1][e], (int)R[0][e], ROR8, 0xf, 0x3, false);
        }
        st_sc1(rs, op + 128 * q, A);
        st_sc1(rs, op + 128 * q + 8 * 65536, B);
      }
      FLAG_SET(v);
      STAMP(6);
    }
    if (a >= 12) {
      unsigned ot = WS_OPT + rd * 8192 + tid * 16, og = WS_OPG + rd * 16384 + tid * 16;
      const bool two = (tid >> 3) < NSL - 64;
      unsigned og2 = two ? og + 8192 : og;
      const unsigned m = (col & 1) ? 0x3f800000u : 0x3f80u;
      v4u sel = {col < 2 ? m : 0u, (col >> 1) == 1 ? m : 0u, col < 2 ? m : 0u, (col >> 1) == 1 ? m : 0u};
      unsigned prw = L_RR + w * 128 + (l & 7) * 16, prr = L_RR + (l & 31) * 4, oy = ((l >> 4) & 1) * 1024 + rd * 32 + col * 2;
      PIN(ot); PIN(sel); PIN(prw); PIN(prr); PIN(oy);
      v4u p0, p1;
      for (unsigned spin = 0;; ++spin) {
        SWEEP_2(p0, p1, og, og2, WAIT0);
        if (!__builtin_amdgcn_ballot_w64((T & p0.y & p0.w & p1.y & p1.w) != (T | p0.y | p0.w | p1.y | p1.w)) || spin > SPIN_CAP) break;
      }
      f4 acc = mfma32(sel, v4u{p0.x, p0.z, two ? p1.x : 0u, two ? p1.z : 0u}, z);
      STAMP(7);
      for (unsigned spin = 0;; ++spin) {
        asm volatile(LD16(0, 1, 2) " sc1" WAIT0 : "=&v"(p0) : "v"(ot), "s"(ws) : "memory");
        if (!__builtin_amdgcn_ballot_w64((T & p0.y & p0.w) != (T | p0.y | p0.w)) || spin > SPIN_CAP) break;
      }
      STAMP(8);
      acc = mfma32(sel, v4u{p0.x, p0.z, 0u, 0u}, acc);
#pragma unroll
      for (int e = 0; e < 4; ++e) acc[e] += dpp<ROR8>(acc[e]);
      if (l < 8) *(LAS f4*)(sm + prw) = acc;
      BAR();
      if (w == 0 && l < 32) {
        float t[8];
#pragma unroll
        for (int i = 0; i < 8; ++i) t[i] = *(LAS float*)(sm + prr + i * 128);
        const float y = blo(xres) + (((t[0] + t[1]) + (t[2] + t[3])) + ((t[4] + t[5]) + (t[6] + t[7])));
        *(u16*)((char*)out + oy) = (u16)(pk(y, y) & 0xffffu);
        if (rd == 0 && l == 0) __builtin_amdgcn_raw_buffer_store_b32(T, rs, WS_E, 0, AUX_SC1);
      }
      STAMP(9);
    }
  }
  STAMP_FLUSH();
}

#define WS_END 0
#define WS_BASE (WS_END ? ((ws_size - (size_t)WS_END * 1048576) & ~(size_t)4095) : 0)
extern "C" void kernel_launch(void* const* d_in, const int* in_sizes, int n_in, void* d_out, int out_size, void* d_ws, size_t ws_size,
                              hipStream_t stream) {
  mla_glu_decode<<<256, 512, 0, stream>>>((const u16*)d_in[0], (const u16*)d_in[1], (const u16*)d_in[2], (const u16*)d_in[3],
                                          (const u16*)d_in[4], (const u16*)d_in[5], (const u16*)d_in[7], (const u16*)d_in[8],
                                          (const u16*)d_in[9], (u16*)d_out, (char*)d_ws + WS_BASE);
}
```

```cpp
#include <hip/hip_runtime.h>
#include <stdint.h>

#define GAS __attribute__((address_space(1)))
#define LAS __attribute__((address_space(3)))
typedef unsigned short u16;
typedef unsigned v4u __attribute__((ext_vector_type(4)));
typedef unsigned v2u __attribute__((ext_vector_type(2)));
typedef float f4 __attribute__((ext_vector_type(4)));
typedef float f2 __attribute__((ext_vector_type(2)));
typedef __bf16 bf8 __attribute__((ext_vector_type(8)));
typedef __bf16 bf2 __attribute__((ext_vector_type(2)));
typedef short s4 __attribute__((ext_vector_type(4)));

namespace {
constexpr int NH = 32, SK = 4096, DH = 128, DM = 512, DC = 512, FF = 1376;
constexpr int NSL = FF / 16;
constexpr int WS_E = 0;
constexpr int WS_QP = 4096;
constexpr int WS_QA = WS_QP + NH * 4 * 1024;
constexpr int WS_LSE = WS_QA + 2 * NH * 2048;
constexpr int WS_OPT = WS_LSE + 2 * 2 * 32 * 128;
constexpr int WS_OPG = WS_OPT + 32 * 64 * 128;
constexpr int WS_PART = WS_OPG + 32 * 128 * 128;
constexpr int WS_BYTES = WS_PART + NH * 2 * 2 * 32 * 512;
constexpr int WS_STAMP = 7 << 19;
constexpr int CP = 1056;
constexpr int XP = 1088, SP = 288, SP2 = 544;
constexpr int L_C = 0, L_Q = 128 * CP, L_P = L_Q + 16 * CP, L_X = L_P + 4096, L_MX = L_X + 2 * XP, L_RR = L_MX + 256,
              L_JUNK = L_RR + 1024, L_FL = L_JUNK + 256, L_END = L_FL + 128;
static_assert(L_END <= 163840, "LDS");
constexpr int L_GG = 0, L_GD = 32768, L_GR = 65536;
constexpr int L_WUV = 0, L_WO = 65536, L_WQ = 0, L_WUK = 32768, L_CC = 131072, L_CT = L_CC + 8 * SP2, L_W = L_CT + 2 * SP;
#define UK_DELAY 16
#define H1_DELAY 8
#define T_DELAY 20
#define GLU_FLAG_Q 1
#define NOCLEAR 0
#define CODE_LINES 0
#define OPT_W 0
#define QA_WARM 0
#define LSE_DELAY 40
#define A_DELAY 24
#define PF_GLU 1
constexpr unsigned SPIN_CAP = 1u << 20;
constexpr int AUX_SC1 = 16;

#define STAMP_W 0
#define STAMP(i)
#define STAMP_FLUSH()
#define PROBE_T(T_)

__device__ __forceinline__ f4 mfma32(v4u a, v4u b, f4 c) {
  return __builtin_amdgcn_mfma_f32_16x16x32_bf16(__builtin_bit_cast(bf8, a), __builtin_bit_cast(bf8, b), c, 0, 0, 0);
}
__device__ __forceinline__ f4 mfma16(v2u a, v2u b, f4 c) {
  return __builtin_amdgcn_mfma_f32_16x16x16bf16_1k(__builtin_bit_cast(s4, a), __builtin_bit_cast(s4, b), c, 0, 0, 0);
}
__device__ __forceinline__ unsigned pk(float lo, float hi) {
  f2 x = {lo, hi};
  return __builtin_bit_cast(unsigned, __builtin_convertvector(x, bf2));
}
__device__ __forceinline__ float asf(unsigned u) { return __builtin_bit_cast(float, u); }
__device__ __forceinline__ float blo(unsigned u) { return __builtin_bit_cast(float, u << 16); }
__device__ __forceinline__ float bhi(unsigned u) { return __builtin_bit_cast(float, u & 0xffff0000u); }
template <int CTRL> __device__ __forceinline__ float dpp(float v) {
  return __builtin_bit_cast(float, __builtin_amdgcn_update_dpp(0, __builtin_bit_cast(int, v), CTRL, 0xf, 0xf, true));
}
__device__ __forceinline__ float fmax2(float a, float b) {
#pragma float_control(precise, off)
  return __builtin_fmaxf(a, b);
}
typedef unsigned short h2 __attribute__((ext_vector_type(2)));
__device__ __forceinline__ unsigned scale_dn(unsigned p, unsigned kk) {
  return __builtin_bit_cast(unsigned, __builtin_elementwise_sub_sat(__builtin_bit_cast(h2, p), __builtin_bit_cast(h2, kk)));
}
#define PIN(x) asm volatile("" : "+v"(x))
constexpr int ROR1 = 0x121, ROR2 = 0x122, ROR4 = 0x124, ROR8 = 0x128, QP_2301 = 0x4e, QP_2222 = 0xaa;
__device__ __forceinline__ float sw32_add(float a, float b) {
  auto r = __builtin_amdgcn_permlane32_swap(__builtin_bit_cast(unsigned, a), __builtin_bit_cast(unsigned, b), false, false);
  return asf(r[0]) + asf(r[1]);
}
__device__ __forceinline__ float sw16_add(float a, float b) {
  auto r = __builtin_amdgcn_permlane16_swap(__builtin_bit_cast(unsigned, a), __builtin_bit_cast(unsigned, b), false, false);
  return asf(r[0]) + asf(r[1]);
}
__device__ __forceinline__ float vmax(unsigned a, unsigned b) {
  float r;
  asm("v_max_f32 %0, %1, %2" : "=v"(r) : "v"(a), "v"(b));
  return r;
}
__device__ __forceinline__ float sw32_max(float a) {
  auto r = __builtin_amdgcn_permlane32_swap(__builtin_bit_cast(unsigned, a), __builtin_bit_cast(unsigned, a), false, false);
  return vmax(r[0], r[1]);
}
__device__ __forceinline__ float sw16_max(float a) {
  auto r = __builtin_amdgcn_permlane16_swap(__builtin_bit_cast(unsigned, a), __builtin_bit_cast(unsigned, a), false, false);
  return vmax(r[0], r[1]);
}
__device__ __forceinline__ float wave_sum(float v) {
  v += dpp<ROR8>(v);
  v += dpp<ROR4>(v);
  v += dpp<ROR2>(v);
  v += dpp<ROR1>(v);
  v = sw16_add(v, v);
  return sw32_add(v, v);
}
__device__ __forceinline__ v2u trrd(LAS char* p) {
  return __builtin_bit_cast(v2u, __builtin_amdgcn_ds_read_tr16_b64_v4i16((LAS s4*)p));
}
__device__ __forceinline__ v4u trrd2(LAS char* p0, LAS char* p1) {
  v2u a = trrd(p0), b = trrd(p1);
  return v4u{a.x, a.y, b.x, b.y};
}
#define BAR() asm volatile("s_waitcnt lgkmcnt(0)\n\ts_barrier" ::: "memory")
#define VMWAIT(k) asm volatile("s_waitcnt vmcnt(%0)" ::"n"(k) : "memory")
__device__ __forceinline__ void gld16(v4u& d, const void* base, unsigned off) {
  asm volatile("global_load_dwordx4 %0, %1, %2" : "=v"(d) : "v"(off), "s"(base) : "memory");
}
__device__ __forceinline__ void gld4_sc1(unsigned& d, const void* base, unsigned off) {
  asm volatile("global_load_dword %0, %1, %2 sc1" : "=v"(d) : "v"(off), "s"(base) : "memory");
}
__device__ __forceinline__ void touch(const void* base, unsigned off, unsigned lds) {
  asm volatile("s_mov_b32 m0, %2\n\ts_nop 0\n\tglobal_load_lds_dword %0, %1" ::"v"(off), "s"(base), "s"(lds) : "memory");
}
__device__ __forceinline__ void touch_sc1(const void* base, unsigned off, unsigned lds) {
  asm volatile("s_mov_b32 m0, %2\n\ts_nop 0\n\tglobal_load_lds_dword %0, %1 sc1" ::"v"(off), "s"(base), "s"(lds) : "memory");
}
#define LD16(i, a, o) "global_load_dwordx4 %" #i ", %" #a ", %" #o
#define SWEEP_4(q, o, o0, o1, o2, o3, W)                                                                                     \
  asm volatile(LD16(0, 4, 5) " offset:" #o0 " sc1\n\t" LD16(1, 4, 5) " offset:" #o1 " sc1\n\t" LD16(2, 4, 5) " offset:" #o2         \
               " sc1\n\t" LD16(3, 4, 5) " offset:" #o3 " sc1" W                                                               \
               : "=&v"((q)[0]), "=&v"((q)[1]), "=&v"((q)[2]), "=&v"((q)[3])                                                  \
               : "v"(o), "s"(ws)                                                                                             \
               : "memory")
#define SWEEP_L(W)                                                                                                          \
  asm volatile("global_load_dwordx2 %0, %3, %5 sc1\n\tglobal_load_dwordx2 %1, %4, %5 sc1\n\t"                              \
               "global_load_dwordx2 %2, %4, %5 offset:512 sc1" W                                                            \
               : "=&v"(la), "=&v"(l0), "=&v"(l1)                                                                            \
               : "v"(ola), "v"(olo), "s"(ws)                                                                                \
               : "memory")
#define SWEEP_2(p, q, oa, ob, W) \
  asm volatile(LD16(0, 2, 4) " sc1\n\t" LD16(1, 3, 4) " sc1" W : "=&v"(p), "=&v"(q) : "v"(oa), "v"(ob), "s"(ws) : "memory")
#define TAGS_BAD4(q) \
  __builtin_amdgcn_ballot_w64((T & (q)[0].y & (q)[0].w & (q)[1].y & (q)[1].w & (q)[2].y & (q)[2].w & (q)[3].y & (q)[3].w) != \
                              (T | (q)[0].y | (q)[0].w | (q)[1].y | (q)[1].w | (q)[2].y | (q)[2].w | (q)[3].y | (q)[3].w))
#define FLAG_SET(i) asm volatile("ds_write_b32 %0, %1" ::"v"(smb + L_FL + 4 * (i)), "v"(1u) : "memory")
#define FLAG_WAIT4(i)                                                                                     \
  for (unsigned spin_ = 0; spin_ < SPIN_CAP; ++spin_) {                                                   \
    v4u f_;                                                                                               \
    asm volatile("ds_read_b128 %0, %1\n\ts_waitcnt lgkmcnt(0)" : "=v"(f_) : "v"(smb + L_FL + 16 * (i)) : "memory"); \
    if (__builtin_amdgcn_readfirstlane(f_.x & f_.y & f_.z & f_.w)) break;                                 \
  }
#define WAIT0 "\n\ts_waitcnt vmcnt(0)"
__device__ __forceinline__ void dma16(const void* base, unsigned off, unsigned lds) {
  asm volatile("s_mov_b32 m0, %2\n\ts_nop 0\n\tglobal_load_lds_dwordx4 %0, %1" ::"v"(off), "s"(base), "s"(lds) : "memory");
}
__device__ __forceinline__ v4u ld_sc1(__amdgpu_buffer_rsrc_t rs, int off) {
  return __builtin_amdgcn_raw_buffer_load_b128(rs, off, 0, AUX_SC1);
}
__device__ __forceinline__ v2u ld8_sc1(__amdgpu_buffer_rsrc_t rs, int off) {
  return __builtin_amdgcn_raw_buffer_load_b64(rs, off, 0, AUX_SC1);
}
__device__ __forceinline__ void st_sc1(__amdgpu_buffer_rsrc_t rs, int off, v4u v) {
  __builtin_amdgcn_raw_buffer_store_b128(v, rs, off, 0, AUX_SC1);
}
__device__ __forceinline__ void ln_stats(v4u r, float& mean, float& rstd) {
  float e[8] = {blo(r.x), bhi(r.x), blo(r.y), bhi(r.y), blo(r.z), bhi(r.z), blo(r.w), bhi(r.w)};
  float s = ((e[0] + e[1]) + (e[2] + e[3])) + ((e[4] + e[5]) + (e[6] + e[7]));
  mean = wave_sum(s) * (1.f / DM);
  float q = 0.f;
#pragma unroll
  for (int i = 0; i < 8; ++i) {
    float d = e[i] - mean;
    q = fmaf(d, d, q);
  }
  rstd = __builtin_amdgcn_rsqf(wave_sum(q) * (1.f / DM) + 1e-5f);
}
}

__global__ void __launch_bounds__(512, 2)
mla_glu_decode(const u16* __restrict__ x, const u16* __restrict__ ckv, const u16* __restrict__ wq, const u16* __restrict__ wuk,
               const u16* __restrict__ wuv, const u16* __restrict__ wo, const u16* __restrict__ wg, const u16* __restrict__ wu,
               const u16* __restrict__ wd, u16* __restrict__ out, char* __restrict__ ws) {
  __shared__ __attribute__((aligned(16))) char smem_[L_END];
  LAS char* sm = (LAS char*)smem_;
  const unsigned smb = (unsigned)(size_t)sm;
  const int tid = threadIdx.x, l = tid & 63, w = __builtin_amdgcn_readfirstlane(tid >> 6);
  const int col = l & 15, g4 = l >> 4, tq = (l & 15) >> 2, tp = l & 3;
  const int xcd = blockIdx.x & 7, r = blockIdx.x >> 3;
  const __amdgpu_buffer_rsrc_t rs = __builtin_amdgcn_make_buffer_rsrc(ws, 0, WS_BYTES, 0x00020000);
  asm volatile("" ::"s"(x), "s"(ckv), "s"(wq), "s"(wuk), "s"(wuv), "s"(wo), "s"(wg), "s"(wu), "s"(wd), "s"(out), "s"(ws));
  STAMP(0);

  unsigned ep, T;
  LAS char* const px = sm + L_X + (col & 1) * XP + 16 * g4;
  if ((w >> 1) == 2) dma16(x, (unsigned)((w & 1) * 1024 + l * 16), smb + L_X + (w & 1) * XP);
  const f4 z = {0.f, 0.f, 0.f, 0.f};
#define LAYERNORM()                                                                                                              \
  {                                                                                                                              \
    const v4u xr = *(LAS v4u*)(sm + L_X + (w & 1) * XP + l * 16);                                                                \
    float e[8] = {blo(xr.x), bhi(xr.x), blo(xr.y), bhi(xr.y), blo(xr.z), bhi(xr.z), blo(xr.w), bhi(xr.w)};                       \
    const float mean = wave_sum(((e[0] + e[1]) + (e[2] + e[3])) + ((e[4] + e[5]) + (e[6] + e[7]))) * (1.f / DM);                 \
    float q = 0.f;                                                                                                               \
    _Pragma("unroll") for (int i = 0; i < 8; ++i) {                                                                              \
      e[i] -= mean;                                                                                                              \
      q = fmaf(e[i], e[i], q);                                                                                                   \
    }                                                                                                                            \
    const float rstd = __builtin_amdgcn_rsqf(wave_sum(q) * (1.f / DM) + 1e-5f);                                                  \
    *(LAS v4u*)(sm + L_X + (w & 1) * XP + l * 16) =                                                                              \
        v4u{pk(e[0] * rstd, e[1] * rstd), pk(e[2] * rstd, e[3] * rstd), pk(e[4] * rstd, e[5] * rstd), pk(e[6] * rstd, e[7] * rstd)}; \
  }

  if (__builtin_expect(r < 16, 1)) {
    const int h = 4 * xcd + (r >> 2), j = r & 3, ca = j >> 1, mb = j & 1;
    const int fl = tq | ((g4 & 1) << 2);
    gld4_sc1(ep, ws, 0u);
    if (__builtin_expect(w < 4, 1)) {
      const unsigned vq = (unsigned)(((128 * j + g4) * 4096 + h * DH + 8 * (col ^ ((g4 | ((w & 2) << 1)) << 1))) * 2);
#pragma unroll 1
      for (int k = w; k < 32; k += 4) dma16(wq, vq + (unsigned)k * 32768u, smb + L_WQ + k * 1024);
    } else if (w < 6) {
      VMWAIT(1);
      LAYERNORM();
    } else {
      __builtin_amdgcn_s_sleep(UK_DELAY);
      const unsigned vu = (unsigned)((((128 * j + g4) * NH + h) * DH) * 2);
#pragma unroll 1
      for (int k = w - 6; k < 32; k += 2) dma16(wuk, vu + (unsigned)k * 32768u + 16u * (col ^ ((4 * k + g4) & 15)), smb + L_WUK + k * 1024);
    }
    unsigned p = L_WQ + 256 * (8 * g4 + tq) + 16 * ((2 * w + (tp >> 1)) ^ (fl << 1)) + 8 * (tp & 1);
    unsigned oqp = WS_QP + ((h * 4 + j) * 8 + w) * 128 + col * 64 + g4 * 16;
    unsigned pxj = L_X + (col & 1) * XP + 16 * g4 + 256 * j;
    unsigned o = WS_QP + ((h * 4 + ((col >> 1) & 3)) * 8 + (g4 >> 1)) * 128 + (col & 1) * 64 + (g4 & 1) * 32;
    unsigned oqa = WS_QA + ((col & 1) * NH + h) * 2048 + (64 * j + 16 * w + 8 * (col >> 1) + 2 * g4) * 8;
    unsigned pu = L_WUK + 256 * (32 * w + col) + 16 * (g4 ^ col);
    PIN(p); PIN(oqp); PIN(pxj); PIN(o); PIN(oqa); PIN(pu);
    if (w < 6) VMWAIT(0);
    else VMWAIT(16);
    PIN(ep);
    T = ep + 1u;
    PIN(T);
    STAMP(1);
    BAR();
    STAMP(2);
    {
      v4u af[4], xf[4];
#pragma unroll
      for (int s = 0; s < 4; ++s) {
        af[s] = trrd2(sm + p + 256 * 32 * s, sm + p + 256 * (32 * s + 4));
        xf[s] = *(LAS v4u*)(sm + pxj + 64 * s);
      }
      __builtin_amdgcn_sched_barrier(0);
      f4 acc = z;
#pragma unroll
      for (int s = 0; s < 4; ++s) acc = mfma32(af[s], xf[s], acc);
      if (col < 2) st_sc1(rs, oqp, v4u{pk(acc[0], acc[1]), T, pk(acc[2], acc[3]), T});
    }
    STAMP(3);
    if (w >= 6) VMWAIT(1);
    if (w >= 4) BAR();
    if (w < 4) {
      v4u a[8], uk[8];
      __builtin_amdgcn_s_sleep(H1_DELAY);
      SWEEP_4(a, o, 0, 16, 256, 272, "");
      SWEEP_4(a + 4, o, 512, 528, 768, 784, "");
      BAR();
#pragma unroll
      for (int t = 0; t < 2; ++t)
#pragma unroll
        for (int s = 0; s < 4; ++s) uk[4 * t + s] = *(LAS v4u*)(sm + (pu ^ (64 * s)) + 4096 * t);
      asm volatile("s_waitcnt vmcnt(%8)"
                   : "+v"(a[0]), "+v"(a[1]), "+v"(a[2]), "+v"(a[3]), "+v"(a[4]), "+v"(a[5]), "+v"(a[6]), "+v"(a[7])
                   : "n"(OPT_W)
                   : "memory");
      for (unsigned spin = 0;; ++spin) {
        if (!(TAGS_BAD4(a) | TAGS_BAD4(a + 4)) || spin > SPIN_CAP) break;
        SWEEP_4(a, o, 0, 16, 256, 272, "");
        SWEEP_4(a + 4, o, 512, 528, 768, 784, WAIT0);
        asm volatile("" : "+v"(a[0]), "+v"(a[1]), "+v"(a[2]), "+v"(a[3]));
      }
      STAMP(4);
      f4 a0 = z, a1 = z;
#pragma unroll
      for (int s = 0; s < 4; ++s) {
        const v4u qf = {a[2 * s].x, a[2 * s].z, a[2 * s + 1].x, a[2 * s + 1].z};
        a0 = mfma32(uk[s], qf, a0);
        a1 = mfma32(uk[4 + s], qf, a1);
      }
      float q[4];
#pragma unroll
      for (int e = 0; e < 4; ++e) {
        const float mine = (col & 2) ? a1[e] : a0[e], other = (col & 2) ? a0[e] : a1[e];
        q[e] = mine + dpp<QP_2301>(other);
        q[e] += dpp<ROR4>(q[e]);
      }
      if (col < 4) st_sc1(rs, oqa, v4u{pk(q[0], q[1]), T, pk(q[2], q[3]), T});
    }
    STAMP(5);
    PROBE_T(T);
    BAR();
    __builtin_amdgcn_s_sleep(T_DELAY);
    {
      const unsigned vv = (unsigned)((((256 * ca + g4) * NH + h) * DH + 8 * (col ^ ((g4 | ((w & 2) << 1)) << 1))) * 2);
      const unsigned vo = (unsigned)(((h * DH + (l >> 5)) * DM + 256 * mb) * 2) + 16u * ((l & 31) ^ (((l >> 5) | ((w & 1) << 1) | (w & 4)) << 1));
      int kv = w + 32 * mb, ko = w + 32 * ca;
#pragma unroll 1
      for (int hf = 0; hf < 2; ++hf) {
#pragma unroll 1
        for (int i = 0; i < 4; ++i, kv = (kv + 8) & 63) dma16(wuv, vv + (unsigned)kv * 32768u, smb + L_WUV + kv * 1024);
#pragma unroll 1
        for (int i = 0; i < 4; ++i, ko = (ko + 8) & 63) dma16(wo, vo + (unsigned)ko * 2048u, smb + L_WO + ko * 1024);
        if (hf == 0) asm volatile("s_barrier" ::: "memory");
      }
    }
    const int tb = w >> 2, nq = w & 3;
    o = WS_PART + (((h * 2 + ca) * 2 + tb) * 32 + 8 * nq + g4) * 512 + 16 * col;
    unsigned pw = L_W + (tb * 32 + 8 * nq + g4) * 4;
    unsigned pv = L_WUV + 256 * (8 * g4 + tq) + 16 * ((2 * w + (tp >> 1)) ^ (fl << 1)) + 8 * (tp & 1);
    unsigned po = L_WO + 512 * (8 * g4 + tq) + 16 * ((4 * w + (tp >> 1)) ^ (fl << 1)) + 8 * (tp & 1);
    unsigned pcw = L_CC + (nq * 2 + tb) * SP2 + 16 * col + 8 * g4, pcr = L_CC + (col & 7) * SP2 + 16 * g4;
    unsigned ptw = L_CT + col * SP + (16 * w + 4 * g4) * 2, ptr = L_CT + (col & 1) * SP + 16 * g4;
    unsigned oo = WS_OPT + ((16 * mb + 2 * w + (col >> 1)) * 64 + 2 * h + ca) * 128 + ((col & 1) * 4 + g4) * 16;
    v4u mk;
#pragma unroll
    for (int d = 0; d < 4; ++d) mk[d] = (col >> 1) == d ? ((col & 1) ? 0xffff0000u : 0xffffu) : 0u;
    PIN(o); PIN(pw); PIN(pv); PIN(po); PIN(pcw); PIN(pcr); PIN(ptw); PIN(ptr); PIN(oo); PIN(mk);
    if (w == 7) {
      const unsigned ol = WS_LSE + (((l >> 5) * 2 + (h >> 4)) * 32 + (l & 31)) * 128 + (h & 15) * 8;
      v2u la;
      for (unsigned spin = 0;; ++spin) {
        asm volatile("global_load_dwordx2 %0, %1, %2 sc1" WAIT0 : "=&v"(la) : "v"(ol), "s"(ws) : "memory");
        if (!__builtin_amdgcn_ballot_w64(la.y != T) || spin > SPIN_CAP) break;
      }
      float M = asf(la.x);
      M = fmax2(M, dpp<ROR8>(M));
      M = fmax2(M, dpp<ROR4>(M));
      M = fmax2(M, dpp<ROR2>(M));
      M = fmax2(M, dpp<ROR1>(M));
      M = sw16_max(M);
      const float e = __builtin_amdgcn_exp2f(asf(la.x) - M);
      float L = e + dpp<ROR8>(e);
      L += dpp<ROR4>(L);
      L += dpp<ROR2>(L);
      L += dpp<ROR1>(L);
      L = sw16_add(L, L);
      const float wt = e * __builtin_amdgcn_rcpf(L);
      *(LAS unsigned*)(sm + L_W + 4 * l) = pk(wt, wt);
      STAMP(7);
    }
    VMWAIT(0);
    STAMP(6);
    BAR();
    v4u a[4];
    SWEEP_4(a, o, 0, 256, 2048, 2304, "");
    v4u A0 = mk & *(LAS unsigned*)(sm + pw), A1 = mk & *(LAS unsigned*)(sm + pw + 16);
    v4u av[8], ao[8];
#pragma unroll
    for (int s = 0; s < 8; ++s) av[s] = trrd2(sm + pv + 256 * 32 * s, sm + pv + 256 * (32 * s + 4));
#pragma unroll
    for (int t = 0; t < 2; ++t)
#pragma unroll
      for (int s = 0; s < 4; ++s) ao[4 * t + s] = trrd2(sm + (po ^ (32 * t)) + 512 * 32 * s, sm + (po ^ (32 * t)) + 512 * (32 * s + 4));
    asm volatile("s_waitcnt vmcnt(0)" : "+v"(a[0]), "+v"(a[1]), "+v"(a[2]), "+v"(a[3]), "+v"(A0), "+v"(A1)::"memory");
    for (unsigned spin = 0;; ++spin) {
      unsigned mx = 0u;
#pragma unroll
      for (int i = 0; i < 4; ++i) mx = max(max(mx, max(a[i].x, a[i].y)), max(a[i].z, a[i].w));
      if (!__builtin_amdgcn_ballot_w64(mx == ~0u) || spin > SPIN_CAP) break;
      SWEEP_4(a, o, 0, 256, 2048, 2304, WAIT0);
    }
    STAMP(8);
    {
      f4 c0 = mfma32(A0, a[0], z), c1 = mfma32(A0, a[1], z);
      c0 = mfma32(A1, a[2], c0);
      c1 = mfma32(A1, a[3], c1);
      if (g4 < 2) {
        *(LAS v2u*)(sm + pcw) = v2u{pk(c0[0], c0[1]), pk(c0[2], c0[3])};
        *(LAS v2u*)(sm + pcw + 256) = v2u{pk(c1[0], c1[1]), pk(c1[2], c1[3])};
      }
    }
    BAR();
    {
      f4 acc = z;
      v4u bf[8];
#pragma unroll
      for (int s = 0; s < 8; ++s) bf[s] = *(LAS v4u*)(sm + pcr + 64 * s);
      __builtin_amdgcn_sched_barrier(0);
#pragma unroll
      for (int s = 0; s < 8; ++s) acc = mfma32(av[s], bf[s], acc);
#pragma unroll
      for (int e = 0; e < 4; ++e) {
        acc[e] += dpp<QP_2301>(acc[e]);
        acc[e] += dpp<ROR4>(acc[e]);
      }
      if (col < 2) *(LAS v2u*)(sm + ptw) = v2u{pk(acc[0], acc[1]), pk(acc[2], acc[3])};
    }
    BAR();
    {
      f4 c0 = z, c1 = z;
      v4u bf[4];
#pragma unroll
      for (int s = 0; s < 4; ++s) bf[s] = *(LAS v4u*)(sm + ptr + 64 * s);
      __builtin_amdgcn_sched_barrier(0);
#pragma unroll
      for (int s = 0; s < 4; ++s) {
        c0 = mfma32(ao[s], bf[s], c0);
        c1 = mfma32(ao[4 + s], bf[s], c1);
      }
      if (col & 2) c0 = c1;
      if (col < 4) st_sc1(rs, oo, v4u{pk(c0[0], c0[1]), T, pk(c0[2], c0[3]), T});
    }
    STAMP(9);
  } else {
    const int a = r - 16, b = xcd >> 2, n = 8 * (xcd & 3) + (a >> 1), g = a & 1;
    const int sl = 12 * xcd + a, rd = (4 * xcd + a) & 31;
    const bool glu = a < 12 && sl < NSL;
    const int v = w & 3;
    unsigned op = WS_PART + ((((16 * g + (col & 7)) * 2 + (v >> 1)) * 2 + b) * 32 + n) * 512 + 256 * (v & 1) + (col >> 3) * 64 +
                  (g4 & 1) * 32 + (g4 >> 1) * 16;
    unsigned xres;
    gld4_sc1(ep, ws, 0u);
    asm volatile("global_load_ushort %0, %1, %2" : "=v"(xres) : "v"((unsigned)(((l >> 4) & 1) * 1024 + rd * 32 + col * 2)), "s"(x) : "memory");
    {
      const unsigned vc = (unsigned)(((b * SK + 128 * n) * DC + 8 * l) * 2);
      int row = w + 64 * g;
      if (w >= 4) {
#pragma unroll
        for (int q = 0; q < 4; ++q)
          if (!NOCLEAR) st_sc1(rs, op + 128 * (q >> 1) + (q & 1) * 8 * 65536, v4u{~0u, ~0u, ~0u, ~0u});
        if (w == 7 && l < 32) *(LAS unsigned*)(sm + L_FL + 4 * l) = 0u;
      }
      __builtin_amdgcn_s_sleep(A_DELAY);
#pragma unroll 1
      for (int q = 0; q < 4; ++q) {
        row = w + ((64 * g + (OPT_W ? 64 * (q & 1) + 32 * (q >> 1) : 32 * q)) & 127);
#pragma unroll 1
        for (int i = 0; i < 4; ++i, row += 8) dma16(ckv, vc + row * 1024u, smb + L_C + row * CP);
        if (q < 3) asm volatile("s_barrier" ::: "memory");
      }
      if ((w >> 1) == 2) {
        VMWAIT(22);
        LAYERNORM();
      }
    }
    unsigned o = WS_QA + (b * NH + 16 * g) * 2048 + w * 4096 + l * 16, pqs = smb + L_Q + 2 * w * CP + l * 8;
    PIN(o); PIN(pqs);
    asm volatile("s_waitcnt vmcnt(0)" : "+v"(ep), "+v"(xres)::"memory");
    T = ep + 1u;
    PROBE_T(T);
    BAR();
    STAMP(1);
    v4u fr[32], qg[4];
    SWEEP_4(qg, o, 0, 1024, 2048, 3072, "");
#define QGATHER()                                                                                                           \
  asm volatile("s_waitcnt vmcnt(0)" : "+v"(qg[0]), "+v"(qg[1]), "+v"(qg[2]), "+v"(qg[3])::"memory");                        \
  for (unsigned spin = 0;; ++spin) {                                                                                        \
    if (!TAGS_BAD4(qg) || spin > SPIN_CAP) break;                                                                           \
    SWEEP_4(qg, o, 0, 1024, 2048, 3072, WAIT0);                                                                             \
  }                                                                                                                         \
  STAMP(3);                                                                                                                 \
  asm volatile("ds_write2_b32 %0, %2, %3 offset0:0 offset1:1\n\tds_write2_b32 %0, %4, %5 offset0:128 offset1:129\n\t"        \
               "ds_write2_b32 %1, %6, %7 offset0:0 offset1:1\n\tds_write2_b32 %1, %8, %9 offset0:128 offset1:129" ::"v"(pqs), \
               "v"(pqs + CP), "v"(qg[0].x), "v"(qg[0].z), "v"(qg[1].x), "v"(qg[1].z), "v"(qg[2].x), "v"(qg[2].z), "v"(qg[3].x), \
               "v"(qg[3].z)                                                                                                 \
               : "memory")
    if (__builtin_expect(w < 4, 1)) {
      LAS char* pc = sm + L_C + (32 * w + col) * CP + 16 * g4;
#pragma unroll
      for (int kt = 0; kt < 2; ++kt)
#pragma unroll
        for (int s = 0; s < 16; ++s) fr[16 * kt + s] = *(LAS v4u*)(pc + 16 * kt * CP + 64 * s);
      unsigned p = L_Q + col * CP + 16 * g4, pp = L_P + w * 1024 + l * 16, pm = L_MX + col * 16 + w * 4;
      PIN(p); PIN(pp); PIN(pm);
      STAMP(2);
      QGATHER();
      BAR();
      STAMP(4);
      f4 s0 = z, s1 = z;
      v4u qf[16];
#pragma unroll
      for (int s = 0; s < 6; ++s) qf[s] = *(LAS v4u*)(sm + p + 64 * s);
      __builtin_amdgcn_sched_barrier(0);
#pragma unroll
      for (int s = 0; s < 16; ++s) {
        if (s + 6 < 16) qf[s + 6] = *(LAS v4u*)(sm + p + 64 * (s + 6));
        s0 = mfma32(fr[s], qf[s], s0);
        s1 = mfma32(fr[16 + s], qf[s], s1);
        __builtin_amdgcn_sched_barrier(0);
      }
      float mx = fmax2(fmax2(fmax2(s0[0], s0[1]), fmax2(s0[2], s0[3])), fmax2(fmax2(s1[0], s1[1]), fmax2(s1[2], s1[3])));
      mx = sw32_max(sw16_max(mx));
      constexpr float SC = 0.08838834764831845f * 1.4426950408889634f;
      const float mi = __builtin_ceilf(mx * SC);
#pragma unroll
      for (int e = 0; e < 4; ++e) {
        s0[e] = __builtin_amdgcn_exp2f(fmaf(s0[e], SC, -mi));
        s1[e] = __builtin_amdgcn_exp2f(fmaf(s1[e], SC, -mi));
      }
      *(LAS float*)(sm + pm) = mi;
      *(LAS v4u*)(sm + pp) = v4u{pk(s0[0], s0[1]), pk(s0[2], s0[3]), pk(s1[0], s1[1]), pk(s1[2], s1[3])};
      BAR();
      STAMP(5);
      if (glu) {
        v4u af[8], xf[8];
#pragma unroll
        for (int s = 0; s < 8; ++s) xf[s] = *(LAS v4u*)(px + 512 * (w >> 1) + 64 * s);
        FLAG_WAIT4(0);
        unsigned og = (unsigned)(((128 * w + (l >> 1)) * FF + 16 * sl + 8 * (l & 1)) * 2);
#pragma unroll 1
        for (int i = 0; i < 4; ++i, og += 32 * FF * 2) {
          dma16(wg, og, smb + L_GG + (4 * w + i) * 1024);
          dma16(wu, og, smb + L_GG + 16384 + (4 * w + i) * 1024);
        }
#pragma unroll 1
        for (int i = 0; i < 4; ++i) dma16(wd, (unsigned)(((16 * sl + 4 * w + i) * DM + 8 * l) * 2), smb + L_GD + (4 * w + i) * CP);
        VMWAIT(4);
        FLAG_SET(4 + w);
        FLAG_WAIT4(1);
        STAMP(7);
        LAS char* pg = sm + L_GG + (w & 1) * 16384 + 32 * (256 * (w >> 1) + 8 * g4 + tq) + 8 * tp;
        f4 acc = z;
#pragma unroll
        for (int s = 0; s < 8; ++s) af[s] = trrd2(pg + 32 * 32 * s, pg + 32 * (32 * s + 4));
        __builtin_amdgcn_sched_barrier(0);
#pragma unroll
        for (int s = 0; s < 8; ++s) acc = mfma32(af[s], xf[s], acc);
        *(LAS f4*)(sm + L_GR + ((w & 1) * 2 + (w >> 1)) * 1024 + l * 16) = acc;
        VMWAIT(0);
        FLAG_SET(8 + w);
        FLAG_WAIT4(2);
        v2u dn[8];
#pragma unroll
        for (int t = 0; t < 8; ++t) dn[t] = trrd(sm + L_GD + (4 * g4 + tq) * CP + 32 * (8 * w + t) + 8 * tp);
        LAS char* pr = sm + L_GR + l * 16;
        const f4 gx = *(LAS f4*)(pr) + *(LAS f4*)(pr + 1024), ux = *(LAS f4*)(pr + 2048) + *(LAS f4*)(pr + 3072);
        float hh[4];
#pragma unroll
        for (int e = 0; e < 4; ++e) hh[e] = ux[e] * __builtin_amdgcn_rcpf(1.f + __builtin_amdgcn_exp2f(-1.4426950408889634f * gx[e]));
        const v2u hb = {pk(hh[0], hh[1]), pk(hh[2], hh[3])};
        f4 d = z;
#pragma unroll
        for (int t = 0; t < 8; ++t) d = mfma16(dn[t], (col >> 1) == t ? hb : v2u{0u, 0u}, d);
        st_sc1(rs, WS_OPG + ((8 * w + (col >> 1)) * 128 + sl) * 128 + ((col & 1) * 4 + g4) * 16, v4u{pk(d[0], d[1]), T, pk(d[2], d[3]), T});
        STAMP(9);
      }
    } else {
      LAS char* p0 = sm + L_C + (4 * g4 + tq) * CP + 256 * v + 8 * tp;
      unsigned p1 = L_C + (64 + 4 * g4 + tq) * CP + 256 * v + 8 * tp;
      PIN(p1);
#pragma unroll
      for (int ct = 0; ct < 8; ++ct)
#pragma unroll
        for (int u = 0; u < 4; ++u) {
          LAS char* p = (u < 2 ? p0 : sm + p1) + 32 * (u & 1) * CP + 32 * ct;
          fr[4 * ct + u] = trrd2(p, p + 16 * CP);
        }
      unsigned ol = WS_LSE + ((b * 2 + g) * 32 + n) * 128 + col * 8, pp = L_P + l * 16, pm = L_MX + col * 16;
      v4u one = {0x3f803f80u, 0x3f803f80u, 0x3f803f80u, 0x3f803f80u};
      PIN(ol); PIN(pp); PIN(pm); PIN(one); PIN(op);
      STAMP(2);
      QGATHER();
      BAR();
      STAMP(4);
      {
        const int ln = 128 * a + 64 * (v >> 1) + l;
        touch((v & 1) ? wu : wg, (unsigned)((ln >> 2) * (FF * 2)) + (unsigned)min(384 * xcd + 128 * (ln & 3), FF * 2 - 4), smb + L_JUNK);
      }
      BAR();
      const f4 m4 = *(LAS f4*)(sm + pm);
      v4u pf[4];
#pragma unroll
      for (int u = 0; u < 4; ++u) pf[u] = *(LAS v4u*)(sm + pp + u * 1024);
      const float M = fmax2(fmax2(m4[0], m4[1]), fmax2(m4[2], m4[3]));
#pragma unroll
      for (int u = 0; u < 4; ++u) {
        const unsigned kk = min((unsigned)(M - m4[u]), 255u) * 0x00800080u;
#pragma unroll
        for (int e = 0; e < 4; ++e) pf[u][e] = scale_dn(pf[u][e], kk);
      }
      f4 ls = z;
#pragma unroll
      for (int u = 0; u < 4; ++u) ls = mfma32(one, pf[u], ls);
      const float il = __builtin_amdgcn_rcpf(ls[0]);
      if (v == 0 && g4 == 0)
        __builtin_amdgcn_raw_buffer_store_b64(v2u{__builtin_bit_cast(unsigned, M + __builtin_amdgcn_logf(ls[0])), T}, rs, ol, 0, AUX_SC1);
#pragma unroll
      for (int q = 0; q < 2; ++q) {
        f4 c[4];
#pragma unroll
        for (int k = 0; k < 4; ++k) c[k] = z;
#pragma unroll
        for (int u = 0; u < 4; ++u)
#pragma unroll
          for (int k = 0; k < 4; ++k) c[k] = mfma32(fr[16 * q + 4 * k + u], pf[u], c[k]);
        v4u R[2];
#pragma unroll
        for (int k = 0; k < 2; ++k) {
          const f4 c0 = c[2 * k] * il, c1 = c[2 * k + 1] * il;
          const unsigned x0 = min(pk(c0[0], c0[1]), 0xfffffffeu), x1 = min(pk(c0[2], c0[3]), 0xfffffffeu);
          const unsigned y0 = min(pk(c1[0], c1[1]), 0xfffffffeu), y1 = min(pk(c1[2], c1[3]), 0xfffffffeu);
          auto r0 = __builtin_amdgcn_permlane16_swap(x0, y0, false, false);
          auto r1 = __builtin_amdgcn_permlane16_swap(x1, y1, false, false);
          R[k] = v4u{r0[0], r1[0], r0[1], r1[1]};
        }
        v4u A, B;
#pragma unroll
        for (int e = 0; e < 4; ++e) {
          A[e] = (unsigned)__builtin_amdgcn_update_dpp((int)R[0][e], (int)R[1][e], ROR8, 0xf, 0xc, false);
          B[e] = (unsigned)__builtin_amdgcn_update_dpp((int)R[1][e], (int)R[0][e], ROR8, 0xf, 0x3, false);
        }
        st_sc1(rs, op + 128 * q, A);
        st_sc1(rs, op + 128 * q + 8 * 65536, B);
      }
      FLAG_SET(v);
      STAMP(6);
    }
    if (a >= 12) {
      unsigned ot = WS_OPT + rd * 8192 + tid * 16, og = WS_OPG + rd * 16384 + tid * 16;
      const bool two = (tid >> 3) < NSL - 64;
      unsigned og2 = two ? og + 8192 : og;
      const unsigned m = (col & 1) ? 0x3f800000u : 0x3f80u;
      v4u sel = {col < 2 ? m : 0u, (col >> 1) == 1 ? m : 0u, col < 2 ? m : 0u, (col >> 1) == 1 ? m : 0u};
      unsigned m2 = two ? ~0u : 0u;
      unsigned prw = L_RR + w * 128 + (l & 7) * 16, prr = L_RR + (l & 31) * 4, oy = ((l >> 4) & 1) * 1024 + rd * 32 + col * 2;
      PIN(ot); PIN(og); PIN(og2); PIN(sel); PIN(m2); PIN(prw); PIN(prr); PIN(oy);
      v4u p0, p1, p2;
      for (unsigned spin = 0;; ++spin) {
        asm volatile(LD16(0, 3, 6) " sc1\n\t" LD16(1, 4, 6) " sc1\n\t" LD16(2, 5, 6) " sc1" WAIT0
                     : "=&v"(p0), "=&v"(p1), "=&v"(p2)
                     : "v"(ot), "v"(og), "v"(og2), "s"(ws)
                     : "memory");
        if (!__builtin_amdgcn_ballot_w64((T & p0.y & p0.w & p1.y & p1.w & p2.y & p2.w) != (T | p0.y | p0.w | p1.y | p1.w | p2.y | p2.w)) ||
            spin > SPIN_CAP)
          break;
      }
      STAMP(8);
      f4 acc = mfma32(sel, v4u{p0.x, p0.z, p1.x, p1.z}, z);
      acc = mfma32(sel, v4u{p2.x & m2, p2.z & m2, 0u, 0u}, acc);
#pragma unroll
      for (int e = 0; e < 4; ++e) acc[e] += dpp<ROR8>(acc[e]);
      if (l < 8) *(LAS f4*)(sm + prw) = acc;
      BAR();
      if (w == 0 && l < 32) {
        float t[8];
#pragma unroll
        for (int i = 0; i < 8; ++i) t[i] = *(LAS float*)(sm + prr + i * 128);
        const float y = blo(xres) + (((t[0] + t[1]) + (t[2] + t[3])) + ((t[4] + t[5]) + (t[6] + t[7])));
        *(u16*)((char*)out + oy) = (u16)(pk(y, y) & 0xffffu);
        if (rd == 0 && l == 0) __builtin_amdgcn_raw_buffer_store_b32(T, rs, WS_E, 0, AUX_SC1);
      }
      STAMP(9);
    }
  }
  STAMP_FLUSH();
}

#define WS_END 0
#define WS_BASE (WS_END ? ((ws_size - (size_t)WS_END * 1048576) & ~(size_t)4095) : 0)
extern "C" void kernel_launch(void* const* d_in, const int* in_sizes, int n_in, void* d_out, int out_size, void* d_ws, size_t ws_size,
                              hipStream_t stream) {
  mla_glu_decode<<<256, 512, 0, stream>>>((const u16*)d_in[0], (const u16*)d_in[1], (const u16*)d_in[2], (const u16*)d_in[3],
                                          (const u16*)d_in[4], (const u16*)d_in[5], (const u16*)d_in[7], (const u16*)d_in[8],
                                          (const u16*)d_in[9], (u16*)d_out, (char*)d_ws + WS_BASE);
}
```

```cpp
#define CODE_LINES 98
#include <hip/hip_runtime.h>
#include <stdint.h>

#define GAS __attribute__((address_space(1)))
#define LAS __attribute__((address_space(3)))
typedef unsigned short u16;
typedef unsigned v4u __attribute__((ext_vector_type(4)));
typedef unsigned v2u __attribute__((ext_vector_type(2)));
typedef float f4 __attribute__((ext_vector_type(4)));
typedef float f2 __attribute__((ext_vector_type(2)));
typedef __bf16 bf8 __attribute__((ext_vector_type(8)));
typedef __bf16 bf2 __attribute__((ext_vector_type(2)));
typedef short s4 __attribute__((ext_vector_type(4)));

namespace {
constexpr int NH = 32, SK = 4096, DH = 128, DM = 512, DC = 512, FF = 1376;
constexpr int NSL = FF / 16;
constexpr int WS_E = 0;
constexpr int WS_QP = 4096;
constexpr int WS_QA = WS_QP + NH * 4 * 1024;
constexpr int WS_LSE = WS_QA + 2 * NH * 2048;
constexpr int WS_OPT = WS_LSE + 2 * 2 * 32 * 128;
constexpr int WS_OPG = WS_OPT + 32 * 64 * 128;
constexpr int WS_PART = WS_OPG + 32 * 128 * 128;
constexpr int WS_BYTES = WS_PART + NH * 2 * 2 * 32 * 512;
constexpr int WS_STAMP = 7 << 19;
constexpr int CP = 1056;
constexpr int XP = 1088, SP = 288, SP2 = 544;
constexpr int L_C = 0, L_Q = 128 * CP, L_P = L_Q + 16 * CP, L_X = L_P + 4096, L_MX = L_X + 2 * XP, L_RR = L_MX + 256,
              L_JUNK = L_RR + 1024, L_FL = L_JUNK + 256, L_END = L_FL + 128;
static_assert(L_END <= 163840, "LDS");
constexpr int L_GG = 0, L_GD = 32768, L_GR = 65536;
constexpr int L_WUV = 0, L_WO = 65536, L_WQ = 0, L_WUK = 32768, L_CC = 131072, L_CT = L_CC + 8 * SP2, L_W = L_CT + 2 * SP;
#define UK_DELAY 16
#define H1_DELAY 8
#define T_DELAY 20
#define GLU_FLAG_Q 1
#define NOCLEAR 0
#define TLB_WARM 0
#define OPT_W 0
#define QA_WARM 0
#define LSE_DELAY 40
#define A_DELAY 24
#define PF_GLU 1
constexpr unsigned SPIN_CAP = 1u << 20;
constexpr int AUX_SC1 = 16;

#define STAMP_W 0
#define STAMP(i)
#define STAMP_FLUSH()
#define PROBE_T(T_)

__device__ __forceinline__ f4 mfma32(v4u a, v4u b, f4 c) {
  return __builtin_amdgcn_mfma_f32_16x16x32_bf16(__builtin_bit_cast(bf8, a), __builtin_bit_cast(bf8, b), c, 0, 0, 0);
}
__device__ __forceinline__ f4 mfma16(v2u a, v2u b, f4 c) {
  return __builtin_amdgcn_mfma_f32_16x16x16bf16_1k(__builtin_bit_cast(s4, a), __builtin_bit_cast(s4, b), c, 0, 0, 0);
}
__device__ __forceinline__ unsigned pk(float lo, float hi) {
  f2 x = {lo, hi};
  return __builtin_bit_cast(unsigned, __builtin_convertvector(x, bf2));
}
__device__ __forceinline__ float asf(unsigned u) { return __builtin_bit_cast(float, u); }
__device__ __forceinline__ float blo(unsigned u) { return __builtin_bit_cast(float, u << 16); }
__device__ __forceinline__ float bhi(unsigned u) { return __builtin_bit_cast(float, u & 0xffff0000u); }
template <int CTRL> __device__ __forceinline__ float dpp(float v) {
  return __builtin_bit_cast(float, __builtin_amdgcn_update_dpp(0, __builtin_bit_cast(int, v), CTRL, 0xf, 0xf, true));
}
__device__ __forceinline__ float fmax2(float a, float b) {
#pragma float_control(precise, off)
  return __builtin_fmaxf(a, b);
}
typedef unsigned short h2 __attribute__((ext_vector_type(2)));
__device__ __forceinline__ unsigned scale_dn(unsigned p, unsigned kk) {
  return __builtin_bit_cast(unsigned, __builtin_elementwise_sub_sat(__builtin_bit_cast(h2, p), __builtin_bit_cast(h2, kk)));
}
#define PIN(x) asm volatile("" : "+v"(x))
constexpr int ROR1 = 0x121, ROR2 = 0x122, ROR4 = 0x124, ROR8 = 0x128, QP_2301 = 0x4e, QP_2222 = 0xaa;
__device__ __forceinline__ float sw32_add(float a, float b) {
  auto r = __builtin_amdgcn_permlane32_swap(__builtin_bit_cast(unsigned, a), __builtin_bit_cast(unsigned, b), false, false);
  return asf(r[0]) + asf(r[1]);
}
__device__ __forceinline__ float sw16_add(float a, float b) {
  auto r = __builtin_amdgcn_permlane16_swap(__builtin_bit_cast(unsigned, a), __builtin_bit_cast(unsigned, b), false, false);
  return asf(r[0]) + asf(r[1]);
}
__device__ __forceinline__ float vmax(unsigned a, unsigned b) {
  float r;
  asm("v_max_f32 %0, %1, %2" : "=v"(r) : "v"(a), "v"(b));
  return r;
}
__device__ __forceinline__ float sw32_max(float a) {
  auto r = __builtin_amdgcn_permlane32_swap(__builtin_bit_cast(unsigned, a), __builtin_bit_cast(unsigned, a), false, false);
  return vmax(r[0], r[1]);
}
__device__ __forceinline__ float sw16_max(float a) {
  auto r = __builtin_amdgcn_permlane16_swap(__builtin_bit_cast(unsigned, a), __builtin_bit_cast(unsigned, a), false, false);
  return vmax(r[0], r[1]);
}
__device__ __forceinline__ float wave_sum(float v) {
  v += dpp<ROR8>(v);
  v += dpp<ROR4>(v);
  v += dpp<ROR2>(v);
  v += dpp<ROR1>(v);
  v = sw16_add(v, v);
  return sw32_add(v, v);
}
__device__ __forceinline__ v2u trrd(LAS char* p) {
  return __builtin_bit_cast(v2u, __builtin_amdgcn_ds_read_tr16_b64_v4i16((LAS s4*)p));
}
__device__ __forceinline__ v4u trrd2(LAS char* p0, LAS char* p1) {
  v2u a = trrd(p0), b = trrd(p1);
  return v4u{a.x, a.y, b.x, b.y};
}
#define BAR() asm volatile("s_waitcnt lgkmcnt(0)\n\ts_barrier" ::: "memory")
#define VMWAIT(k) asm volatile("s_waitcnt vmcnt(%0)" ::"n"(k) : "memory")
__device__ __forceinline__ void gld16(v4u& d, const void* base, unsigned off) {
  asm volatile("global_load_dwordx4 %0, %1, %2" : "=v"(d) : "v"(off), "s"(base) : "memory");
}
__device__ __forceinline__ void gld4_sc1(unsigned& d, const void* base, unsigned off) {
  asm volatile("global_load_dword %0, %1, %2 sc1" : "=v"(d) : "v"(off), "s"(base) : "memory");
}
__device__ __forceinline__ void touch(const void* base, unsigned off, unsigned lds) {
  asm volatile("s_mov_b32 m0, %2\n\ts_nop 0\n\tglobal_load_lds_dword %0, %1" ::"v"(off), "s"(base), "s"(lds) : "memory");
}
__device__ __forceinline__ void touch_sc1(const void* base, unsigned off, unsigned lds) {
  asm volatile("s_mov_b32 m0, %2\n\ts_nop 0\n\tglobal_load_lds_dword %0, %1 sc1" ::"v"(off), "s"(base), "s"(lds) : "memory");
}
#define LD16(i, a, o) "global_load_dwordx4 %" #i ", %" #a ", %" #o
#define SWEEP_4(q, o, o0, o1, o2, o3, W)                                                                                     \
  asm volatile(LD16(0, 4, 5) " offset:" #o0 " sc1\n\t" LD16(1, 4, 5) " offset:" #o1 " sc1\n\t" LD16(2, 4, 5) " offset:" #o2         \
               " sc1\n\t" LD16(3, 4, 5) " offset:" #o3 " sc1" W                                                               \
               : "=&v"((q)[0]), "=&v"((q)[1]), "=&v"((q)[2]), "=&v"((q)[3])                                                  \
               : "v"(o), "s"(ws)                                                                                             \
               : "memory")
#define SWEEP_L(W)                                                                                                          \
  asm volatile("global_load_dwordx2 %0, %3, %5 sc1\n\tglobal_load_dwordx2 %1, %4, %5 sc1\n\t"                              \
               "global_load_dwordx2 %2, %4, %5 offset:512 sc1" W                                                            \
               : "=&v"(la), "=&v"(l0), "=&v"(l1)                                                                            \
               : "v"(ola), "v"(olo), "s"(ws)                                                                                \
               : "memory")
#define SWEEP_2(p, q, oa, ob, W) \
  asm volatile(LD16(0, 2, 4) " sc1\n\t" LD16(1, 3, 4) " sc1" W : "=&v"(p), "=&v"(q) : "v"(oa), "v"(ob), "s"(ws) : "memory")
#define TAGS_BAD4(q) \
  __builtin_amdgcn_ballot_w64((T & (q)[0].y & (q)[0].w & (q)[1].y & (q)[1].w & (q)[2].y & (q)[2].w & (q)[3].y & (q)[3].w) != \
                              (T | (q)[0].y | (q)[0].w | (q)[1].y | (q)[1].w | (q)[2].y | (q)[2].w | (q)[3].y | (q)[3].w))
#define FLAG_SET(i) asm volatile("ds_write_b32 %0, %1" ::"v"(smb + L_FL + 4 * (i)), "v"(1u) : "memory")
#define FLAG_WAIT4(i)                                                                                     \
  for (unsigned spin_ = 0; spin_ < SPIN_CAP; ++spin_) {                                                   \
    v4u f_;                                                                                               \
    asm volatile("ds_read_b128 %0, %1\n\ts_waitcnt lgkmcnt(0)" : "=v"(f_) : "v"(smb + L_FL + 16 * (i)) : "memory"); \
    if (__builtin_amdgcn_readfirstlane(f_.x & f_.y & f_.z & f_.w)) break;                                 \
  }
#define WAIT0 "\n\ts_waitcnt vmcnt(0)"
__device__ __forceinline__ void dma16(const void* base, unsigned off, unsigned lds) {
  asm volatile("s_mov_b32 m0, %2\n\ts_nop 0\n\tglobal_load_lds_dwordx4 %0, %1" ::"v"(off), "s"(base), "s"(lds) : "memory");
}
__device__ __forceinline__ v4u ld_sc1(__amdgpu_buffer_rsrc_t rs, int off) {
  return __builtin_amdgcn_raw_buffer_load_b128(rs, off, 0, AUX_SC1);
}
__device__ __forceinline__ v2u ld8_sc1(__amdgpu_buffer_rsrc_t rs, int off) {
  return __builtin_amdgcn_raw_buffer_load_b64(rs, off, 0, AUX_SC1);
}
__device__ __forceinline__ void st_sc1(__amdgpu_buffer_rsrc_t rs, int off, v4u v) {
  __builtin_amdgcn_raw_buffer_store_b128(v, rs, off, 0, AUX_SC1);
}
__device__ __forceinline__ void ln_stats(v4u r, float& mean, float& rstd) {
  float e[8] = {blo(r.x), bhi(r.x), blo(r.y), bhi(r.y), blo(r.z), bhi(r.z), blo(r.w), bhi(r.w)};
  float s = ((e[0] + e[1]) + (e[2] + e[3])) + ((e[4] + e[5]) + (e[6] + e[7]));
  mean = wave_sum(s) * (1.f / DM);
  float q = 0.f;
#pragma unroll
  for (int i = 0; i < 8; ++i) {
    float d = e[i] - mean;
    q = fmaf(d, d, q);
  }
  rstd = __builtin_amdgcn_rsqf(wave_sum(q) * (1.f / DM) + 1e-5f);
}
}

__global__ void __launch_bounds__(512, 2)
mla_glu_decode(const u16* __restrict__ x, const u16* __restrict__ ckv, const u16* __restrict__ wq, const u16* __restrict__ wuk,
               const u16* __restrict__ wuv, const u16* __restrict__ wo, const u16* __restrict__ wg, const u16* __restrict__ wu,
               const u16* __restrict__ wd, u16* __restrict__ out, char* __restrict__ ws) {
  __shared__ __attribute__((aligned(16))) char smem_[L_END];
  LAS char* sm = (LAS char*)smem_;
  const unsigned smb = (unsigned)(size_t)sm;
  const int tid = threadIdx.x, l = tid & 63, w = __builtin_amdgcn_readfirstlane(tid >> 6);
  const int col = l & 15, g4 = l >> 4, tq = (l & 15) >> 2, tp = l & 3;
  const int xcd = blockIdx.x & 7, r = blockIdx.x >> 3;
  const __amdgpu_buffer_rsrc_t rs = __builtin_amdgcn_make_buffer_rsrc(ws, 0, WS_BYTES, 0x00020000);
  asm volatile("" ::"s"(x), "s"(ckv), "s"(wq), "s"(wuk), "s"(wuv), "s"(wo), "s"(wg), "s"(wu), "s"(wd), "s"(out), "s"(ws));
  STAMP(0);

  const char* pc0;
  asm volatile("s_getpc_b64 %0" : "=s"(pc0));
  unsigned ep, T;
  LAS char* const px = sm + L_X + (col & 1) * XP + 16 * g4;
  if ((w >> 1) == 2) dma16(x, (unsigned)((w & 1) * 1024 + l * 16), smb + L_X + (w & 1) * XP);
  const f4 z = {0.f, 0.f, 0.f, 0.f};
#define LAYERNORM()                                                                                                              \
  {                                                                                                                              \
    const v4u xr = *(LAS v4u*)(sm + L_X + (w & 1) * XP + l * 16);                                                                \
    float e[8] = {blo(xr.x), bhi(xr.x), blo(xr.y), bhi(xr.y), blo(xr.z), bhi(xr.z), blo(xr.w), bhi(xr.w)};                       \
    const float mean = wave_sum(((e[0] + e[1]) + (e[2] + e[3])) + ((e[4] + e[5]) + (e[6] + e[7]))) * (1.f / DM);                 \
    float q = 0.f;                                                                                                               \
    _Pragma("unroll") for (int i = 0; i < 8; ++i) {                                                                              \
      e[i] -= mean;                                                                                                              \
      q = fmaf(e[i], e[i], q);                                                                                                   \
    }                                                                                                                            \
    const float rstd = __builtin_amdgcn_rsqf(wave_sum(q) * (1.f / DM) + 1e-5f);                                                  \
    *(LAS v4u*)(sm + L_X + (w & 1) * XP + l * 16) =                                                                              \
        v4u{pk(e[0] * rstd, e[1] * rstd), pk(e[2] * rstd, e[3] * rstd), pk(e[4] * rstd, e[5] * rstd), pk(e[6] * rstd, e[7] * rstd)}; \
  }

  if (__builtin_expect(r < 16, 1)) {
    const int h = 4 * xcd + (r >> 2), j = r & 3, ca = j >> 1, mb = j & 1;
    const int fl = tq | ((g4 & 1) << 2);
    gld4_sc1(ep, ws, 0u);
    if (__builtin_expect(w < 4, 1)) {
      const unsigned vq = (unsigned)(((128 * j + g4) * 4096 + h * DH + 8 * (col ^ ((g4 | ((w & 2) << 1)) << 1))) * 2);
#pragma unroll 1
      for (int k = w; k < 32; k += 4) dma16(wq, vq + (unsigned)k * 32768u, smb + L_WQ + k * 1024);
    } else if (w < 6) {
      VMWAIT(1);
      LAYERNORM();
    } else {
      __builtin_amdgcn_s_sleep(UK_DELAY);
      const unsigned vu = (unsigned)((((128 * j + g4) * NH + h) * DH) * 2);
#pragma unroll 1
      for (int k = w - 6; k < 32; k += 2) dma16(wuk, vu + (unsigned)k * 32768u + 16u * (col ^ ((4 * k + g4) & 15)), smb + L_WUK + k * 1024);
    }
    unsigned p = L_WQ + 256 * (8 * g4 + tq) + 16 * ((2 * w + (tp >> 1)) ^ (fl << 1)) + 8 * (tp & 1);
    unsigned oqp = WS_QP + ((h * 4 + j) * 8 + w) * 128 + col * 64 + g4 * 16;
    unsigned pxj = L_X + (col & 1) * XP + 16 * g4 + 256 * j;
    unsigned o = WS_QP + ((h * 4 + ((col >> 1) & 3)) * 8 + (g4 >> 1)) * 128 + (col & 1) * 64 + (g4 & 1) * 32;
    unsigned oqa = WS_QA + ((col & 1) * NH + h) * 2048 + (64 * j + 16 * w + 8 * (col >> 1) + 2 * g4) * 8;
    unsigned pu = L_WUK + 256 * (32 * w + col) + 16 * (g4 ^ col);
    PIN(p); PIN(oqp); PIN(pxj); PIN(o); PIN(oqa); PIN(pu);
    if (w < 6) VMWAIT(0);
    else VMWAIT(16);
    PIN(ep);
    T = ep + 1u;
    PIN(T);
    STAMP(1);
    BAR();
    STAMP(2);
    {
      v4u af[4], xf[4];
#pragma unroll
      for (int s = 0; s < 4; ++s) {
        af[s] = trrd2(sm + p + 256 * 32 * s, sm + p + 256 * (32 * s + 4));
        xf[s] = *(LAS v4u*)(sm + pxj + 64 * s);
      }
      __builtin_amdgcn_sched_barrier(0);
      f4 acc = z;
#pragma unroll
      for (int s = 0; s < 4; ++s) acc = mfma32(af[s], xf[s], acc);
      if (col < 2) st_sc1(rs, oqp, v4u{pk(acc[0], acc[1]), T, pk(acc[2], acc[3]), T});
    }
    STAMP(3);
    if (w >= 6) VMWAIT(1);
    if (w >= 4) BAR();
    if (w < 4) {
      v4u a[8], uk[8];
      __builtin_amdgcn_s_sleep(H1_DELAY);
      SWEEP_4(a, o, 0, 16, 256, 272, "");
      SWEEP_4(a + 4, o, 512, 528, 768, 784, "");
      BAR();
#pragma unroll
      for (int t = 0; t < 2; ++t)
#pragma unroll
        for (int s = 0; s < 4; ++s) uk[4 * t + s] = *(LAS v4u*)(sm + (pu ^ (64 * s)) + 4096 * t);
      asm volatile("s_waitcnt vmcnt(%8)"
                   : "+v"(a[0]), "+v"(a[1]), "+v"(a[2]), "+v"(a[3]), "+v"(a[4]), "+v"(a[5]), "+v"(a[6]), "+v"(a[7])
                   : "n"(OPT_W)
                   : "memory");
      for (unsigned spin = 0;; ++spin) {
        if (!(TAGS_BAD4(a) | TAGS_BAD4(a + 4)) || spin > SPIN_CAP) break;
        SWEEP_4(a, o, 0, 16, 256, 272, "");
        SWEEP_4(a + 4, o, 512, 528, 768, 784, WAIT0);
        asm volatile("" : "+v"(a[0]), "+v"(a[1]), "+v"(a[2]), "+v"(a[3]));
      }
      STAMP(4);
      f4 a0 = z, a1 = z;
#pragma unroll
      for (int s = 0; s < 4; ++s) {
        const v4u qf = {a[2 * s].x, a[2 * s].z, a[2 * s + 1].x, a[2 * s + 1].z};
        a0 = mfma32(uk[s], qf, a0);
        a1 = mfma32(uk[4 + s], qf, a1);
      }
      float q[4];
#pragma unroll
      for (int e = 0; e < 4; ++e) {
        const float mine = (col & 2) ? a1[e] : a0[e], other = (col & 2) ? a0[e] : a1[e];
        q[e] = mine + dpp<QP_2301>(other);
        q[e] += dpp<ROR4>(q[e]);
      }
      if (col < 4) st_sc1(rs, oqa, v4u{pk(q[0], q[1]), T, pk(q[2], q[3]), T});
    }
    STAMP(5);
    PROBE_T(T);
    BAR();
    __builtin_amdgcn_s_sleep(T_DELAY);
    {
      const unsigned vv = (unsigned)((((256 * ca + g4) * NH + h) * DH + 8 * (col ^ ((g4 | ((w & 2) << 1)) << 1))) * 2);
      const unsigned vo = (unsigned)(((h * DH + (l >> 5)) * DM + 256 * mb) * 2) + 16u * ((l & 31) ^ (((l >> 5) | ((w & 1) << 1) | (w & 4)) << 1));
      int kv = w + 32 * mb, ko = w + 32 * ca;
#pragma unroll 1
      for (int hf = 0; hf < 2; ++hf) {
#pragma unroll 1
        for (int i = 0; i < 4; ++i, kv = (kv + 8) & 63) dma16(wuv, vv + (unsigned)kv * 32768u, smb + L_WUV + kv * 1024);
#pragma unroll 1
        for (int i = 0; i < 4; ++i, ko = (ko + 8) & 63) dma16(wo, vo + (unsigned)ko * 2048u, smb + L_WO + ko * 1024);
        if (hf == 0) asm volatile("s_barrier" ::: "memory");
      }
    }
    const int tb = w >> 2, nq = w & 3;
    o = WS_PART + (((h * 2 + ca) * 2 + tb) * 32 + 8 * nq + g4) * 512 + 16 * col;
    unsigned pw = L_W + (tb * 32 + 8 * nq + g4) * 4;
    unsigned pv = L_WUV + 256 * (8 * g4 + tq) + 16 * ((2 * w + (tp >> 1)) ^ (fl << 1)) + 8 * (tp & 1);
    unsigned po = L_WO + 512 * (8 * g4 + tq) + 16 * ((4 * w + (tp >> 1)) ^ (fl << 1)) + 8 * (tp & 1);
    unsigned pcw = L_CC + (nq * 2 + tb) * SP2 + 16 * col + 8 * g4, pcr = L_CC + (col & 7) * SP2 + 16 * g4;
    unsigned ptw = L_CT + col * SP + (16 * w + 4 * g4) * 2, ptr = L_CT + (col & 1) * SP + 16 * g4;
    unsigned oo = WS_OPT + ((16 * mb + 2 * w + (col >> 1)) * 64 + 2 * h + ca) * 128 + ((col & 1) * 4 + g4) * 16;
    v4u mk;
#pragma unroll
    for (int d = 0; d < 4; ++d) mk[d] = (col >> 1) == d ? ((col & 1) ? 0xffff0000u : 0xffffu) : 0u;
    PIN(o); PIN(pw); PIN(pv); PIN(po); PIN(pcw); PIN(pcr); PIN(ptw); PIN(ptr); PIN(oo); PIN(mk);
    if (w == 7) {
      const unsigned ol = WS_LSE + (((l >> 5) * 2 + (h >> 4)) * 32 + (l & 31)) * 128 + (h & 15) * 8;
      v2u la;
      for (unsigned spin = 0;; ++spin) {
        asm volatile("global_load_dwordx2 %0, %1, %2 sc1" WAIT0 : "=&v"(la) : "v"(ol), "s"(ws) : "memory");
        if (!__builtin_amdgcn_ballot_w64(la.y != T) || spin > SPIN_CAP) break;
      }
      float M = asf(la.x);
      M = fmax2(M, dpp<ROR8>(M));
      M = fmax2(M, dpp<ROR4>(M));
      M = fmax2(M, dpp<ROR2>(M));
      M = fmax2(M, dpp<ROR1>(M));
      M = sw16_max(M);
      const float e = __builtin_amdgcn_exp2f(asf(la.x) - M);
      float L = e + dpp<ROR8>(e);
      L += dpp<ROR4>(L);
      L += dpp<ROR2>(L);
      L += dpp<ROR1>(L);
      L = sw16_add(L, L);
      const float wt = e * __builtin_amdgcn_rcpf(L);
      *(LAS unsigned*)(sm + L_W + 4 * l) = pk(wt, wt);
      STAMP(7);
    }
    VMWAIT(0);
    STAMP(6);
    BAR();
    v4u a[4];
    SWEEP_4(a, o, 0, 256, 2048, 2304, "");
    v4u A0 = mk & *(LAS unsigned*)(sm + pw), A1 = mk & *(LAS unsigned*)(sm + pw + 16);
    v4u av[8], ao[8];
#pragma unroll
    for (int s = 0; s < 8; ++s) av[s] = trrd2(sm + pv + 256 * 32 * s, sm + pv + 256 * (32 * s + 4));
#pragma unroll
    for (int t = 0; t < 2; ++t)
#pragma unroll
      for (int s = 0; s < 4; ++s) ao[4 * t + s] = trrd2(sm + (po ^ (32 * t)) + 512 * 32 * s, sm + (po ^ (32 * t)) + 512 * (32 * s + 4));
    asm volatile("s_waitcnt vmcnt(0)" : "+v"(a[0]), "+v"(a[1]), "+v"(a[2]), "+v"(a[3]), "+v"(A0), "+v"(A1)::"memory");
    for (unsigned spin = 0;; ++spin) {
      unsigned mx = 0u;
#pragma unroll
      for (int i = 0; i < 4; ++i) mx = max(max(mx, max(a[i].x, a[i].y)), max(a[i].z, a[i].w));
      if (!__builtin_amdgcn_ballot_w64(mx == ~0u) || spin > SPIN_CAP) break;
      SWEEP_4(a, o, 0, 256, 2048, 2304, WAIT0);
    }
    STAMP(8);
    {
      f4 c0 = mfma32(A0, a[0], z), c1 = mfma32(A0, a[1], z);
      c0 = mfma32(A1, a[2], c0);
      c1 = mfma32(A1, a[3], c1);
      if (g4 < 2) {
        *(LAS v2u*)(sm + pcw) = v2u{pk(c0[0], c0[1]), pk(c0[2], c0[3])};
        *(LAS v2u*)(sm + pcw + 256) = v2u{pk(c1[0], c1[1]), pk(c1[2], c1[3])};
      }
    }
    BAR();
    {
      f4 acc = z;
      v4u bf[8];
#pragma unroll
      for (int s = 0; s < 8; ++s) bf[s] = *(LAS v4u*)(sm + pcr + 64 * s);
      __builtin_amdgcn_sched_barrier(0);
#pragma unroll
      for (int s = 0; s < 8; ++s) acc = mfma32(av[s], bf[s], acc);
#pragma unroll
      for (int e = 0; e < 4; ++e) {
        acc[e] += dpp<QP_2301>(acc[e]);
        acc[e] += dpp<ROR4>(acc[e]);
      }
      if (col < 2) *(LAS v2u*)(sm + ptw) = v2u{pk(acc[0], acc[1]), pk(acc[2], acc[3])};
    }
    BAR();
    {
      f4 c0 = z, c1 = z;
      v4u bf[4];
#pragma unroll
      for (int s = 0; s < 4; ++s) bf[s] = *(LAS v4u*)(sm + ptr + 64 * s);
      __builtin_amdgcn_sched_barrier(0);
#pragma unroll
      for (int s = 0; s < 4; ++s) {
        c0 = mfma32(ao[s], bf[s], c0);
        c1 = mfma32(ao[4 + s], bf[s], c1);
      }
      if (col & 2) c0 = c1;
      if (col < 4) st_sc1(rs, oo, v4u{pk(c0[0], c0[1]), T, pk(c0[2], c0[3]), T});
    }
    STAMP(9);
  } else {
    const int a = r - 16, b = xcd >> 2, n = 8 * (xcd & 3) + (a >> 1), g = a & 1;
    const int sl = 12 * xcd + a, rd = (4 * xcd + a) & 31;
    const bool glu = a < 12 && sl < NSL;
    const int v = w & 3;
    unsigned op = WS_PART + ((((16 * g + (col & 7)) * 2 + (v >> 1)) * 2 + b) * 32 + n) * 512 + 256 * (v & 1) + (col >> 3) * 64 +
                  (g4 & 1) * 32 + (g4 >> 1) * 16;
    unsigned xres, sdummy = 0u;
    gld4_sc1(ep, ws, 0u);
    asm volatile("global_load_ushort %0, %1, %2" : "=v"(xres) : "v"((unsigned)(((l >> 4) & 1) * 1024 + rd * 32 + col * 2)), "s"(x) : "memory");
    {
      const unsigned vc = (unsigned)(((b * SK + 128 * n) * DC + 8 * l) * 2);
      int row = w + 64 * g;
#define STOUCH(base, off) asm volatile("s_load_dword %0, %1, %2" : "+s"(sdummy) : "s"(base), "s"((unsigned)(off)) : "memory")
      if (w < 4) {
        const int i = 4 * a + w;
        STOUCH(pc0, i * 128);
        STOUCH(pc0, min(64 + i, CODE_LINES - 1) * 128);
      }
      if (w >= 4) {
#pragma unroll
        for (int q = 0; q < 4; ++q)
          if (!NOCLEAR) st_sc1(rs, op + 128 * (q >> 1) + (q & 1) * 8 * 65536, v4u{~0u, ~0u, ~0u, ~0u});
        if (w == 7 && l < 32) *(LAS unsigned*)(sm + L_FL + 4 * l) = 0u;
      }
      __builtin_amdgcn_s_sleep(A_DELAY);
#pragma unroll 1
      for (int q = 0; q < 4; ++q) {
        row = w + ((64 * g + (OPT_W ? 64 * (q & 1) + 32 * (q >> 1) : 32 * q)) & 127);
#pragma unroll 1
        for (int i = 0; i < 4; ++i, row += 8) dma16(ckv, vc + row * 1024u, smb + L_C + row * CP);
        if (q < 3) asm volatile("s_barrier" ::: "memory");
      }
      if ((w >> 1) == 2) {
        VMWAIT(22);
        LAYERNORM();
      }
    }
    unsigned o = WS_QA + (b * NH + 16 * g) * 2048 + w * 4096 + l * 16, pqs = smb + L_Q + 2 * w * CP + l * 8;
    PIN(o); PIN(pqs);
    asm volatile("s_waitcnt vmcnt(0) lgkmcnt(0)" : "+v"(ep), "+v"(xres), "+s"(sdummy)::"memory");
    T = ep + 1u;
    PROBE_T(T);
    BAR();
    STAMP(1);
    v4u fr[32], qg[4];
    SWEEP_4(qg, o, 0, 1024, 2048, 3072, "");
#define QGATHER()                                                                                                           \
  asm volatile("s_waitcnt vmcnt(0)" : "+v"(qg[0]), "+v"(qg[1]), "+v"(qg[2]), "+v"(qg[3])::"memory");                        \
  for (unsigned spin = 0;; ++spin) {                                                                                        \
    if (!TAGS_BAD4(qg) || spin > SPIN_CAP) break;                                                                           \
    SWEEP_4(qg, o, 0, 1024, 2048, 3072, WAIT0);                                                                             \
  }                                                                                                                         \
  STAMP(3);                                                                                                                 \
  asm volatile("ds_write2_b32 %0, %2, %3 offset0:0 offset1:1\n\tds_write2_b32 %0, %4, %5 offset0:128 offset1:129\n\t"        \
               "ds_write2_b32 %1, %6, %7 offset0:0 offset1:1\n\tds_write2_b32 %1, %8, %9 offset0:128 offset1:129" ::"v"(pqs), \
               "v"(pqs + CP), "v"(qg[0].x), "v"(qg[0].z), "v"(qg[1].x), "v"(qg[1].z), "v"(qg[2].x), "v"(qg[2].z), "v"(qg[3].x), \
               "v"(qg[3].z)                                                                                                 \
               : "memory")
    if (__builtin_expect(w < 4, 1)) {
      LAS char* pc = sm + L_C + (32 * w + col) * CP + 16 * g4;
#pragma unroll
      for (int kt = 0; kt < 2; ++kt)
#pragma unroll
        for (int s = 0; s < 16; ++s) fr[16 * kt + s] = *(LAS v4u*)(pc + 16 * kt * CP + 64 * s);
      unsigned p = L_Q + col * CP + 16 * g4, pp = L_P + w * 1024 + l * 16, pm = L_MX + col * 16 + w * 4;
      PIN(p); PIN(pp); PIN(pm);
      STAMP(2);
      QGATHER();
      BAR();
      STAMP(4);
      f4 s0 = z, s1 = z;
      v4u qf[16];
#pragma unroll
      for (int s = 0; s < 6; ++s) qf[s] = *(LAS v4u*)(sm + p + 64 * s);
      __builtin_amdgcn_sched_barrier(0);
#pragma unroll
      for (int s = 0; s < 16; ++s) {
        if (s + 6 < 16) qf[s + 6] = *(LAS v4u*)(sm + p + 64 * (s + 6));
        s0 = mfma32(fr[s], qf[s], s0);
        s1 = mfma32(fr[16 + s], qf[s], s1);
        __builtin_amdgcn_sched_barrier(0);
      }
      float mx = fmax2(fmax2(fmax2(s0[0], s0[1]), fmax2(s0[2], s0[3])), fmax2(fmax2(s1[0], s1[1]), fmax2(s1[2], s1[3])));
      mx = sw32_max(sw16_max(mx));
      constexpr float SC = 0.08838834764831845f * 1.4426950408889634f;
      const float mi = __builtin_ceilf(mx * SC);
#pragma unroll
      for (int e = 0; e < 4; ++e) {
        s0[e] = __builtin_amdgcn_exp2f(fmaf(s0[e], SC, -mi));
        s1[e] = __builtin_amdgcn_exp2f(fmaf(s1[e], SC, -mi));
      }
      *(LAS float*)(sm + pm) = mi;
      *(LAS v4u*)(sm + pp) = v4u{pk(s0[0], s0[1]), pk(s0[2], s0[3]), pk(s1[0], s1[1]), pk(s1[2], s1[3])};
      BAR();
      STAMP(5);
      if (glu) {
        v4u af[8], xf[8];
#pragma unroll
        for (int s = 0; s < 8; ++s) xf[s] = *(LAS v4u*)(px + 512 * (w >> 1) + 64 * s);
        FLAG_WAIT4(0);
        unsigned og = (unsigned)(((128 * w + (l >> 1)) * FF + 16 * sl + 8 * (l & 1)) * 2);
#pragma unroll 1
        for (int i = 0; i < 4; ++i, og += 32 * FF * 2) {
          dma16(wg, og, smb + L_GG + (4 * w + i) * 1024);
          dma16(wu, og, smb + L_GG + 16384 + (4 * w + i) * 1024);
        }
#pragma unroll 1
        for (int i = 0; i < 4; ++i) dma16(wd, (unsigned)(((16 * sl + 4 * w + i) * DM + 8 * l) * 2), smb + L_GD + (4 * w + i) * CP);
        VMWAIT(4);
        FLAG_SET(4 + w);
        FLAG_WAIT4(1);
        STAMP(7);
        LAS char* pg = sm + L_GG + (w & 1) * 16384 + 32 * (256 * (w >> 1) + 8 * g4 + tq) + 8 * tp;
        f4 acc = z;
#pragma unroll
        for (int s = 0; s < 8; ++s) af[s] = trrd2(pg + 32 * 32 * s, pg + 32 * (32 * s + 4));
        __builtin_amdgcn_sched_barrier(0);
#pragma unroll
        for (int s = 0; s < 8; ++s) acc = mfma32(af[s], xf[s], acc);
        *(LAS f4*)(sm + L_GR + ((w & 1) * 2 + (w >> 1)) * 1024 + l * 16) = acc;
        VMWAIT(0);
        FLAG_SET(8 + w);
        FLAG_WAIT4(2);
        v2u dn[8];
#pragma unroll
        for (int t = 0; t < 8; ++t) dn[t] = trrd(sm + L_GD + (4 * g4 + tq) * CP + 32 * (8 * w + t) + 8 * tp);
        LAS char* pr = sm + L_GR + l * 16;
        const f4 gx = *(LAS f4*)(pr) + *(LAS f4*)(pr + 1024), ux = *(LAS f4*)(pr + 2048) + *(LAS f4*)(pr + 3072);
        float hh[4];
#pragma unroll
        for (int e = 0; e < 4; ++e) hh[e] = ux[e] * __builtin_amdgcn_rcpf(1.f + __builtin_amdgcn_exp2f(-1.4426950408889634f * gx[e]));
        const v2u hb = {pk(hh[0], hh[1]), pk(hh[2], hh[3])};
        f4 d = z;
#pragma unroll
        for (int t = 0; t < 8; ++t) d = mfma16(dn[t], (col >> 1) == t ? hb : v2u{0u, 0u}, d);
        st_sc1(rs, WS_OPG + ((8 * w + (col >> 1)) * 128 + sl) * 128 + ((col & 1) * 4 + g4) * 16, v4u{pk(d[0], d[1]), T, pk(d[2], d[3]), T});
        STAMP(9);
      }
    } else {
      LAS char* p0 = sm + L_C + (4 * g4 + tq) * CP + 256 * v + 8 * tp;
      unsigned p1 = L_C + (64 + 4 * g4 + tq) * CP + 256 * v + 8 * tp;
      PIN(p1);
#pragma unroll
      for (int ct = 0; ct < 8; ++ct)
#pragma unroll
        for (int u = 0; u < 4; ++u) {
          LAS char* p = (u < 2 ? p0 : sm + p1) + 32 * (u & 1) * CP + 32 * ct;
          fr[4 * ct + u] = trrd2(p, p + 16 * CP);
        }
      unsigned ol = WS_LSE + ((b * 2 + g) * 32 + n) * 128 + col * 8, pp = L_P + l * 16, pm = L_MX + col * 16;
      v4u one = {0x3f803f80u, 0x3f803f80u, 0x3f803f80u, 0x3f803f80u};
      PIN(ol); PIN(pp); PIN(pm); PIN(one); PIN(op);
      STAMP(2);
      QGATHER();
      BAR();
      STAMP(4);
      {
        const int ln = 128 * a + 64 * (v >> 1) + l;
        touch((v & 1) ? wu : wg, (unsigned)((ln >> 2) * (FF * 2)) + (unsigned)min(384 * xcd + 128 * (ln & 3), FF * 2 - 4), smb + L_JUNK);
      }
      BAR();
      const f4 m4 = *(LAS f4*)(sm + pm);
      v4u pf[4];
#pragma unroll
      for (int u = 0; u < 4; ++u) pf[u] = *(LAS v4u*)(sm + pp + u * 1024);
      const float M = fmax2(fmax2(m4[0], m4[1]), fmax2(m4[2], m4[3]));
#pragma unroll
      for (int u = 0; u < 4; ++u) {
        const unsigned kk = min((unsigned)(M - m4[u]), 255u) * 0x00800080u;
#pragma unroll
        for (int e = 0; e < 4; ++e) pf[u][e] = scale_dn(pf[u][e], kk);
      }
      f4 ls = z;
#pragma unroll
      for (int u = 0; u < 4; ++u) ls = mfma32(one, pf[u], ls);
      const float il = __builtin_amdgcn_rcpf(ls[0]);
      if (v == 0 && g4 == 0)
        __builtin_amdgcn_raw_buffer_store_b64(v2u{__builtin_bit_cast(unsigned, M + __builtin_amdgcn_logf(ls[0])), T}, rs, ol, 0, AUX_SC1);
#pragma unroll
      for (int q = 0; q < 2; ++q) {
        f4 c[4];
#pragma unroll
        for (int k = 0; k < 4; ++k) c[k] = z;
#pragma unroll
        for (int u = 0; u < 4; ++u)
#pragma unroll
          for (int k = 0; k < 4; ++k) c[k] = mfma32(fr[16 * q + 4 * k + u], pf[u], c[k]);
        v4u R[2];
#pragma unroll
        for (int k = 0; k < 2; ++k) {
          const f4 c0 = c[2 * k] * il, c1 = c[2 * k + 1] * il;
          const unsigned x0 = min(pk(c0[0], c0[1]), 0xfffffffeu), x1 = min(pk(c0[2], c0[3]), 0xfffffffeu);
          const unsigned y0 = min(pk(c1[0], c1[1]), 0xfffffffeu), y1 = min(pk(c1[2], c1[3]), 0xfffffffeu);
          auto r0 = __builtin_amdgcn_permlane16_swap(x0, y0, false, false);
          auto r1 = __builtin_amdgcn_permlane16_swap(x1, y1, false, false);
          R[k] = v4u{r0[0], r1[0], r0[1], r1[1]};
        }
        v4u A, B;
#pragma unroll
        for (int e = 0; e < 4; ++e) {
          A[e] = (unsigned)__builtin_amdgcn_update_dpp((int)R[0][e], (int)R[1][e], ROR8, 0xf, 0xc, false);
          B[e] = (unsigned)__builtin_amdgcn_update_dpp((int)R[1][e], (int)R[0][e], ROR8, 0xf, 0x3, false);
        }
        st_sc1(rs, op + 128 * q, A);
        st_sc1(rs, op + 128 * q + 8 * 65536, B);
      }
      FLAG_SET(v);
      STAMP(6);
    }
    if (a >= 12) {
      unsigned ot = WS_OPT + rd * 8192 + tid * 16, og = WS_OPG + rd * 16384 + tid * 16;
      const bool two = (tid >> 3) < NSL - 64;
      unsigned og2 = two ? og + 8192 : og;
      const unsigned m = (col & 1) ? 0x3f800000u : 0x3f80u;
      v4u sel = {col < 2 ? m : 0u, (col >> 1) == 1 ? m : 0u, col < 2 ? m : 0u, (col >> 1) == 1 ? m : 0u};
      unsigned m2 = two ? ~0u : 0u;
      unsigned prw = L_RR + w * 128 + (l & 7) * 16, prr = L_RR + (l & 31) * 4, oy = ((l >> 4) & 1) * 1024 + rd * 32 + col * 2;
      PIN(ot); PIN(og); PIN(og2); PIN(sel); PIN(m2); PIN(prw); PIN(prr); PIN(oy);
      v4u p0, p1, p2;
      for (unsigned spin = 0;; ++spin) {
        asm volatile(LD16(0, 3, 6) " sc1\n\t" LD16(1, 4, 6) " sc1\n\t" LD16(2, 5, 6) " sc1" WAIT0
                     : "=&v"(p0), "=&v"(p1), "=&v"(p2)
                     : "v"(ot), "v"(og), "v"(og2), "s"(ws)
                     : "memory");
        if (!__builtin_amdgcn_ballot_w64((T & p0.y & p0.w & p1.y & p1.w & p2.y & p2.w) != (T | p0.y | p0.w | p1.y | p1.w | p2.y | p2.w)) ||
            spin > SPIN_CAP)
          break;
      }
      STAMP(8);
      f4 acc = mfma32(sel, v4u{p0.x, p0.z, p1.x, p1.z}, z);
      acc = mfma32(sel, v4u{p2.x & m2, p2.z & m2, 0u, 0u}, acc);
#pragma unroll
      for (int e = 0; e < 4; ++e) acc[e] += dpp<ROR8>(acc[e]);
      if (l < 8) *(LAS f4*)(sm + prw) = acc;
      BAR();
      if (w == 0 && l < 32) {
        float t[8];
#pragma unroll
        for (int i = 0; i < 8; ++i) t[i] = *(LAS float*)(sm + prr + i * 128);
        const float y = blo(xres) + (((t[0] + t[1]) + (t[2] + t[3])) + ((t[4] + t[5]) + (t[6] + t[7])));
        *(u16*)((char*)out + oy) = (u16)(pk(y, y) & 0xffffu);
        if (rd == 0 && l == 0) __builtin_amdgcn_raw_buffer_store_b32(T, rs, WS_E, 0, AUX_SC1);
      }
      STAMP(9);
    }
  }
  STAMP_FLUSH();
}

#define WS_END 0
#define WS_BASE (WS_END ? ((ws_size - (size_t)WS_END * 1048576) & ~(size_t)4095) : 0)
extern "C" void kernel_launch(void* const* d_in, const int* in_sizes, int n_in, void* d_out, int out_size, void* d_ws, size_t ws_size,
                              hipStream_t stream) {
  mla_glu_decode<<<256, 512, 0, stream>>>((const u16*)d_in[0], (const u16*)d_in[1], (const u16*)d_in[2], (const u16*)d_in[3],
                                          (const u16*)d_in[4], (const u16*)d_in[5], (const u16*)d_in[7], (const u16*)d_in[8],
                                          (const u16*)d_in[9], (u16*)d_out, (char*)d_ws + WS_BASE);
}
```

```cpp
#define PROBE_CLASS 0
#define PROBE_IDX 3
#define PROBE_WAVE 0
#define PROBE_K 10
#include <hip/hip_runtime.h>
#include <stdint.h>

#define GAS __attribute__((address_space(1)))
#define LAS __attribute__((address_space(3)))
typedef unsigned short u16;
typedef unsigned v4u __attribute__((ext_vector_type(4)));
typedef unsigned v2u __attribute__((ext_vector_type(2)));
typedef float f4 __attribute__((ext_vector_type(4)));
typedef float f2 __attribute__((ext_vector_type(2)));
typedef __bf16 bf8 __attribute__((ext_vector_type(8)));
typedef __bf16 bf2 __attribute__((ext_vector_type(2)));
typedef short s4 __attribute__((ext_vector_type(4)));

namespace {
constexpr int NH = 32, SK = 4096, DH = 128, DM = 512, DC = 512, FF = 1376;
constexpr int NSL = FF / 16;
constexpr int WS_E = 0;
constexpr int WS_QP = 4096;
constexpr int WS_QA = WS_QP + NH * 4 * 1024;
constexpr int WS_LSE = WS_QA + 2 * NH * 2048;
constexpr int WS_OPT = WS_LSE + 2 * 2 * 32 * 128;
constexpr int WS_OPG = WS_OPT + 32 * 64 * 128;
constexpr int WS_PART = WS_OPG + 32 * 128 * 128;
constexpr int WS_BYTES = WS_PART + NH * 2 * 2 * 32 * 512;
constexpr int WS_STAMP = 7 << 19;
constexpr int CP = 1056;
constexpr int XP = 1088, SP = 288, SP2 = 544;
constexpr int L_C = 0, L_Q = 128 * CP, L_P = L_Q + 16 * CP, L_X = L_P + 4096, L_MX = L_X + 2 * XP, L_RR = L_MX + 256,
              L_JUNK = L_RR + 1024, L_FL = L_JUNK + 256, L_END = L_FL + 128;
static_assert(L_END <= 163840, "LDS");
constexpr int L_GG = 0, L_GD = 32768, L_GR = 65536;
constexpr int L_WUV = 0, L_WO = 65536, L_WQ = 0, L_WUK = 32768, L_CC = 131072, L_CT = L_CC + 8 * SP2, L_W = L_CT + 2 * SP;
#define UK_DELAY 16
#define H1_DELAY 8
#define T_DELAY 20
#define GLU_FLAG_Q 1
#define NOCLEAR 0
#define CODE_LINES 0
#define TLB_WARM 0
#define OPT_W 0
#define QA_WARM 0
#define LSE_DELAY 40
#define A_DELAY 24
#define PF_GLU 1
constexpr unsigned SPIN_CAP = 1u << 20;
constexpr int AUX_SC1 = 16;

#define STAMPS
#define STAMP_W 0
#define STAMP(i) asm volatile("s_memrealtime %0\n\ts_waitcnt lgkmcnt(0)" : "=s"(ts[i])::"memory")
#define STAMP_FLUSH()                                                                      \
  do {                                                                                     \
    const int cls_ = r < 16 ? 0 : (r < 28 ? 1 : 2);                                        \
    if ((cls_ == PROBE_CLASS || PROBE_CLASS == 3) && w == PROBE_WAVE && ts[PROBE_IDX] != 0) {   \
      v4u g_;                                                                              \
      for (unsigned sp_ = 0; sp_ < SPIN_CAP; ++sp_) { asm volatile("" ::: "memory"); g_ = ld_sc1(rs, 2048); if (g_.z == Tg) break; } \
      const unsigned long long t0_ = ((unsigned long long)__builtin_amdgcn_readfirstlane(g_.y) << 32) | (unsigned)__builtin_amdgcn_readfirstlane(g_.x); \
      const long long d_ = (long long)(ts[PROBE_IDX] - t0_);                               \
      const unsigned long long until_ = t0_ + 4000ull + PROBE_K * d_;                      \
      while (__builtin_amdgcn_s_memrealtime() < until_) __builtin_amdgcn_s_sleep(1);       \
    }                                                                                      \
  } while (0)
#define PROBE_T(T_) do { Tg = T_; if (blockIdx.x == 0 && tid == 0) st_sc1(rs, 2048, v4u{(unsigned)tstart, (unsigned)(tstart >> 32), T_, 0u}); } while (0)

__device__ __forceinline__ f4 mfma32(v4u a, v4u b, f4 c) {
  return __builtin_amdgcn_mfma_f32_16x16x32_bf16(__builtin_bit_cast(bf8, a), __builtin_bit_cast(bf8, b), c, 0, 0, 0);
}
__device__ __forceinline__ f4 mfma16(v2u a, v2u b, f4 c) {
  return __builtin_amdgcn_mfma_f32_16x16x16bf16_1k(__builtin_bit_cast(s4, a), __builtin_bit_cast(s4, b), c, 0, 0, 0);
}
__device__ __forceinline__ unsigned pk(float lo, float hi) {
  f2 x = {lo, hi};
  return __builtin_bit_cast(unsigned, __builtin_convertvector(x, bf2));
}
__device__ __forceinline__ float asf(unsigned u) { return __builtin_bit_cast(float, u); }
__device__ __forceinline__ float blo(unsigned u) { return __builtin_bit_cast(float, u << 16); }
__device__ __forceinline__ float bhi(unsigned u) { return __builtin_bit_cast(float, u & 0xffff0000u); }
template <int CTRL> __device__ __forceinline__ float dpp(float v) {
  return __builtin_bit_cast(float, __builtin_amdgcn_update_dpp(0, __builtin_bit_cast(int, v), CTRL, 0xf, 0xf, true));
}
__device__ __forceinline__ float fmax2(float a, float b) {
#pragma float_control(precise, off)
  return __builtin_fmaxf(a, b);
}
typedef unsigned short h2 __attribute__((ext_vector_type(2)));
__device__ __forceinline__ unsigned scale_dn(unsigned p, unsigned kk) {
  return __builtin_bit_cast(unsigned, __builtin_elementwise_sub_sat(__builtin_bit_cast(h2, p), __builtin_bit_cast(h2, kk)));
}
#define PIN(x) asm volatile("" : "+v"(x))
constexpr int ROR1 = 0x121, ROR2 = 0x122, ROR4 = 0x124, ROR8 = 0x128, QP_2301 = 0x4e, QP_2222 = 0xaa;
__device__ __forceinline__ float sw32_add(float a, float b) {
  auto r = __builtin_amdgcn_permlane32_swap(__builtin_bit_cast(unsigned, a), __builtin_bit_cast(unsigned, b), false, false);
  return asf(r[0]) + asf(r[1]);
}
__device__ __forceinline__ float sw16_add(float a, float b) {
  auto r = __builtin_amdgcn_permlane16_swap(__builtin_bit_cast(unsigned, a), __builtin_bit_cast(unsigned, b), false, false);
  return asf(r[0]) + asf(r[1]);
}
__device__ __forceinline__ float vmax(unsigned a, unsigned b) {
  float r;
  asm("v_max_f32 %0, %1, %2" : "=v"(r) : "v"(a), "v"(b));
  return r;
}
__device__ __forceinline__ float sw32_max(float a) {
  auto r = __builtin_amdgcn_permlane32_swap(__builtin_bit_cast(unsigned, a), __builtin_bit_cast(unsigned, a), false, false);
  return vmax(r[0], r[1]);
}
__device__ __forceinline__ float sw16_max(float a) {
  auto r = __builtin_amdgcn_permlane16_swap(__builtin_bit_cast(unsigned, a), __builtin_bit_cast(unsigned, a), false, false);
  return vmax(r[0], r[1]);
}
__device__ __forceinline__ float wave_sum(float v) {
  v += dpp<ROR8>(v);
  v += dpp<ROR4>(v);
  v += dpp<ROR2>(v);
  v += dpp<ROR1>(v);
  v = sw16_add(v, v);
  return sw32_add(v, v);
}
__device__ __forceinline__ v2u trrd(LAS char* p) {
  return __builtin_bit_cast(v2u, __builtin_amdgcn_ds_read_tr16_b64_v4i16((LAS s4*)p));
}
__device__ __forceinline__ v4u trrd2(LAS char* p0, LAS char* p1) {
  v2u a = trrd(p0), b = trrd(p1);
  return v4u{a.x, a.y, b.x, b.y};
}
#define BAR() asm volatile("s_waitcnt lgkmcnt(0)\n\ts_barrier" ::: "memory")
#define VMWAIT(k) asm volatile("s_waitcnt vmcnt(%0)" ::"n"(k) : "memory")
__device__ __forceinline__ void gld16(v4u& d, const void* base, unsigned off) {
  asm volatile("global_load_dwordx4 %0, %1, %2" : "=v"(d) : "v"(off), "s"(base) : "memory");
}
__device__ __forceinline__ void gld4_sc1(unsigned& d, const void* base, unsigned off) {
  asm volatile("global_load_dword %0, %1, %2 sc1" : "=v"(d) : "v"(off), "s"(base) : "memory");
}
__device__ __forceinline__ void touch(const void* base, unsigned off, unsigned lds) {
  asm volatile("s_mov_b32 m0, %2\n\ts_nop 0\n\tglobal_load_lds_dword %0, %1" ::"v"(off), "s"(base), "s"(lds) : "memory");
}
__device__ __forceinline__ void touch_sc1(const void* base, unsigned off, unsigned lds) {
  asm volatile("s_mov_b32 m0, %2\n\ts_nop 0\n\tglobal_load_lds_dword %0, %1 sc1" ::"v"(off), "s"(base), "s"(lds) : "memory");
}
#define LD16(i, a, o) "global_load_dwordx4 %" #i ", %" #a ", %" #o
#define SWEEP_4(q, o, o0, o1, o2, o3, W)                                                                                     \
  asm volatile(LD16(0, 4, 5) " offset:" #o0 " sc1\n\t" LD16(1, 4, 5) " offset:" #o1 " sc1\n\t" LD16(2, 4, 5) " offset:" #o2         \
               " sc1\n\t" LD16(3, 4, 5) " offset:" #o3 " sc1" W                                                               \
               : "=&v"((q)[0]), "=&v"((q)[1]), "=&v"((q)[2]), "=&v"((q)[3])                                                  \
               : "v"(o), "s"(ws)                                                                                             \
               : "memory")
#define SWEEP_L(W)                                                                                                          \
  asm volatile("global_load_dwordx2 %0, %3, %5 sc1\n\tglobal_load_dwordx2 %1, %4, %5 sc1\n\t"                              \
               "global_load_dwordx2 %2, %4, %5 offset:512 sc1" W                                                            \
               : "=&v"(la), "=&v"(l0), "=&v"(l1)                                                                            \
               : "v"(ola), "v"(olo), "s"(ws)                                                                                \
               : "memory")
#define SWEEP_2(p, q, oa, ob, W) \
  asm volatile(LD16(0, 2, 4) " sc1\n\t" LD16(1, 3, 4) " sc1" W : "=&v"(p), "=&v"(q) : "v"(oa), "v"(ob), "s"(ws) : "memory")
#define TAGS_BAD4(q) \
  __builtin_amdgcn_ballot_w64((T & (q)[0].y & (q)[0].w & (q)[1].y & (q)[1].w & (q)[2].y & (q)[2].w & (q)[3].y & (q)[3].w) != \
                              (T | (q)[0].y | (q)[0].w | (q)[1].y | (q)[1].w | (q)[2].y | (q)[2].w | (q)[3].y | (q)[3].w))
#define FLAG_SET(i) asm volatile("ds_write_b32 %0, %1" ::"v"(smb + L_FL + 4 * (i)), "v"(1u) : "memory")
#define FLAG_WAIT4(i)                                                                                     \
  for (unsigned spin_ = 0; spin_ < SPIN_CAP; ++spin_) {                                                   \
    v4u f_;                                                                                               \
    asm volatile("ds_read_b128 %0, %1\n\ts_waitcnt lgkmcnt(0)" : "=v"(f_) : "v"(smb + L_FL + 16 * (i)) : "memory"); \
    if (__builtin_amdgcn_readfirstlane(f_.x & f_.y & f_.z & f_.w)) break;                                 \
  }
#define WAIT0 "\n\ts_waitcnt vmcnt(0)"
__device__ __forceinline__ void dma16(const void* base, unsigned off, unsigned lds) {
  asm volatile("s_mov_b32 m0, %2\n\ts_nop 0\n\tglobal_load_lds_dwordx4 %0, %1" ::"v"(off), "s"(base), "s"(lds) : "memory");
}
__device__ __forceinline__ v4u ld_sc1(__amdgpu_buffer_rsrc_t rs, int off) {
  return __builtin_amdgcn_raw_buffer_load_b128(rs, off, 0, AUX_SC1);
}
__device__ __forceinline__ v2u ld8_sc1(__amdgpu_buffer_rsrc_t rs, int off) {
  return __builtin_amdgcn_raw_buffer_load_b64(rs, off, 0, AUX_SC1);
}
__device__ __forceinline__ void st_sc1(__amdgpu_buffer_rsrc_t rs, int off, v4u v) {
  __builtin_amdgcn_raw_buffer_store_b128(v, rs, off, 0, AUX_SC1);
}
__device__ __forceinline__ void ln_stats(v4u r, float& mean, float& rstd) {
  float e[8] = {blo(r.x), bhi(r.x), blo(r.y), bhi(r.y), blo(r.z), bhi(r.z), blo(r.w), bhi(r.w)};
  float s = ((e[0] + e[1]) + (e[2] + e[3])) + ((e[4] + e[5]) + (e[6] + e[7]));
  mean = wave_sum(s) * (1.f / DM);
  float q = 0.f;
#pragma unroll
  for (int i = 0; i < 8; ++i) {
    float d = e[i] - mean;
    q = fmaf(d, d, q);
  }
  rstd = __builtin_amdgcn_rsqf(wave_sum(q) * (1.f / DM) + 1e-5f);
}
}

__global__ void __launch_bounds__(512, 2)
mla_glu_decode(const u16* __restrict__ x, const u16* __restrict__ ckv, const u16* __restrict__ wq, const u16* __restrict__ wuk,
               const u16* __restrict__ wuv, const u16* __restrict__ wo, const u16* __restrict__ wg, const u16* __restrict__ wu,
               const u16* __restrict__ wd, u16* __restrict__ out, char* __restrict__ ws) {
  const unsigned long long tstart = __builtin_amdgcn_s_memrealtime();
  __builtin_amdgcn_sched_barrier(0);
  __shared__ __attribute__((aligned(16))) char smem_[L_END];
  LAS char* sm = (LAS char*)smem_;
  const unsigned smb = (unsigned)(size_t)sm;
  const int tid = threadIdx.x, l = tid & 63, w = __builtin_amdgcn_readfirstlane(tid >> 6);
  const int col = l & 15, g4 = l >> 4, tq = (l & 15) >> 2, tp = l & 3;
  const int xcd = blockIdx.x & 7, r = blockIdx.x >> 3;
  const __amdgpu_buffer_rsrc_t rs = __builtin_amdgcn_make_buffer_rsrc(ws, 0, WS_BYTES, 0x00020000);
  asm volatile("" ::"s"(x), "s"(ckv), "s"(wq), "s"(wuk), "s"(wuv), "s"(wo), "s"(wg), "s"(wu), "s"(wd), "s"(out), "s"(ws));
  unsigned long long ts[16] = {0, 0, 0, 0, 0, 0, 0, 0, 0, 0, 0, 0, 0, 0, 0, 0};
  STAMP(0);
  unsigned Tg = 0;
  ts[0] = tstart;

  unsigned ep, T;
  LAS char* const px = sm + L_X + (col & 1) * XP + 16 * g4;
  if ((w >> 1) == 2) dma16(x, (unsigned)((w & 1) * 1024 + l * 16), smb + L_X + (w & 1) * XP);
  const f4 z = {0.f, 0.f, 0.f, 0.f};
#define LAYERNORM()                                                                                                              \
  {                                                                                                                              \
    const v4u xr = *(LAS v4u*)(sm + L_X + (w & 1) * XP + l * 16);                                                                \
    float e[8] = {blo(xr.x), bhi(xr.x), blo(xr.y), bhi(xr.y), blo(xr.z), bhi(xr.z), blo(xr.w), bhi(xr.w)};                       \
    const float mean = wave_sum(((e[0] + e[1]) + (e[2] + e[3])) + ((e[4] + e[5]) + (e[6] + e[7]))) * (1.f / DM);                 \
    float q = 0.f;                                                                                                               \
    _Pragma("unroll") for (int i = 0; i < 8; ++i) {                                                                              \
      e[i] -= mean;                                                                                                              \
      q = fmaf(e[i], e[i], q);                                                                                                   \
    }                                                                                                                            \
    const float rstd = __builtin_amdgcn_rsqf(wave_sum(q) * (1.f / DM) + 1e-5f);                                                  \
    *(LAS v4u*)(sm + L_X + (w & 1) * XP + l * 16) =                                                                              \
        v4u{pk(e[0] * rstd, e[1] * rstd), pk(e[2] * rstd, e[3] * rstd), pk(e[4] * rstd, e[5] * rstd), pk(e[6] * rstd, e[7] * rstd)}; \
  }

  if (__builtin_expect(r < 16, 1)) {
    const int h = 4 * xcd + (r >> 2), j = r & 3, ca = j >> 1, mb = j & 1;
    const int fl = tq | ((g4 & 1) << 2);
    gld4_sc1(ep, ws, 0u);
    if (__builtin_expect(w < 4, 1)) {
      const unsigned vq = (unsigned)(((128 * j + g4) * 4096 + h * DH + 8 * (col ^ ((g4 | ((w & 2) << 1)) << 1))) * 2);
#pragma unroll 1
      for (int k = w; k < 32; k += 4) dma16(wq, vq + (unsigned)k * 32768u, smb + L_WQ + k * 1024);
    } else if (w < 6) {
      VMWAIT(1);
      LAYERNORM();
    } else {
      __builtin_amdgcn_s_sleep(UK_DELAY);
      const unsigned vu = (unsigned)((((128 * j + g4) * NH + h) * DH) * 2);
#pragma unroll 1
      for (int k = w - 6; k < 32; k += 2) dma16(wuk, vu + (unsigned)k * 32768u + 16u * (col ^ ((4 * k + g4) & 15)), smb + L_WUK + k * 1024);
    }
    unsigned p = L_WQ + 256 * (8 * g4 + tq) + 16 * ((2 * w + (tp >> 1)) ^ (fl << 1)) + 8 * (tp & 1);
    unsigned oqp = WS_QP + ((h * 4 + j) * 8 + w) * 128 + col * 64 + g4 * 16;
    unsigned pxj = L_X + (col & 1) * XP + 16 * g4 + 256 * j;
    unsigned o = WS_QP + ((h * 4 + ((col >> 1) & 3)) * 8 + (g4 >> 1)) * 128 + (col & 1) * 64 + (g4 & 1) * 32;
    unsigned oqa = WS_QA + ((col & 1) * NH + h) * 2048 + (64 * j + 16 * w + 8 * (col >> 1) + 2 * g4) * 8;
    unsigned pu = L_WUK + 256 * (32 * w + col) + 16 * (g4 ^ col);
    PIN(p); PIN(oqp); PIN(pxj); PIN(o); PIN(oqa); PIN(pu);
    if (w < 6) VMWAIT(0);
    else VMWAIT(16);
    PIN(ep);
    T = ep + 1u;
    PIN(T);
    STAMP(1);
    BAR();
    STAMP(2);
    {
      v4u af[4], xf[4];
#pragma unroll
      for (int s = 0; s < 4; ++s) {
        af[s] = trrd2(sm + p + 256 * 32 * s, sm + p + 256 * (32 * s + 4));
        xf[s] = *(LAS v4u*)(sm + pxj + 64 * s);
      }
      __builtin_amdgcn_sched_barrier(0);
      f4 acc = z;
#pragma unroll
      for (int s = 0; s < 4; ++s) acc = mfma32(af[s], xf[s], acc);
      if (col < 2) st_sc1(rs, oqp, v4u{pk(acc[0], acc[1]), T, pk(acc[2], acc[3]), T});
    }
    STAMP(3);
    if (w >= 6) VMWAIT(1);
    if (w >= 4) BAR();
    if (w < 4) {
      v4u a[8], uk[8];
      __builtin_amdgcn_s_sleep(H1_DELAY);
      SWEEP_4(a, o, 0, 16, 256, 272, "");
      SWEEP_4(a + 4, o, 512, 528, 768, 784, "");
      BAR();
#pragma unroll
      for (int t = 0; t < 2; ++t)
#pragma unroll
        for (int s = 0; s < 4; ++s) uk[4 * t + s] = *(LAS v4u*)(sm + (pu ^ (64 * s)) + 4096 * t);
      asm volatile("s_waitcnt vmcnt(%8)"
                   : "+v"(a[0]), "+v"(a[1]), "+v"(a[2]), "+v"(a[3]), "+v"(a[4]), "+v"(a[5]), "+v"(a[6]), "+v"(a[7])
                   : "n"(OPT_W)
                   : "memory");
      for (unsigned spin = 0;; ++spin) {
        if (!(TAGS_BAD4(a) | TAGS_BAD4(a + 4)) || spin > SPIN_CAP) break;
        SWEEP_4(a, o, 0, 16, 256, 272, "");
        SWEEP_4(a + 4, o, 512, 528, 768, 784, WAIT0);
        asm volatile("" : "+v"(a[0]), "+v"(a[1]), "+v"(a[2]), "+v"(a[3]));
      }
      STAMP(4);
      f4 a0 = z, a1 = z;
#pragma unroll
      for (int s = 0; s < 4; ++s) {
        const v4u qf = {a[2 * s].x, a[2 * s].z, a[2 * s + 1].x, a[2 * s + 1].z};
        a0 = mfma32(uk[s], qf, a0);
        a1 = mfma32(uk[4 + s], qf, a1);
      }
      float q[4];
#pragma unroll
      for (int e = 0; e < 4; ++e) {
        const float mine = (col & 2) ? a1[e] : a0[e], other = (col & 2) ? a0[e] : a1[e];
        q[e] = mine + dpp<QP_2301>(other);
        q[e] += dpp<ROR4>(q[e]);
      }
      if (col < 4) st_sc1(rs, oqa, v4u{pk(q[0], q[1]), T, pk(q[2], q[3]), T});
    }
    STAMP(5);
    PROBE_T(T);
    BAR();
    __builtin_amdgcn_s_sleep(T_DELAY);
    {
      const unsigned vv = (unsigned)((((256 * ca + g4) * NH + h) * DH + 8 * (col ^ ((g4 | ((w & 2) << 1)) << 1))) * 2);
      const unsigned vo = (unsigned)(((h * DH + (l >> 5)) * DM + 256 * mb) * 2) + 16u * ((l & 31) ^ (((l >> 5) | ((w & 1) << 1) | (w & 4)) << 1));
      int kv = w + 32 * mb, ko = w + 32 * ca;
#pragma unroll 1
      for (int hf = 0; hf < 2; ++hf) {
#pragma unroll 1
        for (int i = 0; i < 4; ++i, kv = (kv + 8) & 63) dma16(wuv, vv + (unsigned)kv * 32768u, smb + L_WUV + kv * 1024);
#pragma unroll 1
        for (int i = 0; i < 4; ++i, ko = (ko + 8) & 63) dma16(wo, vo + (unsigned)ko * 2048u, smb + L_WO + ko * 1024);
        if (hf == 0) asm volatile("s_barrier" ::: "memory");
      }
    }
    const int tb = w >> 2, nq = w & 3;
    o = WS_PART + (((h * 2 + ca) * 2 + tb) * 32 + 8 * nq + g4) * 512 + 16 * col;
    unsigned pw = L_W + (tb * 32 + 8 * nq + g4) * 4;
    unsigned pv = L_WUV + 256 * (8 * g4 + tq) + 16 * ((2 * w + (tp >> 1)) ^ (fl << 1)) + 8 * (tp & 1);
    unsigned po = L_WO + 512 * (8 * g4 + tq) + 16 * ((4 * w + (tp >> 1)) ^ (fl << 1)) + 8 * (tp & 1);
    unsigned pcw = L_CC + (nq * 2 + tb) * SP2 + 16 * col + 8 * g4, pcr = L_CC + (col & 7) * SP2 + 16 * g4;
    unsigned ptw = L_CT + col * SP + (16 * w + 4 * g4) * 2, ptr = L_CT + (col & 1) * SP + 16 * g4;
    unsigned oo = WS_OPT + ((16 * mb + 2 * w + (col >> 1)) * 64 + 2 * h + ca) * 128 + ((col & 1) * 4 + g4) * 16;
    v4u mk;
#pragma unroll
    for (int d = 0; d < 4; ++d) mk[d] = (col >> 1) == d ? ((col & 1) ? 0xffff0000u : 0xffffu) : 0u;
    PIN(o); PIN(pw); PIN(pv); PIN(po); PIN(pcw); PIN(pcr); PIN(ptw); PIN(ptr); PIN(oo); PIN(mk);
    if (w == 7) {
      const unsigned ol = WS_LSE + (((l >> 5) * 2 + (h >> 4)) * 32 + (l & 31)) * 128 + (h & 15) * 8;
      v2u la;
      for (unsigned spin = 0;; ++spin) {
        asm volatile("global_load_dwordx2 %0, %1, %2 sc1" WAIT0 : "=&v"(la) : "v"(ol), "s"(ws) : "memory");
        if (!__builtin_amdgcn_ballot_w64(la.y != T) || spin > SPIN_CAP) break;
      }
      float M = asf(la.x);
      M = fmax2(M, dpp<ROR8>(M));
      M = fmax2(M, dpp<ROR4>(M));
      M = fmax2(M, dpp<ROR2>(M));
      M = fmax2(M, dpp<ROR1>(M));
      M = sw16_max(M);
      const float e = __builtin_amdgcn_exp2f(asf(la.x) - M);
      float L = e + dpp<ROR8>(e);
      L += dpp<ROR4>(L);
      L += dpp<ROR2>(L);
      L += dpp<ROR1>(L);
      L = sw16_add(L, L);
      const float wt = e * __builtin_amdgcn_rcpf(L);
      *(LAS unsigned*)(sm + L_W + 4 * l) = pk(wt, wt);
      STAMP(7);
    }
    VMWAIT(0);
    STAMP(6);
    BAR();
    v4u a[4];
    SWEEP_4(a, o, 0, 256, 2048, 2304, "");
    v4u A0 = mk & *(LAS unsigned*)(sm + pw), A1 = mk & *(LAS unsigned*)(sm + pw + 16);
    v4u av[8], ao[8];
#pragma unroll
    for (int s = 0; s < 8; ++s) av[s] = trrd2(sm + pv + 256 * 32 * s, sm + pv + 256 * (32 * s + 4));
#pragma unroll
    for (int t = 0; t < 2; ++t)
#pragma unroll
      for (int s = 0; s < 4; ++s) ao[4 * t + s] = trrd2(sm + (po ^ (32 * t)) + 512 * 32 * s, sm + (po ^ (32 * t)) + 512 * (32 * s + 4));
    asm volatile("s_waitcnt vmcnt(0)" : "+v"(a[0]), "+v"(a[1]), "+v"(a[2]), "+v"(a[3]), "+v"(A0), "+v"(A1)::"memory");
    for (unsigned spin = 0;; ++spin) {
      unsigned mx = 0u;
#pragma unroll
      for (int i = 0; i < 4; ++i) mx = max(max(mx, max(a[i].x, a[i].y)), max(a[i].z, a[i].w));
      if (!__builtin_amdgcn_ballot_w64(mx == ~0u) || spin > SPIN_CAP) break;
      SWEEP_4(a, o, 0, 256, 2048, 2304, WAIT0);
    }
    STAMP(8);
    {
      f4 c0 = mfma32(A0, a[0], z), c1 = mfma32(A0, a[1], z);
      c0 = mfma32(A1, a[2], c0);
      c1 = mfma32(A1, a[3], c1);
      if (g4 < 2) {
        *(LAS v2u*)(sm + pcw) = v2u{pk(c0[0], c0[1]), pk(c0[2], c0[3])};
        *(LAS v2u*)(sm + pcw + 256) = v2u{pk(c1[0], c1[1]), pk(c1[2], c1[3])};
      }
    }
    BAR();
    {
      f4 acc = z;
      v4u bf[8];
#pragma unroll
      for (int s = 0; s < 8; ++s) bf[s] = *(LAS v4u*)(sm + pcr + 64 * s);
      __builtin_amdgcn_sched_barrier(0);
#pragma unroll
      for (int s = 0; s < 8; ++s) acc = mfma32(av[s], bf[s], acc);
#pragma unroll
      for (int e = 0; e < 4; ++e) {
        acc[e] += dpp<QP_2301>(acc[e]);
        acc[e] += dpp<ROR4>(acc[e]);
      }
      if (col < 2) *(LAS v2u*)(sm + ptw) = v2u{pk(acc[0], acc[1]), pk(acc[2], acc[3])};
    }
    BAR();
    {
      f4 c0 = z, c1 = z;
      v4u bf[4];
#pragma unroll
      for (int s = 0; s < 4; ++s) bf[s] = *(LAS v4u*)(sm + ptr + 64 * s);
      __builtin_amdgcn_sched_barrier(0);
#pragma unroll
      for (int s = 0; s < 4; ++s) {
        c0 = mfma32(ao[s], bf[s], c0);
        c1 = mfma32(ao[4 + s], bf[s], c1);
      }
      if (col & 2) c0 = c1;
      if (col < 4) st_sc1(rs, oo, v4u{pk(c0[0], c0[1]), T, pk(c0[2], c0[3]), T});
    }
    STAMP(9);
  } else {
    const int a = r - 16, b = xcd >> 2, n = 8 * (xcd & 3) + (a >> 1), g = a & 1;
    const int sl = 12 * xcd + a, rd = (4 * xcd + a) & 31;
    const bool glu = a < 12 && sl < NSL;
    const int v = w & 3;
    unsigned op = WS_PART + ((((16 * g + (col & 7)) * 2 + (v >> 1)) * 2 + b) * 32 + n) * 512 + 256 * (v & 1) + (col >> 3) * 64 +
                  (g4 & 1) * 32 + (g4 >> 1) * 16;
    unsigned xres, sdummy = 0u;
    gld4_sc1(ep, ws, 0u);
    asm volatile("global_load_ushort %0, %1, %2" : "=v"(xres) : "v"((unsigned)(((l >> 4) & 1) * 1024 + rd * 32 + col * 2)), "s"(x) : "memory");
    {
      const unsigned vc = (unsigned)(((b * SK + 128 * n) * DC + 8 * l) * 2);
      int row = w + 64 * g;
#define STOUCH(base, off) asm volatile("s_load_dword %0, %1, %2" : "+s"(sdummy) : "s"(base), "s"((unsigned)(off)) : "memory")
      if (w < 4) {
        const int i = 4 * a + w;
      }
      if (w >= 4) {
#pragma unroll
        for (int q = 0; q < 4; ++q)
          if (!NOCLEAR) st_sc1(rs, op + 128 * (q >> 1) + (q & 1) * 8 * 65536, v4u{~0u, ~0u, ~0u, ~0u});
        if (w == 7 && l < 32) *(LAS unsigned*)(sm + L_FL + 4 * l) = 0u;
      }
      __builtin_amdgcn_s_sleep(A_DELAY);
#pragma unroll 1
      for (int q = 0; q < 4; ++q) {
        row = w + ((64 * g + (OPT_W ? 64 * (q & 1) + 32 * (q >> 1) : 32 * q)) & 127);
#pragma unroll 1
        for (int i = 0; i < 4; ++i, row += 8) dma16(ckv, vc + row * 1024u, smb + L_C + row * CP);
        if (q < 3) asm volatile("s_barrier" ::: "memory");
      }
      if ((w >> 1) == 2) {
        VMWAIT(22);
        LAYERNORM();
      }
    }
    unsigned o = WS_QA + (b * NH + 16 * g) * 2048 + w * 4096 + l * 16, pqs = smb + L_Q + 2 * w * CP + l * 8;
    PIN(o); PIN(pqs);
    asm volatile("s_waitcnt vmcnt(0) lgkmcnt(0)" : "+v"(ep), "+v"(xres), "+s"(sdummy)::"memory");
    T = ep + 1u;
    PROBE_T(T);
    BAR();
    STAMP(1);
    v4u fr[32], qg[4];
    SWEEP_4(qg, o, 0, 1024, 2048, 3072, "");
#define QGATHER()                                                                                                           \
  asm volatile("s_waitcnt vmcnt(0)" : "+v"(qg[0]), "+v"(qg[1]), "+v"(qg[2]), "+v"(qg[3])::"memory");                        \
  for (unsigned spin = 0;; ++spin) {                                                                                        \
    if (!TAGS_BAD4(qg) || spin > SPIN_CAP) break;                                                                           \
    SWEEP_4(qg, o, 0, 1024, 2048, 3072, WAIT0);                                                                             \
  }                                                                                                                         \
  STAMP(3);                                                                                                                 \
  asm volatile("ds_write2_b32 %0, %2, %3 offset0:0 offset1:1\n\tds_write2_b32 %0, %4, %5 offset0:128 offset1:129\n\t"        \
               "ds_write2_b32 %1, %6, %7 offset0:0 offset1:1\n\tds_write2_b32 %1, %8, %9 offset0:128 offset1:129" ::"v"(pqs), \
               "v"(pqs + CP), "v"(qg[0].x), "v"(qg[0].z), "v"(qg[1].x), "v"(qg[1].z), "v"(qg[2].x), "v"(qg[2].z), "v"(qg[3].x), \
               "v"(qg[3].z)                                                                                                 \
               : "memory")
    if (__builtin_expect(w < 4, 1)) {
      LAS char* pc = sm + L_C + (32 * w + col) * CP + 16 * g4;
#pragma unroll
      for (int kt = 0; kt < 2; ++kt)
#pragma unroll
        for (int s = 0; s < 16; ++s) fr[16 * kt + s] = *(LAS v4u*)(pc + 16 * kt * CP + 64 * s);
      unsigned p = L_Q + col * CP + 16 * g4, pp = L_P + w * 1024 + l * 16, pm = L_MX + col * 16 + w * 4;
      PIN(p); PIN(pp); PIN(pm);
      STAMP(2);
      QGATHER();
      BAR();
      STAMP(4);
      f4 s0 = z, s1 = z;
      v4u qf[16];
#pragma unroll
      for (int s = 0; s < 6; ++s) qf[s] = *(LAS v4u*)(sm + p + 64 * s);
      __builtin_amdgcn_sched_barrier(0);
#pragma unroll
      for (int s = 0; s < 16; ++s) {
        if (s + 6 < 16) qf[s + 6] = *(LAS v4u*)(sm + p + 64 * (s + 6));
        s0 = mfma32(fr[s], qf[s], s0);
        s1 = mfma32(fr[16 + s], qf[s], s1);
        __builtin_amdgcn_sched_barrier(0);
      }
      float mx = fmax2(fmax2(fmax2(s0[0], s0[1]), fmax2(s0[2], s0[3])), fmax2(fmax2(s1[0], s1[1]), fmax2(s1[2], s1[3])));
      mx = sw32_max(sw16_max(mx));
      constexpr float SC = 0.08838834764831845f * 1.4426950408889634f;
      const float mi = __builtin_ceilf(mx * SC);
#pragma unroll
      for (int e = 0; e < 4; ++e) {
        s0[e] = __builtin_amdgcn_exp2f(fmaf(s0[e], SC, -mi));
        s1[e] = __builtin_amdgcn_exp2f(fmaf(s1[e], SC, -mi));
      }
      *(LAS float*)(sm + pm) = mi;
      *(LAS v4u*)(sm + pp) = v4u{pk(s0[0], s0[1]), pk(s0[2], s0[3]), pk(s1[0], s1[1]), pk(s1[2], s1[3])};
      BAR();
      STAMP(5);
      if (glu) {
        v4u af[8], xf[8];
#pragma unroll
        for (int s = 0; s < 8; ++s) xf[s] = *(LAS v4u*)(px + 512 * (w >> 1) + 64 * s);
        FLAG_WAIT4(0);
        unsigned og = (unsigned)(((128 * w + (l >> 1)) * FF + 16 * sl + 8 * (l & 1)) * 2);
#pragma unroll 1
        for (int i = 0; i < 4; ++i, og += 32 * FF * 2) {
          dma16(wg, og, smb + L_GG + (4 * w + i) * 1024);
          dma16(wu, og, smb + L_GG + 16384 + (4 * w + i) * 1024);
        }
#pragma unroll 1
        for (int i = 0; i < 4; ++i) dma16(wd, (unsigned)(((16 * sl + 4 * w + i) * DM + 8 * l) * 2), smb + L_GD + (4 * w + i) * CP);
        VMWAIT(4);
        FLAG_SET(4 + w);
        FLAG_WAIT4(1);
        STAMP(7);
        LAS char* pg = sm + L_GG + (w & 1) * 16384 + 32 * (256 * (w >> 1) + 8 * g4 + tq) + 8 * tp;
        f4 acc = z;
#pragma unroll
        for (int s = 0; s < 8; ++s) af[s] = trrd2(pg + 32 * 32 * s, pg + 32 * (32 * s + 4));
        __builtin_amdgcn_sched_barrier(0);
#pragma unroll
        for (int s = 0; s < 8; ++s) acc = mfma32(af[s], xf[s], acc);
        *(LAS f4*)(sm + L_GR + ((w & 1) * 2 + (w >> 1)) * 1024 + l * 16) = acc;
        VMWAIT(0);
        FLAG_SET(8 + w);
        FLAG_WAIT4(2);
        v2u dn[8];
#pragma unroll
        for (int t = 0; t < 8; ++t) dn[t] = trrd(sm + L_GD + (4 * g4 + tq) * CP + 32 * (8 * w + t) + 8 * tp);
        LAS char* pr = sm + L_GR + l * 16;
        const f4 gx = *(LAS f4*)(pr) + *(LAS f4*)(pr + 1024), ux = *(LAS f4*)(pr + 2048) + *(LAS f4*)(pr + 3072);
        float hh[4];
#pragma unroll
        for (int e = 0; e < 4; ++e) hh[e] = ux[e] * __builtin_amdgcn_rcpf(1.f + __builtin_amdgcn_exp2f(-1.4426950408889634f * gx[e]));
        const v2u hb = {pk(hh[0], hh[1]), pk(hh[2], hh[3])};
        f4 d = z;
#pragma unroll
        for (int t = 0; t < 8; ++t) d = mfma16(dn[t], (col >> 1) == t ? hb : v2u{0u, 0u}, d);
        st_sc1(rs, WS_OPG + ((8 * w + (col >> 1)) * 128 + sl) * 128 + ((col & 1) * 4 + g4) * 16, v4u{pk(d[0], d[1]), T, pk(d[2], d[3]), T});
        STAMP(9);
      }
    } else {
      LAS char* p0 = sm + L_C + (4 * g4 + tq) * CP + 256 * v + 8 * tp;
      unsigned p1 = L_C + (64 + 4 * g4 + tq) * CP + 256 * v + 8 * tp;
      PIN(p1);
#pragma unroll
      for (int ct = 0; ct < 8; ++ct)
#pragma unroll
        for (int u = 0; u < 4; ++u) {
          LAS char* p = (u < 2 ? p0 : sm + p1) + 32 * (u & 1) * CP + 32 * ct;
          fr[4 * ct + u] = trrd2(p, p + 16 * CP);
        }
      unsigned ol = WS_LSE + ((b * 2 + g) * 32 + n) * 128 + col * 8, pp = L_P + l * 16, pm = L_MX + col * 16;
      v4u one = {0x3f803f80u, 0x3f803f80u, 0x3f803f80u, 0x3f803f80u};
      PIN(ol); PIN(pp); PIN(pm); PIN(one); PIN(op);
      STAMP(2);
      QGATHER();
      BAR();
      STAMP(4);
      {
        const int ln = 128 * a + 64 * (v >> 1) + l;
        touch((v & 1) ? wu : wg, (unsigned)((ln >> 2) * (FF * 2)) + (unsigned)min(384 * xcd + 128 * (ln & 3), FF * 2 - 4), smb + L_JUNK);
      }
      BAR();
      const f4 m4 = *(LAS f4*)(sm + pm);
      v4u pf[4];
#pragma unroll
      for (int u = 0; u < 4; ++u) pf[u] = *(LAS v4u*)(sm + pp + u * 1024);
      const float M = fmax2(fmax2(m4[0], m4[1]), fmax2(m4[2], m4[3]));
#pragma unroll
      for (int u = 0; u < 4; ++u) {
        const unsigned kk = min((unsigned)(M - m4[u]), 255u) * 0x00800080u;
#pragma unroll
        for (int e = 0; e < 4; ++e) pf[u][e] = scale_dn(pf[u][e], kk);
      }
      f4 ls = z;
#pragma unroll
      for (int u = 0; u < 4; ++u) ls = mfma32(one, pf[u], ls);
      const float il = __builtin_amdgcn_rcpf(ls[0]);
      if (v == 0 && g4 == 0)
        __builtin_amdgcn_raw_buffer_store_b64(v2u{__builtin_bit_cast(unsigned, M + __builtin_amdgcn_logf(ls[0])), T}, rs, ol, 0, AUX_SC1);
#pragma unroll
      for (int q = 0; q < 2; ++q) {
        f4 c[4];
#pragma unroll
        for (int k = 0; k < 4; ++k) c[k] = z;
#pragma unroll
        for (int u = 0; u < 4; ++u)
#pragma unroll
          for (int k = 0; k < 4; ++k) c[k] = mfma32(fr[16 * q + 4 * k + u], pf[u], c[k]);
        v4u R[2];
#pragma unroll
        for (int k = 0; k < 2; ++k) {
          const f4 c0 = c[2 * k] * il, c1 = c[2 * k + 1] * il;
          const unsigned x0 = min(pk(c0[0], c0[1]), 0xfffffffeu), x1 = min(pk(c0[2], c0[3]), 0xfffffffeu);
          const unsigned y0 = min(pk(c1[0], c1[1]), 0xfffffffeu), y1 = min(pk(c1[2], c1[3]), 0xfffffffeu);
          auto r0 = __builtin_amdgcn_permlane16_swap(x0, y0, false, false);
          auto r1 = __builtin_amdgcn_permlane16_swap(x1, y1, false, false);
          R[k] = v4u{r0[0], r1[0], r0[1], r1[1]};
        }
        v4u A, B;
#pragma unroll
        for (int e = 0; e < 4; ++e) {
          A[e] = (unsigned)__builtin_amdgcn_update_dpp((int)R[0][e], (int)R[1][e], ROR8, 0xf, 0xc, false);
          B[e] = (unsigned)__builtin_amdgcn_update_dpp((int)R[1][e], (int)R[0][e], ROR8, 0xf, 0x3, false);
        }
        st_sc1(rs, op + 128 * q, A);
        st_sc1(rs, op + 128 * q + 8 * 65536, B);
      }
      FLAG_SET(v);
      STAMP(6);
    }
    if (a >= 12) {
      unsigned ot = WS_OPT + rd * 8192 + tid * 16, og = WS_OPG + rd * 16384 + tid * 16;
      const bool two = (tid >> 3) < NSL - 64;
      unsigned og2 = two ? og + 8192 : og;
      const unsigned m = (col & 1) ? 0x3f800000u : 0x3f80u;
      v4u sel = {col < 2 ? m : 0u, (col >> 1) == 1 ? m : 0u, col < 2 ? m : 0u, (col >> 1) == 1 ? m : 0u};
      unsigned m2 = two ? ~0u : 0u;
      unsigned prw = L_RR + w * 128 + (l & 7) * 16, prr = L_RR + (l & 31) * 4, oy = ((l >> 4) & 1) * 1024 + rd * 32 + col * 2;
      PIN(ot); PIN(og); PIN(og2); PIN(sel); PIN(m2); PIN(prw); PIN(prr); PIN(oy);
      v4u p0, p1, p2;
      for (unsigned spin = 0;; ++spin) {
        asm volatile(LD16(0, 3, 6) " sc1\n\t" LD16(1, 4, 6) " sc1\n\t" LD16(2, 5, 6) " sc1" WAIT0
                     : "=&v"(p0), "=&v"(p1), "=&v"(p2)
                     : "v"(ot), "v"(og), "v"(og2), "s"(ws)
                     : "memory");
        if (!__builtin_amdgcn_ballot_w64((T & p0.y & p0.w & p1.y & p1.w & p2.y & p2.w) != (T | p0.y | p0.w | p1.y | p1.w | p2.y | p2.w)) ||
            spin > SPIN_CAP)
          break;
      }
      STAMP(8);
      f4 acc = mfma32(sel, v4u{p0.x, p0.z, p1.x, p1.z}, z);
      acc = mfma32(sel, v4u{p2.x & m2, p2.z & m2, 0u, 0u}, acc);
#pragma unroll
      for (int e = 0; e < 4; ++e) acc[e] += dpp<ROR8>(acc[e]);
      if (l < 8) *(LAS f4*)(sm + prw) = acc;
      BAR();
      if (w == 0 && l < 32) {
        float t[8];
#pragma unroll
        for (int i = 0; i < 8; ++i) t[i] = *(LAS float*)(sm + prr + i * 128);
        const float y = blo(xres) + (((t[0] + t[1]) + (t[2] + t[3])) + ((t[4] + t[5]) + (t[6] + t[7])));
        *(u16*)((char*)out + oy) = (u16)(pk(y, y) & 0xffffu);
        if (rd == 0 && l == 0) __builtin_amdgcn_raw_buffer_store_b32(T, rs, WS_E, 0, AUX_SC1);
      }
      STAMP(9);
    }
  }
  STAMP_FLUSH();
}

#define WS_END 0
#define WS_BASE (WS_END ? ((ws_size - (size_t)WS_END * 1048576) & ~(size_t)4095) : 0)
extern "C" void kernel_launch(void* const* d_in, const int* in_sizes, int n_in, void* d_out, int out_size, void* d_ws, size_t ws_size,
                              hipStream_t stream) {
  mla_glu_decode<<<256, 512, 0, stream>>>((const u16*)d_in[0], (const u16*)d_in[1], (const u16*)d_in[2], (const u16*)d_in[3],
                                          (const u16*)d_in[4], (const u16*)d_in[5], (const u16*)d_in[7], (const u16*)d_in[8],
                                          (const u16*)d_in[9], (u16*)d_out, (char*)d_ws + WS_BASE);
}
```

```cpp
#define DRY_A 0
#define A_DELAY 28
#define DRY_F 0
#define DRY_T 0
#define DRY_G 0
#define DRY_R 0
#include <hip/hip_runtime.h>
#include <stdint.h>

#define GAS __attribute__((address_space(1)))
#define LAS __attribute__((address_space(3)))
typedef unsigned short u16;
typedef unsigned v4u __attribute__((ext_vector_type(4)));
typedef unsigned v2u __attribute__((ext_vector_type(2)));
typedef float f4 __attribute__((ext_vector_type(4)));
typedef float f2 __attribute__((ext_vector_type(2)));
typedef __bf16 bf8 __attribute__((ext_vector_type(8)));
typedef __bf16 bf2 __attribute__((ext_vector_type(2)));
typedef short s4 __attribute__((ext_vector_type(4)));

namespace {
constexpr int NH = 32, SK = 4096, DH = 128, DM = 512, DC = 512, FF = 1376;
constexpr int NSL = FF / 16;
constexpr int WS_E = 0;
constexpr int WS_QP = 4096;
constexpr int WS_QA = WS_QP + NH * 4 * 1024;
constexpr int WS_LSE = WS_QA + 2 * NH * 2048;
constexpr int WS_OPT = WS_LSE + 2 * 2 * 32 * 128;
constexpr int WS_OPG = WS_OPT + 32 * 64 * 128;
constexpr int WS_PART = WS_OPG + 32 * 128 * 128;
constexpr int WS_BYTES = WS_PART + NH * 2 * 2 * 32 * 512;
constexpr int WS_STAMP = 7 << 19;
constexpr int CP = 1056;
constexpr int XP = 1088, SP = 288, SP2 = 544;
constexpr int L_C = 0, L_Q = 128 * CP, L_P = L_Q + 16 * CP, L_X = L_P + 4096, L_MX = L_X + 2 * XP, L_RR = L_MX + 256,
              L_JUNK = L_RR + 1024, L_FL = L_JUNK + 256, L_END = L_FL + 128;
static_assert(L_END <= 163840, "LDS");
constexpr int L_GG = 0, L_GD = 32768, L_GR = 65536;
constexpr int L_WUV = 0, L_WO = 65536, L_WQ = 0, L_WUK = 32768, L_CC = 131072, L_CT = L_CC + 8 * SP2, L_W = L_CT + 2 * SP;
#define UK_DELAY 4
#define H1_DELAY 4
#define T_DELAY 30
#define GLU_FLAG_Q 1
#define NOCLEAR 0
#define CODE_LINES 0
#define TLB_WARM 0
#define CLEAR_A 1
#define CLEAR_B 1
#define CLEAR_MODE 0
#define CLEAR_DELAY 0
#define OPT_W 0
#define QA_WARM 0
#define LSE_DELAY 40
#define PF_GLU 1
constexpr unsigned SPIN_CAP = 1u << 20;
constexpr int AUX_SC1 = 16;

#define STAMP_W 0
#define STAMP(i)
#define STAMP_FLUSH()
#define PROBE_T(T_)

__device__ __forceinline__ f4 mfma32(v4u a, v4u b, f4 c) {
  return __builtin_amdgcn_mfma_f32_16x16x32_bf16(__builtin_bit_cast(bf8, a), __builtin_bit_cast(bf8, b), c, 0, 0, 0);
}
__device__ __forceinline__ f4 mfma16(v2u a, v2u b, f4 c) {
  return __builtin_amdgcn_mfma_f32_16x16x16bf16_1k(__builtin_bit_cast(s4, a), __builtin_bit_cast(s4, b), c, 0, 0, 0);
}
__device__ __forceinline__ unsigned pk(float lo, float hi) {
  f2 x = {lo, hi};
  return __builtin_bit_cast(unsigned, __builtin_convertvector(x, bf2));
}
__device__ __forceinline__ float asf(unsigned u) { return __builtin_bit_cast(float, u); }
__device__ __forceinline__ float blo(unsigned u) { return __builtin_bit_cast(float, u << 16); }
__device__ __forceinline__ float bhi(unsigned u) { return __builtin_bit_cast(float, u & 0xffff0000u); }
template <int CTRL> __device__ __forceinline__ float dpp(float v) {
  return __builtin_bit_cast(float, __builtin_amdgcn_update_dpp(0, __builtin_bit_cast(int, v), CTRL, 0xf, 0xf, true));
}
__device__ __forceinline__ float fmax2(float a, float b) {
#pragma float_control(precise, off)
  return __builtin_fmaxf(a, b);
}
typedef unsigned short h2 __attribute__((ext_vector_type(2)));
__device__ __forceinline__ unsigned scale_dn(unsigned p, unsigned kk) {
  return __builtin_bit_cast(unsigned, __builtin_elementwise_sub_sat(__builtin_bit_cast(h2, p), __builtin_bit_cast(h2, kk)));
}
#define PIN(x) asm volatile("" : "+v"(x))
constexpr int ROR1 = 0x121, ROR2 = 0x122, ROR4 = 0x124, ROR8 = 0x128, QP_2301 = 0x4e, QP_2222 = 0xaa;
__device__ __forceinline__ float sw32_add(float a, float b) {
  auto r = __builtin_amdgcn_permlane32_swap(__builtin_bit_cast(unsigned, a), __builtin_bit_cast(unsigned, b), false, false);
  return asf(r[0]) + asf(r[1]);
}
__device__ __forceinline__ float sw16_add(float a, float b) {
  auto r = __builtin_amdgcn_permlane16_swap(__builtin_bit_cast(unsigned, a), __builtin_bit_cast(unsigned, b), false, false);
  return asf(r[0]) + asf(r[1]);
}
__device__ __forceinline__ float vmax(unsigned a, unsigned b) {
  float r;
  asm("v_max_f32 %0, %1, %2" : "=v"(r) : "v"(a), "v"(b));
  return r;
}
__device__ __forceinline__ float sw32_max(float a) {
  auto r = __builtin_amdgcn_permlane32_swap(__builtin_bit_cast(unsigned, a), __builtin_bit_cast(unsigned, a), false, false);
  return vmax(r[0], r[1]);
}
__device__ __forceinline__ float sw16_max(float a) {
  auto r = __builtin_amdgcn_permlane16_swap(__builtin_bit_cast(unsigned, a), __builtin_bit_cast(unsigned, a), false, false);
  return vmax(r[0], r[1]);
}
__device__ __forceinline__ float wave_sum(float v) {
  v += dpp<ROR8>(v);
  v += dpp<ROR4>(v);
  v += dpp<ROR2>(v);
  v += dpp<ROR1>(v);
  v = sw16_add(v, v);
  return sw32_add(v, v);
}
__device__ __forceinline__ v2u trrd(LAS char* p) {
  return __builtin_bit_cast(v2u, __builtin_amdgcn_ds_read_tr16_b64_v4i16((LAS s4*)p));
}
__device__ __forceinline__ v4u trrd2(LAS char* p0, LAS char* p1) {
  v2u a = trrd(p0), b = trrd(p1);
  return v4u{a.x, a.y, b.x, b.y};
}
#define BAR() asm volatile("s_waitcnt lgkmcnt(0)\n\ts_barrier" ::: "memory")
#define VMWAIT(k) asm volatile("s_waitcnt vmcnt(%0)" ::"n"(k) : "memory")
__device__ __forceinline__ void gld16(v4u& d, const void* base, unsigned off) {
  asm volatile("global_load_dwordx4 %0, %1, %2" : "=v"(d) : "v"(off), "s"(base) : "memory");
}
__device__ __forceinline__ void gld4_sc1(unsigned& d, const void* base, unsigned off) {
  asm volatile("global_load_dword %0, %1, %2 sc1" : "=v"(d) : "v"(off), "s"(base) : "memory");
}
__device__ __forceinline__ void touch(const void* base, unsigned off, unsigned lds) {
  asm volatile("s_mov_b32 m0, %2\n\ts_nop 0\n\tglobal_load_lds_dword %0, %1" ::"v"(off), "s"(base), "s"(lds) : "memory");
}
__device__ __forceinline__ void touch_sc1(const void* base, unsigned off, unsigned lds) {
  asm volatile("s_mov_b32 m0, %2\n\ts_nop 0\n\tglobal_load_lds_dword %0, %1 sc1" ::"v"(off), "s"(base), "s"(lds) : "memory");
}
#define LD16(i, a, o) "global_load_dwordx4 %" #i ", %" #a ", %" #o
#define SWEEP_4(q, o, o0, o1, o2, o3, W)                                                                                     \
  asm volatile(LD16(0, 4, 5) " offset:" #o0 " sc1\n\t" LD16(1, 4, 5) " offset:" #o1 " sc1\n\t" LD16(2, 4, 5) " offset:" #o2         \
               " sc1\n\t" LD16(3, 4, 5) " offset:" #o3 " sc1" W                                                               \
               : "=&v"((q)[0]), "=&v"((q)[1]), "=&v"((q)[2]), "=&v"((q)[3])                                                  \
               : "v"(o), "s"(ws)                                                                                             \
               : "memory")
#define RESWEEP_4(q, o, o0, o1, o2, o3)                                                                                      \
  asm volatile(LD16(0, 4, 5) " offset:" #o0 " sc1\n\t" LD16(1, 4, 5) " offset:" #o1 " sc1\n\t" LD16(2, 4, 5) " offset:" #o2         \
               " sc1\n\t" LD16(3, 4, 5) " offset:" #o3 " sc1\n\ts_waitcnt vmcnt(0)"                                            \
               : "+v"((q)[0]), "+v"((q)[1]), "+v"((q)[2]), "+v"((q)[3])                                                      \
               : "v"(o), "s"(ws)                                                                                             \
               : "memory")
#define SWEEP_L(W)                                                                                                          \
  asm volatile("global_load_dwordx2 %0, %3, %5 sc1\n\tglobal_load_dwordx2 %1, %4, %5 sc1\n\t"                              \
               "global_load_dwordx2 %2, %4, %5 offset:512 sc1" W                                                            \
               : "=&v"(la), "=&v"(l0), "=&v"(l1)                                                                            \
               : "v"(ola), "v"(olo), "s"(ws)                                                                                \
               : "memory")
#define SWEEP_2(p, q, oa, ob, W) \
  asm volatile(LD16(0, 2, 4) " sc1\n\t" LD16(1, 3, 4) " sc1" W : "=&v"(p), "=&v"(q) : "v"(oa), "v"(ob), "s"(ws) : "memory")
#define TAGS_BAD4(q) \
  __builtin_amdgcn_ballot_w64((T & (q)[0].y & (q)[0].w & (q)[1].y & (q)[1].w & (q)[2].y & (q)[2].w & (q)[3].y & (q)[3].w) != \
                              (T | (q)[0].y | (q)[0].w | (q)[1].y | (q)[1].w | (q)[2].y | (q)[2].w | (q)[3].y | (q)[3].w))
#define FLAG_SET(i) asm volatile("ds_write_b32 %0, %1" ::"v"(smb + L_FL + 4 * (i)), "v"(1u) : "memory")
#define FLAG_WAIT4(i)                                                                                     \
  for (unsigned spin_ = 0; spin_ < SPIN_CAP; ++spin_) {                                                   \
    v4u f_;                                                                                               \
    asm volatile("ds_read_b128 %0, %1\n\ts_waitcnt lgkmcnt(0)" : "=v"(f_) : "v"(smb + L_FL + 16 * (i)) : "memory"); \
    if (__builtin_amdgcn_readfirstlane(f_.x & f_.y & f_.z & f_.w)) break;                                 \
  }
#define TWICE(first)                                              \
  int live;                                                       \
  asm volatile("s_mov_b32 %0, %1" : "=s"(live) : "s"(first));     \
  do {
#define TWICE_END                                                 \
  asm volatile("s_add_u32 %0, %0, 1" : "+s"(live)::"scc");        \
  }                                                               \
  while (live < 2)
#define UNDEF(x) asm volatile("" : "=v"(x))
#define EPOCH() asm volatile("v_add_u32 %0, 1, %1" : "=v"(T) : "v"(ep) : "memory")
#define WAIT0 "\n\ts_waitcnt vmcnt(0)"
__device__ __forceinline__ void dma16(const void* base, unsigned off, unsigned lds) {
  asm volatile("s_mov_b32 m0, %2\n\ts_nop 0\n\tglobal_load_lds_dwordx4 %0, %1" ::"v"(off), "s"(base), "s"(lds) : "memory");
}
__device__ __forceinline__ v4u ld_sc1(__amdgpu_buffer_rsrc_t rs, int off) {
  return __builtin_amdgcn_raw_buffer_load_b128(rs, off, 0, AUX_SC1);
}
__device__ __forceinline__ v2u ld8_sc1(__amdgpu_buffer_rsrc_t rs, int off) {
  return __builtin_amdgcn_raw_buffer_load_b64(rs, off, 0, AUX_SC1);
}
__device__ __forceinline__ void st_sc1(__amdgpu_buffer_rsrc_t rs, int off, v4u v) {
  __builtin_amdgcn_raw_buffer_store_b128(v, rs, off, 0, AUX_SC1);
}
__device__ __forceinline__ void ln_stats(v4u r, float& mean, float& rstd) {
  float e[8] = {blo(r.x), bhi(r.x), blo(r.y), bhi(r.y), blo(r.z), bhi(r.z), blo(r.w), bhi(r.w)};
  float s = ((e[0] + e[1]) + (e[2] + e[3])) + ((e[4] + e[5]) + (e[6] + e[7]));
  mean = wave_sum(s) * (1.f / DM);
  float q = 0.f;
#pragma unroll
  for (int i = 0; i < 8; ++i) {
    float d = e[i] - mean;
    q = fmaf(d, d, q);
  }
  rstd = __builtin_amdgcn_rsqf(wave_sum(q) * (1.f / DM) + 1e-5f);
}
}

__global__ void __launch_bounds__(512, 2)
mla_glu_decode(const u16* __restrict__ x, const u16* __restrict__ ckv, const u16* __restrict__ wq, const u16* __restrict__ wuk,
               const u16* __restrict__ wuv, const u16* __restrict__ wo, const u16* __restrict__ wg, const u16* __restrict__ wu,
               const u16* __restrict__ wd, u16* __restrict__ out, char* __restrict__ ws) {
  __shared__ __attribute__((aligned(16))) char smem_[L_END];
  LAS char* sm = (LAS char*)smem_;
  const unsigned smb = (unsigned)(size_t)sm;
  const int tid = threadIdx.x, l = tid & 63, w = __builtin_amdgcn_readfirstlane(tid >> 6);
  const int col = l & 15, g4 = l >> 4, tq = (l & 15) >> 2, tp = l & 3;
  const int xcd = blockIdx.x & 7, r = blockIdx.x >> 3;
  const __amdgpu_buffer_rsrc_t rs = __builtin_amdgcn_make_buffer_rsrc(ws, 0, WS_BYTES, 0x00020000);
  asm volatile("" ::"s"(x), "s"(ckv), "s"(wq), "s"(wuk), "s"(wuv), "s"(wo), "s"(wg), "s"(wu), "s"(wd), "s"(out), "s"(ws));
  STAMP(0);

  unsigned ep, T;
  LAS char* const px = sm + L_X + (col & 1) * XP + 16 * g4;
  if ((w >> 1) == 2) dma16(x, (unsigned)((w & 1) * 1024 + l * 16), smb + L_X + (w & 1) * XP);
  const f4 z = {0.f, 0.f, 0.f, 0.f};
#define LAYERNORM()                                                                                                              \
  {                                                                                                                              \
    const v4u xr = *(LAS v4u*)(sm + L_X + (w & 1) * XP + l * 16);                                                                \
    float e[8] = {blo(xr.x), bhi(xr.x), blo(xr.y), bhi(xr.y), blo(xr.z), bhi(xr.z), blo(xr.w), bhi(xr.w)};                       \
    const float mean = wave_sum(((e[0] + e[1]) + (e[2] + e[3])) + ((e[4] + e[5]) + (e[6] + e[7]))) * (1.f / DM);                 \
    float q = 0.f;                                                                                                               \
    _Pragma("unroll") for (int i = 0; i < 8; ++i) {                                                                              \
      e[i] -= mean;                                                                                                              \
      q = fmaf(e[i], e[i], q);                                                                                                   \
    }                                                                                                                            \
    const float rstd = __builtin_amdgcn_rsqf(wave_sum(q) * (1.f / DM) + 1e-5f);                                                  \
    *(LAS v4u*)(sm + L_X + (w & 1) * XP + l * 16) =                                                                              \
        v4u{pk(e[0] * rstd, e[1] * rstd), pk(e[2] * rstd, e[3] * rstd), pk(e[4] * rstd, e[5] * rstd), pk(e[6] * rstd, e[7] * rstd)}; \
  }

  if (__builtin_expect(r < 16, 1)) {
    const int h = 4 * xcd + (r >> 2), j = r & 3, ca = j >> 1, mb = j & 1;
    const int fl = tq | ((g4 & 1) << 2);
    gld4_sc1(ep, ws, 0u);
    if (__builtin_expect(w < 4, 1)) {
      const unsigned vq = (unsigned)(((128 * j + g4) * 4096 + h * DH + 8 * (col ^ ((g4 | ((w & 2) << 1)) << 1))) * 2);
#pragma unroll 1
      for (int k = w; k < 32; k += 4) dma16(wq, vq + (unsigned)k * 32768u, smb + L_WQ + k * 1024);
    } else if (w < 6) {
      VMWAIT(1);
      LAYERNORM();
    } else {
      __builtin_amdgcn_s_sleep(UK_DELAY);
      const unsigned vu = (unsigned)((((128 * j + g4) * NH + h) * DH) * 2);
#pragma unroll 1
      for (int k = w - 6; k < 32; k += 2) dma16(wuk, vu + (unsigned)k * 32768u + 16u * (col ^ ((4 * k + g4) & 15)), smb + L_WUK + k * 1024);
    }
    unsigned p = L_WQ + 256 * (8 * g4 + tq) + 16 * ((2 * w + (tp >> 1)) ^ (fl << 1)) + 8 * (tp & 1);
    unsigned oqp = WS_QP + ((h * 4 + j) * 8 + w) * 128 + col * 64 + g4 * 16;
    unsigned pxj = L_X + (col & 1) * XP + 16 * g4 + 256 * j;
    unsigned o = WS_QP + (h * 4 + (w & 3)) * 1024 + l * 16;
    unsigned pqw = smb + L_CC + (2 * (w & 3) + ((l >> 2) & 1)) * SP + (l >> 3) * 32 + (l & 3) * 8, pqr = L_CC + (col & 7) * SP + 16 * g4;
    unsigned oqa = WS_QA + ((col & 1) * NH + h) * 2048 + (64 * j + 16 * w + 8 * (col >> 1) + 2 * g4) * 8;
    unsigned pu = L_WUK + 256 * (32 * w + col) + 16 * (g4 ^ col);
    PIN(p); PIN(oqp); PIN(pxj); PIN(o); PIN(oqa); PIN(pu); PIN(pqw); PIN(pqr);
    {
    TWICE(DRY_F ? (w >> 2) : 1)
    if (live) {
      if (w < 6) VMWAIT(0);
      else VMWAIT(16);
      EPOCH();
      STAMP(1);
      BAR();
      STAMP(2);
    } else {
      UNDEF(T);
    }
    {
      v4u af[4], xf[4];
#pragma unroll
      for (int s = 0; s < 4; ++s) {
        af[s] = trrd2(sm + p + 256 * 32 * s, sm + p + 256 * (32 * s + 4));
        xf[s] = *(LAS v4u*)(sm + pxj + 64 * s);
      }
      __builtin_amdgcn_sched_barrier(0);
      f4 acc = z;
#pragma unroll
      for (int s = 0; s < 4; ++s) acc = mfma32(af[s], xf[s], acc);
      if (live && col < 2) st_sc1(rs, oqp, v4u{pk(acc[0], acc[1]), T, pk(acc[2], acc[3]), T});
    }
    STAMP(3);
    if (w >= 6) VMWAIT(1);
    if (w >= 4) {
      BAR();
      BAR();
    } else {
      v4u a, uk[8], qf[4];
      if (live) {
        __builtin_amdgcn_s_sleep(H1_DELAY);
        asm volatile(LD16(0, 1, 2) " sc1" : "=&v"(a) : "v"(o), "s"(ws) : "memory");
        BAR();
      } else {
        UNDEF(a);
      }
#pragma unroll
      for (int s = 0; s < 4; ++s) {
        uk[s] = *(LAS v4u*)(sm + (pu ^ (64 * s)));
        uk[4 + s] = *(LAS v4u*)(sm + (pu ^ (64 * s)) + 4096);
      }
      if (live) VMWAIT(0);
      PIN(a);
      for (unsigned spin = 0;; ++spin) {
        if (!__builtin_amdgcn_ballot_w64((T & a.y & a.w) != (T | a.y | a.w)) || spin > SPIN_CAP || !live) break;
        asm volatile(LD16(0, 1, 2) " sc1" WAIT0 : "+v"(a) : "v"(o), "s"(ws) : "memory");
      }
      asm volatile("ds_write2_b32 %0, %1, %2 offset0:0 offset1:1" ::"v"(pqw), "v"(a.x), "v"(a.z) : "memory");
      STAMP(4);
      if (live) BAR();
#pragma unroll
      for (int s = 0; s < 4; ++s) qf[s] = *(LAS v4u*)(sm + pqr + 64 * s);
      __builtin_amdgcn_sched_barrier(0);
      f4 a0 = z, a1 = z;
#pragma unroll
      for (int s = 0; s < 4; ++s) {
        a0 = mfma32(uk[s], qf[s], a0);
        a1 = mfma32(uk[4 + s], qf[s], a1);
      }
      float q[4];
#pragma unroll
      for (int e = 0; e < 4; ++e) {
        const float mine = (col & 2) ? a1[e] : a0[e], other = (col & 2) ? a0[e] : a1[e];
        q[e] = mine + dpp<QP_2301>(other);
        q[e] += dpp<ROR4>(q[e]);
      }
      if (live && col < 4) st_sc1(rs, oqa, v4u{pk(q[0], q[1]), T, pk(q[2], q[3]), T});
    }
    TWICE_END;
    }
    STAMP(5);
    PROBE_T(T);
    BAR();
    __builtin_amdgcn_s_sleep(T_DELAY);
    {
      const unsigned vv = (unsigned)((((256 * ca + g4) * NH + h) * DH + 8 * (col ^ ((g4 | ((w & 2) << 1)) << 1))) * 2);
      const unsigned vo = (unsigned)(((h * DH + (l >> 5)) * DM + 256 * mb) * 2) + 16u * ((l & 31) ^ (((l >> 5) | ((w & 1) << 1) | (w & 4)) << 1));
      int kv = w + 32 * mb, ko = w + 32 * ca;
#pragma unroll 1
      for (int hf = 0; hf < 2; ++hf) {
#pragma unroll 1
        for (int i = 0; i < 4; ++i, kv = (kv + 8) & 63) dma16(wuv, vv + (unsigned)kv * 32768u, smb + L_WUV + kv * 1024);
#pragma unroll 1
        for (int i = 0; i < 4; ++i, ko = (ko + 8) & 63) dma16(wo, vo + (unsigned)ko * 2048u, smb + L_WO + ko * 1024);
        if (hf == 0) asm volatile("s_barrier" ::: "memory");
      }
    }
    const int tb = w >> 2, nq = w & 3;
    o = WS_PART + (((h * 2 + ca) * 2 + tb) * 32 + 8 * nq + g4) * 512 + 16 * col;
    unsigned pw = L_W + (tb * 32 + 8 * nq + g4) * 4;
    unsigned pv = L_WUV + 256 * (8 * g4 + tq) + 16 * ((2 * w + (tp >> 1)) ^ (fl << 1)) + 8 * (tp & 1);
    unsigned po = L_WO + 512 * (8 * g4 + tq) + 16 * ((4 * w + (tp >> 1)) ^ (fl << 1)) + 8 * (tp & 1);
    unsigned pcw = L_CC + (nq * 2 + tb) * SP2 + 16 * col + 8 * g4, pcr = L_CC + (col & 7) * SP2 + 16 * g4;
    unsigned ptw = L_CT + col * SP + (16 * w + 4 * g4) * 2, ptr = L_CT + (col & 1) * SP + 16 * g4;
    unsigned oo = WS_OPT + ((16 * mb + 2 * w + (col >> 1)) * 64 + 2 * h + ca) * 128 + ((col & 1) * 4 + g4) * 16;
    v4u mk;
#pragma unroll
    for (int d = 0; d < 4; ++d) mk[d] = (col >> 1) == d ? ((col & 1) ? 0xffff0000u : 0xffffu) : 0u;
    PIN(o); PIN(pw); PIN(pv); PIN(po); PIN(pcw); PIN(pcr); PIN(ptw); PIN(ptr); PIN(oo); PIN(mk);
    TWICE(DRY_T ? 0 : 1)
    if (w == 7) {
      const unsigned ol = WS_LSE + (((l >> 5) * 2 + (h >> 4)) * 32 + (l & 31)) * 128 + (h & 15) * 8;
      v2u la;
      UNDEF(la);
      for (unsigned spin = 0; live; ++spin) {
        asm volatile("global_load_dwordx2 %0, %1, %2 sc1" WAIT0 : "+v"(la) : "v"(ol), "s"(ws) : "memory");
        if (!__builtin_amdgcn_ballot_w64(la.y != T) || spin > SPIN_CAP) break;
      }
      float M = asf(la.x);
      M = fmax2(M, dpp<ROR8>(M));
      M = fmax2(M, dpp<ROR4>(M));
      M = fmax2(M, dpp<ROR2>(M));
      M = fmax2(M, dpp<ROR1>(M));
      M = sw16_max(M);
      const float e = __builtin_amdgcn_exp2f(asf(la.x) - M);
      float L = e + dpp<ROR8>(e);
      L += dpp<ROR4>(L);
      L += dpp<ROR2>(L);
      L += dpp<ROR1>(L);
      L = sw16_add(L, L);
      const float wt = e * __builtin_amdgcn_rcpf(L);
      *(LAS unsigned*)(sm + L_W + 4 * l) = pk(wt, wt);
      STAMP(7);
    }
    v4u a[4];
    if (live) {
      VMWAIT(0);
      STAMP(6);
      BAR();
      SWEEP_4(a, o, 0, 256, 2048, 2304, "");
    } else {
      UNDEF(a[0]); UNDEF(a[1]); UNDEF(a[2]); UNDEF(a[3]);
    }
    v4u A0 = mk & *(LAS unsigned*)(sm + pw), A1 = mk & *(LAS unsigned*)(sm + pw + 16);
    v4u av[8], ao[8];
#pragma unroll
    for (int s = 0; s < 8; ++s) av[s] = trrd2(sm + pv + 256 * 32 * s, sm + pv + 256 * (32 * s + 4));
#pragma unroll
    for (int t = 0; t < 2; ++t)
#pragma unroll
      for (int s = 0; s < 4; ++s) ao[4 * t + s] = trrd2(sm + (po ^ (32 * t)) + 512 * 32 * s, sm + (po ^ (32 * t)) + 512 * (32 * s + 4));
    if (live) VMWAIT(0);
    asm volatile("" : "+v"(a[0]), "+v"(a[1]), "+v"(a[2]), "+v"(a[3]), "+v"(A0), "+v"(A1)::"memory");
    for (unsigned spin = 0;; ++spin) {
      unsigned mx = 0u;
#pragma unroll
      for (int i = 0; i < 4; ++i) mx = max(max(mx, max(a[i].x, a[i].y)), max(a[i].z, a[i].w));
      if (!__builtin_amdgcn_ballot_w64(mx == ~0u) || spin > SPIN_CAP || !live) break;
      RESWEEP_4(a, o, 0, 256, 2048, 2304);
    }
    STAMP(8);
    {
      f4 c0 = mfma32(A0, a[0], z), c1 = mfma32(A0, a[1], z);
      c0 = mfma32(A1, a[2], c0);
      c1 = mfma32(A1, a[3], c1);
      if (g4 < 2) {
        *(LAS v2u*)(sm + pcw) = v2u{pk(c0[0], c0[1]), pk(c0[2], c0[3])};
        *(LAS v2u*)(sm + pcw + 256) = v2u{pk(c1[0], c1[1]), pk(c1[2], c1[3])};
      }
    }
    if (live) BAR();
    {
      f4 acc = z;
      v4u bf[8];
#pragma unroll
      for (int s = 0; s < 8; ++s) bf[s] = *(LAS v4u*)(sm + pcr + 64 * s);
      __builtin_amdgcn_sched_barrier(0);
#pragma unroll
      for (int s = 0; s < 8; ++s) acc = mfma32(av[s], bf[s], acc);
#pragma unroll
      for (int e = 0; e < 4; ++e) {
        acc[e] += dpp<QP_2301>(acc[e]);
        acc[e] += dpp<ROR4>(acc[e]);
      }
      if (col < 2) *(LAS v2u*)(sm + ptw) = v2u{pk(acc[0], acc[1]), pk(acc[2], acc[3])};
    }
    if (live) BAR();
    {
      f4 c0 = z, c1 = z;
      v4u bf[4];
#pragma unroll
      for (int s = 0; s < 4; ++s) bf[s] = *(LAS v4u*)(sm + ptr + 64 * s);
      __builtin_amdgcn_sched_barrier(0);
#pragma unroll
      for (int s = 0; s < 4; ++s) {
        c0 = mfma32(ao[s], bf[s], c0);
        c1 = mfma32(ao[4 + s], bf[s], c1);
      }
      if (col & 2) c0 = c1;
      if (live && col < 4) st_sc1(rs, oo, v4u{pk(c0[0], c0[1]), T, pk(c0[2], c0[3]), T});
    }
    TWICE_END;
    STAMP(9);
  } else {
    const int a = r - 16, b = xcd >> 2, n = 8 * (xcd & 3) + (a >> 1), g = a & 1;
    const int sl = 12 * xcd + a, rd = (4 * xcd + a) & 31;
    const bool glu = a < 12 && sl < NSL;
    const int v = w & 3;
    unsigned op = WS_PART + ((((16 * g + (col & 7)) * 2 + (v >> 1)) * 2 + b) * 32 + n) * 512 + 256 * (v & 1) + (col >> 3) * 64 +
                  (g4 & 1) * 32 + (g4 >> 1) * 16;
    unsigned xres;
    gld4_sc1(ep, ws, 0u);
    asm volatile("global_load_ushort %0, %1, %2" : "=v"(xres) : "v"((unsigned)(((l >> 4) & 1) * 1024 + rd * 32 + col * 2)), "s"(x) : "memory");
    if (w >= 4) {
#pragma unroll
      for (int q = 0; q < 4; ++q) st_sc1(rs, op + 128 * (q >> 1) + (q & 1) * 8 * 65536, v4u{~0u, ~0u, ~0u, ~0u});
      if (w == 7 && l < 32) *(LAS unsigned*)(sm + L_FL + 4 * l) = 0u;
    }
    unsigned o = WS_QA + (b * NH + 16 * g) * 2048 + w * 4096 + l * 16, pqs = smb + L_Q + 2 * w * CP + l * 8;
    PIN(o); PIN(pqs);
    {
    TWICE(DRY_A ? 0 : 1)
    if (live) {
      const unsigned vc = (unsigned)(((b * SK + 128 * n) * DC + 8 * l) * 2);
      STAMP(10);
      __builtin_amdgcn_s_sleep(A_DELAY);
#pragma unroll 1
      for (int q = 0; q < 4; ++q) {
        int row = w + ((64 * g + 32 * q) & 127);
        asm volatile("s_barrier" ::: "memory");
#pragma unroll 1
        for (int i = 0; i < 4; ++i, row += 8) dma16(ckv, vc + row * 1024u, smb + L_C + row * CP);
      }
      if ((w >> 1) == 2) {
        VMWAIT(22);
        LAYERNORM();
      }
      VMWAIT(0);
      EPOCH();
      PROBE_T(T);
      BAR();
      STAMP(1);
    } else {
      UNDEF(T);
    }
#define QISSUE()                                \
  if (live) {                                   \
    SWEEP_4(qg, o, 0, 1024, 2048, 3072, "");    \
  } else {                                      \
    UNDEF(qg[0]); UNDEF(qg[1]); UNDEF(qg[2]); UNDEF(qg[3]); \
  }
#define QGATHER()                                                                                                           \
  if (live) VMWAIT(0);                                                                                                      \
  asm volatile("" : "+v"(qg[0]), "+v"(qg[1]), "+v"(qg[2]), "+v"(qg[3])::"memory");                                          \
  for (unsigned spin = 0;; ++spin) {                                                                                        \
    if (!TAGS_BAD4(qg) || spin > SPIN_CAP || !live) break;                                                                           \
    RESWEEP_4(qg, o, 0, 1024, 2048, 3072);                                                                             \
  }                                                                                                                         \
  STAMP(3);                                                                                                                 \
  asm volatile("ds_write2_b32 %0, %2, %3 offset0:0 offset1:1\n\tds_write2_b32 %0, %4, %5 offset0:128 offset1:129\n\t"        \
               "ds_write2_b32 %1, %6, %7 offset0:0 offset1:1\n\tds_write2_b32 %1, %8, %9 offset0:128 offset1:129" ::"v"(pqs), \
               "v"(pqs + CP), "v"(qg[0].x), "v"(qg[0].z), "v"(qg[1].x), "v"(qg[1].z), "v"(qg[2].x), "v"(qg[2].z), "v"(qg[3].x), \
               "v"(qg[3].z)                                                                                                 \
               : "memory")
    if (__builtin_expect(w < 4, 1)) {
      v4u fr[32], qg[4];
      QISSUE();
      LAS char* pc = sm + L_C + (32 * w + col) * CP + 16 * g4;
#pragma unroll
      for (int kt = 0; kt < 2; ++kt)
#pragma unroll
        for (int s = 0; s < 16; ++s) fr[16 * kt + s] = *(LAS v4u*)(pc + 16 * kt * CP + 64 * s);
      unsigned p = L_Q + col * CP + 16 * g4, pp = L_P + w * 1024 + l * 16, pm = L_MX + col * 16 + w * 4;
      PIN(p); PIN(pp); PIN(pm);
      STAMP(2);
      QGATHER();
      if (live) BAR();
      STAMP(4);
      f4 s0 = z, s1 = z;
      v4u qf[16];
#pragma unroll
      for (int s = 0; s < 6; ++s) qf[s] = *(LAS v4u*)(sm + p + 64 * s);
      __builtin_amdgcn_sched_barrier(0);
#pragma unroll
      for (int s = 0; s < 16; ++s) {
        if (s + 6 < 16) qf[s + 6] = *(LAS v4u*)(sm + p + 64 * (s + 6));
        s0 = mfma32(fr[s], qf[s], s0);
        s1 = mfma32(fr[16 + s], qf[s], s1);
        __builtin_amdgcn_sched_barrier(0);
      }
      float mx = fmax2(fmax2(fmax2(s0[0], s0[1]), fmax2(s0[2], s0[3])), fmax2(fmax2(s1[0], s1[1]), fmax2(s1[2], s1[3])));
      mx = sw32_max(sw16_max(mx));
      constexpr float SC = 0.08838834764831845f * 1.4426950408889634f;
      const float mi = __builtin_ceilf(mx * SC);
#pragma unroll
      for (int e = 0; e < 4; ++e) {
        s0[e] = __builtin_amdgcn_exp2f(fmaf(s0[e], SC, -mi));
        s1[e] = __builtin_amdgcn_exp2f(fmaf(s1[e], SC, -mi));
      }
      *(LAS float*)(sm + pm) = mi;
      *(LAS v4u*)(sm + pp) = v4u{pk(s0[0], s0[1]), pk(s0[2], s0[3]), pk(s1[0], s1[1]), pk(s1[2], s1[3])};
      if (live) BAR();
      STAMP(5);
    } else {
      v4u fr[32], qg[4];
      QISSUE();
      LAS char* p0 = sm + L_C + (4 * g4 + tq) * CP + 256 * v + 8 * tp;
      unsigned p1 = L_C + (64 + 4 * g4 + tq) * CP + 256 * v + 8 * tp;
      PIN(p1);
#pragma unroll
      for (int ct = 0; ct < 8; ++ct)
#pragma unroll
        for (int u = 0; u < 4; ++u) {
          LAS char* p = (u < 2 ? p0 : sm + p1) + 32 * (u & 1) * CP + 32 * ct;
          fr[4 * ct + u] = trrd2(p, p + 16 * CP);
        }
      unsigned ol = WS_LSE + ((b * 2 + g) * 32 + n) * 128 + col * 8, pp = L_P + l * 16, pm = L_MX + col * 16;
      v4u one = {0x3f803f80u, 0x3f803f80u, 0x3f803f80u, 0x3f803f80u};
      PIN(ol); PIN(pp); PIN(pm); PIN(one); PIN(op);
      STAMP(2);
      QGATHER();
      if (live) BAR();
      STAMP(4);
      if (live) {
        const int ln = 128 * a + 64 * (v >> 1) + l;
        touch((v & 1) ? wu : wg, (unsigned)((ln >> 2) * (FF * 2)) + (unsigned)min(384 * xcd + 128 * (ln & 3), FF * 2 - 4), smb + L_JUNK);
      }
      if (live) BAR();
      const f4 m4 = *(LAS f4*)(sm + pm);
      v4u pf[4];
#pragma unroll
      for (int u = 0; u < 4; ++u) pf[u] = *(LAS v4u*)(sm + pp + u * 1024);
      const float M = fmax2(fmax2(m4[0], m4[1]), fmax2(m4[2], m4[3]));
#pragma unroll
      for (int u = 0; u < 4; ++u) {
        const unsigned kk = min((unsigned)(M - m4[u]), 255u) * 0x00800080u;
#pragma unroll
        for (int e = 0; e < 4; ++e) pf[u][e] = scale_dn(pf[u][e], kk);
      }
      f4 ls = z;
#pragma unroll
      for (int u = 0; u < 4; ++u) ls = mfma32(one, pf[u], ls);
      const float il = __builtin_amdgcn_rcpf(ls[0]);
      if (live && v == 0 && g4 == 0)
        __builtin_amdgcn_raw_buffer_store_b64(v2u{__builtin_bit_cast(unsigned, M + __builtin_amdgcn_logf(ls[0])), T}, rs, ol, 0, AUX_SC1);
#pragma unroll
      for (int q = 0; q < 2; ++q) {
        f4 c[4];
#pragma unroll
        for (int k = 0; k < 4; ++k) c[k] = z;
#pragma unroll
        for (int u = 0; u < 4; ++u)
#pragma unroll
          for (int k = 0; k < 4; ++k) c[k] = mfma32(fr[16 * q + 4 * k + u], pf[u], c[k]);
        v4u R[2];
#pragma unroll
        for (int k = 0; k < 2; ++k) {
          const f4 c0 = c[2 * k] * il, c1 = c[2 * k + 1] * il;
          const unsigned x0 = min(pk(c0[0], c0[1]), 0xfffffffeu), x1 = min(pk(c0[2], c0[3]), 0xfffffffeu);
          const unsigned y0 = min(pk(c1[0], c1[1]), 0xfffffffeu), y1 = min(pk(c1[2], c1[3]), 0xfffffffeu);
          auto r0 = __builtin_amdgcn_permlane16_swap(x0, y0, false, false);
          auto r1 = __builtin_amdgcn_permlane16_swap(x1, y1, false, false);
          R[k] = v4u{r0[0], r1[0], r0[1], r1[1]};
        }
        v4u A, B;
#pragma unroll
        for (int e = 0; e < 4; ++e) {
          A[e] = (unsigned)__builtin_amdgcn_update_dpp((int)R[0][e], (int)R[1][e], ROR8, 0xf, 0xc, false);
          B[e] = (unsigned)__builtin_amdgcn_update_dpp((int)R[1][e], (int)R[0][e], ROR8, 0xf, 0x3, false);
        }
        PIN(A); PIN(B);
        if (live) {
          st_sc1(rs, op + 128 * q, A);
          st_sc1(rs, op + 128 * q + 8 * 65536, B);
        }
      }
      if (live) FLAG_SET(v);
      STAMP(6);
    }
    TWICE_END;
    }
      if (glu && w < 4) {
        v4u af[8], xf[8];
#pragma unroll
        for (int s = 0; s < 8; ++s) xf[s] = *(LAS v4u*)(px + 512 * (w >> 1) + 64 * s);
        unsigned oog = WS_OPG + ((8 * w + (col >> 1)) * 128 + sl) * 128 + ((col & 1) * 4 + g4) * 16;
        PIN(oog);
        FLAG_WAIT4(0);
        unsigned og = (unsigned)(((128 * w + (l >> 1)) * FF + 16 * sl + 8 * (l & 1)) * 2);
#pragma unroll 1
        for (int i = 0; i < 4; ++i, og += 32 * FF * 2) {
          dma16(wg, og, smb + L_GG + (4 * w + i) * 1024);
          dma16(wu, og, smb + L_GG + 16384 + (4 * w + i) * 1024);
        }
#pragma unroll 1
        for (int i = 0; i < 4; ++i) dma16(wd, (unsigned)(((16 * sl + 4 * w + i) * DM + 8 * l) * 2), smb + L_GD + (4 * w + i) * CP);
        TWICE(DRY_G ? 0 : 1)
        if (live) {
          VMWAIT(4);
          FLAG_SET(4 + w);
          FLAG_WAIT4(1);
        }
        STAMP(7);
        LAS char* pg = sm + L_GG + (w & 1) * 16384 + 32 * (256 * (w >> 1) + 8 * g4 + tq) + 8 * tp;
        f4 acc = z;
#pragma unroll
        for (int s = 0; s < 8; ++s) af[s] = trrd2(pg + 32 * 32 * s, pg + 32 * (32 * s + 4));
        __builtin_amdgcn_sched_barrier(0);
#pragma unroll
        for (int s = 0; s < 8; ++s) acc = mfma32(af[s], xf[s], acc);
        *(LAS f4*)(sm + L_GR + ((w & 1) * 2 + (w >> 1)) * 1024 + l * 16) = acc;
        if (live) {
          VMWAIT(0);
          FLAG_SET(8 + w);
          FLAG_WAIT4(2);
        }
        v2u dn[8];
#pragma unroll
        for (int t = 0; t < 8; ++t) dn[t] = trrd(sm + L_GD + (4 * g4 + tq) * CP + 32 * (8 * w + t) + 8 * tp);
        LAS char* pr = sm + L_GR + l * 16;
        const f4 gx = *(LAS f4*)(pr) + *(LAS f4*)(pr + 1024), ux = *(LAS f4*)(pr + 2048) + *(LAS f4*)(pr + 3072);
        float hh[4];
#pragma unroll
        for (int e = 0; e < 4; ++e) hh[e] = ux[e] * __builtin_amdgcn_rcpf(1.f + __builtin_amdgcn_exp2f(-1.4426950408889634f * gx[e]));
        const v2u hb = {pk(hh[0], hh[1]), pk(hh[2], hh[3])};
        f4 d = z;
#pragma unroll
        for (int t = 0; t < 8; ++t) d = mfma16(dn[t], (col >> 1) == t ? hb : v2u{0u, 0u}, d);
        v4u dv = {pk(d[0], d[1]), T, pk(d[2], d[3]), T};
        PIN(dv);
        if (live) st_sc1(rs, oog, dv);
        TWICE_END;
        STAMP(9);
      }
    if (a >= 12) {
      unsigned ot = WS_OPT + rd * 8192 + tid * 16, og = WS_OPG + rd * 16384 + tid * 16;
      const bool two = (tid >> 3) < NSL - 64;
      unsigned og2 = two ? og + 8192 : og;
      const unsigned m = (col & 1) ? 0x3f800000u : 0x3f80u;
      v4u sel = {col < 2 ? m : 0u, (col >> 1) == 1 ? m : 0u, col < 2 ? m : 0u, (col >> 1) == 1 ? m : 0u};
      unsigned m2 = two ? ~0u : 0u;
      unsigned prw = L_RR + w * 128 + (l & 7) * 16, prr = L_RR + (l & 31) * 4, oy = ((l >> 4) & 1) * 1024 + rd * 32 + col * 2;
      PIN(ot); PIN(og); PIN(og2); PIN(sel); PIN(m2); PIN(prw); PIN(prr); PIN(oy);
      PIN(xres);
      TWICE(DRY_R ? 0 : 1)
      v4u p0, p1, p2;
      UNDEF(p0); UNDEF(p1); UNDEF(p2);
      for (unsigned spin = 0; live; ++spin) {
        asm volatile(LD16(0, 3, 6) " sc1\n\t" LD16(1, 4, 6) " sc1\n\t" LD16(2, 5, 6) " sc1" WAIT0
                     : "+v"(p0), "+v"(p1), "+v"(p2)
                     : "v"(ot), "v"(og), "v"(og2), "s"(ws)
                     : "memory");
        if (!__builtin_amdgcn_ballot_w64((T & p0.y & p0.w & p1.y & p1.w & p2.y & p2.w) != (T | p0.y | p0.w | p1.y | p1.w | p2.y | p2.w)) ||
            spin > SPIN_CAP)
          break;
      }
      STAMP(8);
      f4 acc = mfma32(sel, v4u{p0.x, p0.z, p1.x, p1.z}, z);
      acc = mfma32(sel, v4u{p2.x & m2, p2.z & m2, 0u, 0u}, acc);
#pragma unroll
      for (int e = 0; e < 4; ++e) acc[e] += dpp<ROR8>(acc[e]);
      if (l < 8) *(LAS f4*)(sm + prw) = acc;
      if (live) BAR();
      if (w == 0 && l < 32) {
        float t[8];
#pragma unroll
        for (int i = 0; i < 8; ++i) t[i] = *(LAS float*)(sm + prr + i * 128);
        const float y = blo(xres) + (((t[0] + t[1]) + (t[2] + t[3])) + ((t[4] + t[5]) + (t[6] + t[7])));
        unsigned yb = pk(y, y);
        PIN(yb);
        if (live) *(u16*)((char*)out + oy) = (u16)yb;
        if (live && rd == 0 && l == 0) __builtin_amdgcn_raw_buffer_store_b32(T, rs, WS_E, 0, AUX_SC1);
      }
      TWICE_END;
      STAMP(9);
    }
  }
  STAMP_FLUSH();
}

#define WS_END 0
#define WS_BASE (WS_END ? ((ws_size - (size_t)WS_END * 1048576) & ~(size_t)4095) : 0)
extern "C" void kernel_launch(void* const* d_in, const int* in_sizes, int n_in, void* d_out, int out_size, void* d_ws, size_t ws_size,
                              hipStream_t stream) {
  mla_glu_decode<<<256, 512, 0, stream>>>((const u16*)d_in[0], (const u16*)d_in[1], (const u16*)d_in[2], (const u16*)d_in[3],
                                          (const u16*)d_in[4], (const u16*)d_in[5], (const u16*)d_in[7], (const u16*)d_in[8],
                                          (const u16*)d_in[9], (u16*)d_out, (char*)d_ws + WS_BASE);
}
```

```cpp
#define PROBE_CLASS 1
#define PROBE_IDX 10
#define PROBE_WAVE 0
#define PROBE_K 10
#define DRY_A 0
#define A_DELAY 28
#define DRY_F 0
#define DRY_T 0
#define DRY_G 0
#define DRY_R 0
#include <hip/hip_runtime.h>
#include <stdint.h>

#define GAS __attribute__((address_space(1)))
#define LAS __attribute__((address_space(3)))
typedef unsigned short u16;
typedef unsigned v4u __attribute__((ext_vector_type(4)));
typedef unsigned v2u __attribute__((ext_vector_type(2)));
typedef float f4 __attribute__((ext_vector_type(4)));
typedef float f2 __attribute__((ext_vector_type(2)));
typedef __bf16 bf8 __attribute__((ext_vector_type(8)));
typedef __bf16 bf2 __attribute__((ext_vector_type(2)));
typedef short s4 __attribute__((ext_vector_type(4)));

namespace {
constexpr int NH = 32, SK = 4096, DH = 128, DM = 512, DC = 512, FF = 1376;
constexpr int NSL = FF / 16;
constexpr int WS_E = 0;
constexpr int WS_QP = 4096;
constexpr int WS_QA = WS_QP + NH * 4 * 1024;
constexpr int WS_LSE = WS_QA + 2 * NH * 2048;
constexpr int WS_OPT = WS_LSE + 2 * 2 * 32 * 128;
constexpr int WS_OPG = WS_OPT + 32 * 64 * 128;
constexpr int WS_PART = WS_OPG + 32 * 128 * 128;
constexpr int WS_BYTES = WS_PART + NH * 2 * 2 * 32 * 512;
constexpr int WS_STAMP = 7 << 19;
constexpr int CP = 1056;
constexpr int XP = 1088, SP = 288, SP2 = 544;
constexpr int L_C = 0, L_Q = 128 * CP, L_P = L_Q + 16 * CP, L_X = L_P + 4096, L_MX = L_X + 2 * XP, L_RR = L_MX + 256,
              L_JUNK = L_RR + 1024, L_FL = L_JUNK + 256, L_END = L_FL + 128;
static_assert(L_END <= 163840, "LDS");
constexpr int L_GG = 0, L_GD = 32768, L_GR = 65536;
constexpr int L_WUV = 0, L_WO = 65536, L_WQ = 0, L_WUK = 32768, L_CC = 131072, L_CT = L_CC + 8 * SP2, L_W = L_CT + 2 * SP;
#define UK_DELAY 4
#define H1_DELAY 4
#define T_DELAY 30
#define GLU_FLAG_Q 1
#define NOCLEAR 0
#define CODE_LINES 0
#define TLB_WARM 0
#define CLEAR_A 1
#define CLEAR_B 1
#define CLEAR_MODE 0
#define CLEAR_DELAY 0
#define OPT_W 0
#define QA_WARM 0
#define LSE_DELAY 40
#define PF_GLU 1
constexpr unsigned SPIN_CAP = 1u << 20;
constexpr int AUX_SC1 = 16;

#define STAMPS
#define STAMP_W 0
#define STAMP(i)                                                                          \
  do {                                                                                    \
    unsigned long long t__;                                                               \
    asm volatile("s_memrealtime %0\n\ts_waitcnt lgkmcnt(0)" : "=s"(t__)::"memory");       \
    asm volatile("v_writelane_b32 %0, %1, " #i : "+v"(tsv) : "s"((unsigned)t__));         \
  } while (0)
#define STAMP_FLUSH()                                                                      \
  do {                                                                                     \
    const int cls_ = r < 16 ? 0 : (r < 28 ? 1 : 2);                                        \
    if ((cls_ == PROBE_CLASS || PROBE_CLASS == 3) && w == PROBE_WAVE && __builtin_amdgcn_readlane(tsv, PROBE_IDX) != 0) {   \
      v4u g_;                                                                              \
      for (unsigned sp_ = 0; sp_ < SPIN_CAP; ++sp_) { asm volatile("" ::: "memory"); g_ = ld_sc1(rs, 2048); if (g_.z == Tg) break; } \
      const unsigned long long t0_ = ((unsigned long long)__builtin_amdgcn_readfirstlane(g_.y) << 32) | (unsigned)__builtin_amdgcn_readfirstlane(g_.x); \
      const long long d_ = (int)(__builtin_amdgcn_readlane(tsv, PROBE_IDX) - (unsigned)t0_);                               \
      const unsigned long long until_ = t0_ + 4000ull + PROBE_K * d_;                      \
      while (__builtin_amdgcn_s_memrealtime() < until_) __builtin_amdgcn_s_sleep(1);       \
    }                                                                                      \
  } while (0)
#define PROBE_T(T_) do { Tg = T_; if (blockIdx.x == 0 && tid == 0) st_sc1(rs, 2048, v4u{(unsigned)tstart, (unsigned)(tstart >> 32), T_, 0u}); } while (0)

__device__ __forceinline__ f4 mfma32(v4u a, v4u b, f4 c) {
  return __builtin_amdgcn_mfma_f32_16x16x32_bf16(__builtin_bit_cast(bf8, a), __builtin_bit_cast(bf8, b), c, 0, 0, 0);
}
__device__ __forceinline__ f4 mfma16(v2u a, v2u b, f4 c) {
  return __builtin_amdgcn_mfma_f32_16x16x16bf16_1k(__builtin_bit_cast(s4, a), __builtin_bit_cast(s4, b), c, 0, 0, 0);
}
__device__ __forceinline__ unsigned pk(float lo, float hi) {
  f2 x = {lo, hi};
  return __builtin_bit_cast(unsigned, __builtin_convertvector(x, bf2));
}
__device__ __forceinline__ float asf(unsigned u) { return __builtin_bit_cast(float, u); }
__device__ __forceinline__ float blo(unsigned u) { return __builtin_bit_cast(float, u << 16); }
__device__ __forceinline__ float bhi(unsigned u) { return __builtin_bit_cast(float, u & 0xffff0000u); }
template <int CTRL> __device__ __forceinline__ float dpp(float v) {
  return __builtin_bit_cast(float, __builtin_amdgcn_update_dpp(0, __builtin_bit_cast(int, v), CTRL, 0xf, 0xf, true));
}
__device__ __forceinline__ float fmax2(float a, float b) {
#pragma float_control(precise, off)
  return __builtin_fmaxf(a, b);
}
typedef unsigned short h2 __attribute__((ext_vector_type(2)));
__device__ __forceinline__ unsigned scale_dn(unsigned p, unsigned kk) {
  return __builtin_bit_cast(unsigned, __builtin_elementwise_sub_sat(__builtin_bit_cast(h2, p), __builtin_bit_cast(h2, kk)));
}
#define PIN(x) asm volatile("" : "+v"(x))
constexpr int ROR1 = 0x121, ROR2 = 0x122, ROR4 = 0x124, ROR8 = 0x128, QP_2301 = 0x4e, QP_2222 = 0xaa;
__device__ __forceinline__ float sw32_add(float a, float b) {
  auto r = __builtin_amdgcn_permlane32_swap(__builtin_bit_cast(unsigned, a), __builtin_bit_cast(unsigned, b), false, false);
  return asf(r[0]) + asf(r[1]);
}
__device__ __forceinline__ float sw16_add(float a, float b) {
  auto r = __builtin_amdgcn_permlane16_swap(__builtin_bit_cast(unsigned, a), __builtin_bit_cast(unsigned, b), false, false);
  return asf(r[0]) + asf(r[1]);
}
__device__ __forceinline__ float vmax(unsigned a, unsigned b) {
  float r;
  asm("v_max_f32 %0, %1, %2" : "=v"(r) : "v"(a), "v"(b));
  return r;
}
__device__ __forceinline__ float sw32_max(float a) {
  auto r = __builtin_amdgcn_permlane32_swap(__builtin_bit_cast(unsigned, a), __builtin_bit_cast(unsigned, a), false, false);
  return vmax(r[0], r[1]);
}
__device__ __forceinline__ float sw16_max(float a) {
  auto r = __builtin_amdgcn_permlane16_swap(__builtin_bit_cast(unsigned, a), __builtin_bit_cast(unsigned, a), false, false);
  return vmax(r[0], r[1]);
}
__device__ __forceinline__ float wave_sum(float v) {
  v += dpp<ROR8>(v);
  v += dpp<ROR4>(v);
  v += dpp<ROR2>(v);
  v += dpp<ROR1>(v);
  v = sw16_add(v, v);
  return sw32_add(v, v);
}
__device__ __forceinline__ v2u trrd(LAS char* p) {
  return __builtin_bit_cast(v2u, __builtin_amdgcn_ds_read_tr16_b64_v4i16((LAS s4*)p));
}
__device__ __forceinline__ v4u trrd2(LAS char* p0, LAS char* p1) {
  v2u a = trrd(p0), b = trrd(p1);
  return v4u{a.x, a.y, b.x, b.y};
}
#define BAR() asm volatile("s_waitcnt lgkmcnt(0)\n\ts_barrier" ::: "memory")
#define VMWAIT(k) asm volatile("s_waitcnt vmcnt(%0)" ::"n"(k) : "memory")
__device__ __forceinline__ void gld16(v4u& d, const void* base, unsigned off) {
  asm volatile("global_load_dwordx4 %0, %1, %2" : "=v"(d) : "v"(off), "s"(base) : "memory");
}
__device__ __forceinline__ void gld4_sc1(unsigned& d, const void* base, unsigned off) {
  asm volatile("global_load_dword %0, %1, %2 sc1" : "=v"(d) : "v"(off), "s"(base) : "memory");
}
__device__ __forceinline__ void touch(const void* base, unsigned off, unsigned lds) {
  asm volatile("s_mov_b32 m0, %2\n\ts_nop 0\n\tglobal_load_lds_dword %0, %1" ::"v"(off), "s"(base), "s"(lds) : "memory");
}
__device__ __forceinline__ void touch_sc1(const void* base, unsigned off, unsigned lds) {
  asm volatile("s_mov_b32 m0, %2\n\ts_nop 0\n\tglobal_load_lds_dword %0, %1 sc1" ::"v"(off), "s"(base), "s"(lds) : "memory");
}
#define LD16(i, a, o) "global_load_dwordx4 %" #i ", %" #a ", %" #o
#define SWEEP_4(q, o, o0, o1, o2, o3, W)                                                                                     \
  asm volatile(LD16(0, 4, 5) " offset:" #o0 " sc1\n\t" LD16(1, 4, 5) " offset:" #o1 " sc1\n\t" LD16(2, 4, 5) " offset:" #o2         \
               " sc1\n\t" LD16(3, 4, 5) " offset:" #o3 " sc1" W                                                               \
               : "=&v"((q)[0]), "=&v"((q)[1]), "=&v"((q)[2]), "=&v"((q)[3])                                                  \
               : "v"(o), "s"(ws)                                                                                             \
               : "memory")
#define RESWEEP_4(q, o, o0, o1, o2, o3)                                                                                      \
  asm volatile(LD16(0, 4, 5) " offset:" #o0 " sc1\n\t" LD16(1, 4, 5) " offset:" #o1 " sc1\n\t" LD16(2, 4, 5) " offset:" #o2         \
               " sc1\n\t" LD16(3, 4, 5) " offset:" #o3 " sc1\n\ts_waitcnt vmcnt(0)"                                            \
               : "+v"((q)[0]), "+v"((q)[1]), "+v"((q)[2]), "+v"((q)[3])                                                      \
               : "v"(o), "s"(ws)                                                                                             \
               : "memory")
#define SWEEP_L(W)                                                                                                          \
  asm volatile("global_load_dwordx2 %0, %3, %5 sc1\n\tglobal_load_dwordx2 %1, %4, %5 sc1\n\t"                              \
               "global_load_dwordx2 %2, %4, %5 offset:512 sc1" W                                                            \
               : "=&v"(la), "=&v"(l0), "=&v"(l1)                                                                            \
               : "v"(ola), "v"(olo), "s"(ws)                                                                                \
               : "memory")
#define SWEEP_2(p, q, oa, ob, W) \
  asm volatile(LD16(0, 2, 4) " sc1\n\t" LD16(1, 3, 4) " sc1" W : "=&v"(p), "=&v"(q) : "v"(oa), "v"(ob), "s"(ws) : "memory")
#define TAGS_BAD4(q) \
  __builtin_amdgcn_ballot_w64((T & (q)[0].y & (q)[0].w & (q)[1].y & (q)[1].w & (q)[2].y & (q)[2].w & (q)[3].y & (q)[3].w) != \
                              (T | (q)[0].y | (q)[0].w | (q)[1].y | (q)[1].w | (q)[2].y | (q)[2].w | (q)[3].y | (q)[3].w))
#define FLAG_SET(i) asm volatile("ds_write_b32 %0, %1" ::"v"(smb + L_FL + 4 * (i)), "v"(1u) : "memory")
#define FLAG_WAIT4(i)                                                                                     \
  for (unsigned spin_ = 0; spin_ < SPIN_CAP; ++spin_) {                                                   \
    v4u f_;                                                                                               \
    asm volatile("ds_read_b128 %0, %1\n\ts_waitcnt lgkmcnt(0)" : "=v"(f_) : "v"(smb + L_FL + 16 * (i)) : "memory"); \
    if (__builtin_amdgcn_readfirstlane(f_.x & f_.y & f_.z & f_.w)) break;                                 \
  }
#define TWICE(first)                                              \
  int live;                                                       \
  asm volatile("s_mov_b32 %0, %1" : "=s"(live) : "s"(first));     \
  do {
#define TWICE_END                                                 \
  asm volatile("s_add_u32 %0, %0, 1" : "+s"(live)::"scc");        \
  }                                                               \
  while (live < 2)
#define UNDEF(x) asm volatile("" : "=v"(x))
#define EPOCH() asm volatile("v_add_u32 %0, 1, %1" : "=v"(T) : "v"(ep) : "memory")
#define WAIT0 "\n\ts_waitcnt vmcnt(0)"
__device__ __forceinline__ void dma16(const void* base, unsigned off, unsigned lds) {
  asm volatile("s_mov_b32 m0, %2\n\ts_nop 0\n\tglobal_load_lds_dwordx4 %0, %1" ::"v"(off), "s"(base), "s"(lds) : "memory");
}
__device__ __forceinline__ v4u ld_sc1(__amdgpu_buffer_rsrc_t rs, int off) {
  return __builtin_amdgcn_raw_buffer_load_b128(rs, off, 0, AUX_SC1);
}
__device__ __forceinline__ v2u ld8_sc1(__amdgpu_buffer_rsrc_t rs, int off) {
  return __builtin_amdgcn_raw_buffer_load_b64(rs, off, 0, AUX_SC1);
}
__device__ __forceinline__ void st_sc1(__amdgpu_buffer_rsrc_t rs, int off, v4u v) {
  __builtin_amdgcn_raw_buffer_store_b128(v, rs, off, 0, AUX_SC1);
}
__device__ __forceinline__ void ln_stats(v4u r, float& mean, float& rstd) {
  float e[8] = {blo(r.x), bhi(r.x), blo(r.y), bhi(r.y), blo(r.z), bhi(r.z), blo(r.w), bhi(r.w)};
  float s = ((e[0] + e[1]) + (e[2] + e[3])) + ((e[4] + e[5]) + (e[6] + e[7]));
  mean = wave_sum(s) * (1.f / DM);
  float q = 0.f;
#pragma unroll
  for (int i = 0; i < 8; ++i) {
    float d = e[i] - mean;
    q = fmaf(d, d, q);
  }
  rstd = __builtin_amdgcn_rsqf(wave_sum(q) * (1.f / DM) + 1e-5f);
}
}

__global__ void __launch_bounds__(512, 2)
mla_glu_decode(const u16* __restrict__ x, const u16* __restrict__ ckv, const u16* __restrict__ wq, const u16* __restrict__ wuk,
               const u16* __restrict__ wuv, const u16* __restrict__ wo, const u16* __restrict__ wg, const u16* __restrict__ wu,
               const u16* __restrict__ wd, u16* __restrict__ out, char* __restrict__ ws) {
  const unsigned long long tstart = __builtin_amdgcn_s_memrealtime();
  __builtin_amdgcn_sched_barrier(0);
  __shared__ __attribute__((aligned(16))) char smem_[L_END];
  LAS char* sm = (LAS char*)smem_;
  const unsigned smb = (unsigned)(size_t)sm;
  const int tid = threadIdx.x, l = tid & 63, w = __builtin_amdgcn_readfirstlane(tid >> 6);
  const int col = l & 15, g4 = l >> 4, tq = (l & 15) >> 2, tp = l & 3;
  const int xcd = blockIdx.x & 7, r = blockIdx.x >> 3;
  const __amdgpu_buffer_rsrc_t rs = __builtin_amdgcn_make_buffer_rsrc(ws, 0, WS_BYTES, 0x00020000);
  asm volatile("" ::"s"(x), "s"(ckv), "s"(wq), "s"(wuk), "s"(wuv), "s"(wo), "s"(wg), "s"(wu), "s"(wd), "s"(out), "s"(ws));
  unsigned tsv = 0u;
  STAMP(0);
  unsigned Tg = 0;

  unsigned ep, T;
  LAS char* const px = sm + L_X + (col & 1) * XP + 16 * g4;
  if ((w >> 1) == 2) dma16(x, (unsigned)((w & 1) * 1024 + l * 16), smb + L_X + (w & 1) * XP);
  const f4 z = {0.f, 0.f, 0.f, 0.f};
#define LAYERNORM()                                                                                                              \
  {                                                                                                                              \
    const v4u xr = *(LAS v4u*)(sm + L_X + (w & 1) * XP + l * 16);                                                                \
    float e[8] = {blo(xr.x), bhi(xr.x), blo(xr.y), bhi(xr.y), blo(xr.z), bhi(xr.z), blo(xr.w), bhi(xr.w)};                       \
    const float mean = wave_sum(((e[0] + e[1]) + (e[2] + e[3])) + ((e[4] + e[5]) + (e[6] + e[7]))) * (1.f / DM);                 \
    float q = 0.f;                                                                                                               \
    _Pragma("unroll") for (int i = 0; i < 8; ++i) {                                                                              \
      e[i] -= mean;                                                                                                              \
      q = fmaf(e[i], e[i], q);                                                                                                   \
    }                                                                                                                            \
    const float rstd = __builtin_amdgcn_rsqf(wave_sum(q) * (1.f / DM) + 1e-5f);                                                  \
    *(LAS v4u*)(sm + L_X + (w & 1) * XP + l * 16) =                                                                              \
        v4u{pk(e[0] * rstd, e[1] * rstd), pk(e[2] * rstd, e[3] * rstd), pk(e[4] * rstd, e[5] * rstd), pk(e[6] * rstd, e[7] * rstd)}; \
  }

  if (__builtin_expect(r < 16, 1)) {
    const int h = 4 * xcd + (r >> 2), j = r & 3, ca = j >> 1, mb = j & 1;
    const int fl = tq | ((g4 & 1) << 2);
    gld4_sc1(ep, ws, 0u);
    if (__builtin_expect(w < 4, 1)) {
      const unsigned vq = (unsigned)(((128 * j + g4) * 4096 + h * DH + 8 * (col ^ ((g4 | ((w & 2) << 1)) << 1))) * 2);
#pragma unroll 1
      for (int k = w; k < 32; k += 4) dma16(wq, vq + (unsigned)k * 32768u, smb + L_WQ + k * 1024);
    } else if (w < 6) {
      VMWAIT(1);
      LAYERNORM();
    } else {
      __builtin_amdgcn_s_sleep(UK_DELAY);
      const unsigned vu = (unsigned)((((128 * j + g4) * NH + h) * DH) * 2);
#pragma unroll 1
      for (int k = w - 6; k < 32; k += 2) dma16(wuk, vu + (unsigned)k * 32768u + 16u * (col ^ ((4 * k + g4) & 15)), smb + L_WUK + k * 1024);
    }
    unsigned p = L_WQ + 256 * (8 * g4 + tq) + 16 * ((2 * w + (tp >> 1)) ^ (fl << 1)) + 8 * (tp & 1);
    unsigned oqp = WS_QP + ((h * 4 + j) * 8 + w) * 128 + col * 64 + g4 * 16;
    unsigned pxj = L_X + (col & 1) * XP + 16 * g4 + 256 * j;
    unsigned o = WS_QP + (h * 4 + (w & 3)) * 1024 + l * 16;
    unsigned pqw = smb + L_CC + (2 * (w & 3) + ((l >> 2) & 1)) * SP + (l >> 3) * 32 + (l & 3) * 8, pqr = L_CC + (col & 7) * SP + 16 * g4;
    unsigned oqa = WS_QA + ((col & 1) * NH + h) * 2048 + (64 * j + 16 * w + 8 * (col >> 1) + 2 * g4) * 8;
    unsigned pu = L_WUK + 256 * (32 * w + col) + 16 * (g4 ^ col);
    PIN(p); PIN(oqp); PIN(pxj); PIN(o); PIN(oqa); PIN(pu); PIN(pqw); PIN(pqr);
    {
    TWICE(DRY_F ? (w >> 2) : 1)
    if (live) {
      if (w < 6) VMWAIT(0);
      else VMWAIT(16);
      EPOCH();
      STAMP(1);
      BAR();
      STAMP(2);
    } else {
      UNDEF(T);
    }
    {
      v4u af[4], xf[4];
#pragma unroll
      for (int s = 0; s < 4; ++s) {
        af[s] = trrd2(sm + p + 256 * 32 * s, sm + p + 256 * (32 * s + 4));
        xf[s] = *(LAS v4u*)(sm + pxj + 64 * s);
      }
      __builtin_amdgcn_sched_barrier(0);
      f4 acc = z;
#pragma unroll
      for (int s = 0; s < 4; ++s) acc = mfma32(af[s], xf[s], acc);
      if (live && col < 2) st_sc1(rs, oqp, v4u{pk(acc[0], acc[1]), T, pk(acc[2], acc[3]), T});
    }
    STAMP(3);
    if (w >= 6) VMWAIT(1);
    if (w >= 4) {
      BAR();
      BAR();
    } else {
      v4u a, uk[8], qf[4];
      if (live) {
        __builtin_amdgcn_s_sleep(H1_DELAY);
        asm volatile(LD16(0, 1, 2) " sc1" : "=&v"(a) : "v"(o), "s"(ws) : "memory");
        BAR();
      } else {
        UNDEF(a);
      }
#pragma unroll
      for (int s = 0; s < 4; ++s) {
        uk[s] = *(LAS v4u*)(sm + (pu ^ (64 * s)));
        uk[4 + s] = *(LAS v4u*)(sm + (pu ^ (64 * s)) + 4096);
      }
      if (live) VMWAIT(0);
      PIN(a);
      for (unsigned spin = 0;; ++spin) {
        if (!__builtin_amdgcn_ballot_w64((T & a.y & a.w) != (T | a.y | a.w)) || spin > SPIN_CAP || !live) break;
        asm volatile(LD16(0, 1, 2) " sc1" WAIT0 : "+v"(a) : "v"(o), "s"(ws) : "memory");
      }
      asm volatile("ds_write2_b32 %0, %1, %2 offset0:0 offset1:1" ::"v"(pqw), "v"(a.x), "v"(a.z) : "memory");
      STAMP(4);
      if (live) BAR();
#pragma unroll
      for (int s = 0; s < 4; ++s) qf[s] = *(LAS v4u*)(sm + pqr + 64 * s);
      __builtin_amdgcn_sched_barrier(0);
      f4 a0 = z, a1 = z;
#pragma unroll
      for (int s = 0; s < 4; ++s) {
        a0 = mfma32(uk[s], qf[s], a0);
        a1 = mfma32(uk[4 + s], qf[s], a1);
      }
      float q[4];
#pragma unroll
      for (int e = 0; e < 4; ++e) {
        const float mine = (col & 2) ? a1[e] : a0[e], other = (col & 2) ? a0[e] : a1[e];
        q[e] = mine + dpp<QP_2301>(other);
        q[e] += dpp<ROR4>(q[e]);
      }
      if (live && col < 4) st_sc1(rs, oqa, v4u{pk(q[0], q[1]), T, pk(q[2], q[3]), T});
    }
    TWICE_END;
    }
    STAMP(5);
    PROBE_T(T);
    BAR();
    __builtin_amdgcn_s_sleep(T_DELAY);
    {
      const unsigned vv = (unsigned)((((256 * ca + g4) * NH + h) * DH + 8 * (col ^ ((g4 | ((w & 2) << 1)) << 1))) * 2);
      const unsigned vo = (unsigned)(((h * DH + (l >> 5)) * DM + 256 * mb) * 2) + 16u * ((l & 31) ^ (((l >> 5) | ((w & 1) << 1) | (w & 4)) << 1));
      int kv = w + 32 * mb, ko = w + 32 * ca;
#pragma unroll 1
      for (int hf = 0; hf < 2; ++hf) {
#pragma unroll 1
        for (int i = 0; i < 4; ++i, kv = (kv + 8) & 63) dma16(wuv, vv + (unsigned)kv * 32768u, smb + L_WUV + kv * 1024);
#pragma unroll 1
        for (int i = 0; i < 4; ++i, ko = (ko + 8) & 63) dma16(wo, vo + (unsigned)ko * 2048u, smb + L_WO + ko * 1024);
        if (hf == 0) asm volatile("s_barrier" ::: "memory");
      }
    }
    const int tb = w >> 2, nq = w & 3;
    o = WS_PART + (((h * 2 + ca) * 2 + tb) * 32 + 8 * nq + g4) * 512 + 16 * col;
    unsigned pw = L_W + (tb * 32 + 8 * nq + g4) * 4;
    unsigned pv = L_WUV + 256 * (8 * g4 + tq) + 16 * ((2 * w + (tp >> 1)) ^ (fl << 1)) + 8 * (tp & 1);
    unsigned po = L_WO + 512 * (8 * g4 + tq) + 16 * ((4 * w + (tp >> 1)) ^ (fl << 1)) + 8 * (tp & 1);
    unsigned pcw = L_CC + (nq * 2 + tb) * SP2 + 16 * col + 8 * g4, pcr = L_CC + (col & 7) * SP2 + 16 * g4;
    unsigned ptw = L_CT + col * SP + (16 * w + 4 * g4) * 2, ptr = L_CT + (col & 1) * SP + 16 * g4;
    unsigned oo = WS_OPT + ((16 * mb + 2 * w + (col >> 1)) * 64 + 2 * h + ca) * 128 + ((col & 1) * 4 + g4) * 16;
    v4u mk;
#pragma unroll
    for (int d = 0; d < 4; ++d) mk[d] = (col >> 1) == d ? ((col & 1) ? 0xffff0000u : 0xffffu) : 0u;
    PIN(o); PIN(pw); PIN(pv); PIN(po); PIN(pcw); PIN(pcr); PIN(ptw); PIN(ptr); PIN(oo); PIN(mk);
    STAMP(11);
    TWICE(DRY_T ? 0 : 1)
    if (live) STAMP(12);
    if (w == 7) {
      const unsigned ol = WS_LSE + (((l >> 5) * 2 + (h >> 4)) * 32 + (l & 31)) * 128 + (h & 15) * 8;
      v2u la;
      UNDEF(la);
      for (unsigned spin = 0; live; ++spin) {
        asm volatile("global_load_dwordx2 %0, %1, %2 sc1" WAIT0 : "+v"(la) : "v"(ol), "s"(ws) : "memory");
        if (!__builtin_amdgcn_ballot_w64(la.y != T) || spin > SPIN_CAP) break;
      }
      float M = asf(la.x);
      M = fmax2(M, dpp<ROR8>(M));
      M = fmax2(M, dpp<ROR4>(M));
      M = fmax2(M, dpp<ROR2>(M));
      M = fmax2(M, dpp<ROR1>(M));
      M = sw16_max(M);
      const float e = __builtin_amdgcn_exp2f(asf(la.x) - M);
      float L = e + dpp<ROR8>(e);
      L += dpp<ROR4>(L);
      L += dpp<ROR2>(L);
      L += dpp<ROR1>(L);
      L = sw16_add(L, L);
      const float wt = e * __builtin_amdgcn_rcpf(L);
      *(LAS unsigned*)(sm + L_W + 4 * l) = pk(wt, wt);
      STAMP(7);
    }
    v4u a[4];
    if (live) {
      VMWAIT(0);
      STAMP(6);
      BAR();
      SWEEP_4(a, o, 0, 256, 2048, 2304, "");
    } else {
      UNDEF(a[0]); UNDEF(a[1]); UNDEF(a[2]); UNDEF(a[3]);
    }
    v4u A0 = mk & *(LAS unsigned*)(sm + pw), A1 = mk & *(LAS unsigned*)(sm + pw + 16);
    v4u av[8], ao[8];
#pragma unroll
    for (int s = 0; s < 8; ++s) av[s] = trrd2(sm + pv + 256 * 32 * s, sm + pv + 256 * (32 * s + 4));
#pragma unroll
    for (int t = 0; t < 2; ++t)
#pragma unroll
      for (int s = 0; s < 4; ++s) ao[4 * t + s] = trrd2(sm + (po ^ (32 * t)) + 512 * 32 * s, sm + (po ^ (32 * t)) + 512 * (32 * s + 4));
    if (live) VMWAIT(0);
    asm volatile("" : "+v"(a[0]), "+v"(a[1]), "+v"(a[2]), "+v"(a[3]), "+v"(A0), "+v"(A1)::"memory");
    for (unsigned spin = 0;; ++spin) {
      unsigned mx = 0u;
#pragma unroll
      for (int i = 0; i < 4; ++i) mx = max(max(mx, max(a[i].x, a[i].y)), max(a[i].z, a[i].w));
      if (!__builtin_amdgcn_ballot_w64(mx == ~0u) || spin > SPIN_CAP || !live) break;
      RESWEEP_4(a, o, 0, 256, 2048, 2304);
    }
    STAMP(8);
    {
      f4 c0 = mfma32(A0, a[0], z), c1 = mfma32(A0, a[1], z);
      c0 = mfma32(A1, a[2], c0);
      c1 = mfma32(A1, a[3], c1);
      if (g4 < 2) {
        *(LAS v2u*)(sm + pcw) = v2u{pk(c0[0], c0[1]), pk(c0[2], c0[3])};
        *(LAS v2u*)(sm + pcw + 256) = v2u{pk(c1[0], c1[1]), pk(c1[2], c1[3])};
      }
    }
    if (live) BAR();
    {
      f4 acc = z;
      v4u bf[8];
#pragma unroll
      for (int s = 0; s < 8; ++s) bf[s] = *(LAS v4u*)(sm + pcr + 64 * s);
      __builtin_amdgcn_sched_barrier(0);
#pragma unroll
      for (int s = 0; s < 8; ++s) acc = mfma32(av[s], bf[s], acc);
#pragma unroll
      for (int e = 0; e < 4; ++e) {
        acc[e] += dpp<QP_2301>(acc[e]);
        acc[e] += dpp<ROR4>(acc[e]);
      }
      if (col < 2) *(LAS v2u*)(sm + ptw) = v2u{pk(acc[0], acc[1]), pk(acc[2], acc[3])};
    }
    if (live) BAR();
    {
      f4 c0 = z, c1 = z;
      v4u bf[4];
#pragma unroll
      for (int s = 0; s < 4; ++s) bf[s] = *(LAS v4u*)(sm + ptr + 64 * s);
      __builtin_amdgcn_sched_barrier(0);
#pragma unroll
      for (int s = 0; s < 4; ++s) {
        c0 = mfma32(ao[s], bf[s], c0);
        c1 = mfma32(ao[4 + s], bf[s], c1);
      }
      if (col & 2) c0 = c1;
      if (live && col < 4) st_sc1(rs, oo, v4u{pk(c0[0], c0[1]), T, pk(c0[2], c0[3]), T});
    }
    TWICE_END;
    STAMP(9);
  } else {
    const int a = r - 16, b = xcd >> 2, n = 8 * (xcd & 3) + (a >> 1), g = a & 1;
    const int sl = 12 * xcd + a, rd = (4 * xcd + a) & 31;
    const bool glu = a < 12 && sl < NSL;
    const int v = w & 3;
    unsigned op = WS_PART + ((((16 * g + (col & 7)) * 2 + (v >> 1)) * 2 + b) * 32 + n) * 512 + 256 * (v & 1) + (col >> 3) * 64 +
                  (g4 & 1) * 32 + (g4 >> 1) * 16;
    unsigned xres;
    gld4_sc1(ep, ws, 0u);
    asm volatile("global_load_ushort %0, %1, %2" : "=v"(xres) : "v"((unsigned)(((l >> 4) & 1) * 1024 + rd * 32 + col * 2)), "s"(x) : "memory");
    if (w >= 4) {
#pragma unroll
      for (int q = 0; q < 4; ++q) st_sc1(rs, op + 128 * (q >> 1) + (q & 1) * 8 * 65536, v4u{~0u, ~0u, ~0u, ~0u});
      if (w == 7 && l < 32) *(LAS unsigned*)(sm + L_FL + 4 * l) = 0u;
    }
    unsigned o = WS_QA + (b * NH + 16 * g) * 2048 + w * 4096 + l * 16, pqs = smb + L_Q + 2 * w * CP + l * 8;
    PIN(o); PIN(pqs);
    {
    TWICE(DRY_A ? 0 : 1)
    if (live) {
      const unsigned vc = (unsigned)(((b * SK + 128 * n) * DC + 8 * l) * 2);
      STAMP(10);
      __builtin_amdgcn_s_sleep(A_DELAY);
#pragma unroll 1
      for (int q = 0; q < 4; ++q) {
        int row = w + ((64 * g + 32 * q) & 127);
        asm volatile("s_barrier" ::: "memory");
#pragma unroll 1
        for (int i = 0; i < 4; ++i, row += 8) dma16(ckv, vc + row * 1024u, smb + L_C + row * CP);
      }
      if ((w >> 1) == 2) {
        VMWAIT(22);
        LAYERNORM();
      }
      VMWAIT(0);
      EPOCH();
      PROBE_T(T);
      BAR();
      STAMP(1);
    } else {
      UNDEF(T);
    }
#define QISSUE()                                \
  if (live) {                                   \
    SWEEP_4(qg, o, 0, 1024, 2048, 3072, "");    \
  } else {                                      \
    UNDEF(qg[0]); UNDEF(qg[1]); UNDEF(qg[2]); UNDEF(qg[3]); \
  }
#define QGATHER()                                                                                                           \
  if (live) VMWAIT(0);                                                                                                      \
  asm volatile("" : "+v"(qg[0]), "+v"(qg[1]), "+v"(qg[2]), "+v"(qg[3])::"memory");                                          \
  for (unsigned spin = 0;; ++spin) {                                                                                        \
    if (!TAGS_BAD4(qg) || spin > SPIN_CAP || !live) break;                                                                           \
    RESWEEP_4(qg, o, 0, 1024, 2048, 3072);                                                                             \
  }                                                                                                                         \
  STAMP(3);                                                                                                                 \
  asm volatile("ds_write2_b32 %0, %2, %3 offset0:0 offset1:1\n\tds_write2_b32 %0, %4, %5 offset0:128 offset1:129\n\t"        \
               "ds_write2_b32 %1, %6, %7 offset0:0 offset1:1\n\tds_write2_b32 %1, %8, %9 offset0:128 offset1:129" ::"v"(pqs), \
               "v"(pqs + CP), "v"(qg[0].x), "v"(qg[0].z), "v"(qg[1].x), "v"(qg[1].z), "v"(qg[2].x), "v"(qg[2].z), "v"(qg[3].x), \
               "v"(qg[3].z)                                                                                                 \
               : "memory")
    if (__builtin_expect(w < 4, 1)) {
      v4u fr[32], qg[4];
      QISSUE();
      LAS char* pc = sm + L_C + (32 * w + col) * CP + 16 * g4;
#pragma unroll
      for (int kt = 0; kt < 2; ++kt)
#pragma unroll
        for (int s = 0; s < 16; ++s) fr[16 * kt + s] = *(LAS v4u*)(pc + 16 * kt * CP + 64 * s);
      unsigned p = L_Q + col * CP + 16 * g4, pp = L_P + w * 1024 + l * 16, pm = L_MX + col * 16 + w * 4;
      PIN(p); PIN(pp); PIN(pm);
      STAMP(2);
      QGATHER();
      if (live) BAR();
      STAMP(4);
      f4 s0 = z, s1 = z;
      v4u qf[16];
#pragma unroll
      for (int s = 0; s < 6; ++s) qf[s] = *(LAS v4u*)(sm + p + 64 * s);
      __builtin_amdgcn_sched_barrier(0);
#pragma unroll
      for (int s = 0; s < 16; ++s) {
        if (s + 6 < 16) qf[s + 6] = *(LAS v4u*)(sm + p + 64 * (s + 6));
        s0 = mfma32(fr[s], qf[s], s0);
        s1 = mfma32(fr[16 + s], qf[s], s1);
        __builtin_amdgcn_sched_barrier(0);
      }
      float mx = fmax2(fmax2(fmax2(s0[0], s0[1]), fmax2(s0[2], s0[3])), fmax2(fmax2(s1[0], s1[1]), fmax2(s1[2], s1[3])));
      mx = sw32_max(sw16_max(mx));
      constexpr float SC = 0.08838834764831845f * 1.4426950408889634f;
      const float mi = __builtin_ceilf(mx * SC);
#pragma unroll
      for (int e = 0; e < 4; ++e) {
        s0[e] = __builtin_amdgcn_exp2f(fmaf(s0[e], SC, -mi));
        s1[e] = __builtin_amdgcn_exp2f(fmaf(s1[e], SC, -mi));
      }
      *(LAS float*)(sm + pm) = mi;
      *(LAS v4u*)(sm + pp) = v4u{pk(s0[0], s0[1]), pk(s0[2], s0[3]), pk(s1[0], s1[1]), pk(s1[2], s1[3])};
      if (live) BAR();
      STAMP(5);
    } else {
      v4u fr[32], qg[4];
      QISSUE();
      LAS char* p0 = sm + L_C + (4 * g4 + tq) * CP + 256 * v + 8 * tp;
      unsigned p1 = L_C + (64 + 4 * g4 + tq) * CP + 256 * v + 8 * tp;
      PIN(p1);
#pragma unroll
      for (int ct = 0; ct < 8; ++ct)
#pragma unroll
        for (int u = 0; u < 4; ++u) {
          LAS char* p = (u < 2 ? p0 : sm + p1) + 32 * (u & 1) * CP + 32 * ct;
          fr[4 * ct + u] = trrd2(p, p + 16 * CP);
        }
      unsigned ol = WS_LSE + ((b * 2 + g) * 32 + n) * 128 + col * 8, pp = L_P + l * 16, pm = L_MX + col * 16;
      v4u one = {0x3f803f80u, 0x3f803f80u, 0x3f803f80u, 0x3f803f80u};
      PIN(ol); PIN(pp); PIN(pm); PIN(one); PIN(op);
      STAMP(2);
      QGATHER();
      if (live) BAR();
      STAMP(4);
      if (live) {
        const int ln = 128 * a + 64 * (v >> 1) + l;
        touch((v & 1) ? wu : wg, (unsigned)((ln >> 2) * (FF * 2)) + (unsigned)min(384 * xcd + 128 * (ln & 3), FF * 2 - 4), smb + L_JUNK);
      }
      if (live) BAR();
      const f4 m4 = *(LAS f4*)(sm + pm);
      v4u pf[4];
#pragma unroll
      for (int u = 0; u < 4; ++u) pf[u] = *(LAS v4u*)(sm + pp + u * 1024);
      const float M = fmax2(fmax2(m4[0], m4[1]), fmax2(m4[2], m4[3]));
#pragma unroll
      for (int u = 0; u < 4; ++u) {
        const unsigned kk = min((unsigned)(M - m4[u]), 255u) * 0x00800080u;
#pragma unroll
        for (int e = 0; e < 4; ++e) pf[u][e] = scale_dn(pf[u][e], kk);
      }
      f4 ls = z;
#pragma unroll
      for (int u = 0; u < 4; ++u) ls = mfma32(one, pf[u], ls);
      const float il = __builtin_amdgcn_rcpf(ls[0]);
      if (live && v == 0 && g4 == 0)
        __builtin_amdgcn_raw_buffer_store_b64(v2u{__builtin_bit_cast(unsigned, M + __builtin_amdgcn_logf(ls[0])), T}, rs, ol, 0, AUX_SC1);
#pragma unroll
      for (int q = 0; q < 2; ++q) {
        f4 c[4];
#pragma unroll
        for (int k = 0; k < 4; ++k) c[k] = z;
#pragma unroll
        for (int u = 0; u < 4; ++u)
#pragma unroll
          for (int k = 0; k < 4; ++k) c[k] = mfma32(fr[16 * q + 4 * k + u], pf[u], c[k]);
        v4u R[2];
#pragma unroll
        for (int k = 0; k < 2; ++k) {
          const f4 c0 = c[2 * k] * il, c1 = c[2 * k + 1] * il;
          const unsigned x0 = min(pk(c0[0], c0[1]), 0xfffffffeu), x1 = min(pk(c0[2], c0[3]), 0xfffffffeu);
          const unsigned y0 = min(pk(c1[0], c1[1]), 0xfffffffeu), y1 = min(pk(c1[2], c1[3]), 0xfffffffeu);
          auto r0 = __builtin_amdgcn_permlane16_swap(x0, y0, false, false);
          auto r1 = __builtin_amdgcn_permlane16_swap(x1, y1, false, false);
          R[k] = v4u{r0[0], r1[0], r0[1], r1[1]};
        }
        v4u A, B;
#pragma unroll
        for (int e = 0; e < 4; ++e) {
          A[e] = (unsigned)__builtin_amdgcn_update_dpp((int)R[0][e], (int)R[1][e], ROR8, 0xf, 0xc, false);
          B[e] = (unsigned)__builtin_amdgcn_update_dpp((int)R[1][e], (int)R[0][e], ROR8, 0xf, 0x3, false);
        }
        PIN(A); PIN(B);
        if (live) {
          st_sc1(rs, op + 128 * q, A);
          st_sc1(rs, op + 128 * q + 8 * 65536, B);
        }
      }
      if (live) FLAG_SET(v);
      STAMP(6);
    }
    TWICE_END;
    }
      if (glu && w < 4) {
        v4u af[8], xf[8];
#pragma unroll
        for (int s = 0; s < 8; ++s) xf[s] = *(LAS v4u*)(px + 512 * (w >> 1) + 64 * s);
        unsigned oog = WS_OPG + ((8 * w + (col >> 1)) * 128 + sl) * 128 + ((col & 1) * 4 + g4) * 16;
        PIN(oog);
        FLAG_WAIT4(0);
        unsigned og = (unsigned)(((128 * w + (l >> 1)) * FF + 16 * sl + 8 * (l & 1)) * 2);
#pragma unroll 1
        for (int i = 0; i < 4; ++i, og += 32 * FF * 2) {
          dma16(wg, og, smb + L_GG + (4 * w + i) * 1024);
          dma16(wu, og, smb + L_GG + 16384 + (4 * w + i) * 1024);
        }
#pragma unroll 1
        for (int i = 0; i < 4; ++i) dma16(wd, (unsigned)(((16 * sl + 4 * w + i) * DM + 8 * l) * 2), smb + L_GD + (4 * w + i) * CP);
        TWICE(DRY_G ? 0 : 1)
        if (live) {
          VMWAIT(4);
          FLAG_SET(4 + w);
          FLAG_WAIT4(1);
        }
        STAMP(7);
        LAS char* pg = sm + L_GG + (w & 1) * 16384 + 32 * (256 * (w >> 1) + 8 * g4 + tq) + 8 * tp;
        f4 acc = z;
#pragma unroll
        for (int s = 0; s < 8; ++s) af[s] = trrd2(pg + 32 * 32 * s, pg + 32 * (32 * s + 4));
        __builtin_amdgcn_sched_barrier(0);
#pragma unroll
        for (int s = 0; s < 8; ++s) acc = mfma32(af[s], xf[s], acc);
        *(LAS f4*)(sm + L_GR + ((w & 1) * 2 + (w >> 1)) * 1024 + l * 16) = acc;
        if (live) {
          VMWAIT(0);
          FLAG_SET(8 + w);
          FLAG_WAIT4(2);
        }
        v2u dn[8];
#pragma unroll
        for (int t = 0; t < 8; ++t) dn[t] = trrd(sm + L_GD + (4 * g4 + tq) * CP + 32 * (8 * w + t) + 8 * tp);
        LAS char* pr = sm + L_GR + l * 16;
        const f4 gx = *(LAS f4*)(pr) + *(LAS f4*)(pr + 1024), ux = *(LAS f4*)(pr + 2048) + *(LAS f4*)(pr + 3072);
        float hh[4];
#pragma unroll
        for (int e = 0; e < 4; ++e) hh[e] = ux[e] * __builtin_amdgcn_rcpf(1.f + __builtin_amdgcn_exp2f(-1.4426950408889634f * gx[e]));
        const v2u hb = {pk(hh[0], hh[1]), pk(hh[2], hh[3])};
        f4 d = z;
#pragma unroll
        for (int t = 0; t < 8; ++t) d = mfma16(dn[t], (col >> 1) == t ? hb : v2u{0u, 0u}, d);
        v4u dv = {pk(d[0], d[1]), T, pk(d[2], d[3]), T};
        PIN(dv);
        if (live) st_sc1(rs, oog, dv);
        TWICE_END;
        STAMP(9);
      }
    if (a >= 12) {
      unsigned ot = WS_OPT + rd * 8192 + tid * 16, og = WS_OPG + rd * 16384 + tid * 16;
      const bool two = (tid >> 3) < NSL - 64;
      unsigned og2 = two ? og + 8192 : og;
      const unsigned m = (col & 1) ? 0x3f800000u : 0x3f80u;
      v4u sel = {col < 2 ? m : 0u, (col >> 1) == 1 ? m : 0u, col < 2 ? m : 0u, (col >> 1) == 1 ? m : 0u};
      unsigned m2 = two ? ~0u : 0u;
      unsigned prw = L_RR + w * 128 + (l & 7) * 16, prr = L_RR + (l & 31) * 4, oy = ((l >> 4) & 1) * 1024 + rd * 32 + col * 2;
      PIN(ot); PIN(og); PIN(og2); PIN(sel); PIN(m2); PIN(prw); PIN(prr); PIN(oy);
      PIN(xres);
      TWICE(DRY_R ? 0 : 1)
      v4u p0, p1, p2;
      UNDEF(p0); UNDEF(p1); UNDEF(p2);
      for (unsigned spin = 0; live; ++spin) {
        asm volatile(LD16(0, 3, 6) " sc1\n\t" LD16(1, 4, 6) " sc1\n\t" LD16(2, 5, 6) " sc1" WAIT0
                     : "+v"(p0), "+v"(p1), "+v"(p2)
                     : "v"(ot), "v"(og), "v"(og2), "s"(ws)
                     : "memory");
        if (!__builtin_amdgcn_ballot_w64((T & p0.y & p0.w & p1.y & p1.w & p2.y & p2.w) != (T | p0.y | p0.w | p1.y | p1.w | p2.y | p2.w)) ||
            spin > SPIN_CAP)
          break;
      }
      STAMP(8);
      f4 acc = mfma32(sel, v4u{p0.x, p0.z, p1.x, p1.z}, z);
      acc = mfma32(sel, v4u{p2.x & m2, p2.z & m2, 0u, 0u}, acc);
#pragma unroll
      for (int e = 0; e < 4; ++e) acc[e] += dpp<ROR8>(acc[e]);
      if (l < 8) *(LAS f4*)(sm + prw) = acc;
      if (live) BAR();
      if (w == 0 && l < 32) {
        float t[8];
#pragma unroll
        for (int i = 0; i < 8; ++i) t[i] = *(LAS float*)(sm + prr + i * 128);
        const float y = blo(xres) + (((t[0] + t[1]) + (t[2] + t[3])) + ((t[4] + t[5]) + (t[6] + t[7])));
        unsigned yb = pk(y, y);
        PIN(yb);
        if (live) *(u16*)((char*)out + oy) = (u16)yb;
        if (live && rd == 0 && l == 0) __builtin_amdgcn_raw_buffer_store_b32(T, rs, WS_E, 0, AUX_SC1);
      }
      TWICE_END;
      STAMP(9);
    }
  }
  STAMP_FLUSH();
}

#define WS_END 0
#define WS_BASE (WS_END ? ((ws_size - (size_t)WS_END * 1048576) & ~(size_t)4095) : 0)
extern "C" void kernel_launch(void* const* d_in, const int* in_sizes, int n_in, void* d_out, int out_size, void* d_ws, size_t ws_size,
                              hipStream_t stream) {
  mla_glu_decode<<<256, 512, 0, stream>>>((const u16*)d_in[0], (const u16*)d_in[1], (const u16*)d_in[2], (const u16*)d_in[3],
                                          (const u16*)d_in[4], (const u16*)d_in[5], (const u16*)d_in[7], (const u16*)d_in[8],
                                          (const u16*)d_in[9], (u16*)d_out, (char*)d_ws + WS_BASE);
}
```

```cpp
#define A_DELAY 16
#include <hip/hip_runtime.h>
#include <stdint.h>

#define GAS __attribute__((address_space(1)))
#define LAS __attribute__((address_space(3)))
typedef unsigned short u16;
typedef unsigned v4u __attribute__((ext_vector_type(4)));
typedef unsigned v2u __attribute__((ext_vector_type(2)));
typedef float f4 __attribute__((ext_vector_type(4)));
typedef float f2 __attribute__((ext_vector_type(2)));
typedef __bf16 bf8 __attribute__((ext_vector_type(8)));
typedef __bf16 bf2 __attribute__((ext_vector_type(2)));
typedef short s4 __attribute__((ext_vector_type(4)));

namespace {
constexpr int NH = 32, SK = 4096, DH = 128, DM = 512, DC = 512, FF = 1376;
constexpr int NSL = FF / 16;
constexpr int WS_E = 0;
constexpr int WS_QP = 4096;
constexpr int WS_QA = WS_QP + NH * 4 * 1024;
constexpr int WS_LSE = WS_QA + 2 * NH * 2048;
constexpr int WS_OPT = WS_LSE + 2 * 2 * 32 * 128;
constexpr int WS_OPG = WS_OPT + 32 * 64 * 128;
constexpr int WS_PART = WS_OPG + 32 * 128 * 128;
constexpr int WS_BYTES = WS_PART + NH * 2 * 2 * 32 * 512;
constexpr int WS_STAMP = 7 << 19;
constexpr int CP = 1056;
constexpr int XP = 1088, SP = 288, SP2 = 544;
constexpr int L_C = 0, L_Q = 128 * CP, L_P = L_Q + 16 * CP, L_X = L_P + 4096, L_MX = L_X + 2 * XP, L_RR = L_MX + 256,
              L_JUNK = L_RR + 1024, L_FL = L_JUNK + 256, L_END = L_FL + 128;
static_assert(L_END <= 163840, "LDS");
constexpr int L_GG = 0, L_GD = 32768, L_GR = 65536;
constexpr int L_WUV = 0, L_WO = 65536, L_WQ = 0, L_WUK = 32768, L_CC = 131072, L_CT = L_CC + 8 * SP2, L_W = L_CT + 2 * SP;
#define UK_DELAY 4
#define H1_DELAY 4
#define T_DELAY 30
#define GLU_FLAG_Q 1
#define NOCLEAR 0
#define CODE_LINES 0
#define TLB_WARM 0
#define CLEAR_A 1
#define CLEAR_B 1
#define CLEAR_MODE 0
#define CLEAR_DELAY 0
#define DRY_F 0
#define DRY_T 0
#define DRY_A 0
#define DRY_G 0
#define DRY_R 0
#define OPT_W 0
#define QA_WARM 0
#define LSE_DELAY 40
#define PF_GLU 1
constexpr unsigned SPIN_CAP = 1u << 20;
constexpr int AUX_SC1 = 16;

#define STAMP_W 0
#define STAMP(i)
#define STAMP_FLUSH()
#define PROBE_T(T_)

__device__ __forceinline__ f4 mfma32(v4u a, v4u b, f4 c) {
  return __builtin_amdgcn_mfma_f32_16x16x32_bf16(__builtin_bit_cast(bf8, a), __builtin_bit_cast(bf8, b), c, 0, 0, 0);
}
__device__ __forceinline__ f4 mfma16(v2u a, v2u b, f4 c) {
  return __builtin_amdgcn_mfma_f32_16x16x16bf16_1k(__builtin_bit_cast(s4, a), __builtin_bit_cast(s4, b), c, 0, 0, 0);
}
__device__ __forceinline__ unsigned pk(float lo, float hi) {
  f2 x = {lo, hi};
  return __builtin_bit_cast(unsigned, __builtin_convertvector(x, bf2));
}
__device__ __forceinline__ float asf(unsigned u) { return __builtin_bit_cast(float, u); }
__device__ __forceinline__ float blo(unsigned u) { return __builtin_bit_cast(float, u << 16); }
__device__ __forceinline__ float bhi(unsigned u) { return __builtin_bit_cast(float, u & 0xffff0000u); }
template <int CTRL> __device__ __forceinline__ float dpp(float v) {
  return __builtin_bit_cast(float, __builtin_amdgcn_update_dpp(0, __builtin_bit_cast(int, v), CTRL, 0xf, 0xf, true));
}
__device__ __forceinline__ float fmax2(float a, float b) {
#pragma float_control(precise, off)
  return __builtin_fmaxf(a, b);
}
typedef unsigned short h2 __attribute__((ext_vector_type(2)));
__device__ __forceinline__ unsigned scale_dn(unsigned p, unsigned kk) {
  return __builtin_bit_cast(unsigned, __builtin_elementwise_sub_sat(__builtin_bit_cast(h2, p), __builtin_bit_cast(h2, kk)));
}
#define PIN(x) asm volatile("" : "+v"(x))
constexpr int ROR1 = 0x121, ROR2 = 0x122, ROR4 = 0x124, ROR8 = 0x128, QP_2301 = 0x4e, QP_2222 = 0xaa;
__device__ __forceinline__ float sw32_add(float a, float b) {
  auto r = __builtin_amdgcn_permlane32_swap(__builtin_bit_cast(unsigned, a), __builtin_bit_cast(unsigned, b), false, false);
  return asf(r[0]) + asf(r[1]);
}
__device__ __forceinline__ float sw16_add(float a, float b) {
  auto r = __builtin_amdgcn_permlane16_swap(__builtin_bit_cast(unsigned, a), __builtin_bit_cast(unsigned, b), false, false);
  return asf(r[0]) + asf(r[1]);
}
__device__ __forceinline__ float vmax(unsigned a, unsigned b) {
  float r;
  asm("v_max_f32 %0, %1, %2" : "=v"(r) : "v"(a), "v"(b));
  return r;
}
__device__ __forceinline__ float sw32_max(float a) {
  auto r = __builtin_amdgcn_permlane32_swap(__builtin_bit_cast(unsigned, a), __builtin_bit_cast(unsigned, a), false, false);
  return vmax(r[0], r[1]);
}
__device__ __forceinline__ float sw16_max(float a) {
  auto r = __builtin_amdgcn_permlane16_swap(__builtin_bit_cast(unsigned, a), __builtin_bit_cast(unsigned, a), false, false);
  return vmax(r[0], r[1]);
}
__device__ __forceinline__ float wave_sum(float v) {
  v += dpp<ROR8>(v);
  v += dpp<ROR4>(v);
  v += dpp<ROR2>(v);
  v += dpp<ROR1>(v);
  v = sw16_add(v, v);
  return sw32_add(v, v);
}
__device__ __forceinline__ v2u trrd(LAS char* p) {
  return __builtin_bit_cast(v2u, __builtin_amdgcn_ds_read_tr16_b64_v4i16((LAS s4*)p));
}
__device__ __forceinline__ v4u trrd2(LAS char* p0, LAS char* p1) {
  v2u a = trrd(p0), b = trrd(p1);
  return v4u{a.x, a.y, b.x, b.y};
}
#define BAR() asm volatile("s_waitcnt lgkmcnt(0)\n\ts_barrier" ::: "memory")
#define VMWAIT(k) asm volatile("s_waitcnt vmcnt(%0)" ::"n"(k) : "memory")
__device__ __forceinline__ void gld16(v4u& d, const void* base, unsigned off) {
  asm volatile("global_load_dwordx4 %0, %1, %2" : "=v"(d) : "v"(off), "s"(base) : "memory");
}
__device__ __forceinline__ void gld4_sc1(unsigned& d, const void* base, unsigned off) {
  asm volatile("global_load_dword %0, %1, %2 sc1" : "=v"(d) : "v"(off), "s"(base) : "memory");
}
__device__ __forceinline__ void touch(const void* base, unsigned off, unsigned lds) {
  asm volatile("s_mov_b32 m0, %2\n\ts_nop 0\n\tglobal_load_lds_dword %0, %1" ::"v"(off), "s"(base), "s"(lds) : "memory");
}
__device__ __forceinline__ void touch_sc1(const void* base, unsigned off, unsigned lds) {
  asm volatile("s_mov_b32 m0, %2\n\ts_nop 0\n\tglobal_load_lds_dword %0, %1 sc1" ::"v"(off), "s"(base), "s"(lds) : "memory");
}
#define LD16(i, a, o) "global_load_dwordx4 %" #i ", %" #a ", %" #o
#define SWEEP_4(q, o, o0, o1, o2, o3, W)                                                                                     \
  asm volatile(LD16(0, 4, 5) " offset:" #o0 " sc1\n\t" LD16(1, 4, 5) " offset:" #o1 " sc1\n\t" LD16(2, 4, 5) " offset:" #o2         \
               " sc1\n\t" LD16(3, 4, 5) " offset:" #o3 " sc1" W                                                               \
               : "=&v"((q)[0]), "=&v"((q)[1]), "=&v"((q)[2]), "=&v"((q)[3])                                                  \
               : "v"(o), "s"(ws)                                                                                             \
               : "memory")
#define RESWEEP_4(q, o, o0, o1, o2, o3)                                                                                      \
  asm volatile(LD16(0, 4, 5) " offset:" #o0 " sc1\n\t" LD16(1, 4, 5) " offset:" #o1 " sc1\n\t" LD16(2, 4, 5) " offset:" #o2         \
               " sc1\n\t" LD16(3, 4, 5) " offset:" #o3 " sc1\n\ts_waitcnt vmcnt(0)"                                            \
               : "+v"((q)[0]), "+v"((q)[1]), "+v"((q)[2]), "+v"((q)[3])                                                      \
               : "v"(o), "s"(ws)                                                                                             \
               : "memory")
#define SWEEP_L(W)                                                                                                          \
  asm volatile("global_load_dwordx2 %0, %3, %5 sc1\n\tglobal_load_dwordx2 %1, %4, %5 sc1\n\t"                              \
               "global_load_dwordx2 %2, %4, %5 offset:512 sc1" W                                                            \
               : "=&v"(la), "=&v"(l0), "=&v"(l1)                                                                            \
               : "v"(ola), "v"(olo), "s"(ws)                                                                                \
               : "memory")
#define SWEEP_2(p, q, oa, ob, W) \
  asm volatile(LD16(0, 2, 4) " sc1\n\t" LD16(1, 3, 4) " sc1" W : "=&v"(p), "=&v"(q) : "v"(oa), "v"(ob), "s"(ws) : "memory")
#define TAGS_BAD4(q) \
  __builtin_amdgcn_ballot_w64((T & (q)[0].y & (q)[0].w & (q)[1].y & (q)[1].w & (q)[2].y & (q)[2].w & (q)[3].y & (q)[3].w) != \
                              (T | (q)[0].y | (q)[0].w | (q)[1].y | (q)[1].w | (q)[2].y | (q)[2].w | (q)[3].y | (q)[3].w))
#define FLAG_SET(i) asm volatile("ds_write_b32 %0, %1" ::"v"(smb + L_FL + 4 * (i)), "v"(1u) : "memory")
#define FLAG_WAIT4(i)                                                                                     \
  for (unsigned spin_ = 0; spin_ < SPIN_CAP; ++spin_) {                                                   \
    v4u f_;                                                                                               \
    asm volatile("ds_read_b128 %0, %1\n\ts_waitcnt lgkmcnt(0)" : "=v"(f_) : "v"(smb + L_FL + 16 * (i)) : "memory"); \
    if (__builtin_amdgcn_readfirstlane(f_.x & f_.y & f_.z & f_.w)) break;                                 \
  }
#define TWICE(first)                                              \
  int live;                                                       \
  asm volatile("s_mov_b32 %0, %1" : "=s"(live) : "s"(first));     \
  do {
#define TWICE_END                                                 \
  asm volatile("s_add_u32 %0, %0, 1" : "+s"(live)::"scc");        \
  }                                                               \
  while (live < 2)
#define UNDEF(x) asm volatile("" : "=v"(x))
#define EPOCH() asm volatile("v_add_u32 %0, 1, %1" : "=v"(T) : "v"(ep) : "memory")
#define WAIT0 "\n\ts_waitcnt vmcnt(0)"
__device__ __forceinline__ void dma16(const void* base, unsigned off, unsigned lds) {
  asm volatile("s_mov_b32 m0, %2\n\ts_nop 0\n\tglobal_load_lds_dwordx4 %0, %1" ::"v"(off), "s"(base), "s"(lds) : "memory");
}
__device__ __forceinline__ v4u ld_sc1(__amdgpu_buffer_rsrc_t rs, int off) {
  return __builtin_amdgcn_raw_buffer_load_b128(rs, off, 0, AUX_SC1);
}
__device__ __forceinline__ v2u ld8_sc1(__amdgpu_buffer_rsrc_t rs, int off) {
  return __builtin_amdgcn_raw_buffer_load_b64(rs, off, 0, AUX_SC1);
}
__device__ __forceinline__ void st_sc1(__amdgpu_buffer_rsrc_t rs, int off, v4u v) {
  __builtin_amdgcn_raw_buffer_store_b128(v, rs, off, 0, AUX_SC1);
}
__device__ __forceinline__ void ln_stats(v4u r, float& mean, float& rstd) {
  float e[8] = {blo(r.x), bhi(r.x), blo(r.y), bhi(r.y), blo(r.z), bhi(r.z), blo(r.w), bhi(r.w)};
  float s = ((e[0] + e[1]) + (e[2] + e[3])) + ((e[4] + e[5]) + (e[6] + e[7]));
  mean = wave_sum(s) * (1.f / DM);
  float q = 0.f;
#pragma unroll
  for (int i = 0; i < 8; ++i) {
    float d = e[i] - mean;
    q = fmaf(d, d, q);
  }
  rstd = __builtin_amdgcn_rsqf(wave_sum(q) * (1.f / DM) + 1e-5f);
}
}

__global__ void __launch_bounds__(512, 2)
mla_glu_decode(const u16* __restrict__ x, const u16* __restrict__ ckv, const u16* __restrict__ wq, const u16* __restrict__ wuk,
               const u16* __restrict__ wuv, const u16* __restrict__ wo, const u16* __restrict__ wg, const u16* __restrict__ wu,
               const u16* __restrict__ wd, u16* __restrict__ out, char* __restrict__ ws) {
  __shared__ __attribute__((aligned(16))) char smem_[L_END];
  LAS char* sm = (LAS char*)smem_;
  const unsigned smb = (unsigned)(size_t)sm;
  const int tid = threadIdx.x, l = tid & 63, w = __builtin_amdgcn_readfirstlane(tid >> 6);
  const int col = l & 15, g4 = l >> 4, tq = (l & 15) >> 2, tp = l & 3;
  const int xcd = blockIdx.x & 7, r = blockIdx.x >> 3;
  const __amdgpu_buffer_rsrc_t rs = __builtin_amdgcn_make_buffer_rsrc(ws, 0, WS_BYTES, 0x00020000);
  asm volatile("" ::"s"(x), "s"(ckv), "s"(wq), "s"(wuk), "s"(wuv), "s"(wo), "s"(wg), "s"(wu), "s"(wd), "s"(out), "s"(ws));
  STAMP(0);

  unsigned ep, T;
  LAS char* const px = sm + L_X + (col & 1) * XP + 16 * g4;
  if ((w >> 1) == 2) dma16(x, (unsigned)((w & 1) * 1024 + l * 16), smb + L_X + (w & 1) * XP);
  const f4 z = {0.f, 0.f, 0.f, 0.f};
#define LAYERNORM()                                                                                                              \
  {                                                                                                                              \
    const v4u xr = *(LAS v4u*)(sm + L_X + (w & 1) * XP + l * 16);                                                                \
    float e[8] = {blo(xr.x), bhi(xr.x), blo(xr.y), bhi(xr.y), blo(xr.z), bhi(xr.z), blo(xr.w), bhi(xr.w)};                       \
    const float mean = wave_sum(((e[0] + e[1]) + (e[2] + e[3])) + ((e[4] + e[5]) + (e[6] + e[7]))) * (1.f / DM);                 \
    float q = 0.f;                                                                                                               \
    _Pragma("unroll") for (int i = 0; i < 8; ++i) {                                                                              \
      e[i] -= mean;                                                                                                              \
      q = fmaf(e[i], e[i], q);                                                                                                   \
    }                                                                                                                            \
    const float rstd = __builtin_amdgcn_rsqf(wave_sum(q) * (1.f / DM) + 1e-5f);                                                  \
    *(LAS v4u*)(sm + L_X + (w & 1) * XP + l * 16) =                                                                              \
        v4u{pk(e[0] * rstd, e[1] * rstd), pk(e[2] * rstd, e[3] * rstd), pk(e[4] * rstd, e[5] * rstd), pk(e[6] * rstd, e[7] * rstd)}; \
  }

  if (__builtin_expect(r < 16, 1)) {
    const int h = 4 * xcd + (r >> 2), j = r & 3, ca = j >> 1, mb = j & 1;
    const int fl = tq | ((g4 & 1) << 2);
    gld4_sc1(ep, ws, 0u);
    if (__builtin_expect(w < 4, 1)) {
      const unsigned vq = (unsigned)(((128 * j + g4) * 4096 + h * DH + 8 * (col ^ ((g4 | ((w & 2) << 1)) << 1))) * 2);
#pragma unroll 1
      for (int k = w; k < 32; k += 4) dma16(wq, vq + (unsigned)k * 32768u, smb + L_WQ + k * 1024);
    } else if (w < 6) {
      VMWAIT(1);
      LAYERNORM();
    } else {
      __builtin_amdgcn_s_sleep(UK_DELAY);
      const unsigned vu = (unsigned)((((128 * j + g4) * NH + h) * DH) * 2);
#pragma unroll 1
      for (int k = w - 6; k < 32; k += 2) dma16(wuk, vu + (unsigned)k * 32768u + 16u * (col ^ ((4 * k + g4) & 15)), smb + L_WUK + k * 1024);
    }
    unsigned p = L_WQ + 256 * (8 * g4 + tq) + 16 * ((2 * w + (tp >> 1)) ^ (fl << 1)) + 8 * (tp & 1);
    unsigned oqp = WS_QP + ((h * 4 + j) * 8 + w) * 128 + col * 64 + g4 * 16;
    unsigned pxj = L_X + (col & 1) * XP + 16 * g4 + 256 * j;
    unsigned o = WS_QP + (h * 4 + (w & 3)) * 1024 + l * 16;
    unsigned pqw = smb + L_CC + (2 * (w & 3) + ((l >> 2) & 1)) * SP + (l >> 3) * 32 + (l & 3) * 8, pqr = L_CC + (col & 7) * SP + 16 * g4;
    unsigned oqa = WS_QA + ((col & 1) * NH + h) * 2048 + (64 * j + 16 * w + 8 * (col >> 1) + 2 * g4) * 8;
    unsigned pu = L_WUK + 256 * (32 * w + col) + 16 * (g4 ^ col);
    PIN(p); PIN(oqp); PIN(pxj); PIN(o); PIN(oqa); PIN(pu); PIN(pqw); PIN(pqr);
    {
    TWICE(DRY_F ? (w >> 2) : 1)
    if (live) {
      if (w < 6) VMWAIT(0);
      else VMWAIT(16);
      EPOCH();
      STAMP(1);
      BAR();
      STAMP(2);
    } else {
      UNDEF(T);
    }
    {
      v4u af[4], xf[4];
#pragma unroll
      for (int s = 0; s < 4; ++s) {
        af[s] = trrd2(sm + p + 256 * 32 * s, sm + p + 256 * (32 * s + 4));
        xf[s] = *(LAS v4u*)(sm + pxj + 64 * s);
      }
      __builtin_amdgcn_sched_barrier(0);
      f4 acc = z;
#pragma unroll
      for (int s = 0; s < 4; ++s) acc = mfma32(af[s], xf[s], acc);
      if (live && col < 2) st_sc1(rs, oqp, v4u{pk(acc[0], acc[1]), T, pk(acc[2], acc[3]), T});
    }
    STAMP(3);
    if (w >= 6) VMWAIT(1);
    if (w >= 4) {
      BAR();
      BAR();
    } else {
      v4u a, uk[8], qf[4];
      if (live) {
        __builtin_amdgcn_s_sleep(H1_DELAY);
        asm volatile(LD16(0, 1, 2) " sc1" : "=&v"(a) : "v"(o), "s"(ws) : "memory");
        BAR();
      } else {
        UNDEF(a);
      }
#pragma unroll
      for (int s = 0; s < 4; ++s) {
        uk[s] = *(LAS v4u*)(sm + (pu ^ (64 * s)));
        uk[4 + s] = *(LAS v4u*)(sm + (pu ^ (64 * s)) + 4096);
      }
      if (live) VMWAIT(0);
      PIN(a);
      for (unsigned spin = 0;; ++spin) {
        if (!__builtin_amdgcn_ballot_w64((T & a.y & a.w) != (T | a.y | a.w)) || spin > SPIN_CAP || !live) break;
        asm volatile(LD16(0, 1, 2) " sc1" WAIT0 : "+v"(a) : "v"(o), "s"(ws) : "memory");
      }
      asm volatile("ds_write2_b32 %0, %1, %2 offset0:0 offset1:1" ::"v"(pqw), "v"(a.x), "v"(a.z) : "memory");
      STAMP(4);
      if (live) BAR();
#pragma unroll
      for (int s = 0; s < 4; ++s) qf[s] = *(LAS v4u*)(sm + pqr + 64 * s);
      __builtin_amdgcn_sched_barrier(0);
      f4 a0 = z, a1 = z;
#pragma unroll
      for (int s = 0; s < 4; ++s) {
        a0 = mfma32(uk[s], qf[s], a0);
        a1 = mfma32(uk[4 + s], qf[s], a1);
      }
      float q[4];
#pragma unroll
      for (int e = 0; e < 4; ++e) {
        const float mine = (col & 2) ? a1[e] : a0[e], other = (col & 2) ? a0[e] : a1[e];
        q[e] = mine + dpp<QP_2301>(other);
        q[e] += dpp<ROR4>(q[e]);
      }
      if (live && col < 4) st_sc1(rs, oqa, v4u{pk(q[0], q[1]), T, pk(q[2], q[3]), T});
    }
    TWICE_END;
    }
    STAMP(5);
    PROBE_T(T);
    BAR();
    __builtin_amdgcn_s_sleep(T_DELAY);
    {
      const unsigned vv = (unsigned)((((256 * ca + g4) * NH + h) * DH + 8 * (col ^ ((g4 | ((w & 2) << 1)) << 1))) * 2);
      const unsigned vo = (unsigned)(((h * DH + (l >> 5)) * DM + 256 * mb) * 2) + 16u * ((l & 31) ^ (((l >> 5) | ((w & 1) << 1) | (w & 4)) << 1));
      int kv = w + 32 * mb, ko = w + 32 * ca;
#pragma unroll 1
      for (int hf = 0; hf < 2; ++hf) {
#pragma unroll 1
        for (int i = 0; i < 4; ++i, kv = (kv + 8) & 63) dma16(wuv, vv + (unsigned)kv * 32768u, smb + L_WUV + kv * 1024);
#pragma unroll 1
        for (int i = 0; i < 4; ++i, ko = (ko + 8) & 63) dma16(wo, vo + (unsigned)ko * 2048u, smb + L_WO + ko * 1024);
        if (hf == 0) asm volatile("s_barrier" ::: "memory");
      }
    }
    const int tb = w >> 2, nq = w & 3;
    o = WS_PART + (((h * 2 + ca) * 2 + tb) * 32 + 8 * nq + g4) * 512 + 16 * col;
    unsigned pw = L_W + (tb * 32 + 8 * nq + g4) * 4;
    unsigned pv = L_WUV + 256 * (8 * g4 + tq) + 16 * ((2 * w + (tp >> 1)) ^ (fl << 1)) + 8 * (tp & 1);
    unsigned po = L_WO + 512 * (8 * g4 + tq) + 16 * ((4 * w + (tp >> 1)) ^ (fl << 1)) + 8 * (tp & 1);
    unsigned pcw = L_CC + (nq * 2 + tb) * SP2 + 16 * col + 8 * g4, pcr = L_CC + (col & 7) * SP2 + 16 * g4;
    unsigned ptw = L_CT + col * SP + (16 * w + 4 * g4) * 2, ptr = L_CT + (col & 1) * SP + 16 * g4;
    unsigned oo = WS_OPT + ((16 * mb + 2 * w + (col >> 1)) * 64 + 2 * h + ca) * 128 + ((col & 1) * 4 + g4) * 16;
    v4u mk;
#pragma unroll
    for (int d = 0; d < 4; ++d) mk[d] = (col >> 1) == d ? ((col & 1) ? 0xffff0000u : 0xffffu) : 0u;
    PIN(o); PIN(pw); PIN(pv); PIN(po); PIN(pcw); PIN(pcr); PIN(ptw); PIN(ptr); PIN(oo); PIN(mk);
    STAMP(11);
    TWICE(DRY_T ? 0 : 1)
    if (live) STAMP(12);
    if (w == 7) {
      const unsigned ol = WS_LSE + (((l >> 5) * 2 + (h >> 4)) * 32 + (l & 31)) * 128 + (h & 15) * 8;
      v2u la;
      UNDEF(la);
      for (unsigned spin = 0; live; ++spin) {
        asm volatile("global_load_dwordx2 %0, %1, %2 sc1" WAIT0 : "+v"(la) : "v"(ol), "s"(ws) : "memory");
        if (!__builtin_amdgcn_ballot_w64(la.y != T) || spin > SPIN_CAP) break;
      }
      float M = asf(la.x);
      M = fmax2(M, dpp<ROR8>(M));
      M = fmax2(M, dpp<ROR4>(M));
      M = fmax2(M, dpp<ROR2>(M));
      M = fmax2(M, dpp<ROR1>(M));
      M = sw16_max(M);
      const float e = __builtin_amdgcn_exp2f(asf(la.x) - M);
      float L = e + dpp<ROR8>(e);
      L += dpp<ROR4>(L);
      L += dpp<ROR2>(L);
      L += dpp<ROR1>(L);
      L = sw16_add(L, L);
      const float wt = e * __builtin_amdgcn_rcpf(L);
      *(LAS unsigned*)(sm + L_W + 4 * l) = pk(wt, wt);
      STAMP(7);
    }
    v4u a[4];
    if (live) {
      VMWAIT(0);
      STAMP(6);
      BAR();
      SWEEP_4(a, o, 0, 256, 2048, 2304, "");
    } else {
      UNDEF(a[0]); UNDEF(a[1]); UNDEF(a[2]); UNDEF(a[3]);
    }
    v4u A0 = mk & *(LAS unsigned*)(sm + pw), A1 = mk & *(LAS unsigned*)(sm + pw + 16);
    v4u av[8], ao[8];
#pragma unroll
    for (int s = 0; s < 8; ++s) av[s] = trrd2(sm + pv + 256 * 32 * s, sm + pv + 256 * (32 * s + 4));
#pragma unroll
    for (int t = 0; t < 2; ++t)
#pragma unroll
      for (int s = 0; s < 4; ++s) ao[4 * t + s] = trrd2(sm + (po ^ (32 * t)) + 512 * 32 * s, sm + (po ^ (32 * t)) + 512 * (32 * s + 4));
    if (live) VMWAIT(0);
    asm volatile("" : "+v"(a[0]), "+v"(a[1]), "+v"(a[2]), "+v"(a[3]), "+v"(A0), "+v"(A1)::"memory");
    for (unsigned spin = 0;; ++spin) {
      unsigned mx = 0u;
#pragma unroll
      for (int i = 0; i < 4; ++i) mx = max(max(mx, max(a[i].x, a[i].y)), max(a[i].z, a[i].w));
      if (!__builtin_amdgcn_ballot_w64(mx == ~0u) || spin > SPIN_CAP || !live) break;
      RESWEEP_4(a, o, 0, 256, 2048, 2304);
    }
    STAMP(8);
    {
      f4 c0 = mfma32(A0, a[0], z), c1 = mfma32(A0, a[1], z);
      c0 = mfma32(A1, a[2], c0);
      c1 = mfma32(A1, a[3], c1);
      if (g4 < 2) {
        *(LAS v2u*)(sm + pcw) = v2u{pk(c0[0], c0[1]), pk(c0[2], c0[3])};
        *(LAS v2u*)(sm + pcw + 256) = v2u{pk(c1[0], c1[1]), pk(c1[2], c1[3])};
      }
    }
    if (live) BAR();
    {
      f4 acc = z;
      v4u bf[8];
#pragma unroll
      for (int s = 0; s < 8; ++s) bf[s] = *(LAS v4u*)(sm + pcr + 64 * s);
      __builtin_amdgcn_sched_barrier(0);
#pragma unroll
      for (int s = 0; s < 8; ++s) acc = mfma32(av[s], bf[s], acc);
#pragma unroll
      for (int e = 0; e < 4; ++e) {
        acc[e] += dpp<QP_2301>(acc[e]);
        acc[e] += dpp<ROR4>(acc[e]);
      }
      if (col < 2) *(LAS v2u*)(sm + ptw) = v2u{pk(acc[0], acc[1]), pk(acc[2], acc[3])};
    }
    if (live) BAR();
    {
      f4 c0 = z, c1 = z;
      v4u bf[4];
#pragma unroll
      for (int s = 0; s < 4; ++s) bf[s] = *(LAS v4u*)(sm + ptr + 64 * s);
      __builtin_amdgcn_sched_barrier(0);
#pragma unroll
      for (int s = 0; s < 4; ++s) {
        c0 = mfma32(ao[s], bf[s], c0);
        c1 = mfma32(ao[4 + s], bf[s], c1);
      }
      if (col & 2) c0 = c1;
      if (live && col < 4) st_sc1(rs, oo, v4u{pk(c0[0], c0[1]), T, pk(c0[2], c0[3]), T});
    }
    TWICE_END;
    STAMP(9);
  } else {
    const int a = r - 16, b = xcd >> 2, n = 8 * (xcd & 3) + (a >> 1), g = a & 1;
    const int sl = 12 * xcd + a, rd = (4 * xcd + a) & 31;
    const bool glu = a < 12 && sl < NSL;
    const int v = w & 3;
    unsigned op = WS_PART + ((((16 * g + (col & 7)) * 2 + (v >> 1)) * 2 + b) * 32 + n) * 512 + 256 * (v & 1) + (col >> 3) * 64 +
                  (g4 & 1) * 32 + (g4 >> 1) * 16;
    unsigned xres;
    gld4_sc1(ep, ws, 0u);
    asm volatile("global_load_ushort %0, %1, %2" : "=v"(xres) : "v"((unsigned)(((l >> 4) & 1) * 1024 + rd * 32 + col * 2)), "s"(x) : "memory");
    if (w >= 4) {
#pragma unroll
      for (int q = 0; q < 4; ++q) st_sc1(rs, op + 128 * (q >> 1) + (q & 1) * 8 * 65536, v4u{~0u, ~0u, ~0u, ~0u});
      if (w == 7 && l < 32) *(LAS unsigned*)(sm + L_FL + 4 * l) = 0u;
    }
    unsigned o = WS_QA + (b * NH + 16 * g) * 2048 + w * 4096 + l * 16, pqs = smb + L_Q + 2 * w * CP + l * 8;
    PIN(o); PIN(pqs);
    {
    TWICE(DRY_A ? 0 : 1)
    if (live) {
      const unsigned vc = (unsigned)(((b * SK + 128 * n) * DC + 8 * l) * 2);
      STAMP(10);
      __builtin_amdgcn_s_sleep(A_DELAY);
#pragma unroll 1
      for (int q = 0; q < 4; ++q) {
        int row = w + ((64 * g + 32 * q) & 127);
        asm volatile("s_barrier" ::: "memory");
#pragma unroll 1
        for (int i = 0; i < 4; ++i, row += 8) dma16(ckv, vc + row * 1024u, smb + L_C + row * CP);
      }
      if ((w >> 1) == 2) {
        VMWAIT(22);
        LAYERNORM();
      }
      VMWAIT(0);
      EPOCH();
      PROBE_T(T);
      BAR();
      STAMP(1);
    } else {
      UNDEF(T);
    }
#define QISSUE()                                \
  if (live) {                                   \
    SWEEP_4(qg, o, 0, 1024, 2048, 3072, "");    \
  } else {                                      \
    UNDEF(qg[0]); UNDEF(qg[1]); UNDEF(qg[2]); UNDEF(qg[3]); \
  }
#define QGATHER()                                                                                                           \
  if (live) VMWAIT(0);                                                                                                      \
  asm volatile("" : "+v"(qg[0]), "+v"(qg[1]), "+v"(qg[2]), "+v"(qg[3])::"memory");                                          \
  for (unsigned spin = 0;; ++spin) {                                                                                        \
    if (!TAGS_BAD4(qg) || spin > SPIN_CAP || !live) break;                                                                           \
    RESWEEP_4(qg, o, 0, 1024, 2048, 3072);                                                                             \
  }                                                                                                                         \
  STAMP(3);                                                                                                                 \
  asm volatile("ds_write2_b32 %0, %2, %3 offset0:0 offset1:1\n\tds_write2_b32 %0, %4, %5 offset0:128 offset1:129\n\t"        \
               "ds_write2_b32 %1, %6, %7 offset0:0 offset1:1\n\tds_write2_b32 %1, %8, %9 offset0:128 offset1:129" ::"v"(pqs), \
               "v"(pqs + CP), "v"(qg[0].x), "v"(qg[0].z), "v"(qg[1].x), "v"(qg[1].z), "v"(qg[2].x), "v"(qg[2].z), "v"(qg[3].x), \
               "v"(qg[3].z)                                                                                                 \
               : "memory")
    if (__builtin_expect(w < 4, 1)) {
      v4u fr[32], qg[4];
      QISSUE();
      LAS char* pc = sm + L_C + (32 * w + col) * CP + 16 * g4;
#pragma unroll
      for (int kt = 0; kt < 2; ++kt)
#pragma unroll
        for (int s = 0; s < 16; ++s) fr[16 * kt + s] = *(LAS v4u*)(pc + 16 * kt * CP + 64 * s);
      unsigned p = L_Q + col * CP + 16 * g4, pp = L_P + w * 1024 + l * 16, pm = L_MX + col * 16 + w * 4;
      PIN(p); PIN(pp); PIN(pm);
      STAMP(2);
      QGATHER();
      if (live) BAR();
      STAMP(4);
      f4 s0 = z, s1 = z;
      v4u qf[16];
#pragma unroll
      for (int s = 0; s < 6; ++s) qf[s] = *(LAS v4u*)(sm + p + 64 * s);
      __builtin_amdgcn_sched_barrier(0);
#pragma unroll
      for (int s = 0; s < 16; ++s) {
        if (s + 6 < 16) qf[s + 6] = *(LAS v4u*)(sm + p + 64 * (s + 6));
        s0 = mfma32(fr[s], qf[s], s0);
        s1 = mfma32(fr[16 + s], qf[s], s1);
        __builtin_amdgcn_sched_barrier(0);
      }
      float mx = fmax2(fmax2(fmax2(s0[0], s0[1]), fmax2(s0[2], s0[3])), fmax2(fmax2(s1[0], s1[1]), fmax2(s1[2], s1[3])));
      mx = sw32_max(sw16_max(mx));
      constexpr float SC = 0.08838834764831845f * 1.4426950408889634f;
      const float mi = __builtin_ceilf(mx * SC);
#pragma unroll
      for (int e = 0; e < 4; ++e) {
        s0[e] = __builtin_amdgcn_exp2f(fmaf(s0[e], SC, -mi));
        s1[e] = __builtin_amdgcn_exp2f(fmaf(s1[e], SC, -mi));
      }
      *(LAS float*)(sm + pm) = mi;
      *(LAS v4u*)(sm + pp) = v4u{pk(s0[0], s0[1]), pk(s0[2], s0[3]), pk(s1[0], s1[1]), pk(s1[2], s1[3])};
      if (live) BAR();
      STAMP(5);
    } else {
      v4u fr[32], qg[4];
      QISSUE();
      LAS char* p0 = sm + L_C + (4 * g4 + tq) * CP + 256 * v + 8 * tp;
      unsigned p1 = L_C + (64 + 4 * g4 + tq) * CP + 256 * v + 8 * tp;
      PIN(p1);
#pragma unroll
      for (int ct = 0; ct < 8; ++ct)
#pragma unroll
        for (int u = 0; u < 4; ++u) {
          LAS char* p = (u < 2 ? p0 : sm + p1) + 32 * (u & 1) * CP + 32 * ct;
          fr[4 * ct + u] = trrd2(p, p + 16 * CP);
        }
      unsigned ol = WS_LSE + ((b * 2 + g) * 32 + n) * 128 + col * 8, pp = L_P + l * 16, pm = L_MX + col * 16;
      v4u one = {0x3f803f80u, 0x3f803f80u, 0x3f803f80u, 0x3f803f80u};
      PIN(ol); PIN(pp); PIN(pm); PIN(one); PIN(op);
      STAMP(2);
      QGATHER();
      if (live) BAR();
      STAMP(4);
      if (live) {
        const int ln = 128 * a + 64 * (v >> 1) + l;
        touch((v & 1) ? wu : wg, (unsigned)((ln >> 2) * (FF * 2)) + (unsigned)min(384 * xcd + 128 * (ln & 3), FF * 2 - 4), smb + L_JUNK);
      }
      if (live) BAR();
      const f4 m4 = *(LAS f4*)(sm + pm);
      v4u pf[4];
#pragma unroll
      for (int u = 0; u < 4; ++u) pf[u] = *(LAS v4u*)(sm + pp + u * 1024);
      const float M = fmax2(fmax2(m4[0], m4[1]), fmax2(m4[2], m4[3]));
#pragma unroll
      for (int u = 0; u < 4; ++u) {
        const unsigned kk = min((unsigned)(M - m4[u]), 255u) * 0x00800080u;
#pragma unroll
        for (int e = 0; e < 4; ++e) pf[u][e] = scale_dn(pf[u][e], kk);
      }
      f4 ls = z;
#pragma unroll
      for (int u = 0; u < 4; ++u) ls = mfma32(one, pf[u], ls);
      const float il = __builtin_amdgcn_rcpf(ls[0]);
      if (live && v == 0 && g4 == 0)
        __builtin_amdgcn_raw_buffer_store_b64(v2u{__builtin_bit_cast(unsigned, M + __builtin_amdgcn_logf(ls[0])), T}, rs, ol, 0, AUX_SC1);
#pragma unroll
      for (int q = 0; q < 2; ++q) {
        f4 c[4];
#pragma unroll
        for (int k = 0; k < 4; ++k) c[k] = z;
#pragma unroll
        for (int u = 0; u < 4; ++u)
#pragma unroll
          for (int k = 0; k < 4; ++k) c[k] = mfma32(fr[16 * q + 4 * k + u], pf[u], c[k]);
        v4u R[2];
#pragma unroll
        for (int k = 0; k < 2; ++k) {
          const f4 c0 = c[2 * k] * il, c1 = c[2 * k + 1] * il;
          const unsigned x0 = min(pk(c0[0], c0[1]), 0xfffffffeu), x1 = min(pk(c0[2], c0[3]), 0xfffffffeu);
          const unsigned y0 = min(pk(c1[0], c1[1]), 0xfffffffeu), y1 = min(pk(c1[2], c1[3]), 0xfffffffeu);
          auto r0 = __builtin_amdgcn_permlane16_swap(x0, y0, false, false);
          auto r1 = __builtin_amdgcn_permlane16_swap(x1, y1, false, false);
          R[k] = v4u{r0[0], r1[0], r0[1], r1[1]};
        }
        v4u A, B;
#pragma unroll
        for (int e = 0; e < 4; ++e) {
          A[e] = (unsigned)__builtin_amdgcn_update_dpp((int)R[0][e], (int)R[1][e], ROR8, 0xf, 0xc, false);
          B[e] = (unsigned)__builtin_amdgcn_update_dpp((int)R[1][e], (int)R[0][e], ROR8, 0xf, 0x3, false);
        }
        PIN(A); PIN(B);
        if (live) {
          st_sc1(rs, op + 128 * q, A);
          st_sc1(rs, op + 128 * q + 8 * 65536, B);
        }
      }
      if (live) FLAG_SET(v);
      STAMP(6);
    }
    TWICE_END;
    }
      if (glu && w < 4) {
        v4u af[8], xf[8];
#pragma unroll
        for (int s = 0; s < 8; ++s) xf[s] = *(LAS v4u*)(px + 512 * (w >> 1) + 64 * s);
        unsigned oog = WS_OPG + ((8 * w + (col >> 1)) * 128 + sl) * 128 + ((col & 1) * 4 + g4) * 16;
        PIN(oog);
        FLAG_WAIT4(0);
        unsigned og = (unsigned)(((128 * w + (l >> 1)) * FF + 16 * sl + 8 * (l & 1)) * 2);
#pragma unroll 1
        for (int i = 0; i < 4; ++i, og += 32 * FF * 2) {
          dma16(wg, og, smb + L_GG + (4 * w + i) * 1024);
          dma16(wu, og, smb + L_GG + 16384 + (4 * w + i) * 1024);
        }
#pragma unroll 1
        for (int i = 0; i < 4; ++i) dma16(wd, (unsigned)(((16 * sl + 4 * w + i) * DM + 8 * l) * 2), smb + L_GD + (4 * w + i) * CP);
        TWICE(DRY_G ? 0 : 1)
        if (live) {
          VMWAIT(4);
          FLAG_SET(4 + w);
          FLAG_WAIT4(1);
        }
        STAMP(7);
        LAS char* pg = sm + L_GG + (w & 1) * 16384 + 32 * (256 * (w >> 1) + 8 * g4 + tq) + 8 * tp;
        f4 acc = z;
#pragma unroll
        for (int s = 0; s < 8; ++s) af[s] = trrd2(pg + 32 * 32 * s, pg + 32 * (32 * s + 4));
        __builtin_amdgcn_sched_barrier(0);
#pragma unroll
        for (int s = 0; s < 8; ++s) acc = mfma32(af[s], xf[s], acc);
        *(LAS f4*)(sm + L_GR + ((w & 1) * 2 + (w >> 1)) * 1024 + l * 16) = acc;
        if (live) {
          VMWAIT(0);
          FLAG_SET(8 + w);
          FLAG_WAIT4(2);
        }
        v2u dn[8];
#pragma unroll
        for (int t = 0; t < 8; ++t) dn[t] = trrd(sm + L_GD + (4 * g4 + tq) * CP + 32 * (8 * w + t) + 8 * tp);
        LAS char* pr = sm + L_GR + l * 16;
        const f4 gx = *(LAS f4*)(pr) + *(LAS f4*)(pr + 1024), ux = *(LAS f4*)(pr + 2048) + *(LAS f4*)(pr + 3072);
        float hh[4];
#pragma unroll
        for (int e = 0; e < 4; ++e) hh[e] = ux[e] * __builtin_amdgcn_rcpf(1.f + __builtin_amdgcn_exp2f(-1.4426950408889634f * gx[e]));
        const v2u hb = {pk(hh[0], hh[1]), pk(hh[2], hh[3])};
        f4 d = z;
#pragma unroll
        for (int t = 0; t < 8; ++t) d = mfma16(dn[t], (col >> 1) == t ? hb : v2u{0u, 0u}, d);
        v4u dv = {pk(d[0], d[1]), T, pk(d[2], d[3]), T};
        PIN(dv);
        if (live) st_sc1(rs, oog, dv);
        TWICE_END;
        STAMP(9);
      }
    if (a >= 12) {
      unsigned ot = WS_OPT + rd * 8192 + tid * 16, og = WS_OPG + rd * 16384 + tid * 16;
      const bool two = (tid >> 3) < NSL - 64;
      unsigned og2 = two ? og + 8192 : og;
      const unsigned m = (col & 1) ? 0x3f800000u : 0x3f80u;
      v4u sel = {col < 2 ? m : 0u, (col >> 1) == 1 ? m : 0u, col < 2 ? m : 0u, (col >> 1) == 1 ? m : 0u};
      unsigned m2 = two ? ~0u : 0u;
      unsigned prw = L_RR + w * 128 + (l & 7) * 16, prr = L_RR + (l & 31) * 4, oy = ((l >> 4) & 1) * 1024 + rd * 32 + col * 2;
      PIN(ot); PIN(og); PIN(og2); PIN(sel); PIN(m2); PIN(prw); PIN(prr); PIN(oy);
      PIN(xres);
      TWICE(DRY_R ? 0 : 1)
      v4u p0, p1, p2;
      UNDEF(p0); UNDEF(p1); UNDEF(p2);
      for (unsigned spin = 0; live; ++spin) {
        asm volatile(LD16(0, 3, 6) " sc1\n\t" LD16(1, 4, 6) " sc1\n\t" LD16(2, 5, 6) " sc1" WAIT0
                     : "+v"(p0), "+v"(p1), "+v"(p2)
                     : "v"(ot), "v"(og), "v"(og2), "s"(ws)
                     : "memory");
        if (!__builtin_amdgcn_ballot_w64((T & p0.y & p0.w & p1.y & p1.w & p2.y & p2.w) != (T | p0.y | p0.w | p1.y | p1.w | p2.y | p2.w)) ||
            spin > SPIN_CAP)
          break;
      }
      STAMP(8);
      f4 acc = mfma32(sel, v4u{p0.x, p0.z, p1.x, p1.z}, z);
      acc = mfma32(sel, v4u{p2.x & m2, p2.z & m2, 0u, 0u}, acc);
#pragma unroll
      for (int e = 0; e < 4; ++e) acc[e] += dpp<ROR8>(acc[e]);
      if (l < 8) *(LAS f4*)(sm + prw) = acc;
      if (live) BAR();
      if (w == 0 && l < 32) {
        float t[8];
#pragma unroll
        for (int i = 0; i < 8; ++i) t[i] = *(LAS float*)(sm + prr + i * 128);
        const float y = blo(xres) + (((t[0] + t[1]) + (t[2] + t[3])) + ((t[4] + t[5]) + (t[6] + t[7])));
        unsigned yb = pk(y, y);
        PIN(yb);
        if (live) *(u16*)((char*)out + oy) = (u16)yb;
        if (live && rd == 0 && l == 0) __builtin_amdgcn_raw_buffer_store_b32(T, rs, WS_E, 0, AUX_SC1);
      }
      TWICE_END;
      STAMP(9);
    }
  }
  STAMP_FLUSH();
}

#define WS_END 0
#define WS_BASE (WS_END ? ((ws_size - (size_t)WS_END * 1048576) & ~(size_t)4095) : 0)
extern "C" void kernel_launch(void* const* d_in, const int* in_sizes, int n_in, void* d_out, int out_size, void* d_ws, size_t ws_size,
                              hipStream_t stream) {
  mla_glu_decode<<<256, 512, 0, stream>>>((const u16*)d_in[0], (const u16*)d_in[1], (const u16*)d_in[2], (const u16*)d_in[3],
                                          (const u16*)d_in[4], (const u16*)d_in[5], (const u16*)d_in[7], (const u16*)d_in[8],
                                          (const u16*)d_in[9], (u16*)d_out, (char*)d_ws + WS_BASE);
}
```

```cpp
#include <hip/hip_runtime.h>
#include <stdint.h>

#define GAS __attribute__((address_space(1)))
#define LAS __attribute__((address_space(3)))
typedef unsigned short u16;
typedef unsigned v4u __attribute__((ext_vector_type(4)));
typedef unsigned v2u __attribute__((ext_vector_type(2)));
typedef float f4 __attribute__((ext_vector_type(4)));
typedef float f2 __attribute__((ext_vector_type(2)));
typedef __bf16 bf8 __attribute__((ext_vector_type(8)));
typedef __bf16 bf2 __attribute__((ext_vector_type(2)));
typedef short s4 __attribute__((ext_vector_type(4)));

namespace {
constexpr int NH = 32, SK = 4096, DH = 128, DM = 512, DC = 512, FF = 1376;
constexpr int NSL = FF / 16;
constexpr int WS_E = 0;
constexpr int WS_QP = 4096;
constexpr int WS_QA = WS_QP + NH * 4 * 1024;
constexpr int WS_LSE = WS_QA + 2 * NH * 2048;
constexpr int WS_OPT = WS_LSE + 2 * 2 * 32 * 128;
constexpr int WS_OPG = WS_OPT + 32 * 64 * 128;
constexpr int WS_PART = WS_OPG + 32 * 128 * 128;
constexpr int WS_BYTES = WS_PART + NH * 2 * 2 * 32 * 512;
constexpr int WS_STAMP = 7 << 19;
constexpr int CP = 1056;
constexpr int XP = 1088, SP = 288, SP2 = 544;
constexpr int L_C = 0, L_Q = 128 * CP, L_P = L_Q + 16 * CP, L_X = L_P + 4096, L_MX = L_X + 2 * XP, L_RR = L_MX + 256,
              L_JUNK = L_RR + 1024, L_FL = L_JUNK + 256, L_END = L_FL + 128;
static_assert(L_END <= 163840, "LDS");
constexpr int L_GG = 0, L_GD = 32768, L_GR = 65536;
constexpr int L_WUV = 0, L_WO = 65536, L_WQ = 0, L_WUK = 32768, L_CC = 131072, L_CT = L_CC + 8 * SP2, L_W = L_CT + 2 * SP;
#define UK_DELAY 4
#define H1_DELAY 4
#define T_DELAY 30
#define GLU_FLAG_Q 1
#define NOCLEAR 0
#define CODE_LINES 104
#define TLB_WARM 0
#define CLEAR_A 1
#define CLEAR_B 1
#define CLEAR_MODE 0
#define CLEAR_DELAY 0
#define DRY_F 0
#define DRY_T 0
#define DRY_A 0
#define DRY_G 0
#define DRY_R 0
#define NT_CKV 0
#define NT_WQ 0
#define NT_WUK 0
#define NT_T 0
#define NT_G 0
#define OPT_W 0
#define QA_WARM 0
#define LSE_DELAY 40
#define A_DELAY 28
#define PF_GLU 1
constexpr unsigned SPIN_CAP = 1u << 20;
constexpr int AUX_SC1 = 16;

#define STAMP_W 0
#define STAMP(i)
#define STAMP_FLUSH()
#define PROBE_T(T_)

__device__ __forceinline__ f4 mfma32(v4u a, v4u b, f4 c) {
  return __builtin_amdgcn_mfma_f32_16x16x32_bf16(__builtin_bit_cast(bf8, a), __builtin_bit_cast(bf8, b), c, 0, 0, 0);
}
__device__ __forceinline__ f4 mfma16(v2u a, v2u b, f4 c) {
  return __builtin_amdgcn_mfma_f32_16x16x16bf16_1k(__builtin_bit_cast(s4, a), __builtin_bit_cast(s4, b), c, 0, 0, 0);
}
__device__ __forceinline__ unsigned pk(float lo, float hi) {
  f2 x = {lo, hi};
  return __builtin_bit_cast(unsigned, __builtin_convertvector(x, bf2));
}
__device__ __forceinline__ float asf(unsigned u) { return __builtin_bit_cast(float, u); }
__device__ __forceinline__ float blo(unsigned u) { return __builtin_bit_cast(float, u << 16); }
__device__ __forceinline__ float bhi(unsigned u) { return __builtin_bit_cast(float, u & 0xffff0000u); }
template <int CTRL> __device__ __forceinline__ float dpp(float v) {
  return __builtin_bit_cast(float, __builtin_amdgcn_update_dpp(0, __builtin_bit_cast(int, v), CTRL, 0xf, 0xf, true));
}
__device__ __forceinline__ float fmax2(float a, float b) {
#pragma float_control(precise, off)
  return __builtin_fmaxf(a, b);
}
typedef unsigned short h2 __attribute__((ext_vector_type(2)));
__device__ __forceinline__ unsigned scale_dn(unsigned p, unsigned kk) {
  return __builtin_bit_cast(unsigned, __builtin_elementwise_sub_sat(__builtin_bit_cast(h2, p), __builtin_bit_cast(h2, kk)));
}
#define PIN(x) asm volatile("" : "+v"(x))
constexpr int ROR1 = 0x121, ROR2 = 0x122, ROR4 = 0x124, ROR8 = 0x128, QP_2301 = 0x4e, QP_2222 = 0xaa;
__device__ __forceinline__ float sw32_add(float a, float b) {
  auto r = __builtin_amdgcn_permlane32_swap(__builtin_bit_cast(unsigned, a), __builtin_bit_cast(unsigned, b), false, false);
  return asf(r[0]) + asf(r[1]);
}
__device__ __forceinline__ float sw16_add(float a, float b) {
  auto r = __builtin_amdgcn_permlane16_swap(__builtin_bit_cast(unsigned, a), __builtin_bit_cast(unsigned, b), false, false);
  return asf(r[0]) + asf(r[1]);
}
__device__ __forceinline__ float vmax(unsigned a, unsigned b) {
  float r;
  asm("v_max_f32 %0, %1, %2" : "=v"(r) : "v"(a), "v"(b));
  return r;
}
__device__ __forceinline__ float sw32_max(float a) {
  auto r = __builtin_amdgcn_permlane32_swap(__builtin_bit_cast(unsigned, a), __builtin_bit_cast(unsigned, a), false, false);
  return vmax(r[0], r[1]);
}
__device__ __forceinline__ float sw16_max(float a) {
  auto r = __builtin_amdgcn_permlane16_swap(__builtin_bit_cast(unsigned, a), __builtin_bit_cast(unsigned, a), false, false);
  return vmax(r[0], r[1]);
}
__device__ __forceinline__ float wave_sum(float v) {
  v += dpp<ROR8>(v);
  v += dpp<ROR4>(v);
  v += dpp<ROR2>(v);
  v += dpp<ROR1>(v);
  v = sw16_add(v, v);
  return sw32_add(v, v);
}
__device__ __forceinline__ v2u trrd(LAS char* p) {
  return __builtin_bit_cast(v2u, __builtin_amdgcn_ds_read_tr16_b64_v4i16((LAS s4*)p));
}
__device__ __forceinline__ v4u trrd2(LAS char* p0, LAS char* p1) {
  v2u a = trrd(p0), b = trrd(p1);
  return v4u{a.x, a.y, b.x, b.y};
}
#define BAR() asm volatile("s_waitcnt lgkmcnt(0)\n\ts_barrier" ::: "memory")
#define VMWAIT(k) asm volatile("s_waitcnt vmcnt(%0)" ::"n"(k) : "memory")
__device__ __forceinline__ void gld16(v4u& d, const void* base, unsigned off) {
  asm volatile("global_load_dwordx4 %0, %1, %2" : "=v"(d) : "v"(off), "s"(base) : "memory");
}
__device__ __forceinline__ void gld4_sc1(unsigned& d, const void* base, unsigned off) {
  asm volatile("global_load_dword %0, %1, %2 sc1" : "=v"(d) : "v"(off), "s"(base) : "memory");
}
template <int NT = 0> __device__ __forceinline__ void touch(const void* base, unsigned off, unsigned lds) {
  if (NT) asm volatile("s_mov_b32 m0, %2\n\ts_nop 0\n\tglobal_load_lds_dword %0, %1 nt" ::"v"(off), "s"(base), "s"(lds) : "memory");
  else asm volatile("s_mov_b32 m0, %2\n\ts_nop 0\n\tglobal_load_lds_dword %0, %1" ::"v"(off), "s"(base), "s"(lds) : "memory");
}
__device__ __forceinline__ void touch_sc1(const void* base, unsigned off, unsigned lds) {
  asm volatile("s_mov_b32 m0, %2\n\ts_nop 0\n\tglobal_load_lds_dword %0, %1 sc1" ::"v"(off), "s"(base), "s"(lds) : "memory");
}
#define LD16(i, a, o) "global_load_dwordx4 %" #i ", %" #a ", %" #o
#define SWEEP_4(q, o, o0, o1, o2, o3, W)                                                                                     \
  asm volatile(LD16(0, 4, 5) " offset:" #o0 " sc1\n\t" LD16(1, 4, 5) " offset:" #o1 " sc1\n\t" LD16(2, 4, 5) " offset:" #o2         \
               " sc1\n\t" LD16(3, 4, 5) " offset:" #o3 " sc1" W                                                               \
               : "=&v"((q)[0]), "=&v"((q)[1]), "=&v"((q)[2]), "=&v"((q)[3])                                                  \
               : "v"(o), "s"(ws)                                                                                             \
               : "memory")
#define RESWEEP_4(q, o, o0, o1, o2, o3)                                                                                      \
  asm volatile(LD16(0, 4, 5) " offset:" #o0 " sc1\n\t" LD16(1, 4, 5) " offset:" #o1 " sc1\n\t" LD16(2, 4, 5) " offset:" #o2         \
               " sc1\n\t" LD16(3, 4, 5) " offset:" #o3 " sc1\n\ts_waitcnt vmcnt(0)"                                            \
               : "+v"((q)[0]), "+v"((q)[1]), "+v"((q)[2]), "+v"((q)[3])                                                      \
               : "v"(o), "s"(ws)                                                                                             \
               : "memory")
#define SWEEP_L(W)                                                                                                          \
  asm volatile("global_load_dwordx2 %0, %3, %5 sc1\n\tglobal_load_dwordx2 %1, %4, %5 sc1\n\t"                              \
               "global_load_dwordx2 %2, %4, %5 offset:512 sc1" W                                                            \
               : "=&v"(la), "=&v"(l0), "=&v"(l1)                                                                            \
               : "v"(ola), "v"(olo), "s"(ws)                                                                                \
               : "memory")
#define SWEEP_2(p, q, oa, ob, W) \
  asm volatile(LD16(0, 2, 4) " sc1\n\t" LD16(1, 3, 4) " sc1" W : "=&v"(p), "=&v"(q) : "v"(oa), "v"(ob), "s"(ws) : "memory")
#define TAGS_BAD4(q) \
  __builtin_amdgcn_ballot_w64((T & (q)[0].y & (q)[0].w & (q)[1].y & (q)[1].w & (q)[2].y & (q)[2].w & (q)[3].y & (q)[3].w) != \
                              (T | (q)[0].y | (q)[0].w | (q)[1].y | (q)[1].w | (q)[2].y | (q)[2].w | (q)[3].y | (q)[3].w))
#define FLAG_SET(i) asm volatile("ds_write_b32 %0, %1" ::"v"(smb + L_FL + 4 * (i)), "v"(1u) : "memory")
#define FLAG_WAIT4(i)                                                                                     \
  for (unsigned spin_ = 0; spin_ < SPIN_CAP; ++spin_) {                                                   \
    v4u f_;                                                                                               \
    asm volatile("ds_read_b128 %0, %1\n\ts_waitcnt lgkmcnt(0)" : "=v"(f_) : "v"(smb + L_FL + 16 * (i)) : "memory"); \
    if (__builtin_amdgcn_readfirstlane(f_.x & f_.y & f_.z & f_.w)) break;                                 \
  }
#define TWICE(first)                                              \
  int live;                                                       \
  asm volatile("s_mov_b32 %0, %1" : "=s"(live) : "s"(first));     \
  do {
#define TWICE_END                                                 \
  asm volatile("s_add_u32 %0, %0, 1" : "+s"(live)::"scc");        \
  }                                                               \
  while (live < 2)
#define UNDEF(x) asm volatile("" : "=v"(x))
#define EPOCH() asm volatile("v_add_u32 %0, 1, %1" : "=v"(T) : "v"(ep) : "memory")
#define WAIT0 "\n\ts_waitcnt vmcnt(0)"
template <int NT = 0> __device__ __forceinline__ void dma16(const void* base, unsigned off, unsigned lds) {
  if (NT) asm volatile("s_mov_b32 m0, %2\n\ts_nop 0\n\tglobal_load_lds_dwordx4 %0, %1 nt" ::"v"(off), "s"(base), "s"(lds) : "memory");
  else asm volatile("s_mov_b32 m0, %2\n\ts_nop 0\n\tglobal_load_lds_dwordx4 %0, %1" ::"v"(off), "s"(base), "s"(lds) : "memory");
}
__device__ __forceinline__ v4u ld_sc1(__amdgpu_buffer_rsrc_t rs, int off) {
  return __builtin_amdgcn_raw_buffer_load_b128(rs, off, 0, AUX_SC1);
}
__device__ __forceinline__ v2u ld8_sc1(__amdgpu_buffer_rsrc_t rs, int off) {
  return __builtin_amdgcn_raw_buffer_load_b64(rs, off, 0, AUX_SC1);
}
__device__ __forceinline__ void st_sc1(__amdgpu_buffer_rsrc_t rs, int off, v4u v) {
  __builtin_amdgcn_raw_buffer_store_b128(v, rs, off, 0, AUX_SC1);
}
__device__ __forceinline__ void ln_stats(v4u r, float& mean, float& rstd) {
  float e[8] = {blo(r.x), bhi(r.x), blo(r.y), bhi(r.y), blo(r.z), bhi(r.z), blo(r.w), bhi(r.w)};
  float s = ((e[0] + e[1]) + (e[2] + e[3])) + ((e[4] + e[5]) + (e[6] + e[7]));
  mean = wave_sum(s) * (1.f / DM);
  float q = 0.f;
#pragma unroll
  for (int i = 0; i < 8; ++i) {
    float d = e[i] - mean;
    q = fmaf(d, d, q);
  }
  rstd = __builtin_amdgcn_rsqf(wave_sum(q) * (1.f / DM) + 1e-5f);
}
}

__global__ void __launch_bounds__(512, 2)
mla_glu_decode(const u16* __restrict__ x, const u16* __restrict__ ckv, const u16* __restrict__ wq, const u16* __restrict__ wuk,
               const u16* __restrict__ wuv, const u16* __restrict__ wo, const u16* __restrict__ wg, const u16* __restrict__ wu,
               const u16* __restrict__ wd, u16* __restrict__ out, char* __restrict__ ws) {
  __shared__ __attribute__((aligned(16))) char smem_[L_END];
  LAS char* sm = (LAS char*)smem_;
  const unsigned smb = (unsigned)(size_t)sm;
  const int tid = threadIdx.x, l = tid & 63, w = __builtin_amdgcn_readfirstlane(tid >> 6);
  const int col = l & 15, g4 = l >> 4, tq = (l & 15) >> 2, tp = l & 3;
  const int xcd = blockIdx.x & 7, r = blockIdx.x >> 3;
  const __amdgpu_buffer_rsrc_t rs = __builtin_amdgcn_make_buffer_rsrc(ws, 0, WS_BYTES, 0x00020000);
  if (r >= 16 && w >= 6) {
    const char* pc0;
    asm volatile("s_getpc_b64 %0" : "=s"(pc0));
    touch(pc0, (unsigned)min(64 * (w - 6) + l, CODE_LINES - 1) * 128u, smb + L_JUNK);
  }
  __builtin_amdgcn_sched_barrier(0);
  asm volatile("" ::"s"(x), "s"(ckv), "s"(wq), "s"(wuk), "s"(wuv), "s"(wo), "s"(wg), "s"(wu), "s"(wd), "s"(out), "s"(ws));
  STAMP(0);

  unsigned ep, T;
  LAS char* const px = sm + L_X + (col & 1) * XP + 16 * g4;
  if ((w >> 1) == 2) dma16(x, (unsigned)((w & 1) * 1024 + l * 16), smb + L_X + (w & 1) * XP);
  const f4 z = {0.f, 0.f, 0.f, 0.f};
#define LAYERNORM()                                                                                                              \
  {                                                                                                                              \
    const v4u xr = *(LAS v4u*)(sm + L_X + (w & 1) * XP + l * 16);                                                                \
    float e[8] = {blo(xr.x), bhi(xr.x), blo(xr.y), bhi(xr.y), blo(xr.z), bhi(xr.z), blo(xr.w), bhi(xr.w)};                       \
    const float mean = wave_sum(((e[0] + e[1]) + (e[2] + e[3])) + ((e[4] + e[5]) + (e[6] + e[7]))) * (1.f / DM);                 \
    float q = 0.f;                                                                                                               \
    _Pragma("unroll") for (int i = 0; i < 8; ++i) {                                                                              \
      e[i] -= mean;                                                                                                              \
      q = fmaf(e[i], e[i], q);                                                                                                   \
    }                                                                                                                            \
    const float rstd = __builtin_amdgcn_rsqf(wave_sum(q) * (1.f / DM) + 1e-5f);                                                  \
    *(LAS v4u*)(sm + L_X + (w & 1) * XP + l * 16) =                                                                              \
        v4u{pk(e[0] * rstd, e[1] * rstd), pk(e[2] * rstd, e[3] * rstd), pk(e[4] * rstd, e[5] * rstd), pk(e[6] * rstd, e[7] * rstd)}; \
  }

  if (__builtin_expect(r < 16, 1)) {
    const int h = 4 * xcd + (r >> 2), j = r & 3, ca = j >> 1, mb = j & 1;
    const int fl = tq | ((g4 & 1) << 2);
    gld4_sc1(ep, ws, 0u);
    if (__builtin_expect(w < 4, 1)) {
      const unsigned vq = (unsigned)(((128 * j + g4) * 4096 + h * DH + 8 * (col ^ ((g4 | ((w & 2) << 1)) << 1))) * 2);
#pragma unroll 1
      for (int k = w; k < 32; k += 4) dma16<NT_WQ>(wq, vq + (unsigned)k * 32768u, smb + L_WQ + k * 1024);
    } else if (w < 6) {
      VMWAIT(1);
      LAYERNORM();
    } else {
      __builtin_amdgcn_s_sleep(UK_DELAY);
      const unsigned vu = (unsigned)((((128 * j + g4) * NH + h) * DH) * 2);
#pragma unroll 1
      for (int k = w - 6; k < 32; k += 2) dma16<NT_WUK>(wuk, vu + (unsigned)k * 32768u + 16u * (col ^ ((4 * k + g4) & 15)), smb + L_WUK + k * 1024);
    }
    unsigned p = L_WQ + 256 * (8 * g4 + tq) + 16 * ((2 * w + (tp >> 1)) ^ (fl << 1)) + 8 * (tp & 1);
    unsigned oqp = WS_QP + ((h * 4 + j) * 8 + w) * 128 + col * 64 + g4 * 16;
    unsigned pxj = L_X + (col & 1) * XP + 16 * g4 + 256 * j;
    unsigned o = WS_QP + (h * 4 + (w & 3)) * 1024 + l * 16;
    unsigned pqw = smb + L_CC + (2 * (w & 3) + ((l >> 2) & 1)) * SP + (l >> 3) * 32 + (l & 3) * 8, pqr = L_CC + (col & 7) * SP + 16 * g4;
    unsigned oqa = WS_QA + ((col & 1) * NH + h) * 2048 + (64 * j + 16 * w + 8 * (col >> 1) + 2 * g4) * 8;
    unsigned pu = L_WUK + 256 * (32 * w + col) + 16 * (g4 ^ col);
    PIN(p); PIN(oqp); PIN(pxj); PIN(o); PIN(oqa); PIN(pu); PIN(pqw); PIN(pqr);
    {
    TWICE(DRY_F ? (w >> 2) : 1)
    if (live) {
      if (w < 6) VMWAIT(0);
      else VMWAIT(16);
      EPOCH();
      STAMP(1);
      BAR();
      STAMP(2);
    } else {
      UNDEF(T);
    }
    {
      v4u af[4], xf[4];
#pragma unroll
      for (int s = 0; s < 4; ++s) {
        af[s] = trrd2(sm + p + 256 * 32 * s, sm + p + 256 * (32 * s + 4));
        xf[s] = *(LAS v4u*)(sm + pxj + 64 * s);
      }
      __builtin_amdgcn_sched_barrier(0);
      f4 acc = z;
#pragma unroll
      for (int s = 0; s < 4; ++s) acc = mfma32(af[s], xf[s], acc);
      if (live && col < 2) st_sc1(rs, oqp, v4u{pk(acc[0], acc[1]), T, pk(acc[2], acc[3]), T});
    }
    STAMP(3);
    if (w >= 6) VMWAIT(1);
    if (w >= 4) {
      BAR();
      BAR();
    } else {
      v4u a, uk[8], qf[4];
      if (live) {
        __builtin_amdgcn_s_sleep(H1_DELAY);
        asm volatile(LD16(0, 1, 2) " sc1" : "=&v"(a) : "v"(o), "s"(ws) : "memory");
        BAR();
      } else {
        UNDEF(a);
      }
#pragma unroll
      for (int s = 0; s < 4; ++s) {
        uk[s] = *(LAS v4u*)(sm + (pu ^ (64 * s)));
        uk[4 + s] = *(LAS v4u*)(sm + (pu ^ (64 * s)) + 4096);
      }
      if (live) VMWAIT(0);
      PIN(a);
      for (unsigned spin = 0;; ++spin) {
        if (!__builtin_amdgcn_ballot_w64((T & a.y & a.w) != (T | a.y | a.w)) || spin > SPIN_CAP || !live) break;
        asm volatile(LD16(0, 1, 2) " sc1" WAIT0 : "+v"(a) : "v"(o), "s"(ws) : "memory");
      }
      asm volatile("ds_write2_b32 %0, %1, %2 offset0:0 offset1:1" ::"v"(pqw), "v"(a.x), "v"(a.z) : "memory");
      STAMP(4);
      if (live) BAR();
#pragma unroll
      for (int s = 0; s < 4; ++s) qf[s] = *(LAS v4u*)(sm + pqr + 64 * s);
      __builtin_amdgcn_sched_barrier(0);
      f4 a0 = z, a1 = z;
#pragma unroll
      for (int s = 0; s < 4; ++s) {
        a0 = mfma32(uk[s], qf[s], a0);
        a1 = mfma32(uk[4 + s], qf[s], a1);
      }
      float q[4];
#pragma unroll
      for (int e = 0; e < 4; ++e) {
        const float mine = (col & 2) ? a1[e] : a0[e], other = (col & 2) ? a0[e] : a1[e];
        q[e] = mine + dpp<QP_2301>(other);
        q[e] += dpp<ROR4>(q[e]);
      }
      if (live && col < 4) st_sc1(rs, oqa, v4u{pk(q[0], q[1]), T, pk(q[2], q[3]), T});
    }
    TWICE_END;
    }
    STAMP(5);
    PROBE_T(T);
    BAR();
    __builtin_amdgcn_s_sleep(T_DELAY);
    {
      const unsigned vv = (unsigned)((((256 * ca + g4) * NH + h) * DH + 8 * (col ^ ((g4 | ((w & 2) << 1)) << 1))) * 2);
      const unsigned vo = (unsigned)(((h * DH + (l >> 5)) * DM + 256 * mb) * 2) + 16u * ((l & 31) ^ (((l >> 5) | ((w & 1) << 1) | (w & 4)) << 1));
      int kv = w + 32 * mb, ko = w + 32 * ca;
#pragma unroll 1
      for (int hf = 0; hf < 2; ++hf) {
#pragma unroll 1
        for (int i = 0; i < 4; ++i, kv = (kv + 8) & 63) dma16<NT_T>(wuv, vv + (unsigned)kv * 32768u, smb + L_WUV + kv * 1024);
#pragma unroll 1
        for (int i = 0; i < 4; ++i, ko = (ko + 8) & 63) dma16<NT_T>(wo, vo + (unsigned)ko * 2048u, smb + L_WO + ko * 1024);
        if (hf == 0) asm volatile("s_barrier" ::: "memory");
      }
    }
    const int tb = w >> 2, nq = w & 3;
    o = WS_PART + (((h * 2 + ca) * 2 + tb) * 32 + 8 * nq + g4) * 512 + 16 * col;
    unsigned pw = L_W + (tb * 32 + 8 * nq + g4) * 4;
    unsigned pv = L_WUV + 256 * (8 * g4 + tq) + 16 * ((2 * w + (tp >> 1)) ^ (fl << 1)) + 8 * (tp & 1);
    unsigned po = L_WO + 512 * (8 * g4 + tq) + 16 * ((4 * w + (tp >> 1)) ^ (fl << 1)) + 8 * (tp & 1);
    unsigned pcw = L_CC + (nq * 2 + tb) * SP2 + 16 * col + 8 * g4, pcr = L_CC + (col & 7) * SP2 + 16 * g4;
    unsigned ptw = L_CT + col * SP + (16 * w + 4 * g4) * 2, ptr = L_CT + (col & 1) * SP + 16 * g4;
    unsigned oo = WS_OPT + ((16 * mb + 2 * w + (col >> 1)) * 64 + 2 * h + ca) * 128 + ((col & 1) * 4 + g4) * 16;
    v4u mk;
#pragma unroll
    for (int d = 0; d < 4; ++d) mk[d] = (col >> 1) == d ? ((col & 1) ? 0xffff0000u : 0xffffu) : 0u;
    PIN(o); PIN(pw); PIN(pv); PIN(po); PIN(pcw); PIN(pcr); PIN(ptw); PIN(ptr); PIN(oo); PIN(mk);
    STAMP(11);
    TWICE(DRY_T ? 0 : 1)
    if (live) STAMP(12);
    if (w == 7) {
      const unsigned ol = WS_LSE + (((l >> 5) * 2 + (h >> 4)) * 32 + (l & 31)) * 128 + (h & 15) * 8;
      v2u la;
      UNDEF(la);
      for (unsigned spin = 0; live; ++spin) {
        asm volatile("global_load_dwordx2 %0, %1, %2 sc1" WAIT0 : "+v"(la) : "v"(ol), "s"(ws) : "memory");
        if (!__builtin_amdgcn_ballot_w64(la.y != T) || spin > SPIN_CAP) break;
      }
      float M = asf(la.x);
      M = fmax2(M, dpp<ROR8>(M));
      M = fmax2(M, dpp<ROR4>(M));
      M = fmax2(M, dpp<ROR2>(M));
      M = fmax2(M, dpp<ROR1>(M));
      M = sw16_max(M);
      const float e = __builtin_amdgcn_exp2f(asf(la.x) - M);
      float L = e + dpp<ROR8>(e);
      L += dpp<ROR4>(L);
      L += dpp<ROR2>(L);
      L += dpp<ROR1>(L);
      L = sw16_add(L, L);
      const float wt = e * __builtin_amdgcn_rcpf(L);
      *(LAS unsigned*)(sm + L_W + 4 * l) = pk(wt, wt);
      STAMP(7);
    }
    v4u a[4];
    if (live) {
      VMWAIT(0);
      STAMP(6);
      BAR();
      SWEEP_4(a, o, 0, 256, 2048, 2304, "");
    } else {
      UNDEF(a[0]); UNDEF(a[1]); UNDEF(a[2]); UNDEF(a[3]);
    }
    v4u A0 = mk & *(LAS unsigned*)(sm + pw), A1 = mk & *(LAS unsigned*)(sm + pw + 16);
    v4u av[8], ao[8];
#pragma unroll
    for (int s = 0; s < 8; ++s) av[s] = trrd2(sm + pv + 256 * 32 * s, sm + pv + 256 * (32 * s + 4));
#pragma unroll
    for (int t = 0; t < 2; ++t)
#pragma unroll
      for (int s = 0; s < 4; ++s) ao[4 * t + s] = trrd2(sm + (po ^ (32 * t)) + 512 * 32 * s, sm + (po ^ (32 * t)) + 512 * (32 * s + 4));
    if (live) VMWAIT(0);
    asm volatile("" : "+v"(a[0]), "+v"(a[1]), "+v"(a[2]), "+v"(a[3]), "+v"(A0), "+v"(A1)::"memory");
    for (unsigned spin = 0;; ++spin) {
      unsigned mx = 0u;
#pragma unroll
      for (int i = 0; i < 4; ++i) mx = max(max(mx, max(a[i].x, a[i].y)), max(a[i].z, a[i].w));
      if (!__builtin_amdgcn_ballot_w64(mx == ~0u) || spin > SPIN_CAP || !live) break;
      RESWEEP_4(a, o, 0, 256, 2048, 2304);
    }
    STAMP(8);
    {
      f4 c0 = mfma32(A0, a[0], z), c1 = mfma32(A0, a[1], z);
      c0 = mfma32(A1, a[2], c0);
      c1 = mfma32(A1, a[3], c1);
      if (g4 < 2) {
        *(LAS v2u*)(sm + pcw) = v2u{pk(c0[0], c0[1]), pk(c0[2], c0[3])};
        *(LAS v2u*)(sm + pcw + 256) = v2u{pk(c1[0], c1[1]), pk(c1[2], c1[3])};
      }
    }
    if (live) BAR();
    {
      f4 acc = z;
      v4u bf[8];
#pragma unroll
      for (int s = 0; s < 8; ++s) bf[s] = *(LAS v4u*)(sm + pcr + 64 * s);
      __builtin_amdgcn_sched_barrier(0);
#pragma unroll
      for (int s = 0; s < 8; ++s) acc = mfma32(av[s], bf[s], acc);
#pragma unroll
      for (int e = 0; e < 4; ++e) {
        acc[e] += dpp<QP_2301>(acc[e]);
        acc[e] += dpp<ROR4>(acc[e]);
      }
      if (col < 2) *(LAS v2u*)(sm + ptw) = v2u{pk(acc[0], acc[1]), pk(acc[2], acc[3])};
    }
    if (live) BAR();
    {
      f4 c0 = z, c1 = z;
      v4u bf[4];
#pragma unroll
      for (int s = 0; s < 4; ++s) bf[s] = *(LAS v4u*)(sm + ptr + 64 * s);
      __builtin_amdgcn_sched_barrier(0);
#pragma unroll
      for (int s = 0; s < 4; ++s) {
        c0 = mfma32(ao[s], bf[s], c0);
        c1 = mfma32(ao[4 + s], bf[s], c1);
      }
      if (col & 2) c0 = c1;
      if (live && col < 4) st_sc1(rs, oo, v4u{pk(c0[0], c0[1]), T, pk(c0[2], c0[3]), T});
    }
    TWICE_END;
    STAMP(9);
  } else {
    const int a = r - 16, b = xcd >> 2, n = 8 * (xcd & 3) + (a >> 1), g = a & 1;
    const int sl = 12 * xcd + a, rd = (4 * xcd + a) & 31;
    const bool glu = a < 12 && sl < NSL;
    const int v = w & 3;
    unsigned op = WS_PART + ((((16 * g + (col & 7)) * 2 + (v >> 1)) * 2 + b) * 32 + n) * 512 + 256 * (v & 1) + (col >> 3) * 64 +
                  (g4 & 1) * 32 + (g4 >> 1) * 16;
    unsigned xres;
    gld4_sc1(ep, ws, 0u);
    asm volatile("global_load_ushort %0, %1, %2" : "=v"(xres) : "v"((unsigned)(((l >> 4) & 1) * 1024 + rd * 32 + col * 2)), "s"(x) : "memory");
    if (w >= 4) {
#pragma unroll
      for (int q = 0; q < 4; ++q) st_sc1(rs, op + 128 * (q >> 1) + (q & 1) * 8 * 65536, v4u{~0u, ~0u, ~0u, ~0u});
      if (w == 7 && l < 32) *(LAS unsigned*)(sm + L_FL + 4 * l) = 0u;
    }
    unsigned o = WS_QA + (b * NH + 16 * g) * 2048 + w * 4096 + l * 16, pqs = smb + L_Q + 2 * w * CP + l * 8;
    PIN(o); PIN(pqs);
    {
    TWICE(DRY_A ? 0 : 1)
    if (live) {
      const unsigned vc = (unsigned)(((b * SK + 128 * n) * DC + 8 * l) * 2);
      STAMP(10);
      __builtin_amdgcn_s_sleep(A_DELAY);
#pragma unroll 1
      for (int q = 0; q < 4; ++q) {
        int row = w + ((64 * g + 32 * q) & 127);
        asm volatile("s_barrier" ::: "memory");
#pragma unroll 1
        for (int i = 0; i < 4; ++i, row += 8) dma16<NT_CKV>(ckv, vc + row * 1024u, smb + L_C + row * CP);
      }
      if ((w >> 1) == 2) {
        VMWAIT(22);
        LAYERNORM();
      }
      VMWAIT(0);
      EPOCH();
      PROBE_T(T);
      BAR();
      STAMP(1);
    } else {
      UNDEF(T);
    }
#define QISSUE()                                \
  if (live) {                                   \
    SWEEP_4(qg, o, 0, 1024, 2048, 3072, "");    \
  } else {                                      \
    UNDEF(qg[0]); UNDEF(qg[1]); UNDEF(qg[2]); UNDEF(qg[3]); \
  }
#define QGATHER()                                                                                                           \
  if (live) VMWAIT(0);                                                                                                      \
  asm volatile("" : "+v"(qg[0]), "+v"(qg[1]), "+v"(qg[2]), "+v"(qg[3])::"memory");                                          \
  for (unsigned spin = 0;; ++spin) {                                                                                        \
    if (!TAGS_BAD4(qg) || spin > SPIN_CAP || !live) break;                                                                           \
    RESWEEP_4(qg, o, 0, 1024, 2048, 3072);                                                                             \
  }                                                                                                                         \
  STAMP(3);                                                                                                                 \
  asm volatile("ds_write2_b32 %0, %2, %3 offset0:0 offset1:1\n\tds_write2_b32 %0, %4, %5 offset0:128 offset1:129\n\t"        \
               "ds_write2_b32 %1, %6, %7 offset0:0 offset1:1\n\tds_write2_b32 %1, %8, %9 offset0:128 offset1:129" ::"v"(pqs), \
               "v"(pqs + CP), "v"(qg[0].x), "v"(qg[0].z), "v"(qg[1].x), "v"(qg[1].z), "v"(qg[2].x), "v"(qg[2].z), "v"(qg[3].x), \
               "v"(qg[3].z)                                                                                                 \
               : "memory")
    if (__builtin_expect(w < 4, 1)) {
      v4u fr[32], qg[4];
      QISSUE();
      LAS char* pc = sm + L_C + (32 * w + col) * CP + 16 * g4;
#pragma unroll
      for (int kt = 0; kt < 2; ++kt)
#pragma unroll
        for (int s = 0; s < 16; ++s) fr[16 * kt + s] = *(LAS v4u*)(pc + 16 * kt * CP + 64 * s);
      unsigned p = L_Q + col * CP + 16 * g4, pp = L_P + w * 1024 + l * 16, pm = L_MX + col * 16 + w * 4;
      PIN(p); PIN(pp); PIN(pm);
      STAMP(2);
      QGATHER();
      if (live) BAR();
      STAMP(4);
      f4 s0 = z, s1 = z;
      v4u qf[16];
#pragma unroll
      for (int s = 0; s < 6; ++s) qf[s] = *(LAS v4u*)(sm + p + 64 * s);
      __builtin_amdgcn_sched_barrier(0);
#pragma unroll
      for (int s = 0; s < 16; ++s) {
        if (s + 6 < 16) qf[s + 6] = *(LAS v4u*)(sm + p + 64 * (s + 6));
        s0 = mfma32(fr[s], qf[s], s0);
        s1 = mfma32(fr[16 + s], qf[s], s1);
        __builtin_amdgcn_sched_barrier(0);
      }
      float mx = fmax2(fmax2(fmax2(s0[0], s0[1]), fmax2(s0[2], s0[3])), fmax2(fmax2(s1[0], s1[1]), fmax2(s1[2], s1[3])));
      mx = sw32_max(sw16_max(mx));
      constexpr float SC = 0.08838834764831845f * 1.4426950408889634f;
      const float mi = __builtin_ceilf(mx * SC);
#pragma unroll
      for (int e = 0; e < 4; ++e) {
        s0[e] = __builtin_amdgcn_exp2f(fmaf(s0[e], SC, -mi));
        s1[e] = __builtin_amdgcn_exp2f(fmaf(s1[e], SC, -mi));
      }
      *(LAS float*)(sm + pm) = mi;
      *(LAS v4u*)(sm + pp) = v4u{pk(s0[0], s0[1]), pk(s0[2], s0[3]), pk(s1[0], s1[1]), pk(s1[2], s1[3])};
      if (live) BAR();
      STAMP(5);
    } else {
      v4u fr[32], qg[4];
      QISSUE();
      LAS char* p0 = sm + L_C + (4 * g4 + tq) * CP + 256 * v + 8 * tp;
      unsigned p1 = L_C + (64 + 4 * g4 + tq) * CP + 256 * v + 8 * tp;
      PIN(p1);
#pragma unroll
      for (int ct = 0; ct < 8; ++ct)
#pragma unroll
        for (int u = 0; u < 4; ++u) {
          LAS char* p = (u < 2 ? p0 : sm + p1) + 32 * (u & 1) * CP + 32 * ct;
          fr[4 * ct + u] = trrd2(p, p + 16 * CP);
        }
      unsigned ol = WS_LSE + ((b * 2 + g) * 32 + n) * 128 + col * 8, pp = L_P + l * 16, pm = L_MX + col * 16;
      v4u one = {0x3f803f80u, 0x3f803f80u, 0x3f803f80u, 0x3f803f80u};
      PIN(ol); PIN(pp); PIN(pm); PIN(one); PIN(op);
      STAMP(2);
      QGATHER();
      if (live) BAR();
      STAMP(4);
      if (live) {
        const int ln = 128 * a + 64 * (v >> 1) + l;
        touch<NT_G>((v & 1) ? wu : wg, (unsigned)((ln >> 2) * (FF * 2)) + (unsigned)min(384 * xcd + 128 * (ln & 3), FF * 2 - 4), smb + L_JUNK);
      }
      if (live) BAR();
      const f4 m4 = *(LAS f4*)(sm + pm);
      v4u pf[4];
#pragma unroll
      for (int u = 0; u < 4; ++u) pf[u] = *(LAS v4u*)(sm + pp + u * 1024);
      const float M = fmax2(fmax2(m4[0], m4[1]), fmax2(m4[2], m4[3]));
#pragma unroll
      for (int u = 0; u < 4; ++u) {
        const unsigned kk = min((unsigned)(M - m4[u]), 255u) * 0x00800080u;
#pragma unroll
        for (int e = 0; e < 4; ++e) pf[u][e] = scale_dn(pf[u][e], kk);
      }
      f4 ls = z;
#pragma unroll
      for (int u = 0; u < 4; ++u) ls = mfma32(one, pf[u], ls);
      const float il = __builtin_amdgcn_rcpf(ls[0]);
      if (live && v == 0 && g4 == 0)
        __builtin_amdgcn_raw_buffer_store_b64(v2u{__builtin_bit_cast(unsigned, M + __builtin_amdgcn_logf(ls[0])), T}, rs, ol, 0, AUX_SC1);
#pragma unroll
      for (int q = 0; q < 2; ++q) {
        f4 c[4];
#pragma unroll
        for (int k = 0; k < 4; ++k) c[k] = z;
#pragma unroll
        for (int u = 0; u < 4; ++u)
#pragma unroll
          for (int k = 0; k < 4; ++k) c[k] = mfma32(fr[16 * q + 4 * k + u], pf[u], c[k]);
        v4u R[2];
#pragma unroll
        for (int k = 0; k < 2; ++k) {
          const f4 c0 = c[2 * k] * il, c1 = c[2 * k + 1] * il;
          const unsigned x0 = min(pk(c0[0], c0[1]), 0xfffffffeu), x1 = min(pk(c0[2], c0[3]), 0xfffffffeu);
          const unsigned y0 = min(pk(c1[0], c1[1]), 0xfffffffeu), y1 = min(pk(c1[2], c1[3]), 0xfffffffeu);
          auto r0 = __builtin_amdgcn_permlane16_swap(x0, y0, false, false);
          auto r1 = __builtin_amdgcn_permlane16_swap(x1, y1, false, false);
          R[k] = v4u{r0[0], r1[0], r0[1], r1[1]};
        }
        v4u A, B;
#pragma unroll
        for (int e = 0; e < 4; ++e) {
          A[e] = (unsigned)__builtin_amdgcn_update_dpp((int)R[0][e], (int)R[1][e], ROR8, 0xf, 0xc, false);
          B[e] = (unsigned)__builtin_amdgcn_update_dpp((int)R[1][e], (int)R[0][e], ROR8, 0xf, 0x3, false);
        }
        PIN(A); PIN(B);
        if (live) {
          st_sc1(rs, op + 128 * q, A);
          st_sc1(rs, op + 128 * q + 8 * 65536, B);
        }
      }
      if (live) FLAG_SET(v);
      STAMP(6);
    }
    TWICE_END;
    }
      if (glu && w < 4) {
        v4u af[8], xf[8];
#pragma unroll
        for (int s = 0; s < 8; ++s) xf[s] = *(LAS v4u*)(px + 512 * (w >> 1) + 64 * s);
        unsigned oog = WS_OPG + ((8 * w + (col >> 1)) * 128 + sl) * 128 + ((col & 1) * 4 + g4) * 16;
        PIN(oog);
        FLAG_WAIT4(0);
        unsigned og = (unsigned)(((128 * w + (l >> 1)) * FF + 16 * sl + 8 * (l & 1)) * 2);
#pragma unroll 1
        for (int i = 0; i < 4; ++i, og += 32 * FF * 2) {
          dma16(wg, og, smb + L_GG + (4 * w + i) * 1024);
          dma16(wu, og, smb + L_GG + 16384 + (4 * w + i) * 1024);
        }
#pragma unroll 1
        for (int i = 0; i < 4; ++i) dma16<NT_G>(wd, (unsigned)(((16 * sl + 4 * w + i) * DM + 8 * l) * 2), smb + L_GD + (4 * w + i) * CP);
        TWICE(DRY_G ? 0 : 1)
        if (live) {
          VMWAIT(4);
          FLAG_SET(4 + w);
          FLAG_WAIT4(1);
        }
        STAMP(7);
        LAS char* pg = sm + L_GG + (w & 1) * 16384 + 32 * (256 * (w >> 1) + 8 * g4 + tq) + 8 * tp;
        f4 acc = z;
#pragma unroll
        for (int s = 0; s < 8; ++s) af[s] = trrd2(pg + 32 * 32 * s, pg + 32 * (32 * s + 4));
        __builtin_amdgcn_sched_barrier(0);
#pragma unroll
        for (int s = 0; s < 8; ++s) acc = mfma32(af[s], xf[s], acc);
        *(LAS f4*)(sm + L_GR + ((w & 1) * 2 + (w >> 1)) * 1024 + l * 16) = acc;
        if (live) {
          VMWAIT(0);
          FLAG_SET(8 + w);
          FLAG_WAIT4(2);
        }
        v2u dn[8];
#pragma unroll
        for (int t = 0; t < 8; ++t) dn[t] = trrd(sm + L_GD + (4 * g4 + tq) * CP + 32 * (8 * w + t) + 8 * tp);
        LAS char* pr = sm + L_GR + l * 16;
        const f4 gx = *(LAS f4*)(pr) + *(LAS f4*)(pr + 1024), ux = *(LAS f4*)(pr + 2048) + *(LAS f4*)(pr + 3072);
        float hh[4];
#pragma unroll
        for (int e = 0; e < 4; ++e) hh[e] = ux[e] * __builtin_amdgcn_rcpf(1.f + __builtin_amdgcn_exp2f(-1.4426950408889634f * gx[e]));
        const v2u hb = {pk(hh[0], hh[1]), pk(hh[2], hh[3])};
        f4 d = z;
#pragma unroll
        for (int t = 0; t < 8; ++t) d = mfma16(dn[t], (col >> 1) == t ? hb : v2u{0u, 0u}, d);
        v4u dv = {pk(d[0], d[1]), T, pk(d[2], d[3]), T};
        PIN(dv);
        if (live) st_sc1(rs, oog, dv);
        TWICE_END;
        STAMP(9);
      }
    if (a >= 12) {
      unsigned ot = WS_OPT + rd * 8192 + tid * 16, og = WS_OPG + rd * 16384 + tid * 16;
      const bool two = (tid >> 3) < NSL - 64;
      unsigned og2 = two ? og + 8192 : og;
      const unsigned m = (col & 1) ? 0x3f800000u : 0x3f80u;
      v4u sel = {col < 2 ? m : 0u, (col >> 1) == 1 ? m : 0u, col < 2 ? m : 0u, (col >> 1) == 1 ? m : 0u};
      unsigned m2 = two ? ~0u : 0u;
      unsigned prw = L_RR + w * 128 + (l & 7) * 16, prr = L_RR + (l & 31) * 4, oy = ((l >> 4) & 1) * 1024 + rd * 32 + col * 2;
      PIN(ot); PIN(og); PIN(og2); PIN(sel); PIN(m2); PIN(prw); PIN(prr); PIN(oy);
      PIN(xres);
      TWICE(DRY_R ? 0 : 1)
      v4u p0, p1, p2;
      UNDEF(p0); UNDEF(p1); UNDEF(p2);
      for (unsigned spin = 0; live; ++spin) {
        asm volatile(LD16(0, 3, 6) " sc1\n\t" LD16(1, 4, 6) " sc1\n\t" LD16(2, 5, 6) " sc1" WAIT0
                     : "+v"(p0), "+v"(p1), "+v"(p2)
                     : "v"(ot), "v"(og), "v"(og2), "s"(ws)
                     : "memory");
        if (!__builtin_amdgcn_ballot_w64((T & p0.y & p0.w & p1.y & p1.w & p2.y & p2.w) != (T | p0.y | p0.w | p1.y | p1.w | p2.y | p2.w)) ||
            spin > SPIN_CAP)
          break;
      }
      STAMP(8);
      f4 acc = mfma32(sel, v4u{p0.x, p0.z, p1.x, p1.z}, z);
      acc = mfma32(sel, v4u{p2.x & m2, p2.z & m2, 0u, 0u}, acc);
#pragma unroll
      for (int e = 0; e < 4; ++e) acc[e] += dpp<ROR8>(acc[e]);
      if (l < 8) *(LAS f4*)(sm + prw) = acc;
      if (live) BAR();
      if (w == 0 && l < 32) {
        float t[8];
#pragma unroll
        for (int i = 0; i < 8; ++i) t[i] = *(LAS float*)(sm + prr + i * 128);
        const float y = blo(xres) + (((t[0] + t[1]) + (t[2] + t[3])) + ((t[4] + t[5]) + (t[6] + t[7])));
        unsigned yb = pk(y, y);
        PIN(yb);
        if (live) *(u16*)((char*)out + oy) = (u16)yb;
        if (live && rd == 0 && l == 0) __builtin_amdgcn_raw_buffer_store_b32(T, rs, WS_E, 0, AUX_SC1);
      }
      TWICE_END;
      STAMP(9);
    }
  }
  STAMP_FLUSH();
}

#define WS_END 0
#define WS_BASE (WS_END ? ((ws_size - (size_t)WS_END * 1048576) & ~(size_t)4095) : 0)
extern "C" void kernel_launch(void* const* d_in, const int* in_sizes, int n_in, void* d_out, int out_size, void* d_ws, size_t ws_size,
                              hipStream_t stream) {
  mla_glu_decode<<<256, 512, 0, stream>>>((const u16*)d_in[0], (const u16*)d_in[1], (const u16*)d_in[2], (const u16*)d_in[3],
                                          (const u16*)d_in[4], (const u16*)d_in[5], (const u16*)d_in[7], (const u16*)d_in[8],
                                          (const u16*)d_in[9], (u16*)d_out, (char*)d_ws + WS_BASE);
}
```

```cpp
#define A_START 110
#define T_START 340
#include <hip/hip_runtime.h>
#include <stdint.h>

#define GAS __attribute__((address_space(1)))
#define LAS __attribute__((address_space(3)))
typedef unsigned short u16;
typedef unsigned v4u __attribute__((ext_vector_type(4)));
typedef unsigned v2u __attribute__((ext_vector_type(2)));
typedef float f4 __attribute__((ext_vector_type(4)));
typedef float f2 __attribute__((ext_vector_type(2)));
typedef __bf16 bf8 __attribute__((ext_vector_type(8)));
typedef __bf16 bf2 __attribute__((ext_vector_type(2)));
typedef short s4 __attribute__((ext_vector_type(4)));

namespace {
constexpr int NH = 32, SK = 4096, DH = 128, DM = 512, DC = 512, FF = 1376;
constexpr int NSL = FF / 16;
constexpr int WS_E = 0;
constexpr int WS_QP = 4096;
constexpr int WS_QA = WS_QP + NH * 4 * 1024;
constexpr int WS_LSE = WS_QA + 2 * NH * 2048;
constexpr int WS_OPT = WS_LSE + 2 * 32 * 8 * 128;
constexpr int WS_OPG = WS_OPT + 32 * 64 * 128;
constexpr int WS_PART = WS_OPG + 32 * 128 * 128;
constexpr int WS_BYTES = WS_PART + NH * 2 * 2 * 32 * 512;
constexpr int WS_STAMP = 7 << 19;
constexpr int CP = 1056;
constexpr int XP = 1088, SP = 288, SP2 = 544;
constexpr int L_C = 0, L_Q = 128 * CP, L_P = L_Q + 16 * CP, L_X = L_P + 4096, L_MX = L_X + 2 * XP, L_RR = L_MX + 256,
              L_JUNK = L_RR + 1024, L_FL = L_JUNK + 256, L_END = L_FL + 128;
static_assert(L_END <= 163840, "LDS");
constexpr int L_GG = 0, L_GD = 32768, L_GR = 65536;
constexpr int L_WUV = 0, L_WO = 65536, L_WQ = 0, L_WUK = 32768, L_CC = 131072, L_CT = L_CC + 8 * SP2, L_W = L_CT + 2 * SP;
#define UK_DELAY 4
#define H1_DELAY 4
#define CODE_TOUCH 1
#define NT_CKV 0
#define NT_WQ 0
#define NT_WUK 0
#define NT_T 0
#define NT_G 0
#define GLU_EARLY 0
#define G_START 0
#define C_START 0
#define LSE_LATE 0
#define LSE_WAVE 3
#define DATA_DELAY 0
#define NEWRED 1
#define R_START 0
#define G_TOUCH 4
#define LSE_PIPE 3
#define PV_MIX 4
#define LSE_QK 1
#define PIECEWISE 1
#define PF_GLU 1
constexpr unsigned SPIN_CAP = 1u << 20;
constexpr int AUX_SC1 = 16;

#define STAMP_W 0
#define STAMP(i)
#define STAMP_FLUSH()
#define PROBE_T(T_)

__device__ __forceinline__ f4 mfma32(v4u a, v4u b, f4 c) {
  return __builtin_amdgcn_mfma_f32_16x16x32_bf16(__builtin_bit_cast(bf8, a), __builtin_bit_cast(bf8, b), c, 0, 0, 0);
}
__device__ __forceinline__ f4 mfma16(v2u a, v2u b, f4 c) {
  return __builtin_amdgcn_mfma_f32_16x16x16bf16_1k(__builtin_bit_cast(s4, a), __builtin_bit_cast(s4, b), c, 0, 0, 0);
}
__device__ __forceinline__ unsigned pk(float lo, float hi) {
  f2 x = {lo, hi};
  return __builtin_bit_cast(unsigned, __builtin_convertvector(x, bf2));
}
__device__ __forceinline__ float asf(unsigned u) { return __builtin_bit_cast(float, u); }
__device__ __forceinline__ float blo(unsigned u) { return __builtin_bit_cast(float, u << 16); }
__device__ __forceinline__ float bhi(unsigned u) { return __builtin_bit_cast(float, u & 0xffff0000u); }
template <int CTRL> __device__ __forceinline__ float dpp(float v) {
  return __builtin_bit_cast(float, __builtin_amdgcn_update_dpp(0, __builtin_bit_cast(int, v), CTRL, 0xf, 0xf, true));
}
__device__ __forceinline__ float fmax2(float a, float b) {
#pragma float_control(precise, off)
  return __builtin_fmaxf(a, b);
}
typedef unsigned short h2 __attribute__((ext_vector_type(2)));
__device__ __forceinline__ unsigned scale_dn(unsigned p, unsigned kk) {
  return __builtin_bit_cast(unsigned, __builtin_elementwise_sub_sat(__builtin_bit_cast(h2, p), __builtin_bit_cast(h2, kk)));
}
#define PIN(x) asm volatile("" : "+v"(x))
constexpr int ROR1 = 0x121, ROR2 = 0x122, ROR4 = 0x124, ROR8 = 0x128, QP_2301 = 0x4e, QP_2222 = 0xaa;
__device__ __forceinline__ float sw32_add(float a, float b) {
  auto r = __builtin_amdgcn_permlane32_swap(__builtin_bit_cast(unsigned, a), __builtin_bit_cast(unsigned, b), false, false);
  return asf(r[0]) + asf(r[1]);
}
__device__ __forceinline__ float sw16_add(float a, float b) {
  auto r = __builtin_amdgcn_permlane16_swap(__builtin_bit_cast(unsigned, a), __builtin_bit_cast(unsigned, b), false, false);
  return asf(r[0]) + asf(r[1]);
}
__device__ __forceinline__ float vmax(unsigned a, unsigned b) {
  float r;
  asm("v_max_f32 %0, %1, %2" : "=v"(r) : "v"(a), "v"(b));
  return r;
}
__device__ __forceinline__ float vmax3(float a, float b, float c) {
  float r;
  asm("v_max3_f32 %0, %1, %2, %3" : "=v"(r) : "v"(a), "v"(b), "v"(c));
  return r;
}
__device__ __forceinline__ float sw32_max(float a) {
  auto r = __builtin_amdgcn_permlane32_swap(__builtin_bit_cast(unsigned, a), __builtin_bit_cast(unsigned, a), false, false);
  return vmax(r[0], r[1]);
}
__device__ __forceinline__ float sw16_max(float a) {
  auto r = __builtin_amdgcn_permlane16_swap(__builtin_bit_cast(unsigned, a), __builtin_bit_cast(unsigned, a), false, false);
  return vmax(r[0], r[1]);
}
__device__ __forceinline__ float wave_sum(float v) {
  v += dpp<ROR8>(v);
  v += dpp<ROR4>(v);
  v += dpp<ROR2>(v);
  v += dpp<ROR1>(v);
  v = sw16_add(v, v);
  return sw32_add(v, v);
}
__device__ __forceinline__ v2u trrd(LAS char* p) {
  return __builtin_bit_cast(v2u, __builtin_amdgcn_ds_read_tr16_b64_v4i16((LAS s4*)p));
}
__device__ __forceinline__ v4u trrd2(LAS char* p0, LAS char* p1) {
  v2u a = trrd(p0), b = trrd(p1);
  return v4u{a.x, a.y, b.x, b.y};
}
#define BAR() asm volatile("s_waitcnt lgkmcnt(0)\n\ts_barrier" ::: "memory")
#define VMWAIT(k) asm volatile("s_waitcnt vmcnt(%0)" ::"n"(k) : "memory")
__device__ __forceinline__ void gld16(v4u& d, const void* base, unsigned off) {
  asm volatile("global_load_dwordx4 %0, %1, %2" : "=v"(d) : "v"(off), "s"(base) : "memory");
}
__device__ __forceinline__ void gld4_sc1(unsigned& d, const void* base, unsigned off) {
  asm volatile("global_load_dword %0, %1, %2 sc1" : "=v"(d) : "v"(off), "s"(base) : "memory");
}
template <int NT = 0> __device__ __forceinline__ void touch(const void* base, unsigned off, unsigned lds) {
  if (NT) asm volatile("s_mov_b32 m0, %2\n\ts_nop 0\n\tglobal_load_lds_dword %0, %1 nt" ::"v"(off), "s"(base), "s"(lds) : "memory");
  else asm volatile("s_mov_b32 m0, %2\n\ts_nop 0\n\tglobal_load_lds_dword %0, %1" ::"v"(off), "s"(base), "s"(lds) : "memory");
}
__device__ __forceinline__ void touch_sc1(const void* base, unsigned off, unsigned lds) {
  asm volatile("s_mov_b32 m0, %2\n\ts_nop 0\n\tglobal_load_lds_dword %0, %1 sc1" ::"v"(off), "s"(base), "s"(lds) : "memory");
}
#define LD16(i, a, o) "global_load_dwordx4 %" #i ", %" #a ", %" #o
#define SWEEP_4(q, o, o0, o1, o2, o3, W)                                                                                     \
  asm volatile(LD16(0, 4, 5) " offset:" #o0 " sc1\n\t" LD16(1, 4, 5) " offset:" #o1 " sc1\n\t" LD16(2, 4, 5) " offset:" #o2         \
               " sc1\n\t" LD16(3, 4, 5) " offset:" #o3 " sc1" W                                                               \
               : "=&v"((q)[0]), "=&v"((q)[1]), "=&v"((q)[2]), "=&v"((q)[3])                                                  \
               : "v"(o), "s"(ws)                                                                                             \
               : "memory")
#define RESWEEP_4(q, o, o0, o1, o2, o3)                                                                                      \
  asm volatile(LD16(0, 4, 5) " offset:" #o0 " sc1\n\t" LD16(1, 4, 5) " offset:" #o1 " sc1\n\t" LD16(2, 4, 5) " offset:" #o2         \
               " sc1\n\t" LD16(3, 4, 5) " offset:" #o3 " sc1\n\ts_waitcnt vmcnt(0)"                                            \
               : "+v"((q)[0]), "+v"((q)[1]), "+v"((q)[2]), "+v"((q)[3])                                                      \
               : "v"(o), "s"(ws)                                                                                             \
               : "memory")
#define RELOAD(q, o, off) asm volatile(LD16(0, 1, 2) " offset:" #off " sc1" : "+v"(q) : "v"(o), "s"(ws) : "memory")
#define PIN4(q) asm volatile("s_waitcnt vmcnt(0)" : "+v"((q)[0]), "+v"((q)[1]), "+v"((q)[2]), "+v"((q)[3])::"memory")
#define TAG_BAD(q) __builtin_amdgcn_ballot_w64((T & (q).y & (q).w) != (T | (q).y | (q).w))
#define HOLE(q) __builtin_amdgcn_ballot_w64(max(max((q).x, (q).y), max((q).z, (q).w)) == ~0u)
#define SWEEP_L(W)                                                                                                          \
  asm volatile("global_load_dwordx2 %0, %3, %5 sc1\n\tglobal_load_dwordx2 %1, %4, %5 sc1\n\t"                              \
               "global_load_dwordx2 %2, %4, %5 offset:512 sc1" W                                                            \
               : "=&v"(la), "=&v"(l0), "=&v"(l1)                                                                            \
               : "v"(ola), "v"(olo), "s"(ws)                                                                                \
               : "memory")
#define SWEEP_2(p, q, oa, ob, W) \
  asm volatile(LD16(0, 2, 4) " sc1\n\t" LD16(1, 3, 4) " sc1" W : "=&v"(p), "=&v"(q) : "v"(oa), "v"(ob), "s"(ws) : "memory")
#define TAGS_BAD4(q) \
  __builtin_amdgcn_ballot_w64((T & (q)[0].y & (q)[0].w & (q)[1].y & (q)[1].w & (q)[2].y & (q)[2].w & (q)[3].y & (q)[3].w) != \
                              (T | (q)[0].y | (q)[0].w | (q)[1].y | (q)[1].w | (q)[2].y | (q)[2].w | (q)[3].y | (q)[3].w))
#define FLAG_SET(i) asm volatile("ds_write_b32 %0, %1" ::"v"(smb + L_FL + 4 * (i)), "v"(1u) : "memory")
#define FLAG_WAIT4(i)                                                                                     \
  for (unsigned spin_ = 0; spin_ < SPIN_CAP; ++spin_) {                                                   \
    v4u f_;                                                                                               \
    asm volatile("ds_read_b128 %0, %1\n\ts_waitcnt lgkmcnt(0)" : "=v"(f_) : "v"(smb + L_FL + 16 * (i)) : "memory"); \
    if (__builtin_amdgcn_readfirstlane(f_.x & f_.y & f_.z & f_.w)) break;                                 \
  }
#define HOLD_UNTIL(ticks) \
  while ((unsigned)__builtin_amdgcn_s_memrealtime() - (unsigned)tstart < (ticks)) __builtin_amdgcn_s_sleep(1)
#define MARK(i) asm volatile(".Lmla_e" #i ":")
#define UNDEF(x) asm volatile("" : "=v"(x))
#define EPOCH() asm volatile("v_add_u32 %0, 1, %1" : "=v"(T) : "v"(ep) : "memory")
#define WAIT0 "\n\ts_waitcnt vmcnt(0)"
template <int NT = 0> __device__ __forceinline__ void dma16(const void* base, unsigned off, unsigned lds) {
  if (NT) asm volatile("s_mov_b32 m0, %2\n\ts_nop 0\n\tglobal_load_lds_dwordx4 %0, %1 nt" ::"v"(off), "s"(base), "s"(lds) : "memory");
  else asm volatile("s_mov_b32 m0, %2\n\ts_nop 0\n\tglobal_load_lds_dwordx4 %0, %1" ::"v"(off), "s"(base), "s"(lds) : "memory");
}
__device__ __forceinline__ v4u ld_sc1(__amdgpu_buffer_rsrc_t rs, int off) {
  return __builtin_amdgcn_raw_buffer_load_b128(rs, off, 0, AUX_SC1);
}
__device__ __forceinline__ v2u ld8_sc1(__amdgpu_buffer_rsrc_t rs, int off) {
  return __builtin_amdgcn_raw_buffer_load_b64(rs, off, 0, AUX_SC1);
}
__device__ __forceinline__ void st_sc1(__amdgpu_buffer_rsrc_t rs, int off, v4u v) {
  __builtin_amdgcn_raw_buffer_store_b128(v, rs, off, 0, AUX_SC1);
}
__device__ __forceinline__ void ln_stats(v4u r, float& mean, float& rstd) {
  float e[8] = {blo(r.x), bhi(r.x), blo(r.y), bhi(r.y), blo(r.z), bhi(r.z), blo(r.w), bhi(r.w)};
  float s = ((e[0] + e[1]) + (e[2] + e[3])) + ((e[4] + e[5]) + (e[6] + e[7]));
  mean = wave_sum(s) * (1.f / DM);
  float q = 0.f;
#pragma unroll
  for (int i = 0; i < 8; ++i) {
    float d = e[i] - mean;
    q = fmaf(d, d, q);
  }
  rstd = __builtin_amdgcn_rsqf(wave_sum(q) * (1.f / DM) + 1e-5f);
}
}

__global__ void __launch_bounds__(512, 2)
mla_glu_decode(const u16* __restrict__ x, const u16* __restrict__ ckv, const u16* __restrict__ wq, const u16* __restrict__ wuk,
               const u16* __restrict__ wuv, const u16* __restrict__ wo, const u16* __restrict__ wg, const u16* __restrict__ wu,
               const u16* __restrict__ wd, u16* __restrict__ out, char* __restrict__ ws) {
  const unsigned long long tstart = __builtin_amdgcn_s_memrealtime();
  __builtin_amdgcn_sched_barrier(0);
  __shared__ __attribute__((aligned(16))) char smem_[L_END];
  LAS char* sm = (LAS char*)smem_;
  const unsigned smb = (unsigned)(size_t)sm;
  const int tid = threadIdx.x, l = tid & 63, w = __builtin_amdgcn_readfirstlane(tid >> 6);
  const int col = l & 15, g4 = l >> 4, tq = (l & 15) >> 2, tp = l & 3;
  const int xcd = blockIdx.x & 7, r = blockIdx.x >> 3;
  const __amdgpu_buffer_rsrc_t rs = __builtin_amdgcn_make_buffer_rsrc(ws, 0, WS_BYTES, 0x00020000);
  int bw = (blockIdx.x >> 7) * w;
  asm volatile("" : "+s"(bw));
  if (bw >= 5) {
    const char* pc0;
    unsigned len;
    asm volatile("s_getpc_b64 %0\n.Lmla_pc:\n\ts_mov_b32 %1, .Lmla_e0-.Lmla_pc\n\ts_max_i32 %1, %1, .Lmla_e1-.Lmla_pc\n\t"
                 "s_max_i32 %1, %1, .Lmla_e2-.Lmla_pc\n\ts_max_i32 %1, %1, .Lmla_e3-.Lmla_pc\n\ts_max_i32 %1, %1, .Lmla_e4-.Lmla_pc\n\t"
                 "s_max_i32 %1, %1, .Lmla_e5-.Lmla_pc"
                 : "=s"(pc0), "=s"(len)::"scc");
    touch(pc0, min((unsigned)(64 * (bw - 5) + l) * 128u, len), smb + L_JUNK);
  }
  __builtin_amdgcn_sched_barrier(0);
  asm volatile("" ::"s"(x), "s"(ckv), "s"(wq), "s"(wuk), "s"(wuv), "s"(wo), "s"(wg), "s"(wu), "s"(wd), "s"(out), "s"(ws));
  STAMP(0);

  unsigned ep, T;
  LAS char* const px = sm + L_X + (col & 1) * XP + 16 * g4;
  if ((w >> 1) == 2) dma16(x, (unsigned)((w & 1) * 1024 + l * 16), smb + L_X + (w & 1) * XP);
  const f4 z = {0.f, 0.f, 0.f, 0.f};
#define LAYERNORM()                                                                                                              \
  {                                                                                                                              \
    const v4u xr = *(LAS v4u*)(sm + L_X + (w & 1) * XP + l * 16);                                                                \
    float e[8] = {blo(xr.x), bhi(xr.x), blo(xr.y), bhi(xr.y), blo(xr.z), bhi(xr.z), blo(xr.w), bhi(xr.w)};                       \
    const float mean = wave_sum(((e[0] + e[1]) + (e[2] + e[3])) + ((e[4] + e[5]) + (e[6] + e[7]))) * (1.f / DM);                 \
    float q = 0.f;                                                                                                               \
    _Pragma("unroll") for (int i = 0; i < 8; ++i) {                                                                              \
      e[i] -= mean;                                                                                                              \
      q = fmaf(e[i], e[i], q);                                                                                                   \
    }                                                                                                                            \
    const float rstd = __builtin_amdgcn_rsqf(wave_sum(q) * (1.f / DM) + 1e-5f);                                                  \
    *(LAS v4u*)(sm + L_X + (w & 1) * XP + l * 16) =                                                                              \
        v4u{pk(e[0] * rstd, e[1] * rstd), pk(e[2] * rstd, e[3] * rstd), pk(e[4] * rstd, e[5] * rstd), pk(e[6] * rstd, e[7] * rstd)}; \
  }

  if (__builtin_expect(r >= 16, 0)) {
    const int a = r - 16, b = xcd >> 2, n = 8 * (xcd & 3) + (a >> 1), g = a & 1;
    const int sl = 12 * xcd + a, rd = (4 * xcd + a) & 31;
    const bool glu = a < 12 && sl < NSL;
    const int v = w & 3;
    unsigned op = WS_PART + ((((16 * g + (col & 7)) * 2 + (v >> 1)) * 2 + b) * 32 + n) * 512 + 256 * (v & 1) + (col >> 3) * 64 +
                  (g4 & 1) * 32 + (g4 >> 1) * 16;
    gld4_sc1(ep, ws, 0u);
    unsigned o = WS_QA + (b * NH + 16 * g) * 2048 + w * 4096 + l * 16, pqs = smb + L_Q + 2 * w * CP + l * 8;
    PIN(o); PIN(pqs);
    {
    {
      const unsigned vc = (unsigned)(((b * SK + 128 * n) * DC + 8 * l) * 2);
      STAMP(10);
      HOLD_UNTIL(C_START);
    if (w >= 4) {
#pragma unroll
      for (int q = 0; q < 4; ++q) st_sc1(rs, op + 128 * (q >> 1) + (q & 1) * 8 * 65536, v4u{~0u, ~0u, ~0u, ~0u});
      if (w == 7 && l < 32) *(LAS unsigned*)(sm + L_FL + 4 * l) = 0u;
    }
      HOLD_UNTIL(A_START);
#pragma unroll 1
      for (int q = 0; q < 4; ++q) {
        int row = w + ((64 * g + 32 * q) & 127);
        asm volatile("s_barrier" ::: "memory");
#pragma unroll 1
        for (int i = 0; i < 4; ++i, row += 8) dma16<NT_CKV>(ckv, vc + row * 1024u, smb + L_C + row * CP);
      }
      if ((w >> 1) == 2) {
        VMWAIT(21);
        LAYERNORM();
      }
      VMWAIT(0);
      EPOCH();
      PROBE_T(T);
      BAR();
      STAMP(1);
    }
#define QISSUE()                                \
  {                                   \
    SWEEP_4(qg, o, 0, 1024, 2048, 3072, "");    \
  }
#define QRETRY()                              \
  if (TAG_BAD(qg[0])) RELOAD(qg[0], o, 0);    \
  if (TAG_BAD(qg[1])) RELOAD(qg[1], o, 1024); \
  if (TAG_BAD(qg[2])) RELOAD(qg[2], o, 2048); \
  if (TAG_BAD(qg[3])) RELOAD(qg[3], o, 3072); \
  PIN4(qg)
#define QGATHER()                                                                                                           \
  VMWAIT(0);                                                                                                      \
  asm volatile("" : "+v"(qg[0]), "+v"(qg[1]), "+v"(qg[2]), "+v"(qg[3])::"memory");                                          \
  for (unsigned spin = 0;; ++spin) {                                                                                        \
    if (!TAGS_BAD4(qg) || spin > SPIN_CAP) break;                                                                           \
    QRETRY();                                                                                                               \
  }                                                                                                                         \
  STAMP(3);                                                                                                                 \
  asm volatile("ds_write2_b32 %0, %2, %3 offset0:0 offset1:1\n\tds_write2_b32 %0, %4, %5 offset0:128 offset1:129\n\t"        \
               "ds_write2_b32 %1, %6, %7 offset0:0 offset1:1\n\tds_write2_b32 %1, %8, %9 offset0:128 offset1:129" ::"v"(pqs), \
               "v"(pqs + CP), "v"(qg[0].x), "v"(qg[0].z), "v"(qg[1].x), "v"(qg[1].z), "v"(qg[2].x), "v"(qg[2].z), "v"(qg[3].x), \
               "v"(qg[3].z)                                                                                                 \
               : "memory")
    if (__builtin_expect(w < 4, 1)) {
      v4u fr[32], qg[4];
      QISSUE();
      LAS char* pc = sm + L_C + (32 * w + col) * CP + 16 * g4;
#pragma unroll
      for (int kt = 0; kt < 2; ++kt)
#pragma unroll
        for (int s = 0; s < 16; ++s) fr[16 * kt + s] = *(LAS v4u*)(pc + 16 * kt * CP + 64 * s);
      unsigned p = L_Q + col * CP + 16 * g4, pp = L_P + w * 1024 + l * 16, pm = L_MX + col * 16 + w * 4;
      unsigned ol = WS_LSE + ((b * 32 + n) * 8 + 4 * g + (col >> 2)) * 128 + (col & 3) * 8;
      PIN(p); PIN(pp); PIN(pm); PIN(ol);
      STAMP(2);
      QGATHER();
      BAR();
      STAMP(4);
      f4 s0 = z, s1 = z;
      v4u qf[16];
#pragma unroll
      for (int s = 0; s < 6; ++s) qf[s] = *(LAS v4u*)(sm + p + 64 * s);
      __builtin_amdgcn_sched_barrier(0);
#pragma unroll
      for (int s = 0; s < 16; ++s) {
        if (s + 6 < 16) qf[s + 6] = *(LAS v4u*)(sm + p + 64 * (s + 6));
        s0 = mfma32(fr[s], qf[s], s0);
        s1 = mfma32(fr[16 + s], qf[s], s1);
        __builtin_amdgcn_sched_barrier(0);
      }
      float mx = fmax2(fmax2(fmax2(s0[0], s0[1]), fmax2(s0[2], s0[3])), fmax2(fmax2(s1[0], s1[1]), fmax2(s1[2], s1[3])));
      mx = sw32_max(sw16_max(mx));
      constexpr float SC = 0.08838834764831845f * 1.4426950408889634f;
      const float mi = __builtin_ceilf(mx * SC);
#pragma unroll
      for (int e = 0; e < 4; ++e) {
        s0[e] = __builtin_amdgcn_exp2f(fmaf(s0[e], SC, -mi));
        s1[e] = __builtin_amdgcn_exp2f(fmaf(s1[e], SC, -mi));
      }
      *(LAS float*)(sm + pm) = mi;
      *(LAS v4u*)(sm + pp) = v4u{pk(s0[0], s0[1]), pk(s0[2], s0[3]), pk(s1[0], s1[1]), pk(s1[2], s1[3])};
      BAR();
      STAMP(5);
      if (w == LSE_QK - 1) {
        const f4 m4 = *(LAS f4*)(sm + pm - 4 * w);
        v4u pf[4];
#pragma unroll
        for (int u = 0; u < 4; ++u) pf[u] = *(LAS v4u*)(sm + pp + (u - w) * 1024);
        __builtin_amdgcn_sched_barrier(0);
        const float M = vmax3(vmax3(m4[0], m4[1], m4[2]), m4[3], m4[3]);
        const v4u one = {0x3f803f80u, 0x3f803f80u, 0x3f803f80u, 0x3f803f80u};
        f4 ls = z;
#pragma unroll
        for (int u = 0; u < 4; ++u) {
          const unsigned kk = min((unsigned)(M - m4[u]), 255u) * 0x00800080u;
#pragma unroll
          for (int e = 0; e < 4; ++e) pf[u][e] = scale_dn(pf[u][e], kk);
          ls = mfma32(one, pf[u], ls);
        }
        if (g4 == 0)
          __builtin_amdgcn_raw_buffer_store_b64(v2u{__builtin_bit_cast(unsigned, M + __builtin_amdgcn_logf(ls[0])), T}, rs, ol, 0, AUX_SC1);
      }
    } else {
      v4u fr[32], qg[4];
      QISSUE();
      LAS char* p0 = sm + L_C + (4 * g4 + tq) * CP + 256 * v + 8 * tp;
      unsigned p1 = L_C + (64 + 4 * g4 + tq) * CP + 256 * v + 8 * tp;
      PIN(p1);
#pragma unroll
      for (int ct = 0; ct < 8; ++ct)
#pragma unroll
        for (int u = 0; u < 4; ++u) {
          LAS char* p = (u < 2 ? p0 : sm + p1) + 32 * (u & 1) * CP + 32 * ct;
          fr[4 * ct + u] = trrd2(p, p + 16 * CP);
        }
      unsigned ol = WS_LSE + ((b * 32 + n) * 8 + 4 * g + (col >> 2)) * 128 + (col & 3) * 8, pp = L_P + l * 16, pm = L_MX + col * 16;
      v4u one = {0x3f803f80u, 0x3f803f80u, 0x3f803f80u, 0x3f803f80u};
      unsigned op2 = op + 8 * 65536;
      PIN(ol); PIN(pp); PIN(pm); PIN(one); PIN(op); PIN(op2);
      STAMP(2);
      QGATHER();
      BAR();
      STAMP(4);
      if (v < G_TOUCH) {
        const int ln = 128 * a + 64 * (v >> 1) + l;
        touch((v & 1) ? wu : wg, (unsigned)((ln >> 2) * (FF * 2)) + (unsigned)min(384 * xcd + 128 * (ln & 3), FF * 2 - 4), smb + L_JUNK);
      }
      BAR();
      STAMP(5);
      const f4 m4 = *(LAS f4*)(sm + pm);
      v4u pf[4];
#pragma unroll
      for (int u = 0; u < 4; ++u) pf[u] = *(LAS v4u*)(sm + pp + u * 1024);
      __builtin_amdgcn_sched_barrier(0);
      const float M = vmax3(vmax3(m4[0], m4[1], m4[2]), m4[3], m4[3]);
      f4 ls = z, c[4] = {z, z, z, z};
#pragma unroll
      for (int u = 0; u < 4; ++u) {
        const unsigned kk = min((unsigned)(M - m4[u]), 255u) * 0x00800080u;
#pragma unroll
        for (int e = 0; e < 4; ++e) pf[u][e] = scale_dn(pf[u][e], kk);
        ls = mfma32(one, pf[u], ls);
        if (u < 3) {
#pragma unroll
          for (int k = 0; k < 4; ++k) c[k] = mfma32(fr[4 * k + u], pf[u], c[k]);
        }
      }
      __builtin_amdgcn_sched_barrier(0);
      c[0] = mfma32(fr[3], pf[3], c[0]);
      c[1] = mfma32(fr[7], pf[3], c[1]);
      __builtin_amdgcn_sched_barrier(0);
      float il = __builtin_amdgcn_rcpf(ls[0]);
      unsigned lse = __builtin_bit_cast(unsigned, M + __builtin_amdgcn_logf(ls[0]));
      c[2] = mfma32(fr[11], pf[3], c[2]);
      c[3] = mfma32(fr[15], pf[3], c[3]);
#define PV_OUT(c, q)                                                                                          \
  {                                                                                                           \
    v4u R[2];                                                                                                 \
    _Pragma("unroll") for (int k = 0; k < 2; ++k) {                                                           \
      const f4 c0 = c[2 * k] * il, c1 = c[2 * k + 1] * il;                                                    \
      const unsigned x0 = min(pk(c0[0], c0[1]), 0xfffffffeu), x1 = min(pk(c0[2], c0[3]), 0xfffffffeu);        \
      const unsigned y0 = min(pk(c1[0], c1[1]), 0xfffffffeu), y1 = min(pk(c1[2], c1[3]), 0xfffffffeu);        \
      auto r0 = __builtin_amdgcn_permlane16_swap(x0, y0, false, false);                                       \
      auto r1 = __builtin_amdgcn_permlane16_swap(x1, y1, false, false);                                       \
      R[k] = v4u{r0[0], r1[0], r0[1], r1[1]};                                                                 \
    }                                                                                                         \
    v4u A, B;                                                                                                 \
    _Pragma("unroll") for (int e = 0; e < 4; ++e) {                                                           \
      A[e] = (unsigned)__builtin_amdgcn_update_dpp((int)R[0][e], (int)R[1][e], ROR8, 0xf, 0xc, false);        \
      B[e] = (unsigned)__builtin_amdgcn_update_dpp((int)R[1][e], (int)R[0][e], ROR8, 0xf, 0x3, false);        \
    }                                                                                                         \
    st_sc1(rs, op + 128 * q, A);                                                                              \
    st_sc1(rs, op2 + 128 * q, B);                                                                             \
  }
      __builtin_amdgcn_sched_barrier(0);
      f4 d[4] = {z, z, z, z};
#pragma unroll
      for (int u = 0; u < 4; ++u)
#pragma unroll
        for (int k = 0; k < 4; ++k) d[k] = mfma32(fr[16 + 4 * k + u], pf[u], d[k]);
      PV_OUT(c, 0);
#pragma unroll
      for (int i = 0; i < 16; ++i) {
        __builtin_amdgcn_sched_group_barrier(0x8, 1, 0);
        __builtin_amdgcn_sched_group_barrier(0x2, PV_MIX, 0);
      }
      __builtin_amdgcn_sched_barrier(0);
      PV_OUT(d, 1);
      FLAG_SET(v);
      MARK(3);
      STAMP(6);
    }
    }
      if (glu && w < 4) {
        v4u af[8], xf[8];
#pragma unroll
        for (int s = 0; s < 8; ++s) xf[s] = *(LAS v4u*)(px + 512 * (w >> 1) + 64 * s);
        unsigned oog = WS_OPG + ((8 * w + (col >> 1)) * 128 + sl) * 128 + ((col & 1) * 4 + g4) * 16;
        PIN(oog);
        FLAG_WAIT4(0);
#pragma unroll 1
        for (int i = 0; i < 4; ++i) dma16<NT_G>(wd, (unsigned)(((16 * sl + 4 * w + i) * DM + 8 * l) * 2), smb + L_GD + (4 * w + i) * CP);
        unsigned og = (unsigned)(((128 * w + (l >> 1)) * FF + 16 * sl + 8 * (l & 1)) * 2);
#pragma unroll 1
        for (int i = 0; i < 4; ++i, og += 32 * FF * 2) {
          dma16(wg, og, smb + L_GG + (4 * w + i) * 1024);
          dma16(wu, og, smb + L_GG + 16384 + (4 * w + i) * 1024);
        }
        unsigned pg = L_GG + (w & 1) * 16384 + 32 * (256 * (w >> 1) + 8 * g4 + tq) + 8 * tp;
        unsigned pd = L_GD + (4 * g4 + tq) * CP + 256 * w + 8 * tp, pgw = L_GR + ((w & 1) * 2 + (w >> 1)) * 1024 + l * 16;
        PIN(pg); PIN(pd); PIN(pgw);
        VMWAIT(0);
        FLAG_SET(4 + w);
        FLAG_WAIT4(1);
        STAMP(7);
        f4 acc = z;
#pragma unroll
        for (int s = 0; s < 8; ++s) af[s] = trrd2(sm + pg + 32 * 32 * s, sm + pg + 32 * (32 * s + 4));
        __builtin_amdgcn_sched_barrier(0);
        v2u dn[8];
#pragma unroll
        for (int s = 0; s < 8; ++s) {
          acc = mfma32(af[s], xf[s], acc);
          dn[s] = trrd(sm + pd + 32 * s);
          __builtin_amdgcn_sched_barrier(0);
        }
        *(LAS f4*)(sm + pgw) = acc;
        FLAG_SET(8 + w);
        FLAG_WAIT4(2);
        LAS char* pr = sm + L_GR + l * 16;
        const f4 gx = *(LAS f4*)(pr) + *(LAS f4*)(pr + 1024), ux = *(LAS f4*)(pr + 2048) + *(LAS f4*)(pr + 3072);
        float hh[4];
#pragma unroll
        for (int e = 0; e < 4; ++e) hh[e] = ux[e] * __builtin_amdgcn_rcpf(1.f + __builtin_amdgcn_exp2f(-1.4426950408889634f * gx[e]));
        const v2u hb = {pk(hh[0], hh[1]), pk(hh[2], hh[3])};
        f4 d = z;
#pragma unroll
        for (int t = 0; t < 8; ++t) d = mfma16(dn[t], (col >> 1) == t ? hb : v2u{0u, 0u}, d);
        v4u dv = {pk(d[0], d[1]), T, pk(d[2], d[3]), T};
        PIN(dv);
        st_sc1(rs, oog, dv);
        MARK(4);
        STAMP(9);
      }
    if (a >= 12) {
      const unsigned m = (col & 1) ? 0x3f800000u : 0x3f80u;
      v4u sel = {col < 2 ? m : 0u, (col >> 1) == 1 ? m : 0u, col < 2 ? m : 0u, (col >> 1) == 1 ? m : 0u};
      PIN(sel);
      if (w == 0) {
        unsigned ot = WS_OPT + rd * 8192 + 4096 + l * 16, oy = (l >> 2) * 1024 + rd * 32 + (l & 3) * 8, pr = smb + L_RR + (l & 7) * 16;
        PIN(ot); PIN(oy); PIN(pr);
        v4u p[8];
        HOLD_UNTIL(R_START);
        SWEEP_4(p, ot, -4096, -3072, -2048, -1024, "");
        SWEEP_4(p + 4, ot, 0, 1024, 2048, 3072, "");
        PIN4(p);
        PIN4(p + 4);
        for (unsigned spin = 0;; ++spin) {
          if (!(TAGS_BAD4(p) | TAGS_BAD4(p + 4)) || spin > SPIN_CAP) break;
          if (TAG_BAD(p[0])) RELOAD(p[0], ot, -4096);
          if (TAG_BAD(p[1])) RELOAD(p[1], ot, -3072);
          if (TAG_BAD(p[2])) RELOAD(p[2], ot, -2048);
          if (TAG_BAD(p[3])) RELOAD(p[3], ot, -1024);
          if (TAG_BAD(p[4])) RELOAD(p[4], ot, 0);
          if (TAG_BAD(p[5])) RELOAD(p[5], ot, 1024);
          if (TAG_BAD(p[6])) RELOAD(p[6], ot, 2048);
          if (TAG_BAD(p[7])) RELOAD(p[7], ot, 3072);
          PIN4(p);
          PIN4(p + 4);
        }
        STAMP(8);
        f4 acc = z;
#pragma unroll
        for (int i = 0; i < 4; ++i) acc = mfma32(sel, v4u{p[2 * i].x, p[2 * i].z, p[2 * i + 1].x, p[2 * i + 1].z}, acc);
        f4 rest[6];
        for (unsigned spin = 0; spin < SPIN_CAP; ++spin) {
          v4u f0;
          v2u f1;
          asm volatile("ds_read_b128 %0, %8\n\tds_read_b64 %1, %8 offset:16\n\tds_read_b128 %2, %9\n\tds_read_b128 %3, %9 offset:128\n\t"
                       "ds_read_b128 %4, %9 offset:256\n\tds_read_b128 %5, %9 offset:384\n\tds_read_b128 %6, %9 offset:512\n\t"
                       "ds_read_b128 %7, %9 offset:640\n\ts_waitcnt lgkmcnt(0)"
                       : "=&v"(f0), "=&v"(f1), "=&v"(rest[0]), "=&v"(rest[1]), "=&v"(rest[2]), "=&v"(rest[3]), "=&v"(rest[4]), "=&v"(rest[5])
                       : "v"(smb + L_FL + 64), "v"(pr)
                       : "memory");
          if (__builtin_amdgcn_readfirstlane(f0.x & f0.y & f0.z & f0.w & f1.x & f1.y)) break;
        }
        const f4 gl = ((rest[0] + rest[1]) + (rest[2] + rest[3])) + (rest[4] + rest[5]);
#pragma unroll
        for (int e = 0; e < 4; ++e) acc[e] = (acc[e] + dpp<ROR8>(acc[e])) + gl[e];
        if (l < 8) *(v2u*)((char*)out + oy) = v2u{pk(acc[0], acc[1]), pk(acc[2], acc[3])};
        if (rd == 0 && l == 0) __builtin_amdgcn_raw_buffer_store_b32(T, rs, WS_E, 0, AUX_SC1);
      } else if (w < 7) {
        const bool last = w == 6;
        unsigned og = WS_OPG + rd * 16384 + (w - 1) * 2048 + (last ? min(l, 47) : l) * 16, og2 = last ? og : og + 1024;
        const v2u xr = *(const v2u*)((const char*)x + ((l >> 2) & 1) * 1024 + rd * 32 + (l & 3) * 8);
        unsigned m0 = (last && l >= 48) ? 0u : ~0u, m1 = last ? 0u : ~0u;
        f4 xv = {blo(xr.x), bhi(xr.x), blo(xr.y), bhi(xr.y)};
        if (!last) xv = z;
        PIN(og); PIN(og2); PIN(m0); PIN(m1); PIN(xv);
        v4u p0, p1;
        HOLD_UNTIL(R_START);
        for (unsigned spin = 0;; ++spin) {
          SWEEP_2(p0, p1, og, og2, WAIT0);
          if (!(TAG_BAD(p0) | TAG_BAD(p1)) || spin > SPIN_CAP) break;
        }
        STAMP(8);
        f4 acc = mfma32(sel, v4u{p0.x & m0, p0.z & m0, p1.x & m1, p1.z & m1}, z);
#pragma unroll
        for (int e = 0; e < 4; ++e) acc[e] = (acc[e] + dpp<ROR8>(acc[e])) + xv[e];
        if (l < 8) *(LAS f4*)(sm + L_RR + (w - 1) * 128 + l * 16) = acc;
        FLAG_SET(15 + w);
      }
      MARK(5);
      STAMP(9);
    }
    STAMP_FLUSH();
    __builtin_amdgcn_endpgm();
  }
  {
    const int h = 4 * xcd + (r >> 2), j = r & 3, ca = j >> 1, mb = j & 1;
    const int fl = tq | ((g4 & 1) << 2);
    gld4_sc1(ep, ws, 0u);
    if (__builtin_expect(w < 4, 1)) {
      const unsigned vq = (unsigned)(((128 * j + g4) * 4096 + h * DH + 8 * (col ^ ((g4 | ((w & 2) << 1)) << 1))) * 2);
#pragma unroll 1
      for (int k = w; k < 32; k += 4) dma16<NT_WQ>(wq, vq + (unsigned)k * 32768u, smb + L_WQ + k * 1024);
    } else if (w < 6) {
      VMWAIT(1);
      LAYERNORM();
      MARK(0);
    } else {
      __builtin_amdgcn_s_sleep(UK_DELAY);
      const unsigned vu = (unsigned)((((128 * j + g4) * NH + h) * DH) * 2);
#pragma unroll 1
      for (int k = w - 6; k < 32; k += 2) dma16<NT_WUK>(wuk, vu + (unsigned)k * 32768u + 16u * (col ^ ((4 * k + g4) & 15)), smb + L_WUK + k * 1024);
      MARK(1);
    }
    unsigned p = L_WQ + 256 * (8 * g4 + tq) + 16 * ((2 * w + (tp >> 1)) ^ (fl << 1)) + 8 * (tp & 1);
    unsigned oqp = WS_QP + ((h * 4 + j) * 8 + w) * 128 + col * 64 + g4 * 16;
    unsigned pxj = L_X + (col & 1) * XP + 16 * g4 + 256 * j;
    unsigned o = WS_QP + (h * 4 + (w & 3)) * 1024 + l * 16;
    unsigned pqw = smb + L_CC + (2 * (w & 3) + ((l >> 2) & 1)) * SP + (l >> 3) * 32 + (l & 3) * 8, pqr = L_CC + (col & 7) * SP + 16 * g4;
    unsigned oqa = WS_QA + ((col & 1) * NH + h) * 2048 + (64 * j + 16 * w + 8 * (col >> 1) + 2 * g4) * 8;
    unsigned pu = L_WUK + 256 * (32 * w + col) + 16 * (g4 ^ col);
    PIN(p); PIN(oqp); PIN(pxj); PIN(o); PIN(oqa); PIN(pu); PIN(pqw); PIN(pqr);
    {
    {
      if (w < 6) VMWAIT(0);
      else VMWAIT(16);
      EPOCH();
      STAMP(1);
      BAR();
      STAMP(2);
    }
    {
      v4u af[4], xf[4];
#pragma unroll
      for (int s = 0; s < 4; ++s) {
        af[s] = trrd2(sm + p + 256 * 32 * s, sm + p + 256 * (32 * s + 4));
        xf[s] = *(LAS v4u*)(sm + pxj + 64 * s);
      }
      __builtin_amdgcn_sched_barrier(0);
      f4 acc = z;
#pragma unroll
      for (int s = 0; s < 4; ++s) acc = mfma32(af[s], xf[s], acc);
      if (col < 2) st_sc1(rs, oqp, v4u{pk(acc[0], acc[1]), T, pk(acc[2], acc[3]), T});
    }
    STAMP(3);
    if (w >= 6) VMWAIT(1);
    if (w >= 4) {
      BAR();
      BAR();
    } else {
      v4u a, uk[8], qf[4];
      {
        __builtin_amdgcn_s_sleep(H1_DELAY);
        asm volatile(LD16(0, 1, 2) " sc1" : "=&v"(a) : "v"(o), "s"(ws) : "memory");
        BAR();
      }
#pragma unroll
      for (int s = 0; s < 4; ++s) {
        uk[s] = *(LAS v4u*)(sm + (pu ^ (64 * s)));
        uk[4 + s] = *(LAS v4u*)(sm + (pu ^ (64 * s)) + 4096);
      }
      VMWAIT(0);
      PIN(a);
      for (unsigned spin = 0;; ++spin) {
        if (!__builtin_amdgcn_ballot_w64((T & a.y & a.w) != (T | a.y | a.w)) || spin > SPIN_CAP) break;
        asm volatile(LD16(0, 1, 2) " sc1" WAIT0 : "+v"(a) : "v"(o), "s"(ws) : "memory");
      }
      asm volatile("ds_write2_b32 %0, %1, %2 offset0:0 offset1:1" ::"v"(pqw), "v"(a.x), "v"(a.z) : "memory");
      STAMP(4);
      BAR();
#pragma unroll
      for (int s = 0; s < 4; ++s) qf[s] = *(LAS v4u*)(sm + pqr + 64 * s);
      __builtin_amdgcn_sched_barrier(0);
      f4 a0 = z, a1 = z;
#pragma unroll
      for (int s = 0; s < 4; ++s) {
        a0 = mfma32(uk[s], qf[s], a0);
        a1 = mfma32(uk[4 + s], qf[s], a1);
      }
      float q[4];
#pragma unroll
      for (int e = 0; e < 4; ++e) {
        const float mine = (col & 2) ? a1[e] : a0[e], other = (col & 2) ? a0[e] : a1[e];
        q[e] = mine + dpp<QP_2301>(other);
        q[e] += dpp<ROR4>(q[e]);
      }
      if (col < 4) st_sc1(rs, oqa, v4u{pk(q[0], q[1]), T, pk(q[2], q[3]), T});
    }
    }
    STAMP(5);
    PROBE_T(T);
    BAR();
    HOLD_UNTIL(T_START);
    {
      const unsigned vv = (unsigned)((((256 * ca + g4) * NH + h) * DH + 8 * (col ^ ((g4 | ((w & 2) << 1)) << 1))) * 2);
      const unsigned vo = (unsigned)(((h * DH + (l >> 5)) * DM + 256 * mb) * 2) + 16u * ((l & 31) ^ (((l >> 5) | ((w & 1) << 1) | (w & 4)) << 1));
      int kv = w + 32 * mb, ko = w + 32 * ca;
#pragma unroll 1
      for (int hf = 0; hf < 2; ++hf) {
#pragma unroll 1
        for (int i = 0; i < 4; ++i, kv = (kv + 8) & 63) dma16<NT_T>(wuv, vv + (unsigned)kv * 32768u, smb + L_WUV + kv * 1024);
#pragma unroll 1
        for (int i = 0; i < 4; ++i, ko = (ko + 8) & 63) dma16<NT_T>(wo, vo + (unsigned)ko * 2048u, smb + L_WO + ko * 1024);
        if (hf == 0) asm volatile("s_barrier" ::: "memory");
      }
    }
    const int tb = w >> 2, nq = w & 3;
    o = WS_PART + (((h * 2 + ca) * 2 + tb) * 32 + 8 * nq + g4) * 512 + 16 * col;
    unsigned pw = L_W + (tb * 32 + 8 * nq + g4) * 4;
    unsigned pv = L_WUV + 256 * (8 * g4 + tq) + 16 * ((2 * w + (tp >> 1)) ^ (fl << 1)) + 8 * (tp & 1);
    unsigned po = L_WO + 512 * (8 * g4 + tq) + 16 * ((4 * w + (tp >> 1)) ^ (fl << 1)) + 8 * (tp & 1);
    unsigned pcw = L_CC + (nq * 2 + tb) * SP2 + 16 * col + 8 * g4, pcr = L_CC + (col & 7) * SP2 + 16 * g4;
    unsigned ptw = L_CT + col * SP + (16 * w + 4 * g4) * 2, ptr = L_CT + (col & 1) * SP + 16 * g4;
    unsigned oo = WS_OPT + ((16 * mb + 2 * w + (col >> 1)) * 64 + 2 * h + ca) * 128 + ((col & 1) * 4 + g4) * 16;
    v4u mk;
#pragma unroll
    for (int d = 0; d < 4; ++d) mk[d] = (col >> 1) == d ? ((col & 1) ? 0xffff0000u : 0xffffu) : 0u;
    PIN(o); PIN(pw); PIN(pv); PIN(po); PIN(pcw); PIN(pcr); PIN(ptw); PIN(ptr); PIN(oo); PIN(mk);
    STAMP(11);
        if (w == 7) {
      const unsigned ol = WS_LSE + (l * 8 + (h >> 2)) * 128 + (h & 3) * 8;
      v2u la;
      {
        unsigned spin = SPIN_CAP;
#define LSE_STEP(lo, hi)                                                                                     \
  "s_waitcnt vmcnt(2)\n\tv_cmp_ne_u32 vcc, v" #hi ", %4\n\tv_mov_b32 %0, v" #lo "\n\ts_cbranch_vccz 2f\n\t" \
  "s_sleep %5\n\tglobal_load_dwordx2 v[" #lo ":" #hi "], %2, %3 sc1\n\t"
        asm volatile("global_load_dwordx2 v[200:201], %2, %3 sc1\n\ts_waitcnt vmcnt(1)\n\t"
                     "global_load_dwordx2 v[202:203], %2, %3 sc1\n\ts_sleep %5\n\t"
                     "global_load_dwordx2 v[204:205], %2, %3 sc1\n"
                     "1:\n\t" LSE_STEP(200, 201) LSE_STEP(202, 203) LSE_STEP(204, 205)
                     "s_sub_u32 %1, %1, 1\n\ts_cbranch_scc0 1b\n"
                     "2:"
                     : "=&v"(la.x), "+s"(spin)
                     : "v"(ol), "s"(ws), "v"(T), "n"(LSE_PIPE)
                     : "memory", "vcc", "scc", "v200", "v201", "v202", "v203", "v204", "v205");
      }
      float M = asf(la.x);
      M = fmax2(M, dpp<ROR8>(M));
      M = fmax2(M, dpp<ROR4>(M));
      M = fmax2(M, dpp<ROR2>(M));
      M = fmax2(M, dpp<ROR1>(M));
      M = sw16_max(M);
      const float e = __builtin_amdgcn_exp2f(asf(la.x) - M);
      float L = e + dpp<ROR8>(e);
      L += dpp<ROR4>(L);
      L += dpp<ROR2>(L);
      L += dpp<ROR1>(L);
      L = sw16_add(L, L);
      const float wt = e * __builtin_amdgcn_rcpf(L);
      *(LAS unsigned*)(sm + L_W + 4 * l) = pk(wt, wt);
      STAMP(7);
    }
    v4u a[4];
    {
      if (LSE_PIPE && w == 7) VMWAIT(2);
      else VMWAIT(0);
      STAMP(6);
      BAR();
      SWEEP_4(a, o, 0, 256, 2048, 2304, "");
    }
    v4u A0 = mk & *(LAS unsigned*)(sm + pw), A1 = mk & *(LAS unsigned*)(sm + pw + 16);
    v4u av[8], ao[8];
#pragma unroll
    for (int s = 0; s < 8; ++s) av[s] = trrd2(sm + pv + 256 * 32 * s, sm + pv + 256 * (32 * s + 4));
#pragma unroll
    for (int t = 0; t < 2; ++t)
#pragma unroll
      for (int s = 0; s < 4; ++s) ao[4 * t + s] = trrd2(sm + (po ^ (32 * t)) + 512 * 32 * s, sm + (po ^ (32 * t)) + 512 * (32 * s + 4));
    VMWAIT(0);
    asm volatile("" : "+v"(a[0]), "+v"(a[1]), "+v"(a[2]), "+v"(a[3]), "+v"(A0), "+v"(A1)::"memory");
    for (unsigned spin = 0;; ++spin) {
      unsigned mx = 0u;
#pragma unroll
      for (int i = 0; i < 4; ++i) mx = max(max(mx, max(a[i].x, a[i].y)), max(a[i].z, a[i].w));
      if (!__builtin_amdgcn_ballot_w64(mx == ~0u) || spin > SPIN_CAP) break;
      if (HOLE(a[0])) RELOAD(a[0], o, 0);
      if (HOLE(a[1])) RELOAD(a[1], o, 256);
      if (HOLE(a[2])) RELOAD(a[2], o, 2048);
      if (HOLE(a[3])) RELOAD(a[3], o, 2304);
      PIN4(a);
    }
    STAMP(8);
    {
      f4 c0 = mfma32(A0, a[0], z), c1 = mfma32(A0, a[1], z);
      c0 = mfma32(A1, a[2], c0);
      c1 = mfma32(A1, a[3], c1);
      if (g4 < 2) {
        *(LAS v2u*)(sm + pcw) = v2u{pk(c0[0], c0[1]), pk(c0[2], c0[3])};
        *(LAS v2u*)(sm + pcw + 256) = v2u{pk(c1[0], c1[1]), pk(c1[2], c1[3])};
      }
    }
    BAR();
    {
      f4 acc = z;
      v4u bf[8];
#pragma unroll
      for (int s = 0; s < 8; ++s) bf[s] = *(LAS v4u*)(sm + pcr + 64 * s);
      __builtin_amdgcn_sched_barrier(0);
#pragma unroll
      for (int s = 0; s < 8; ++s) acc = mfma32(av[s], bf[s], acc);
#pragma unroll
      for (int e = 0; e < 4; ++e) {
        acc[e] += dpp<QP_2301>(acc[e]);
        acc[e] += dpp<ROR4>(acc[e]);
      }
      if (col < 2) *(LAS v2u*)(sm + ptw) = v2u{pk(acc[0], acc[1]), pk(acc[2], acc[3])};
    }
    BAR();
    {
      f4 c0 = z, c1 = z;
      v4u bf[4];
#pragma unroll
      for (int s = 0; s < 4; ++s) bf[s] = *(LAS v4u*)(sm + ptr + 64 * s);
      __builtin_amdgcn_sched_barrier(0);
#pragma unroll
      for (int s = 0; s < 4; ++s) {
        c0 = mfma32(ao[s], bf[s], c0);
        c1 = mfma32(ao[4 + s], bf[s], c1);
      }
      if (col & 2) c0 = c1;
      if (col < 4) st_sc1(rs, oo, v4u{pk(c0[0], c0[1]), T, pk(c0[2], c0[3]), T});
    }
    MARK(2);
    STAMP(9);
  }
  STAMP_FLUSH();
}

#define WS_END 0
#define WS_BASE (WS_END ? ((ws_size - (size_t)WS_END * 1048576) & ~(size_t)4095) : 0)
extern "C" void kernel_launch(void* const* d_in, const int* in_sizes, int n_in, void* d_out, int out_size, void* d_ws, size_t ws_size,
                              hipStream_t stream) {
  mla_glu_decode<<<256, 512, 0, stream>>>((const u16*)d_in[0], (const u16*)d_in[1], (const u16*)d_in[2], (const u16*)d_in[3],
                                          (const u16*)d_in[4], (const u16*)d_in[5], (const u16*)d_in[7], (const u16*)d_in[8],
                                          (const u16*)d_in[9], (u16*)d_out, (char*)d_ws + WS_BASE);
}
```

```cpp
#define PROBE_CLASS 0
#define PROBE_IDX 3
#define PROBE_WAVE 0
#define PROBE_K 10
#define A_START 110
#define T_START 340
#include <hip/hip_runtime.h>
#include <stdint.h>

#define GAS __attribute__((address_space(1)))
#define LAS __attribute__((address_space(3)))
typedef unsigned short u16;
typedef unsigned v4u __attribute__((ext_vector_type(4)));
typedef unsigned v2u __attribute__((ext_vector_type(2)));
typedef float f4 __attribute__((ext_vector_type(4)));
typedef float f2 __attribute__((ext_vector_type(2)));
typedef __bf16 bf8 __attribute__((ext_vector_type(8)));
typedef __bf16 bf2 __attribute__((ext_vector_type(2)));
typedef short s4 __attribute__((ext_vector_type(4)));

namespace {
constexpr int NH = 32, SK = 4096, DH = 128, DM = 512, DC = 512, FF = 1376;
constexpr int NSL = FF / 16;
constexpr int WS_E = 0;
constexpr int WS_QP = 4096;
constexpr int WS_QA = WS_QP + NH * 4 * 1024;
constexpr int WS_LSE = WS_QA + 2 * NH * 2048;
constexpr int WS_OPT = WS_LSE + 2 * 32 * 8 * 128;
constexpr int WS_OPG = WS_OPT + 32 * 64 * 128;
constexpr int WS_PART = WS_OPG + 32 * 128 * 128;
constexpr int WS_BYTES = WS_PART + NH * 2 * 2 * 32 * 512;
constexpr int WS_STAMP = 7 << 19;
constexpr int CP = 1056;
constexpr int XP = 1088, SP = 288, SP2 = 544;
constexpr int L_C = 0, L_Q = 128 * CP, L_P = L_Q + 16 * CP, L_X = L_P + 4096, L_MX = L_X + 2 * XP, L_RR = L_MX + 256,
              L_JUNK = L_RR + 1024, L_FL = L_JUNK + 256, L_END = L_FL + 128;
static_assert(L_END <= 163840, "LDS");
constexpr int L_GG = 0, L_GD = 32768, L_GR = 65536;
constexpr int L_WUV = 0, L_WO = 65536, L_WQ = 0, L_WUK = 32768, L_CC = 131072, L_CT = L_CC + 8 * SP2, L_W = L_CT + 2 * SP;
#define UK_DELAY 4
#define H1_DELAY 4
#define CODE_TOUCH 1
#define NT_CKV 0
#define NT_WQ 0
#define NT_WUK 0
#define NT_T 0
#define NT_G 0
#define GLU_EARLY 0
#define G_START 0
#define C_START 0
#define LSE_LATE 0
#define LSE_WAVE 3
#define DATA_DELAY 0
#define NEWRED 1
#define R_START 0
#define G_TOUCH 4
#define LSE_PIPE 3
#define PV_MIX 4
#define LSE_QK 1
#define PIECEWISE 1
#define PF_GLU 1
constexpr unsigned SPIN_CAP = 1u << 20;
constexpr int AUX_SC1 = 16;

#define STAMPS
#define STAMP_W 0
#define STAMP(i)                                                                          \
  do {                                                                                    \
    unsigned long long t__;                                                               \
    asm volatile("s_memrealtime %0\n\ts_waitcnt lgkmcnt(0)" : "=s"(t__)::"memory");       \
    asm volatile("v_writelane_b32 %0, %1, " #i : "+v"(tsv) : "s"((unsigned)t__));         \
  } while (0)
#define STAMP_FLUSH()                                                                      \
  do {                                                                                     \
    const int cls_ = r < 16 ? 0 : (r < 28 ? 1 : 2);                                        \
    if ((cls_ == PROBE_CLASS || PROBE_CLASS == 3) && w == PROBE_WAVE && __builtin_amdgcn_readlane(tsv, PROBE_IDX) != 0) {   \
      v4u g_;                                                                              \
      for (unsigned sp_ = 0; sp_ < SPIN_CAP; ++sp_) { asm volatile("" ::: "memory"); g_ = ld_sc1(rs, 2048); if (g_.z == Tg) break; } \
      const unsigned long long t0_ = ((unsigned long long)__builtin_amdgcn_readfirstlane(g_.y) << 32) | (unsigned)__builtin_amdgcn_readfirstlane(g_.x); \
      const long long d_ = (int)(__builtin_amdgcn_readlane(tsv, PROBE_IDX) - (unsigned)t0_);                               \
      const unsigned long long until_ = t0_ + 4000ull + PROBE_K * d_;                      \
      while (__builtin_amdgcn_s_memrealtime() < until_) __builtin_amdgcn_s_sleep(1);       \
    }                                                                                      \
  } while (0)
#define PROBE_T(T_) do { Tg = T_; if (blockIdx.x == 0 && tid == 0) st_sc1(rs, 2048, v4u{(unsigned)tstart, (unsigned)(tstart >> 32), T_, 0u}); } while (0)

__device__ __forceinline__ f4 mfma32(v4u a, v4u b, f4 c) {
  return __builtin_amdgcn_mfma_f32_16x16x32_bf16(__builtin_bit_cast(bf8, a), __builtin_bit_cast(bf8, b), c, 0, 0, 0);
}
__device__ __forceinline__ f4 mfma16(v2u a, v2u b, f4 c) {
  return __builtin_amdgcn_mfma_f32_16x16x16bf16_1k(__builtin_bit_cast(s4, a), __builtin_bit_cast(s4, b), c, 0, 0, 0);
}
__device__ __forceinline__ unsigned pk(float lo, float hi) {
  f2 x = {lo, hi};
  return __builtin_bit_cast(unsigned, __builtin_convertvector(x, bf2));
}
__device__ __forceinline__ float asf(unsigned u) { return __builtin_bit_cast(float, u); }
__device__ __forceinline__ float blo(unsigned u) { return __builtin_bit_cast(float, u << 16); }
__device__ __forceinline__ float bhi(unsigned u) { return __builtin_bit_cast(float, u & 0xffff0000u); }
template <int CTRL> __device__ __forceinline__ float dpp(float v) {
  return __builtin_bit_cast(float, __builtin_amdgcn_update_dpp(0, __builtin_bit_cast(int, v), CTRL, 0xf, 0xf, true));
}
__device__ __forceinline__ float fmax2(float a, float b) {
#pragma float_control(precise, off)
  return __builtin_fmaxf(a, b);
}
typedef unsigned short h2 __attribute__((ext_vector_type(2)));
__device__ __forceinline__ unsigned scale_dn(unsigned p, unsigned kk) {
  return __builtin_bit_cast(unsigned, __builtin_elementwise_sub_sat(__builtin_bit_cast(h2, p), __builtin_bit_cast(h2, kk)));
}
#define PIN(x) asm volatile("" : "+v"(x))
constexpr int ROR1 = 0x121, ROR2 = 0x122, ROR4 = 0x124, ROR8 = 0x128, QP_2301 = 0x4e, QP_2222 = 0xaa;
__device__ __forceinline__ float sw32_add(float a, float b) {
  auto r = __builtin_amdgcn_permlane32_swap(__builtin_bit_cast(unsigned, a), __builtin_bit_cast(unsigned, b), false, false);
  return asf(r[0]) + asf(r[1]);
}
__device__ __forceinline__ float sw16_add(float a, float b) {
  auto r = __builtin_amdgcn_permlane16_swap(__builtin_bit_cast(unsigned, a), __builtin_bit_cast(unsigned, b), false, false);
  return asf(r[0]) + asf(r[1]);
}
__device__ __forceinline__ float vmax(unsigned a, unsigned b) {
  float r;
  asm("v_max_f32 %0, %1, %2" : "=v"(r) : "v"(a), "v"(b));
  return r;
}
__device__ __forceinline__ float vmax3(float a, float b, float c) {
  float r;
  asm("v_max3_f32 %0, %1, %2, %3" : "=v"(r) : "v"(a), "v"(b), "v"(c));
  return r;
}
__device__ __forceinline__ float sw32_max(float a) {
  auto r = __builtin_amdgcn_permlane32_swap(__builtin_bit_cast(unsigned, a), __builtin_bit_cast(unsigned, a), false, false);
  return vmax(r[0], r[1]);
}
__device__ __forceinline__ float sw16_max(float a) {
  auto r = __builtin_amdgcn_permlane16_swap(__builtin_bit_cast(unsigned, a), __builtin_bit_cast(unsigned, a), false, false);
  return vmax(r[0], r[1]);
}
__device__ __forceinline__ float wave_sum(float v) {
  v += dpp<ROR8>(v);
  v += dpp<ROR4>(v);
  v += dpp<ROR2>(v);
  v += dpp<ROR1>(v);
  v = sw16_add(v, v);
  return sw32_add(v, v);
}
__device__ __forceinline__ v2u trrd(LAS char* p) {
  return __builtin_bit_cast(v2u, __builtin_amdgcn_ds_read_tr16_b64_v4i16((LAS s4*)p));
}
__device__ __forceinline__ v4u trrd2(LAS char* p0, LAS char* p1) {
  v2u a = trrd(p0), b = trrd(p1);
  return v4u{a.x, a.y, b.x, b.y};
}
#define BAR() asm volatile("s_waitcnt lgkmcnt(0)\n\ts_barrier" ::: "memory")
#define VMWAIT(k) asm volatile("s_waitcnt vmcnt(%0)" ::"n"(k) : "memory")
__device__ __forceinline__ void gld16(v4u& d, const void* base, unsigned off) {
  asm volatile("global_load_dwordx4 %0, %1, %2" : "=v"(d) : "v"(off), "s"(base) : "memory");
}
__device__ __forceinline__ void gld4_sc1(unsigned& d, const void* base, unsigned off) {
  asm volatile("global_load_dword %0, %1, %2 sc1" : "=v"(d) : "v"(off), "s"(base) : "memory");
}
template <int NT = 0> __device__ __forceinline__ void touch(const void* base, unsigned off, unsigned lds) {
  if (NT) asm volatile("s_mov_b32 m0, %2\n\ts_nop 0\n\tglobal_load_lds_dword %0, %1 nt" ::"v"(off), "s"(base), "s"(lds) : "memory");
  else asm volatile("s_mov_b32 m0, %2\n\ts_nop 0\n\tglobal_load_lds_dword %0, %1" ::"v"(off), "s"(base), "s"(lds) : "memory");
}
__device__ __forceinline__ void touch_sc1(const void* base, unsigned off, unsigned lds) {
  asm volatile("s_mov_b32 m0, %2\n\ts_nop 0\n\tglobal_load_lds_dword %0, %1 sc1" ::"v"(off), "s"(base), "s"(lds) : "memory");
}
#define LD16(i, a, o) "global_load_dwordx4 %" #i ", %" #a ", %" #o
#define SWEEP_4(q, o, o0, o1, o2, o3, W)                                                                                     \
  asm volatile(LD16(0, 4, 5) " offset:" #o0 " sc1\n\t" LD16(1, 4, 5) " offset:" #o1 " sc1\n\t" LD16(2, 4, 5) " offset:" #o2         \
               " sc1\n\t" LD16(3, 4, 5) " offset:" #o3 " sc1" W                                                               \
               : "=&v"((q)[0]), "=&v"((q)[1]), "=&v"((q)[2]), "=&v"((q)[3])                                                  \
               : "v"(o), "s"(ws)                                                                                             \
               : "memory")
#define RESWEEP_4(q, o, o0, o1, o2, o3)                                                                                      \
  asm volatile(LD16(0, 4, 5) " offset:" #o0 " sc1\n\t" LD16(1, 4, 5) " offset:" #o1 " sc1\n\t" LD16(2, 4, 5) " offset:" #o2         \
               " sc1\n\t" LD16(3, 4, 5) " offset:" #o3 " sc1\n\ts_waitcnt vmcnt(0)"                                            \
               : "+v"((q)[0]), "+v"((q)[1]), "+v"((q)[2]), "+v"((q)[3])                                                      \
               : "v"(o), "s"(ws)                                                                                             \
               : "memory")
#define RELOAD(q, o, off) asm volatile(LD16(0, 1, 2) " offset:" #off " sc1" : "+v"(q) : "v"(o), "s"(ws) : "memory")
#define PIN4(q) asm volatile("s_waitcnt vmcnt(0)" : "+v"((q)[0]), "+v"((q)[1]), "+v"((q)[2]), "+v"((q)[3])::"memory")
#define TAG_BAD(q) __builtin_amdgcn_ballot_w64((T & (q).y & (q).w) != (T | (q).y | (q).w))
#define HOLE(q) __builtin_amdgcn_ballot_w64(max(max((q).x, (q).y), max((q).z, (q).w)) == ~0u)
#define SWEEP_L(W)                                                                                                          \
  asm volatile("global_load_dwordx2 %0, %3, %5 sc1\n\tglobal_load_dwordx2 %1, %4, %5 sc1\n\t"                              \
               "global_load_dwordx2 %2, %4, %5 offset:512 sc1" W                                                            \
               : "=&v"(la), "=&v"(l0), "=&v"(l1)                                                                            \
               : "v"(ola), "v"(olo), "s"(ws)                                                                                \
               : "memory")
#define SWEEP_2(p, q, oa, ob, W) \
  asm volatile(LD16(0, 2, 4) " sc1\n\t" LD16(1, 3, 4) " sc1" W : "=&v"(p), "=&v"(q) : "v"(oa), "v"(ob), "s"(ws) : "memory")
#define TAGS_BAD4(q) \
  __builtin_amdgcn_ballot_w64((T & (q)[0].y & (q)[0].w & (q)[1].y & (q)[1].w & (q)[2].y & (q)[2].w & (q)[3].y & (q)[3].w) != \
                              (T | (q)[0].y | (q)[0].w | (q)[1].y | (q)[1].w | (q)[2].y | (q)[2].w | (q)[3].y | (q)[3].w))
#define FLAG_SET(i) asm volatile("ds_write_b32 %0, %1" ::"v"(smb + L_FL + 4 * (i)), "v"(1u) : "memory")
#define FLAG_WAIT4(i)                                                                                     \
  for (unsigned spin_ = 0; spin_ < SPIN_CAP; ++spin_) {                                                   \
    v4u f_;                                                                                               \
    asm volatile("ds_read_b128 %0, %1\n\ts_waitcnt lgkmcnt(0)" : "=v"(f_) : "v"(smb + L_FL + 16 * (i)) : "memory"); \
    if (__builtin_amdgcn_readfirstlane(f_.x & f_.y & f_.z & f_.w)) break;                                 \
  }
#define HOLD_UNTIL(ticks) \
  while ((unsigned)__builtin_amdgcn_s_memrealtime() - (unsigned)tstart < (ticks)) __builtin_amdgcn_s_sleep(1)
#define MARK(i) asm volatile(".Lmla_e" #i ":")
#define UNDEF(x) asm volatile("" : "=v"(x))
#define EPOCH() asm volatile("v_add_u32 %0, 1, %1" : "=v"(T) : "v"(ep) : "memory")
#define WAIT0 "\n\ts_waitcnt vmcnt(0)"
template <int NT = 0> __device__ __forceinline__ void dma16(const void* base, unsigned off, unsigned lds) {
  if (NT) asm volatile("s_mov_b32 m0, %2\n\ts_nop 0\n\tglobal_load_lds_dwordx4 %0, %1 nt" ::"v"(off), "s"(base), "s"(lds) : "memory");
  else asm volatile("s_mov_b32 m0, %2\n\ts_nop 0\n\tglobal_load_lds_dwordx4 %0, %1" ::"v"(off), "s"(base), "s"(lds) : "memory");
}
__device__ __forceinline__ v4u ld_sc1(__amdgpu_buffer_rsrc_t rs, int off) {
  return __builtin_amdgcn_raw_buffer_load_b128(rs, off, 0, AUX_SC1);
}
__device__ __forceinline__ v2u ld8_sc1(__amdgpu_buffer_rsrc_t rs, int off) {
  return __builtin_amdgcn_raw_buffer_load_b64(rs, off, 0, AUX_SC1);
}
__device__ __forceinline__ void st_sc1(__amdgpu_buffer_rsrc_t rs, int off, v4u v) {
  __builtin_amdgcn_raw_buffer_store_b128(v, rs, off, 0, AUX_SC1);
}
__device__ __forceinline__ void ln_stats(v4u r, float& mean, float& rstd) {
  float e[8] = {blo(r.x), bhi(r.x), blo(r.y), bhi(r.y), blo(r.z), bhi(r.z), blo(r.w), bhi(r.w)};
  float s = ((e[0] + e[1]) + (e[2] + e[3])) + ((e[4] + e[5]) + (e[6] + e[7]));
  mean = wave_sum(s) * (1.f / DM);
  float q = 0.f;
#pragma unroll
  for (int i = 0; i < 8; ++i) {
    float d = e[i] - mean;
    q = fmaf(d, d, q);
  }
  rstd = __builtin_amdgcn_rsqf(wave_sum(q) * (1.f / DM) + 1e-5f);
}
}

__global__ void __launch_bounds__(512, 2)
mla_glu_decode(const u16* __restrict__ x, const u16* __restrict__ ckv, const u16* __restrict__ wq, const u16* __restrict__ wuk,
               const u16* __restrict__ wuv, const u16* __restrict__ wo, const u16* __restrict__ wg, const u16* __restrict__ wu,
               const u16* __restrict__ wd, u16* __restrict__ out, char* __restrict__ ws) {
  const unsigned long long tstart = __builtin_amdgcn_s_memrealtime();
  __builtin_amdgcn_sched_barrier(0);
  __shared__ __attribute__((aligned(16))) char smem_[L_END];
  LAS char* sm = (LAS char*)smem_;
  const unsigned smb = (unsigned)(size_t)sm;
  const int tid = threadIdx.x, l = tid & 63, w = __builtin_amdgcn_readfirstlane(tid >> 6);
  const int col = l & 15, g4 = l >> 4, tq = (l & 15) >> 2, tp = l & 3;
  const int xcd = blockIdx.x & 7, r = blockIdx.x >> 3;
  const __amdgpu_buffer_rsrc_t rs = __builtin_amdgcn_make_buffer_rsrc(ws, 0, WS_BYTES, 0x00020000);
  int bw = (blockIdx.x >> 7) * w;
  asm volatile("" : "+s"(bw));
  if (bw >= 5) {
    const char* pc0;
    unsigned len;
    asm volatile("s_getpc_b64 %0\n.Lmla_pc:\n\ts_mov_b32 %1, .Lmla_e0-.Lmla_pc\n\ts_max_i32 %1, %1, .Lmla_e1-.Lmla_pc\n\t"
                 "s_max_i32 %1, %1, .Lmla_e2-.Lmla_pc\n\ts_max_i32 %1, %1, .Lmla_e3-.Lmla_pc\n\ts_max_i32 %1, %1, .Lmla_e4-.Lmla_pc\n\t"
                 "s_max_i32 %1, %1, .Lmla_e5-.Lmla_pc"
                 : "=s"(pc0), "=s"(len)::"scc");
    touch(pc0, min((unsigned)(64 * (bw - 5) + l) * 128u, len), smb + L_JUNK);
  }
  __builtin_amdgcn_sched_barrier(0);
  asm volatile("" ::"s"(x), "s"(ckv), "s"(wq), "s"(wuk), "s"(wuv), "s"(wo), "s"(wg), "s"(wu), "s"(wd), "s"(out), "s"(ws));
  unsigned tsv = 0u;
  STAMP(0);
  unsigned Tg = 0;

  unsigned ep, T;
  LAS char* const px = sm + L_X + (col & 1) * XP + 16 * g4;
  if ((w >> 1) == 2) dma16(x, (unsigned)((w & 1) * 1024 + l * 16), smb + L_X + (w & 1) * XP);
  const f4 z = {0.f, 0.f, 0.f, 0.f};
#define LAYERNORM()                                                                                                              \
  {                                                                                                                              \
    const v4u xr = *(LAS v4u*)(sm + L_X + (w & 1) * XP + l * 16);                                                                \
    float e[8] = {blo(xr.x), bhi(xr.x), blo(xr.y), bhi(xr.y), blo(xr.z), bhi(xr.z), blo(xr.w), bhi(xr.w)};                       \
    const float mean = wave_sum(((e[0] + e[1]) + (e[2] + e[3])) + ((e[4] + e[5]) + (e[6] + e[7]))) * (1.f / DM);                 \
    float q = 0.f;                                                                                                               \
    _Pragma("unroll") for (int i = 0; i < 8; ++i) {                                                                              \
      e[i] -= mean;                                                                                                              \
      q = fmaf(e[i], e[i], q);                                                                                                   \
    }                                                                                                                            \
    const float rstd = __builtin_amdgcn_rsqf(wave_sum(q) * (1.f / DM) + 1e-5f);                                                  \
    *(LAS v4u*)(sm + L_X + (w & 1) * XP + l * 16) =                                                                              \
        v4u{pk(e[0] * rstd, e[1] * rstd), pk(e[2] * rstd, e[3] * rstd), pk(e[4] * rstd, e[5] * rstd), pk(e[6] * rstd, e[7] * rstd)}; \
  }

  if (__builtin_expect(r >= 16, 0)) {
    const int a = r - 16, b = xcd >> 2, n = 8 * (xcd & 3) + (a >> 1), g = a & 1;
    const int sl = 12 * xcd + a, rd = (4 * xcd + a) & 31;
    const bool glu = a < 12 && sl < NSL;
    const int v = w & 3;
    unsigned op = WS_PART + ((((16 * g + (col & 7)) * 2 + (v >> 1)) * 2 + b) * 32 + n) * 512 + 256 * (v & 1) + (col >> 3) * 64 +
                  (g4 & 1) * 32 + (g4 >> 1) * 16;
    gld4_sc1(ep, ws, 0u);
    unsigned o = WS_QA + (b * NH + 16 * g) * 2048 + w * 4096 + l * 16, pqs = smb + L_Q + 2 * w * CP + l * 8;
    PIN(o); PIN(pqs);
    {
    {
      const unsigned vc = (unsigned)(((b * SK + 128 * n) * DC + 8 * l) * 2);
      STAMP(10);
      HOLD_UNTIL(C_START);
    if (w >= 4) {
#pragma unroll
      for (int q = 0; q < 4; ++q) st_sc1(rs, op + 128 * (q >> 1) + (q & 1) * 8 * 65536, v4u{~0u, ~0u, ~0u, ~0u});
      if (w == 7 && l < 32) *(LAS unsigned*)(sm + L_FL + 4 * l) = 0u;
    }
      HOLD_UNTIL(A_START);
#pragma unroll 1
      for (int q = 0; q < 4; ++q) {
        int row = w + ((64 * g + 32 * q) & 127);
        asm volatile("s_barrier" ::: "memory");
#pragma unroll 1
        for (int i = 0; i < 4; ++i, row += 8) dma16<NT_CKV>(ckv, vc + row * 1024u, smb + L_C + row * CP);
      }
      if ((w >> 1) == 2) {
        VMWAIT(21);
        LAYERNORM();
      }
      VMWAIT(0);
      EPOCH();
      PROBE_T(T);
      BAR();
      STAMP(1);
    }
#define QISSUE()                                \
  {                                   \
    SWEEP_4(qg, o, 0, 1024, 2048, 3072, "");    \
  }
#define QRETRY()                              \
  if (TAG_BAD(qg[0])) RELOAD(qg[0], o, 0);    \
  if (TAG_BAD(qg[1])) RELOAD(qg[1], o, 1024); \
  if (TAG_BAD(qg[2])) RELOAD(qg[2], o, 2048); \
  if (TAG_BAD(qg[3])) RELOAD(qg[3], o, 3072); \
  PIN4(qg)
#define QGATHER()                                                                                                           \
  VMWAIT(0);                                                                                                      \
  asm volatile("" : "+v"(qg[0]), "+v"(qg[1]), "+v"(qg[2]), "+v"(qg[3])::"memory");                                          \
  for (unsigned spin = 0;; ++spin) {                                                                                        \
    if (!TAGS_BAD4(qg) || spin > SPIN_CAP) break;                                                                           \
    QRETRY();                                                                                                               \
  }                                                                                                                         \
  STAMP(3);                                                                                                                 \
  asm volatile("ds_write2_b32 %0, %2, %3 offset0:0 offset1:1\n\tds_write2_b32 %0, %4, %5 offset0:128 offset1:129\n\t"        \
               "ds_write2_b32 %1, %6, %7 offset0:0 offset1:1\n\tds_write2_b32 %1, %8, %9 offset0:128 offset1:129" ::"v"(pqs), \
               "v"(pqs + CP), "v"(qg[0].x), "v"(qg[0].z), "v"(qg[1].x), "v"(qg[1].z), "v"(qg[2].x), "v"(qg[2].z), "v"(qg[3].x), \
               "v"(qg[3].z)                                                                                                 \
               : "memory")
    if (__builtin_expect(w < 4, 1)) {
      v4u fr[32], qg[4];
      QISSUE();
      LAS char* pc = sm + L_C + (32 * w + col) * CP + 16 * g4;
#pragma unroll
      for (int kt = 0; kt < 2; ++kt)
#pragma unroll
        for (int s = 0; s < 16; ++s) fr[16 * kt + s] = *(LAS v4u*)(pc + 16 * kt * CP + 64 * s);
      unsigned p = L_Q + col * CP + 16 * g4, pp = L_P + w * 1024 + l * 16, pm = L_MX + col * 16 + w * 4;
      unsigned ol = WS_LSE + ((b * 32 + n) * 8 + 4 * g + (col >> 2)) * 128 + (col & 3) * 8;
      PIN(p); PIN(pp); PIN(pm); PIN(ol);
      STAMP(2);
      QGATHER();
      BAR();
      STAMP(4);
      f4 s0 = z, s1 = z;
      v4u qf[16];
#pragma unroll
      for (int s = 0; s < 6; ++s) qf[s] = *(LAS v4u*)(sm + p + 64 * s);
      __builtin_amdgcn_sched_barrier(0);
#pragma unroll
      for (int s = 0; s < 16; ++s) {
        if (s + 6 < 16) qf[s + 6] = *(LAS v4u*)(sm + p + 64 * (s + 6));
        s0 = mfma32(fr[s], qf[s], s0);
        s1 = mfma32(fr[16 + s], qf[s], s1);
        __builtin_amdgcn_sched_barrier(0);
      }
      float mx = fmax2(fmax2(fmax2(s0[0], s0[1]), fmax2(s0[2], s0[3])), fmax2(fmax2(s1[0], s1[1]), fmax2(s1[2], s1[3])));
      mx = sw32_max(sw16_max(mx));
      constexpr float SC = 0.08838834764831845f * 1.4426950408889634f;
      const float mi = __builtin_ceilf(mx * SC);
#pragma unroll
      for (int e = 0; e < 4; ++e) {
        s0[e] = __builtin_amdgcn_exp2f(fmaf(s0[e], SC, -mi));
        s1[e] = __builtin_amdgcn_exp2f(fmaf(s1[e], SC, -mi));
      }
      *(LAS float*)(sm + pm) = mi;
      *(LAS v4u*)(sm + pp) = v4u{pk(s0[0], s0[1]), pk(s0[2], s0[3]), pk(s1[0], s1[1]), pk(s1[2], s1[3])};
      BAR();
      STAMP(5);
      if (w == LSE_QK - 1) {
        const f4 m4 = *(LAS f4*)(sm + pm - 4 * w);
        v4u pf[4];
#pragma unroll
        for (int u = 0; u < 4; ++u) pf[u] = *(LAS v4u*)(sm + pp + (u - w) * 1024);
        __builtin_amdgcn_sched_barrier(0);
        const float M = vmax3(vmax3(m4[0], m4[1], m4[2]), m4[3], m4[3]);
        const v4u one = {0x3f803f80u, 0x3f803f80u, 0x3f803f80u, 0x3f803f80u};
        f4 ls = z;
#pragma unroll
        for (int u = 0; u < 4; ++u) {
          const unsigned kk = min((unsigned)(M - m4[u]), 255u) * 0x00800080u;
#pragma unroll
          for (int e = 0; e < 4; ++e) pf[u][e] = scale_dn(pf[u][e], kk);
          ls = mfma32(one, pf[u], ls);
        }
        if (g4 == 0)
          __builtin_amdgcn_raw_buffer_store_b64(v2u{__builtin_bit_cast(unsigned, M + __builtin_amdgcn_logf(ls[0])), T}, rs, ol, 0, AUX_SC1);
      }
    } else {
      v4u fr[32], qg[4];
      QISSUE();
      LAS char* p0 = sm + L_C + (4 * g4 + tq) * CP + 256 * v + 8 * tp;
      unsigned p1 = L_C + (64 + 4 * g4 + tq) * CP + 256 * v + 8 * tp;
      PIN(p1);
#pragma unroll
      for (int ct = 0; ct < 8; ++ct)
#pragma unroll
        for (int u = 0; u < 4; ++u) {
          LAS char* p = (u < 2 ? p0 : sm + p1) + 32 * (u & 1) * CP + 32 * ct;
          fr[4 * ct + u] = trrd2(p, p + 16 * CP);
        }
      unsigned ol = WS_LSE + ((b * 32 + n) * 8 + 4 * g + (col >> 2)) * 128 + (col & 3) * 8, pp = L_P + l * 16, pm = L_MX + col * 16;
      v4u one = {0x3f803f80u, 0x3f803f80u, 0x3f803f80u, 0x3f803f80u};
      unsigned op2 = op + 8 * 65536;
      PIN(ol); PIN(pp); PIN(pm); PIN(one); PIN(op); PIN(op2);
      STAMP(2);
      QGATHER();
      BAR();
      STAMP(4);
      if (v < G_TOUCH) {
        const int ln = 128 * a + 64 * (v >> 1) + l;
        touch((v & 1) ? wu : wg, (unsigned)((ln >> 2) * (FF * 2)) + (unsigned)min(384 * xcd + 128 * (ln & 3), FF * 2 - 4), smb + L_JUNK);
      }
      BAR();
      STAMP(5);
      const f4 m4 = *(LAS f4*)(sm + pm);
      v4u pf[4];
#pragma unroll
      for (int u = 0; u < 4; ++u) pf[u] = *(LAS v4u*)(sm + pp + u * 1024);
      __builtin_amdgcn_sched_barrier(0);
      const float M = vmax3(vmax3(m4[0], m4[1], m4[2]), m4[3], m4[3]);
      f4 ls = z, c[4] = {z, z, z, z};
#pragma unroll
      for (int u = 0; u < 4; ++u) {
        const unsigned kk = min((unsigned)(M - m4[u]), 255u) * 0x00800080u;
#pragma unroll
        for (int e = 0; e < 4; ++e) pf[u][e] = scale_dn(pf[u][e], kk);
        ls = mfma32(one, pf[u], ls);
        if (u < 3) {
#pragma unroll
          for (int k = 0; k < 4; ++k) c[k] = mfma32(fr[4 * k + u], pf[u], c[k]);
        }
      }
      __builtin_amdgcn_sched_barrier(0);
      c[0] = mfma32(fr[3], pf[3], c[0]);
      c[1] = mfma32(fr[7], pf[3], c[1]);
      __builtin_amdgcn_sched_barrier(0);
      float il = __builtin_amdgcn_rcpf(ls[0]);
      unsigned lse = __builtin_bit_cast(unsigned, M + __builtin_amdgcn_logf(ls[0]));
      c[2] = mfma32(fr[11], pf[3], c[2]);
      c[3] = mfma32(fr[15], pf[3], c[3]);
#define PV_OUT(c, q)                                                                                          \
  {                                                                                                           \
    v4u R[2];                                                                                                 \
    _Pragma("unroll") for (int k = 0; k < 2; ++k) {                                                           \
      const f4 c0 = c[2 * k] * il, c1 = c[2 * k + 1] * il;                                                    \
      const unsigned x0 = min(pk(c0[0], c0[1]), 0xfffffffeu), x1 = min(pk(c0[2], c0[3]), 0xfffffffeu);        \
      const unsigned y0 = min(pk(c1[0], c1[1]), 0xfffffffeu), y1 = min(pk(c1[2], c1[3]), 0xfffffffeu);        \
      auto r0 = __builtin_amdgcn_permlane16_swap(x0, y0, false, false);                                       \
      auto r1 = __builtin_amdgcn_permlane16_swap(x1, y1, false, false);                                       \
      R[k] = v4u{r0[0], r1[0], r0[1], r1[1]};                                                                 \
    }                                                                                                         \
    v4u A, B;                                                                                                 \
    _Pragma("unroll") for (int e = 0; e < 4; ++e) {                                                           \
      A[e] = (unsigned)__builtin_amdgcn_update_dpp((int)R[0][e], (int)R[1][e], ROR8, 0xf, 0xc, false);        \
      B[e] = (unsigned)__builtin_amdgcn_update_dpp((int)R[1][e], (int)R[0][e], ROR8, 0xf, 0x3, false);        \
    }                                                                                                         \
    st_sc1(rs, op + 128 * q, A);                                                                              \
    st_sc1(rs, op2 + 128 * q, B);                                                                             \
  }
      __builtin_amdgcn_sched_barrier(0);
      f4 d[4] = {z, z, z, z};
#pragma unroll
      for (int u = 0; u < 4; ++u)
#pragma unroll
        for (int k = 0; k < 4; ++k) d[k] = mfma32(fr[16 + 4 * k + u], pf[u], d[k]);
      PV_OUT(c, 0);
#pragma unroll
      for (int i = 0; i < 16; ++i) {
        __builtin_amdgcn_sched_group_barrier(0x8, 1, 0);
        __builtin_amdgcn_sched_group_barrier(0x2, PV_MIX, 0);
      }
      __builtin_amdgcn_sched_barrier(0);
      PV_OUT(d, 1);
      FLAG_SET(v);
      MARK(3);
      STAMP(6);
    }
    }
      if (glu && w < 4) {
        v4u af[8], xf[8];
#pragma unroll
        for (int s = 0; s < 8; ++s) xf[s] = *(LAS v4u*)(px + 512 * (w >> 1) + 64 * s);
        unsigned oog = WS_OPG + ((8 * w + (col >> 1)) * 128 + sl) * 128 + ((col & 1) * 4 + g4) * 16;
        PIN(oog);
        FLAG_WAIT4(0);
#pragma unroll 1
        for (int i = 0; i < 4; ++i) dma16<NT_G>(wd, (unsigned)(((16 * sl + 4 * w + i) * DM + 8 * l) * 2), smb + L_GD + (4 * w + i) * CP);
        unsigned og = (unsigned)(((128 * w + (l >> 1)) * FF + 16 * sl + 8 * (l & 1)) * 2);
#pragma unroll 1
        for (int i = 0; i < 4; ++i, og += 32 * FF * 2) {
          dma16(wg, og, smb + L_GG + (4 * w + i) * 1024);
          dma16(wu, og, smb + L_GG + 16384 + (4 * w + i) * 1024);
        }
        unsigned pg = L_GG + (w & 1) * 16384 + 32 * (256 * (w >> 1) + 8 * g4 + tq) + 8 * tp;
        unsigned pd = L_GD + (4 * g4 + tq) * CP + 256 * w + 8 * tp, pgw = L_GR + ((w & 1) * 2 + (w >> 1)) * 1024 + l * 16;
        PIN(pg); PIN(pd); PIN(pgw);
        VMWAIT(0);
        FLAG_SET(4 + w);
        FLAG_WAIT4(1);
        STAMP(7);
        f4 acc = z;
#pragma unroll
        for (int s = 0; s < 8; ++s) af[s] = trrd2(sm + pg + 32 * 32 * s, sm + pg + 32 * (32 * s + 4));
        __builtin_amdgcn_sched_barrier(0);
        v2u dn[8];
#pragma unroll
        for (int s = 0; s < 8; ++s) {
          acc = mfma32(af[s], xf[s], acc);
          dn[s] = trrd(sm + pd + 32 * s);
          __builtin_amdgcn_sched_barrier(0);
        }
        *(LAS f4*)(sm + pgw) = acc;
        FLAG_SET(8 + w);
        FLAG_WAIT4(2);
        LAS char* pr = sm + L_GR + l * 16;
        const f4 gx = *(LAS f4*)(pr) + *(LAS f4*)(pr + 1024), ux = *(LAS f4*)(pr + 2048) + *(LAS f4*)(pr + 3072);
        float hh[4];
#pragma unroll
        for (int e = 0; e < 4; ++e) hh[e] = ux[e] * __builtin_amdgcn_rcpf(1.f + __builtin_amdgcn_exp2f(-1.4426950408889634f * gx[e]));
        const v2u hb = {pk(hh[0], hh[1]), pk(hh[2], hh[3])};
        f4 d = z;
#pragma unroll
        for (int t = 0; t < 8; ++t) d = mfma16(dn[t], (col >> 1) == t ? hb : v2u{0u, 0u}, d);
        v4u dv = {pk(d[0], d[1]), T, pk(d[2], d[3]), T};
        PIN(dv);
        st_sc1(rs, oog, dv);
        MARK(4);
        STAMP(9);
      }
    if (a >= 12) {
      const unsigned m = (col & 1) ? 0x3f800000u : 0x3f80u;
      v4u sel = {col < 2 ? m : 0u, (col >> 1) == 1 ? m : 0u, col < 2 ? m : 0u, (col >> 1) == 1 ? m : 0u};
      PIN(sel);
      if (w == 0) {
        unsigned ot = WS_OPT + rd * 8192 + 4096 + l * 16, oy = (l >> 2) * 1024 + rd * 32 + (l & 3) * 8, pr = smb + L_RR + (l & 7) * 16;
        PIN(ot); PIN(oy); PIN(pr);
        v4u p[8];
        HOLD_UNTIL(R_START);
        SWEEP_4(p, ot, -4096, -3072, -2048, -1024, "");
        SWEEP_4(p + 4, ot, 0, 1024, 2048, 3072, "");
        PIN4(p);
        PIN4(p + 4);
        for (unsigned spin = 0;; ++spin) {
          if (!(TAGS_BAD4(p) | TAGS_BAD4(p + 4)) || spin > SPIN_CAP) break;
          if (TAG_BAD(p[0])) RELOAD(p[0], ot, -4096);
          if (TAG_BAD(p[1])) RELOAD(p[1], ot, -3072);
          if (TAG_BAD(p[2])) RELOAD(p[2], ot, -2048);
          if (TAG_BAD(p[3])) RELOAD(p[3], ot, -1024);
          if (TAG_BAD(p[4])) RELOAD(p[4], ot, 0);
          if (TAG_BAD(p[5])) RELOAD(p[5], ot, 1024);
          if (TAG_BAD(p[6])) RELOAD(p[6], ot, 2048);
          if (TAG_BAD(p[7])) RELOAD(p[7], ot, 3072);
          PIN4(p);
          PIN4(p + 4);
        }
        STAMP(8);
        f4 acc = z;
#pragma unroll
        for (int i = 0; i < 4; ++i) acc = mfma32(sel, v4u{p[2 * i].x, p[2 * i].z, p[2 * i + 1].x, p[2 * i + 1].z}, acc);
        f4 rest[6];
        for (unsigned spin = 0; spin < SPIN_CAP; ++spin) {
          v4u f0;
          v2u f1;
          asm volatile("ds_read_b128 %0, %8\n\tds_read_b64 %1, %8 offset:16\n\tds_read_b128 %2, %9\n\tds_read_b128 %3, %9 offset:128\n\t"
                       "ds_read_b128 %4, %9 offset:256\n\tds_read_b128 %5, %9 offset:384\n\tds_read_b128 %6, %9 offset:512\n\t"
                       "ds_read_b128 %7, %9 offset:640\n\ts_waitcnt lgkmcnt(0)"
                       : "=&v"(f0), "=&v"(f1), "=&v"(rest[0]), "=&v"(rest[1]), "=&v"(rest[2]), "=&v"(rest[3]), "=&v"(rest[4]), "=&v"(rest[5])
                       : "v"(smb + L_FL + 64), "v"(pr)
                       : "memory");
          if (__builtin_amdgcn_readfirstlane(f0.x & f0.y & f0.z & f0.w & f1.x & f1.y)) break;
        }
        const f4 gl = ((rest[0] + rest[1]) + (rest[2] + rest[3])) + (rest[4] + rest[5]);
#pragma unroll
        for (int e = 0; e < 4; ++e) acc[e] = (acc[e] + dpp<ROR8>(acc[e])) + gl[e];
        if (l < 8) *(v2u*)((char*)out + oy) = v2u{pk(acc[0], acc[1]), pk(acc[2], acc[3])};
        if (rd == 0 && l == 0) __builtin_amdgcn_raw_buffer_store_b32(T, rs, WS_E, 0, AUX_SC1);
      } else if (w < 7) {
        const bool last = w == 6;
        unsigned og = WS_OPG + rd * 16384 + (w - 1) * 2048 + (last ? min(l, 47) : l) * 16, og2 = last ? og : og + 1024;
        const v2u xr = *(const v2u*)((const char*)x + ((l >> 2) & 1) * 1024 + rd * 32 + (l & 3) * 8);
        unsigned m0 = (last && l >= 48) ? 0u : ~0u, m1 = last ? 0u : ~0u;
        f4 xv = {blo(xr.x), bhi(xr.x), blo(xr.y), bhi(xr.y)};
        if (!last) xv = z;
        PIN(og); PIN(og2); PIN(m0); PIN(m1); PIN(xv);
        v4u p0, p1;
        HOLD_UNTIL(R_START);
        for (unsigned spin = 0;; ++spin) {
          SWEEP_2(p0, p1, og, og2, WAIT0);
          if (!(TAG_BAD(p0) | TAG_BAD(p1)) || spin > SPIN_CAP) break;
        }
        STAMP(8);
        f4 acc = mfma32(sel, v4u{p0.x & m0, p0.z & m0, p1.x & m1, p1.z & m1}, z);
#pragma unroll
        for (int e = 0; e < 4; ++e) acc[e] = (acc[e] + dpp<ROR8>(acc[e])) + xv[e];
        if (l < 8) *(LAS f4*)(sm + L_RR + (w - 1) * 128 + l * 16) = acc;
        FLAG_SET(15 + w);
      }
      MARK(5);
      STAMP(9);
    }
    STAMP_FLUSH();
    __builtin_amdgcn_endpgm();
  }
  {
    const int h = 4 * xcd + (r >> 2), j = r & 3, ca = j >> 1, mb = j & 1;
    const int fl = tq | ((g4 & 1) << 2);
    gld4_sc1(ep, ws, 0u);
    if (__builtin_expect(w < 4, 1)) {
      const unsigned vq = (unsigned)(((128 * j + g4) * 4096 + h * DH + 8 * (col ^ ((g4 | ((w & 2) << 1)) << 1))) * 2);
#pragma unroll 1
      for (int k = w; k < 32; k += 4) dma16<NT_WQ>(wq, vq + (unsigned)k * 32768u, smb + L_WQ + k * 1024);
    } else if (w < 6) {
      VMWAIT(1);
      LAYERNORM();
      MARK(0);
    } else {
      __builtin_amdgcn_s_sleep(UK_DELAY);
      const unsigned vu = (unsigned)((((128 * j + g4) * NH + h) * DH) * 2);
#pragma unroll 1
      for (int k = w - 6; k < 32; k += 2) dma16<NT_WUK>(wuk, vu + (unsigned)k * 32768u + 16u * (col ^ ((4 * k + g4) & 15)), smb + L_WUK + k * 1024);
      MARK(1);
    }
    unsigned p = L_WQ + 256 * (8 * g4 + tq) + 16 * ((2 * w + (tp >> 1)) ^ (fl << 1)) + 8 * (tp & 1);
    unsigned oqp = WS_QP + ((h * 4 + j) * 8 + w) * 128 + col * 64 + g4 * 16;
    unsigned pxj = L_X + (col & 1) * XP + 16 * g4 + 256 * j;
    unsigned o = WS_QP + (h * 4 + (w & 3)) * 1024 + l * 16;
    unsigned pqw = smb + L_CC + (2 * (w & 3) + ((l >> 2) & 1)) * SP + (l >> 3) * 32 + (l & 3) * 8, pqr = L_CC + (col & 7) * SP + 16 * g4;
    unsigned oqa = WS_QA + ((col & 1) * NH + h) * 2048 + (64 * j + 16 * w + 8 * (col >> 1) + 2 * g4) * 8;
    unsigned pu = L_WUK + 256 * (32 * w + col) + 16 * (g4 ^ col);
    PIN(p); PIN(oqp); PIN(pxj); PIN(o); PIN(oqa); PIN(pu); PIN(pqw); PIN(pqr);
    {
    {
      if (w < 6) VMWAIT(0);
      else VMWAIT(16);
      EPOCH();
      STAMP(1);
      BAR();
      STAMP(2);
    }
    {
      v4u af[4], xf[4];
#pragma unroll
      for (int s = 0; s < 4; ++s) {
        af[s] = trrd2(sm + p + 256 * 32 * s, sm + p + 256 * (32 * s + 4));
        xf[s] = *(LAS v4u*)(sm + pxj + 64 * s);
      }
      __builtin_amdgcn_sched_barrier(0);
      f4 acc = z;
#pragma unroll
      for (int s = 0; s < 4; ++s) acc = mfma32(af[s], xf[s], acc);
      if (col < 2) st_sc1(rs, oqp, v4u{pk(acc[0], acc[1]), T, pk(acc[2], acc[3]), T});
    }
    STAMP(3);
    if (w >= 6) VMWAIT(1);
    if (w >= 4) {
      BAR();
      BAR();
    } else {
      v4u a, uk[8], qf[4];
      {
        __builtin_amdgcn_s_sleep(H1_DELAY);
        asm volatile(LD16(0, 1, 2) " sc1" : "=&v"(a) : "v"(o), "s"(ws) : "memory");
        BAR();
      }
#pragma unroll
      for (int s = 0; s < 4; ++s) {
        uk[s] = *(LAS v4u*)(sm + (pu ^ (64 * s)));
        uk[4 + s] = *(LAS v4u*)(sm + (pu ^ (64 * s)) + 4096);
      }
      VMWAIT(0);
      PIN(a);
      for (unsigned spin = 0;; ++spin) {
        if (!__builtin_amdgcn_ballot_w64((T & a.y & a.w) != (T | a.y | a.w)) || spin > SPIN_CAP) break;
        asm volatile(LD16(0, 1, 2) " sc1" WAIT0 : "+v"(a) : "v"(o), "s"(ws) : "memory");
      }
      asm volatile("ds_write2_b32 %0, %1, %2 offset0:0 offset1:1" ::"v"(pqw), "v"(a.x), "v"(a.z) : "memory");
      STAMP(4);
      BAR();
#pragma unroll
      for (int s = 0; s < 4; ++s) qf[s] = *(LAS v4u*)(sm + pqr + 64 * s);
      __builtin_amdgcn_sched_barrier(0);
      f4 a0 = z, a1 = z;
#pragma unroll
      for (int s = 0; s < 4; ++s) {
        a0 = mfma32(uk[s], qf[s], a0);
        a1 = mfma32(uk[4 + s], qf[s], a1);
      }
      float q[4];
#pragma unroll
      for (int e = 0; e < 4; ++e) {
        const float mine = (col & 2) ? a1[e] : a0[e], other = (col & 2) ? a0[e] : a1[e];
        q[e] = mine + dpp<QP_2301>(other);
        q[e] += dpp<ROR4>(q[e]);
      }
      if (col < 4) st_sc1(rs, oqa, v4u{pk(q[0], q[1]), T, pk(q[2], q[3]), T});
    }
    }
    STAMP(5);
    PROBE_T(T);
    BAR();
    HOLD_UNTIL(T_START);
    {
      const unsigned vv = (unsigned)((((256 * ca + g4) * NH + h) * DH + 8 * (col ^ ((g4 | ((w & 2) << 1)) << 1))) * 2);
      const unsigned vo = (unsigned)(((h * DH + (l >> 5)) * DM + 256 * mb) * 2) + 16u * ((l & 31) ^ (((l >> 5) | ((w & 1) << 1) | (w & 4)) << 1));
      int kv = w + 32 * mb, ko = w + 32 * ca;
#pragma unroll 1
      for (int hf = 0; hf < 2; ++hf) {
#pragma unroll 1
        for (int i = 0; i < 4; ++i, kv = (kv + 8) & 63) dma16<NT_T>(wuv, vv + (unsigned)kv * 32768u, smb + L_WUV + kv * 1024);
#pragma unroll 1
        for (int i = 0; i < 4; ++i, ko = (ko + 8) & 63) dma16<NT_T>(wo, vo + (unsigned)ko * 2048u, smb + L_WO + ko * 1024);
        if (hf == 0) asm volatile("s_barrier" ::: "memory");
      }
    }
    const int tb = w >> 2, nq = w & 3;
    o = WS_PART + (((h * 2 + ca) * 2 + tb) * 32 + 8 * nq + g4) * 512 + 16 * col;
    unsigned pw = L_W + (tb * 32 + 8 * nq + g4) * 4;
    unsigned pv = L_WUV + 256 * (8 * g4 + tq) + 16 * ((2 * w + (tp >> 1)) ^ (fl << 1)) + 8 * (tp & 1);
    unsigned po = L_WO + 512 * (8 * g4 + tq) + 16 * ((4 * w + (tp >> 1)) ^ (fl << 1)) + 8 * (tp & 1);
    unsigned pcw = L_CC + (nq * 2 + tb) * SP2 + 16 * col + 8 * g4, pcr = L_CC + (col & 7) * SP2 + 16 * g4;
    unsigned ptw = L_CT + col * SP + (16 * w + 4 * g4) * 2, ptr = L_CT + (col & 1) * SP + 16 * g4;
    unsigned oo = WS_OPT + ((16 * mb + 2 * w + (col >> 1)) * 64 + 2 * h + ca) * 128 + ((col & 1) * 4 + g4) * 16;
    v4u mk;
#pragma unroll
    for (int d = 0; d < 4; ++d) mk[d] = (col >> 1) == d ? ((col & 1) ? 0xffff0000u : 0xffffu) : 0u;
    PIN(o); PIN(pw); PIN(pv); PIN(po); PIN(pcw); PIN(pcr); PIN(ptw); PIN(ptr); PIN(oo); PIN(mk);
    STAMP(11);
        if (w == 7) {
      const unsigned ol = WS_LSE + (l * 8 + (h >> 2)) * 128 + (h & 3) * 8;
      v2u la;
      {
        unsigned spin = SPIN_CAP;
#define LSE_STEP(lo, hi)                                                                                     \
  "s_waitcnt vmcnt(2)\n\tv_cmp_ne_u32 vcc, v" #hi ", %4\n\tv_mov_b32 %0, v" #lo "\n\ts_cbranch_vccz 2f\n\t" \
  "s_sleep %5\n\tglobal_load_dwordx2 v[" #lo ":" #hi "], %2, %3 sc1\n\t"
        asm volatile("global_load_dwordx2 v[200:201], %2, %3 sc1\n\ts_waitcnt vmcnt(1)\n\t"
                     "global_load_dwordx2 v[202:203], %2, %3 sc1\n\ts_sleep %5\n\t"
                     "global_load_dwordx2 v[204:205], %2, %3 sc1\n"
                     "1:\n\t" LSE_STEP(200, 201) LSE_STEP(202, 203) LSE_STEP(204, 205)
                     "s_sub_u32 %1, %1, 1\n\ts_cbranch_scc0 1b\n"
                     "2:"
                     : "=&v"(la.x), "+s"(spin)
                     : "v"(ol), "s"(ws), "v"(T), "n"(LSE_PIPE)
                     : "memory", "vcc", "scc", "v200", "v201", "v202", "v203", "v204", "v205");
      }
      float M = asf(la.x);
      M = fmax2(M, dpp<ROR8>(M));
      M = fmax2(M, dpp<ROR4>(M));
      M = fmax2(M, dpp<ROR2>(M));
      M = fmax2(M, dpp<ROR1>(M));
      M = sw16_max(M);
      const float e = __builtin_amdgcn_exp2f(asf(la.x) - M);
      float L = e + dpp<ROR8>(e);
      L += dpp<ROR4>(L);
      L += dpp<ROR2>(L);
      L += dpp<ROR1>(L);
      L = sw16_add(L, L);
      const float wt = e * __builtin_amdgcn_rcpf(L);
      *(LAS unsigned*)(sm + L_W + 4 * l) = pk(wt, wt);
      STAMP(7);
    }
    v4u a[4];
    {
      if (LSE_PIPE && w == 7) VMWAIT(2);
      else VMWAIT(0);
      STAMP(6);
      BAR();
      SWEEP_4(a, o, 0, 256, 2048, 2304, "");
    }
    v4u A0 = mk & *(LAS unsigned*)(sm + pw), A1 = mk & *(LAS unsigned*)(sm + pw + 16);
    v4u av[8], ao[8];
#pragma unroll
    for (int s = 0; s < 8; ++s) av[s] = trrd2(sm + pv + 256 * 32 * s, sm + pv + 256 * (32 * s + 4));
#pragma unroll
    for (int t = 0; t < 2; ++t)
#pragma unroll
      for (int s = 0; s < 4; ++s) ao[4 * t + s] = trrd2(sm + (po ^ (32 * t)) + 512 * 32 * s, sm + (po ^ (32 * t)) + 512 * (32 * s + 4));
    VMWAIT(0);
    asm volatile("" : "+v"(a[0]), "+v"(a[1]), "+v"(a[2]), "+v"(a[3]), "+v"(A0), "+v"(A1)::"memory");
    for (unsigned spin = 0;; ++spin) {
      unsigned mx = 0u;
#pragma unroll
      for (int i = 0; i < 4; ++i) mx = max(max(mx, max(a[i].x, a[i].y)), max(a[i].z, a[i].w));
      if (!__builtin_amdgcn_ballot_w64(mx == ~0u) || spin > SPIN_CAP) break;
      if (HOLE(a[0])) RELOAD(a[0], o, 0);
      if (HOLE(a[1])) RELOAD(a[1], o, 256);
      if (HOLE(a[2])) RELOAD(a[2], o, 2048);
      if (HOLE(a[3])) RELOAD(a[3], o, 2304);
      PIN4(a);
    }
    STAMP(8);
    {
      f4 c0 = mfma32(A0, a[0], z), c1 = mfma32(A0, a[1], z);
      c0 = mfma32(A1, a[2], c0);
      c1 = mfma32(A1, a[3], c1);
      if (g4 < 2) {
        *(LAS v2u*)(sm + pcw) = v2u{pk(c0[0], c0[1]), pk(c0[2], c0[3])};
        *(LAS v2u*)(sm + pcw + 256) = v2u{pk(c1[0], c1[1]), pk(c1[2], c1[3])};
      }
    }
    BAR();
    {
      f4 acc = z;
      v4u bf[8];
#pragma unroll
      for (int s = 0; s < 8; ++s) bf[s] = *(LAS v4u*)(sm + pcr + 64 * s);
      __builtin_amdgcn_sched_barrier(0);
#pragma unroll
      for (int s = 0; s < 8; ++s) acc = mfma32(av[s], bf[s], acc);
#pragma unroll
      for (int e = 0; e < 4; ++e) {
        acc[e] += dpp<QP_2301>(acc[e]);
        acc[e] += dpp<ROR4>(acc[e]);
      }
      if (col < 2) *(LAS v2u*)(sm + ptw) = v2u{pk(acc[0], acc[1]), pk(acc[2], acc[3])};
    }
    BAR();
    {
      f4 c0 = z, c1 = z;
      v4u bf[4];
#pragma unroll
      for (int s = 0; s < 4; ++s) bf[s] = *(LAS v4u*)(sm + ptr + 64 * s);
      __builtin_amdgcn_sched_barrier(0);
#pragma unroll
      for (int s = 0; s < 4; ++s) {
        c0 = mfma32(ao[s], bf[s], c0);
        c1 = mfma32(ao[4 + s], bf[s], c1);
      }
      if (col & 2) c0 = c1;
      if (col < 4) st_sc1(rs, oo, v4u{pk(c0[0], c0[1]), T, pk(c0[2], c0[3]), T});
    }
    MARK(2);
    STAMP(9);
  }
  STAMP_FLUSH();
}

#define WS_END 0
#define WS_BASE (WS_END ? ((ws_size - (size_t)WS_END * 1048576) & ~(size_t)4095) : 0)
extern "C" void kernel_launch(void* const* d_in, const int* in_sizes, int n_in, void* d_out, int out_size, void* d_ws, size_t ws_size,
                              hipStream_t stream) {
  mla_glu_decode<<<256, 512, 0, stream>>>((const u16*)d_in[0], (const u16*)d_in[1], (const u16*)d_in[2], (const u16*)d_in[3],
                                          (const u16*)d_in[4], (const u16*)d_in[5], (const u16*)d_in[7], (const u16*)d_in[8],
                                          (const u16*)d_in[9], (u16*)d_out, (char*)d_ws + WS_BASE);
}
```

```cpp
#define A_START 70
#define T_START 340
#include <hip/hip_runtime.h>
#include <stdint.h>

#define GAS __attribute__((address_space(1)))
#define LAS __attribute__((address_space(3)))
typedef unsigned short u16;
typedef unsigned v4u __attribute__((ext_vector_type(4)));
typedef unsigned v2u __attribute__((ext_vector_type(2)));
typedef float f4 __attribute__((ext_vector_type(4)));
typedef float f2 __attribute__((ext_vector_type(2)));
typedef __bf16 bf8 __attribute__((ext_vector_type(8)));
typedef __bf16 bf2 __attribute__((ext_vector_type(2)));
typedef short s4 __attribute__((ext_vector_type(4)));

#define ROT 1
namespace {
constexpr int NH = 32, SK = 4096, DH = 128, DM = 512, DC = 512, FF = 1376;
constexpr int NSL = FF / 16;
constexpr int WS_E = 0;
constexpr int WS_QP = 4096;
constexpr int WS_QA = WS_QP + NH * 4 * 1024;
constexpr int WS_LSE = WS_QA + 2 * NH * 2048;
constexpr int WS_GO = WS_LSE + 2 * 32 * 16 * 128;
constexpr int WS_OPT = WS_GO + 2 * 32 * 16 * 128;
constexpr int WS_OPG = WS_OPT + 32 * 64 * 128;
constexpr int OGP = 16384 + ROT * 384;
constexpr int WS_PART = (WS_OPG + 32 * OGP + 4095) / 4096 * 4096;
constexpr int WS_BYTES = WS_PART + NH * 2 * 2 * 32 * 512;
constexpr int WS_STAMP = 7 << 19;
constexpr int CP = 1056;
constexpr int XP = 1088, SP = 288, SP2 = 544;
constexpr int L_C = 0, L_Q = 128 * CP, L_P = L_Q + 16 * CP, L_X = L_P + 4096, L_MX = L_X + 2 * XP, L_RR = L_MX + 256,
              L_JUNK = L_RR + 1024, L_FL = L_JUNK + 256, L_END = L_FL + 128;
static_assert(L_END <= 163840, "LDS");
constexpr int L_GG = 0, L_GD = 32768, L_GR = 65536;
constexpr int L_WUV = 0, L_WO = 65536, L_WQ = 0, L_WUK = 32768, L_CC = 131072, L_CT = L_CC + 8 * SP2, L_W = L_CT + 2 * SP;
#define UK_DELAY 4
#define H1_DELAY 12
#define CODE_TOUCH 1
#define NT_CKV 0
#define NT_WQ 0
#define NT_WUK 0
#define NT_T 0
#define NT_G 0
#define GLU_EARLY 0
#define G_START 0
#define C_START 0
#define LSE_LATE 0
#define LSE_WAVE 3
#define DATA_DELAY 0
#define NEWRED 1
#define R_START 0
#define G_TOUCH 4
#define LSE_PIPE 0
#define PV_MIX 4
#define LSE_QK 1
#define HMAP 1
#define GQ 1
#define DOWN_FIRST 1
#define EARLY_SWEEP 0
#define GO_DELAY 0
#define PIECEWISE 1
#define PF_GLU 1
constexpr unsigned SPIN_CAP = 1u << 20;
constexpr int AUX_SC1 = 16;

#define STAMP_W 0
#define STAMP(i)
#define STAMP_FLUSH()
#define PROBE_T(T_)

__device__ __forceinline__ f4 mfma32(v4u a, v4u b, f4 c) {
  return __builtin_amdgcn_mfma_f32_16x16x32_bf16(__builtin_bit_cast(bf8, a), __builtin_bit_cast(bf8, b), c, 0, 0, 0);
}
__device__ __forceinline__ f4 mfma16(v2u a, v2u b, f4 c) {
  return __builtin_amdgcn_mfma_f32_16x16x16bf16_1k(__builtin_bit_cast(s4, a), __builtin_bit_cast(s4, b), c, 0, 0, 0);
}
__device__ __forceinline__ unsigned pk(float lo, float hi) {
  f2 x = {lo, hi};
  return __builtin_bit_cast(unsigned, __builtin_convertvector(x, bf2));
}
__device__ __forceinline__ float asf(unsigned u) { return __builtin_bit_cast(float, u); }
__device__ __forceinline__ float blo(unsigned u) { return __builtin_bit_cast(float, u << 16); }
__device__ __forceinline__ float bhi(unsigned u) { return __builtin_bit_cast(float, u & 0xffff0000u); }
template <int CTRL> __device__ __forceinline__ float dpp(float v) {
  return __builtin_bit_cast(float, __builtin_amdgcn_update_dpp(0, __builtin_bit_cast(int, v), CTRL, 0xf, 0xf, true));
}
__device__ __forceinline__ float fmax2(float a, float b) {
#pragma float_control(precise, off)
  return __builtin_fmaxf(a, b);
}
typedef unsigned short h2 __attribute__((ext_vector_type(2)));
__device__ __forceinline__ unsigned scale_dn(unsigned p, unsigned kk) {
  return __builtin_bit_cast(unsigned, __builtin_elementwise_sub_sat(__builtin_bit_cast(h2, p), __builtin_bit_cast(h2, kk)));
}
#define PIN(x) asm volatile("" : "+v"(x))
constexpr int ROR1 = 0x121, ROR2 = 0x122, ROR4 = 0x124, ROR8 = 0x128, QP_2301 = 0x4e, QP_2222 = 0xaa;
__device__ __forceinline__ float sw32_add(float a, float b) {
  auto r = __builtin_amdgcn_permlane32_swap(__builtin_bit_cast(unsigned, a), __builtin_bit_cast(unsigned, b), false, false);
  return asf(r[0]) + asf(r[1]);
}
__device__ __forceinline__ float sw16_add(float a, float b) {
  auto r = __builtin_amdgcn_permlane16_swap(__builtin_bit_cast(unsigned, a), __builtin_bit_cast(unsigned, b), false, false);
  return asf(r[0]) + asf(r[1]);
}
__device__ __forceinline__ float vmax(unsigned a, unsigned b) {
  float r;
  asm("v_max_f32 %0, %1, %2" : "=v"(r) : "v"(a), "v"(b));
  return r;
}
__device__ __forceinline__ float vmax3(float a, float b, float c) {
  float r;
  asm("v_max3_f32 %0, %1, %2, %3" : "=v"(r) : "v"(a), "v"(b), "v"(c));
  return r;
}
__device__ __forceinline__ float sw32_max(float a) {
  auto r = __builtin_amdgcn_permlane32_swap(__builtin_bit_cast(unsigned, a), __builtin_bit_cast(unsigned, a), false, false);
  return vmax(r[0], r[1]);
}
__device__ __forceinline__ float sw16_max(float a) {
  auto r = __builtin_amdgcn_permlane16_swap(__builtin_bit_cast(unsigned, a), __builtin_bit_cast(unsigned, a), false, false);
  return vmax(r[0], r[1]);
}
__device__ __forceinline__ float wave_sum(float v) {
  v += dpp<ROR8>(v);
  v += dpp<ROR4>(v);
  v += dpp<ROR2>(v);
  v += dpp<ROR1>(v);
  v = sw16_add(v, v);
  return sw32_add(v, v);
}
__device__ __forceinline__ v2u trrd(LAS char* p) {
  return __builtin_bit_cast(v2u, __builtin_amdgcn_ds_read_tr16_b64_v4i16((LAS s4*)p));
}
__device__ __forceinline__ v4u trrd2(LAS char* p0, LAS char* p1) {
  v2u a = trrd(p0), b = trrd(p1);
  return v4u{a.x, a.y, b.x, b.y};
}
#define BAR() asm volatile("s_waitcnt lgkmcnt(0)\n\ts_barrier" ::: "memory")
#define VMWAIT(k) asm volatile("s_waitcnt vmcnt(%0)" ::"n"(k) : "memory")
__device__ __forceinline__ void gld16(v4u& d, const void* base, unsigned off) {
  asm volatile("global_load_dwordx4 %0, %1, %2" : "=v"(d) : "v"(off), "s"(base) : "memory");
}
__device__ __forceinline__ void gld4_sc1(unsigned& d, const void* base, unsigned off) {
  asm volatile("global_load_dword %0, %1, %2 sc1" : "=v"(d) : "v"(off), "s"(base) : "memory");
}
template <int NT = 0> __device__ __forceinline__ void touch(const void* base, unsigned off, unsigned lds) {
  if (NT) asm volatile("s_mov_b32 m0, %2\n\ts_nop 0\n\tglobal_load_lds_dword %0, %1 nt" ::"v"(off), "s"(base), "s"(lds) : "memory");
  else asm volatile("s_mov_b32 m0, %2\n\ts_nop 0\n\tglobal_load_lds_dword %0, %1" ::"v"(off), "s"(base), "s"(lds) : "memory");
}
__device__ __forceinline__ void touch_sc1(const void* base, unsigned off, unsigned lds) {
  asm volatile("s_mov_b32 m0, %2\n\ts_nop 0\n\tglobal_load_lds_dword %0, %1 sc1" ::"v"(off), "s"(base), "s"(lds) : "memory");
}
#define LD16(i, a, o) "global_load_dwordx4 %" #i ", %" #a ", %" #o
#define SWEEP_4(q, o, o0, o1, o2, o3, W)                                                                                     \
  asm volatile(LD16(0, 4, 5) " offset:" #o0 " sc1\n\t" LD16(1, 4, 5) " offset:" #o1 " sc1\n\t" LD16(2, 4, 5) " offset:" #o2         \
               " sc1\n\t" LD16(3, 4, 5) " offset:" #o3 " sc1" W                                                               \
               : "=&v"((q)[0]), "=&v"((q)[1]), "=&v"((q)[2]), "=&v"((q)[3])                                                  \
               : "v"(o), "s"(ws)                                                                                             \
               : "memory")
#define RESWEEP_4(q, o, o0, o1, o2, o3)                                                                                      \
  asm volatile(LD16(0, 4, 5) " offset:" #o0 " sc1\n\t" LD16(1, 4, 5) " offset:" #o1 " sc1\n\t" LD16(2, 4, 5) " offset:" #o2         \
               " sc1\n\t" LD16(3, 4, 5) " offset:" #o3 " sc1\n\ts_waitcnt vmcnt(0)"                                            \
               : "+v"((q)[0]), "+v"((q)[1]), "+v"((q)[2]), "+v"((q)[3])                                                      \
               : "v"(o), "s"(ws)                                                                                             \
               : "memory")
#define RELOAD(q, o, off) asm volatile(LD16(0, 1, 2) " offset:" #off " sc1" : "+v"(q) : "v"(o), "s"(ws) : "memory")
#define PIN4(q) asm volatile("s_waitcnt vmcnt(0)" : "+v"((q)[0]), "+v"((q)[1]), "+v"((q)[2]), "+v"((q)[3])::"memory")
#define TAG_BAD(q) __builtin_amdgcn_ballot_w64((T & (q).y & (q).w) != (T | (q).y | (q).w))
#define HOLE(q) __builtin_amdgcn_ballot_w64(max(max((q).x, (q).y), max((q).z, (q).w)) == ~0u)
#define SWEEP_L(W)                                                                                                          \
  asm volatile("global_load_dwordx2 %0, %3, %5 sc1\n\tglobal_load_dwordx2 %1, %4, %5 sc1\n\t"                              \
               "global_load_dwordx2 %2, %4, %5 offset:512 sc1" W                                                            \
               : "=&v"(la), "=&v"(l0), "=&v"(l1)                                                                            \
               : "v"(ola), "v"(olo), "s"(ws)                                                                                \
               : "memory")
#define SWEEP_2(p, q, oa, ob, W) \
  asm volatile(LD16(0, 2, 4) " sc1\n\t" LD16(1, 3, 4) " sc1" W : "=&v"(p), "=&v"(q) : "v"(oa), "v"(ob), "s"(ws) : "memory")
#define TAGS_BAD4(q) \
  __builtin_amdgcn_ballot_w64((T & (q)[0].y & (q)[0].w & (q)[1].y & (q)[1].w & (q)[2].y & (q)[2].w & (q)[3].y & (q)[3].w) != \
                              (T | (q)[0].y | (q)[0].w | (q)[1].y | (q)[1].w | (q)[2].y | (q)[2].w | (q)[3].y | (q)[3].w))
#define FLAG_SET(i) asm volatile("ds_write_b32 %0, %1" ::"v"(smb + L_FL + 4 * (i)), "v"(1u) : "memory")
#define FLAG_WAIT4(i)                                                                                     \
  for (unsigned spin_ = 0; spin_ < SPIN_CAP; ++spin_) {                                                   \
    v4u f_;                                                                                               \
    asm volatile("ds_read_b128 %0, %1\n\ts_waitcnt lgkmcnt(0)" : "=v"(f_) : "v"(smb + L_FL + 16 * (i)) : "memory"); \
    if (__builtin_amdgcn_readfirstlane(f_.x & f_.y & f_.z & f_.w)) break;                                 \
  }
#define HOLD_UNTIL(ticks) \
  while ((unsigned)__builtin_amdgcn_s_memrealtime() - (unsigned)tstart < (ticks)) __builtin_amdgcn_s_sleep(1)
#define MARK(i) asm volatile(".Lmla_e" #i ":")
#define UNDEF(x) asm volatile("" : "=v"(x))
#define EPOCH() asm volatile("v_add_u32 %0, 1, %1" : "=v"(T) : "v"(ep) : "memory")
#define WAIT0 "\n\ts_waitcnt vmcnt(0)"
template <int NT = 0> __device__ __forceinline__ void dma16(const void* base, unsigned off, unsigned lds) {
  if (NT) asm volatile("s_mov_b32 m0, %2\n\ts_nop 0\n\tglobal_load_lds_dwordx4 %0, %1 nt" ::"v"(off), "s"(base), "s"(lds) : "memory");
  else asm volatile("s_mov_b32 m0, %2\n\ts_nop 0\n\tglobal_load_lds_dwordx4 %0, %1" ::"v"(off), "s"(base), "s"(lds) : "memory");
}
__device__ __forceinline__ v4u ld_sc1(__amdgpu_buffer_rsrc_t rs, int off) {
  return __builtin_amdgcn_raw_buffer_load_b128(rs, off, 0, AUX_SC1);
}
__device__ __forceinline__ v2u ld8_sc1(__amdgpu_buffer_rsrc_t rs, int off) {
  return __builtin_amdgcn_raw_buffer_load_b64(rs, off, 0, AUX_SC1);
}
__device__ __forceinline__ void st_sc1(__amdgpu_buffer_rsrc_t rs, int off, v4u v) {
  __builtin_amdgcn_raw_buffer_store_b128(v, rs, off, 0, AUX_SC1);
}
__device__ __forceinline__ void ln_stats(v4u r, float& mean, float& rstd) {
  float e[8] = {blo(r.x), bhi(r.x), blo(r.y), bhi(r.y), blo(r.z), bhi(r.z), blo(r.w), bhi(r.w)};
  float s = ((e[0] + e[1]) + (e[2] + e[3])) + ((e[4] + e[5]) + (e[6] + e[7]));
  mean = wave_sum(s) * (1.f / DM);
  float q = 0.f;
#pragma unroll
  for (int i = 0; i < 8; ++i) {
    float d = e[i] - mean;
    q = fmaf(d, d, q);
  }
  rstd = __builtin_amdgcn_rsqf(wave_sum(q) * (1.f / DM) + 1e-5f);
}
}

__global__ void __launch_bounds__(512, 2)
mla_glu_decode(const u16* __restrict__ x, const u16* __restrict__ ckv, const u16* __restrict__ wq, const u16* __restrict__ wuk,
               const u16* __restrict__ wuv, const u16* __restrict__ wo, const u16* __restrict__ wg, const u16* __restrict__ wu,
               const u16* __restrict__ wd, u16* __restrict__ out, char* __restrict__ ws) {
  const unsigned long long tstart = __builtin_amdgcn_s_memrealtime();
  __builtin_amdgcn_sched_barrier(0);
  __shared__ __attribute__((aligned(16))) char smem_[L_END];
  LAS char* sm = (LAS char*)smem_;
  const unsigned smb = (unsigned)(size_t)sm;
  const int tid = threadIdx.x, l = tid & 63, w = __builtin_amdgcn_readfirstlane(tid >> 6);
  const int col = l & 15, g4 = l >> 4, tq = (l & 15) >> 2, tp = l & 3;
  const int xcd = blockIdx.x & 7, r = blockIdx.x >> 3;
  const __amdgpu_buffer_rsrc_t rs = __builtin_amdgcn_make_buffer_rsrc(ws, 0, WS_BYTES, 0x00020000);
  int bw = (blockIdx.x >> 7) * w;
  asm volatile("" : "+s"(bw));
  if (bw >= 5) {
    const char* pc0;
    unsigned len;
    asm volatile("s_getpc_b64 %0\n.Lmla_pc:\n\ts_mov_b32 %1, .Lmla_e0-.Lmla_pc\n\ts_max_i32 %1, %1, .Lmla_e1-.Lmla_pc\n\t"
                 "s_max_i32 %1, %1, .Lmla_e2-.Lmla_pc\n\ts_max_i32 %1, %1, .Lmla_e3-.Lmla_pc\n\ts_max_i32 %1, %1, .Lmla_e4-.Lmla_pc\n\t"
                 "s_max_i32 %1, %1, .Lmla_e5-.Lmla_pc"
                 : "=s"(pc0), "=s"(len)::"scc");
    touch(pc0, min((unsigned)(64 * (bw - 5) + l) * 128u, len), smb + L_JUNK);
  }
  __builtin_amdgcn_sched_barrier(0);
  asm volatile("" ::"s"(x), "s"(ckv), "s"(wq), "s"(wuk), "s"(wuv), "s"(wo), "s"(wg), "s"(wu), "s"(wd), "s"(out), "s"(ws));
  STAMP(0);

  unsigned ep, T;
  LAS char* const px = sm + L_X + (col & 1) * XP + 16 * g4;
  if ((w >> 1) == 2) dma16(x, (unsigned)((w & 1) * 1024 + l * 16), smb + L_X + (w & 1) * XP);
  const f4 z = {0.f, 0.f, 0.f, 0.f};
#define LAYERNORM()                                                                                                              \
  {                                                                                                                              \
    const v4u xr = *(LAS v4u*)(sm + L_X + (w & 1) * XP + l * 16);                                                                \
    float e[8] = {blo(xr.x), bhi(xr.x), blo(xr.y), bhi(xr.y), blo(xr.z), bhi(xr.z), blo(xr.w), bhi(xr.w)};                       \
    const float mean = wave_sum(((e[0] + e[1]) + (e[2] + e[3])) + ((e[4] + e[5]) + (e[6] + e[7]))) * (1.f / DM);                 \
    float q = 0.f;                                                                                                               \
    _Pragma("unroll") for (int i = 0; i < 8; ++i) {                                                                              \
      e[i] -= mean;                                                                                                              \
      q = fmaf(e[i], e[i], q);                                                                                                   \
    }                                                                                                                            \
    const float rstd = __builtin_amdgcn_rsqf(wave_sum(q) * (1.f / DM) + 1e-5f);                                                  \
    *(LAS v4u*)(sm + L_X + (w & 1) * XP + l * 16) =                                                                              \
        v4u{pk(e[0] * rstd, e[1] * rstd), pk(e[2] * rstd, e[3] * rstd), pk(e[4] * rstd, e[5] * rstd), pk(e[6] * rstd, e[7] * rstd)}; \
  }

  if (__builtin_expect(r >= 16, 0)) {
    const int a = r - 16, b = xcd >> 2, n = 8 * (xcd & 3) + (a >> 1), g = a & 1;
    const int sl = 12 * xcd + a, rd = (4 * xcd + a) & 31;
    const bool glu = a < 12 && sl < NSL;
    const int v = w & 3;
    unsigned op = WS_PART + ((((16 * g + (col & 7)) * 2 + (v >> 1)) * 2 + b) * 32 + ((n + ROT * 4 * (col & 7)) & 31)) * 512 + 256 * (v & 1) +
                  (col >> 3) * 64 + (g4 & 1) * 32 + (g4 >> 1) * 16;
    gld4_sc1(ep, ws, 0u);
    unsigned o = WS_QA + (b * NH + 16 * g) * 2048 + w * 4096 + l * 16, pqs = smb + L_Q + 2 * w * CP + l * 8;
    PIN(o); PIN(pqs);
    {
    {
      const unsigned vc = (unsigned)(((b * SK + 128 * n) * DC + 8 * l) * 2);
      STAMP(10);
      HOLD_UNTIL(C_START);
    if (w >= 4) {
#pragma unroll
      for (int q = 0; q < 4; ++q) st_sc1(rs, op + 128 * (q >> 1) + (q & 1) * 8 * 65536, v4u{~0u, ~0u, ~0u, ~0u});
      if (w == 7 && l < 32) *(LAS unsigned*)(sm + L_FL + 4 * l) = 0u;
    }
      HOLD_UNTIL(A_START);
#pragma unroll 1
      for (int q = 0; q < 4; ++q) {
        int row = w + ((64 * g + 32 * q) & 127);
        asm volatile("s_barrier" ::: "memory");
#pragma unroll 1
        for (int i = 0; i < 4; ++i, row += 8) dma16<NT_CKV>(ckv, vc + row * 1024u, smb + L_C + row * CP);
      }
    }
#define CLAND()                          \
  if ((w >> 1) == 2) {                   \
    VMWAIT(21 + 4 * EARLY_SWEEP);        \
    LAYERNORM();                         \
  }                                      \
  VMWAIT(4 * EARLY_SWEEP);               \
  EPOCH();                               \
  PROBE_T(T);                            \
  BAR();                                 \
  STAMP(1)
#define QISSUE() \
  CLAND();       \
  SWEEP_4(qg, o, 0, 1024, 2048, 3072, "")
#define QRETRY()                              \
  if (TAG_BAD(qg[0])) RELOAD(qg[0], o, 0);    \
  if (TAG_BAD(qg[1])) RELOAD(qg[1], o, 1024); \
  if (TAG_BAD(qg[2])) RELOAD(qg[2], o, 2048); \
  if (TAG_BAD(qg[3])) RELOAD(qg[3], o, 3072); \
  PIN4(qg)
#define QGATHER()                                                                                                           \
  VMWAIT(0);                                                                                                      \
  asm volatile("" : "+v"(qg[0]), "+v"(qg[1]), "+v"(qg[2]), "+v"(qg[3])::"memory");                                          \
  for (unsigned spin = 0;; ++spin) {                                                                                        \
    if (!TAGS_BAD4(qg) || spin > SPIN_CAP) break;                                                                           \
    QRETRY();                                                                                                               \
  }                                                                                                                         \
  STAMP(3);                                                                                                                 \
  asm volatile("ds_write2_b32 %0, %2, %3 offset0:0 offset1:1\n\tds_write2_b32 %0, %4, %5 offset0:128 offset1:129\n\t"        \
               "ds_write2_b32 %1, %6, %7 offset0:0 offset1:1\n\tds_write2_b32 %1, %8, %9 offset0:128 offset1:129" ::"v"(pqs), \
               "v"(pqs + CP), "v"(qg[0].x), "v"(qg[0].z), "v"(qg[1].x), "v"(qg[1].z), "v"(qg[2].x), "v"(qg[2].z), "v"(qg[3].x), \
               "v"(qg[3].z)                                                                                                 \
               : "memory")
    if (__builtin_expect(w < 4, 1)) {
      v4u fr[32], qg[4];
      QISSUE();
      LAS char* pc = sm + L_C + (32 * w + col) * CP + 16 * g4;
#pragma unroll
      for (int kt = 0; kt < 2; ++kt)
#pragma unroll
        for (int s = 0; s < 16; ++s) fr[16 * kt + s] = *(LAS v4u*)(pc + 16 * kt * CP + 64 * s);
      unsigned p = L_Q + col * CP + 16 * g4, pp = L_P + w * 1024 + l * 16, pm = L_MX + col * 16 + w * 4;
      unsigned ol = WS_LSE + (HMAP ? (((b * 32 + n) * 2 + g) * 8 + ((col + ROT * n) & 7)) * 128 + (col >> 3) * 8 : ((b * 32 + n) * 8 + 4 * g + (col >> 2)) * 128 + (col & 3) * 8);
      PIN(p); PIN(pp); PIN(pm); PIN(ol);
      STAMP(2);
      QGATHER();
      BAR();
      STAMP(4);
      f4 s0 = z, s1 = z;
      v4u qf[16];
#pragma unroll
      for (int s = 0; s < 6; ++s) qf[s] = *(LAS v4u*)(sm + p + 64 * s);
      __builtin_amdgcn_sched_barrier(0);
#pragma unroll
      for (int s = 0; s < 16; ++s) {
        if (s + 6 < 16) qf[s + 6] = *(LAS v4u*)(sm + p + 64 * (s + 6));
        s0 = mfma32(fr[s], qf[s], s0);
        s1 = mfma32(fr[16 + s], qf[s], s1);
        __builtin_amdgcn_sched_barrier(0);
      }
      float mx = fmax2(fmax2(fmax2(s0[0], s0[1]), fmax2(s0[2], s0[3])), fmax2(fmax2(s1[0], s1[1]), fmax2(s1[2], s1[3])));
      mx = sw32_max(sw16_max(mx));
      constexpr float SC = 0.08838834764831845f * 1.4426950408889634f;
      const float mi = __builtin_ceilf(mx * SC);
#pragma unroll
      for (int e = 0; e < 4; ++e) {
        s0[e] = __builtin_amdgcn_exp2f(fmaf(s0[e], SC, -mi));
        s1[e] = __builtin_amdgcn_exp2f(fmaf(s1[e], SC, -mi));
      }
      *(LAS float*)(sm + pm) = mi;
      *(LAS v4u*)(sm + pp) = v4u{pk(s0[0], s0[1]), pk(s0[2], s0[3]), pk(s1[0], s1[1]), pk(s1[2], s1[3])};
      BAR();
      STAMP(5);
      if (w == LSE_QK - 1) {
        const f4 m4 = *(LAS f4*)(sm + pm - 4 * w);
        v4u pf[4];
#pragma unroll
        for (int u = 0; u < 4; ++u) pf[u] = *(LAS v4u*)(sm + pp + (u - w) * 1024);
        __builtin_amdgcn_sched_barrier(0);
        const float M = vmax3(vmax3(m4[0], m4[1], m4[2]), m4[3], m4[3]);
        const v4u one = {0x3f803f80u, 0x3f803f80u, 0x3f803f80u, 0x3f803f80u};
        f4 ls = z;
#pragma unroll
        for (int u = 0; u < 4; ++u) {
          const unsigned kk = min((unsigned)(M - m4[u]), 255u) * 0x00800080u;
#pragma unroll
          for (int e = 0; e < 4; ++e) pf[u][e] = scale_dn(pf[u][e], kk);
          ls = mfma32(one, pf[u], ls);
        }
        if (g4 == 0)
          __builtin_amdgcn_raw_buffer_store_b64(v2u{__builtin_bit_cast(unsigned, M + __builtin_amdgcn_logf(ls[0])), T}, rs, ol, 0, AUX_SC1);
      }
    } else {
      v4u fr[32], qg[4];
      QISSUE();
      LAS char* p0 = sm + L_C + (4 * g4 + tq) * CP + 256 * v + 8 * tp;
      unsigned p1 = L_C + (64 + 4 * g4 + tq) * CP + 256 * v + 8 * tp;
      PIN(p1);
#pragma unroll
      for (int ct = 0; ct < 8; ++ct)
#pragma unroll
        for (int u = 0; u < 4; ++u) {
          LAS char* p = (u < 2 ? p0 : sm + p1) + 32 * (u & 1) * CP + 32 * ct;
          fr[4 * ct + u] = trrd2(p, p + 16 * CP);
        }
      unsigned ol = WS_LSE + (HMAP ? (((b * 32 + n) * 2 + g) * 8 + ((col + ROT * n) & 7)) * 128 + (col >> 3) * 8 : ((b * 32 + n) * 8 + 4 * g + (col >> 2)) * 128 + (col & 3) * 8), pp = L_P + l * 16, pm = L_MX + col * 16;
      v4u one = {0x3f803f80u, 0x3f803f80u, 0x3f803f80u, 0x3f803f80u};
      unsigned op2 = op + 8 * 65536;
      PIN(ol); PIN(pp); PIN(pm); PIN(one); PIN(op); PIN(op2);
      STAMP(2);
      QGATHER();
      BAR();
      STAMP(4);
      if (GQ && glu) {
        const int k = 4 * (a & 3) + v;
        const unsigned og = (unsigned)(((32 * k + (l >> 1)) * FF + 16 * sl + 8 * (l & 1)) * 2);
        dma16(wg, og, smb + L_GG + k * 1024);
        dma16(wu, og, smb + L_GG + 16384 + k * 1024);
      } else if (v < G_TOUCH) {
        const int ln = 128 * a + 64 * (v >> 1) + l;
        touch((v & 1) ? wu : wg, (unsigned)((ln >> 2) * (FF * 2)) + (unsigned)min(384 * xcd + 128 * (ln & 3), FF * 2 - 4), smb + L_JUNK);
      }
      BAR();
      STAMP(5);
      const f4 m4 = *(LAS f4*)(sm + pm);
      v4u pf[4];
#pragma unroll
      for (int u = 0; u < 4; ++u) pf[u] = *(LAS v4u*)(sm + pp + u * 1024);
      __builtin_amdgcn_sched_barrier(0);
      const float M = vmax3(vmax3(m4[0], m4[1], m4[2]), m4[3], m4[3]);
      f4 ls = z, c[4] = {z, z, z, z};
#pragma unroll
      for (int u = 0; u < 4; ++u) {
        const unsigned kk = min((unsigned)(M - m4[u]), 255u) * 0x00800080u;
#pragma unroll
        for (int e = 0; e < 4; ++e) pf[u][e] = scale_dn(pf[u][e], kk);
        ls = mfma32(one, pf[u], ls);
        if (u < 3) {
#pragma unroll
          for (int k = 0; k < 4; ++k) c[k] = mfma32(fr[4 * k + u], pf[u], c[k]);
        }
      }
      __builtin_amdgcn_sched_barrier(0);
      c[0] = mfma32(fr[3], pf[3], c[0]);
      c[1] = mfma32(fr[7], pf[3], c[1]);
      __builtin_amdgcn_sched_barrier(0);
      float il = __builtin_amdgcn_rcpf(ls[0]);
      unsigned lse = __builtin_bit_cast(unsigned, M + __builtin_amdgcn_logf(ls[0]));
      c[2] = mfma32(fr[11], pf[3], c[2]);
      c[3] = mfma32(fr[15], pf[3], c[3]);
#define PV_OUT(c, q)                                                                                          \
  {                                                                                                           \
    v4u R[2];                                                                                                 \
    _Pragma("unroll") for (int k = 0; k < 2; ++k) {                                                           \
      const f4 c0 = c[2 * k] * il, c1 = c[2 * k + 1] * il;                                                    \
      const unsigned x0 = min(pk(c0[0], c0[1]), 0xfffffffeu), x1 = min(pk(c0[2], c0[3]), 0xfffffffeu);        \
      const unsigned y0 = min(pk(c1[0], c1[1]), 0xfffffffeu), y1 = min(pk(c1[2], c1[3]), 0xfffffffeu);        \
      auto r0 = __builtin_amdgcn_permlane16_swap(x0, y0, false, false);                                       \
      auto r1 = __builtin_amdgcn_permlane16_swap(x1, y1, false, false);                                       \
      R[k] = v4u{r0[0], r1[0], r0[1], r1[1]};                                                                 \
    }                                                                                                         \
    v4u A, B;                                                                                                 \
    _Pragma("unroll") for (int e = 0; e < 4; ++e) {                                                           \
      A[e] = (unsigned)__builtin_amdgcn_update_dpp((int)R[0][e], (int)R[1][e], ROR8, 0xf, 0xc, false);        \
      B[e] = (unsigned)__builtin_amdgcn_update_dpp((int)R[1][e], (int)R[0][e], ROR8, 0xf, 0x3, false);        \
    }                                                                                                         \
    st_sc1(rs, op + 128 * q, A);                                                                              \
    st_sc1(rs, op2 + 128 * q, B);                                                                             \
  }
      __builtin_amdgcn_sched_barrier(0);
      f4 d[4] = {z, z, z, z};
#pragma unroll
      for (int u = 0; u < 4; ++u)
#pragma unroll
        for (int k = 0; k < 4; ++k) d[k] = mfma32(fr[16 + 4 * k + u], pf[u], d[k]);
      PV_OUT(c, 0);
#pragma unroll
      for (int i = 0; i < 16; ++i) {
        __builtin_amdgcn_sched_group_barrier(0x8, 1, 0);
        __builtin_amdgcn_sched_group_barrier(0x2, PV_MIX, 0);
      }
      __builtin_amdgcn_sched_barrier(0);
      PV_OUT(d, 1);
      FLAG_SET(v);
      if (GQ && glu) {
        VMWAIT(4);
        FLAG_SET(12 + v);
      }
      MARK(3);
      STAMP(6);
    }
    }
      if (glu && w < 4) {
        v4u af[8], xf[8];
#pragma unroll
        for (int s = 0; s < 8; ++s) xf[s] = *(LAS v4u*)(px + 512 * (w >> 1) + 64 * s);
        unsigned oog = WS_OPG + (8 * w + (col >> 1)) * OGP + sl * 128 + ((col & 1) * 4 + g4) * 16;
        PIN(oog);
        FLAG_WAIT4(0);
#pragma unroll 1
        for (int i = 0; i < 4; ++i) dma16<NT_G>(wd, (unsigned)(((16 * sl + 4 * w + i) * DM + 8 * l) * 2), smb + L_GD + (4 * w + i) * CP);
#pragma unroll 1
        for (int i = GQ; i < 4; ++i) {
          const int k = 4 * ((a + i) & 3) + w;
          const unsigned og = (unsigned)(((32 * k + (l >> 1)) * FF + 16 * sl + 8 * (l & 1)) * 2);
          dma16(wg, og, smb + L_GG + k * 1024);
          dma16(wu, og, smb + L_GG + 16384 + k * 1024);
        }
        unsigned pg = L_GG + (w & 1) * 16384 + 32 * (256 * (w >> 1) + 8 * g4 + tq) + 8 * tp;
        unsigned pd = L_GD + (4 * g4 + tq) * CP + 256 * w + 8 * tp, pgw = L_GR + ((w & 1) * 2 + (w >> 1)) * 1024 + l * 16;
        PIN(pg); PIN(pd); PIN(pgw);
        VMWAIT(0);
        FLAG_SET(4 + w);
        FLAG_WAIT4(1);
        if (GQ) FLAG_WAIT4(3);
        STAMP(7);
        f4 acc = z;
#pragma unroll
        for (int s = 0; s < 8; ++s) af[s] = trrd2(sm + pg + 32 * 32 * s, sm + pg + 32 * (32 * s + 4));
        __builtin_amdgcn_sched_barrier(0);
        v2u dn[8];
#pragma unroll
        for (int s = 0; s < 8; ++s) {
          acc = mfma32(af[s], xf[s], acc);
          dn[s] = trrd(sm + pd + 32 * s);
          __builtin_amdgcn_sched_barrier(0);
        }
        *(LAS f4*)(sm + pgw) = acc;
        FLAG_SET(8 + w);
        FLAG_WAIT4(2);
        LAS char* pr = sm + L_GR + l * 16;
        const f4 gx = *(LAS f4*)(pr) + *(LAS f4*)(pr + 1024), ux = *(LAS f4*)(pr + 2048) + *(LAS f4*)(pr + 3072);
        float hh[4];
#pragma unroll
        for (int e = 0; e < 4; ++e) hh[e] = ux[e] * __builtin_amdgcn_rcpf(1.f + __builtin_amdgcn_exp2f(-1.4426950408889634f * gx[e]));
        const v2u hb = {pk(hh[0], hh[1]), pk(hh[2], hh[3])};
        f4 d = z;
#pragma unroll
        for (int t = 0; t < 8; ++t) d = mfma16(dn[t], (col >> 1) == t ? hb : v2u{0u, 0u}, d);
        v4u dv = {pk(d[0], d[1]), T, pk(d[2], d[3]), T};
        PIN(dv);
        st_sc1(rs, oog, dv);
        MARK(4);
        STAMP(9);
      }
    if (a >= 12) {
      const unsigned m = (col & 1) ? 0x3f800000u : 0x3f80u;
      v4u sel = {col < 2 ? m : 0u, (col >> 1) == 1 ? m : 0u, col < 2 ? m : 0u, (col >> 1) == 1 ? m : 0u};
      PIN(sel);
      if (w == 0) {
        unsigned ot = WS_OPT + rd * 8192 + 4096 + l * 16, oy = (l >> 2) * 1024 + rd * 32 + (l & 3) * 8, pr = smb + L_RR + (l & 7) * 16;
        PIN(ot); PIN(oy); PIN(pr);
        v4u p[8];
        HOLD_UNTIL(R_START);
        SWEEP_4(p, ot, -4096, -3072, -2048, -1024, "");
        SWEEP_4(p + 4, ot, 0, 1024, 2048, 3072, "");
        PIN4(p);
        PIN4(p + 4);
        for (unsigned spin = 0;; ++spin) {
          if (!(TAGS_BAD4(p) | TAGS_BAD4(p + 4)) || spin > SPIN_CAP) break;
          if (TAG_BAD(p[0])) RELOAD(p[0], ot, -4096);
          if (TAG_BAD(p[1])) RELOAD(p[1], ot, -3072);
          if (TAG_BAD(p[2])) RELOAD(p[2], ot, -2048);
          if (TAG_BAD(p[3])) RELOAD(p[3], ot, -1024);
          if (TAG_BAD(p[4])) RELOAD(p[4], ot, 0);
          if (TAG_BAD(p[5])) RELOAD(p[5], ot, 1024);
          if (TAG_BAD(p[6])) RELOAD(p[6], ot, 2048);
          if (TAG_BAD(p[7])) RELOAD(p[7], ot, 3072);
          PIN4(p);
          PIN4(p + 4);
        }
        STAMP(8);
        f4 acc = z;
#pragma unroll
        for (int i = 0; i < 4; ++i) acc = mfma32(sel, v4u{p[2 * i].x, p[2 * i].z, p[2 * i + 1].x, p[2 * i + 1].z}, acc);
        f4 rest[6];
        for (unsigned spin = 0; spin < SPIN_CAP; ++spin) {
          v4u f0;
          v2u f1;
          asm volatile("ds_read_b128 %0, %8\n\tds_read_b64 %1, %8 offset:16\n\tds_read_b128 %2, %9\n\tds_read_b128 %3, %9 offset:128\n\t"
                       "ds_read_b128 %4, %9 offset:256\n\tds_read_b128 %5, %9 offset:384\n\tds_read_b128 %6, %9 offset:512\n\t"
                       "ds_read_b128 %7, %9 offset:640\n\ts_waitcnt lgkmcnt(0)"
                       : "=&v"(f0), "=&v"(f1), "=&v"(rest[0]), "=&v"(rest[1]), "=&v"(rest[2]), "=&v"(rest[3]), "=&v"(rest[4]), "=&v"(rest[5])
                       : "v"(smb + L_FL + 64), "v"(pr)
                       : "memory");
          if (__builtin_amdgcn_readfirstlane(f0.x & f0.y & f0.z & f0.w & f1.x & f1.y)) break;
        }
        const f4 gl = ((rest[0] + rest[1]) + (rest[2] + rest[3])) + (rest[4] + rest[5]);
#pragma unroll
        for (int e = 0; e < 4; ++e) acc[e] = (acc[e] + dpp<ROR8>(acc[e])) + gl[e];
        if (l < 8) *(v2u*)((char*)out + oy) = v2u{pk(acc[0], acc[1]), pk(acc[2], acc[3])};
        if (rd == 0 && l == 0) __builtin_amdgcn_raw_buffer_store_b32(T, rs, WS_E, 0, AUX_SC1);
      } else if (w < 7) {
        const bool last = w == 6;
        unsigned og = WS_OPG + rd * OGP + (w - 1) * 2048 + (last ? min(l, 47) : l) * 16, og2 = last ? og : og + 1024;
        const v2u xr = *(const v2u*)((const char*)x + ((l >> 2) & 1) * 1024 + rd * 32 + (l & 3) * 8);
        unsigned m0 = (last && l >= 48) ? 0u : ~0u, m1 = last ? 0u : ~0u;
        f4 xv = {blo(xr.x), bhi(xr.x), blo(xr.y), bhi(xr.y)};
        if (!last) xv = z;
        PIN(og); PIN(og2); PIN(m0); PIN(m1); PIN(xv);
        v4u p0, p1;
        HOLD_UNTIL(R_START);
        for (unsigned spin = 0;; ++spin) {
          SWEEP_2(p0, p1, og, og2, WAIT0);
          if (!(TAG_BAD(p0) | TAG_BAD(p1)) || spin > SPIN_CAP) break;
        }
        STAMP(8);
        f4 acc = mfma32(sel, v4u{p0.x & m0, p0.z & m0, p1.x & m1, p1.z & m1}, z);
#pragma unroll
        for (int e = 0; e < 4; ++e) acc[e] = (acc[e] + dpp<ROR8>(acc[e])) + xv[e];
        if (l < 8) *(LAS f4*)(sm + L_RR + (w - 1) * 128 + l * 16) = acc;
        FLAG_SET(15 + w);
      }
      MARK(5);
      STAMP(9);
    }
    STAMP_FLUSH();
    __builtin_amdgcn_endpgm();
  }
  {
    const int h = HMAP ? xcd + 8 * (r >> 2) : 4 * xcd + (r >> 2), j = r & 3, ca = j >> 1, mb = j & 1;
    const int fl = tq | ((g4 & 1) << 2);
    gld4_sc1(ep, ws, 0u);
    if (__builtin_expect(w < 4, 1)) {
      const unsigned vq = (unsigned)(((128 * j + g4) * 4096 + h * DH + 8 * (col ^ ((g4 | ((w & 2) << 1)) << 1))) * 2);
#pragma unroll 1
      for (int k = w; k < 32; k += 4) dma16<NT_WQ>(wq, vq + (unsigned)k * 32768u, smb + L_WQ + k * 1024);
    } else if (w < 6) {
      VMWAIT(1);
      LAYERNORM();
      MARK(0);
    } else {
      __builtin_amdgcn_s_sleep(UK_DELAY);
      const unsigned vu = (unsigned)((((128 * j + g4) * NH + h) * DH) * 2);
#pragma unroll 1
      for (int k = w - 6; k < 32; k += 2) dma16<NT_WUK>(wuk, vu + (unsigned)k * 32768u + 16u * (col ^ ((4 * k + g4) & 15)), smb + L_WUK + k * 1024);
      MARK(1);
    }
    unsigned p = L_WQ + 256 * (8 * g4 + tq) + 16 * ((2 * w + (tp >> 1)) ^ (fl << 1)) + 8 * (tp & 1);
    unsigned oqp = WS_QP + ((h * 4 + j) * 8 + w) * 128 + col * 64 + g4 * 16;
    unsigned pxj = L_X + (col & 1) * XP + 16 * g4 + 256 * j;
    unsigned o = WS_QP + (h * 4 + (w & 3)) * 1024 + l * 16;
    unsigned pqw = smb + L_CC + (2 * (w & 3) + ((l >> 2) & 1)) * SP + (l >> 3) * 32 + (l & 3) * 8, pqr = L_CC + (col & 7) * SP + 16 * g4;
    unsigned oqa = WS_QA + ((col & 1) * NH + h) * 2048 + (64 * j + 16 * w + 8 * (col >> 1) + 2 * g4) * 8;
    unsigned pu = L_WUK + 256 * (32 * w + col) + 16 * (g4 ^ col);
    PIN(p); PIN(oqp); PIN(pxj); PIN(o); PIN(oqa); PIN(pu); PIN(pqw); PIN(pqr);
    {
    {
      if (w < 6) VMWAIT(0);
      else VMWAIT(16);
      EPOCH();
      STAMP(1);
      BAR();
      STAMP(2);
    }
    {
      v4u af[4], xf[4];
#pragma unroll
      for (int s = 0; s < 4; ++s) {
        af[s] = trrd2(sm + p + 256 * 32 * s, sm + p + 256 * (32 * s + 4));
        xf[s] = *(LAS v4u*)(sm + pxj + 64 * s);
      }
      __builtin_amdgcn_sched_barrier(0);
      f4 acc = z;
#pragma unroll
      for (int s = 0; s < 4; ++s) acc = mfma32(af[s], xf[s], acc);
      if (col < 2) st_sc1(rs, oqp, v4u{pk(acc[0], acc[1]), T, pk(acc[2], acc[3]), T});
    }
    STAMP(3);
    if (w >= 6) VMWAIT(1);
    if (w >= 4) {
      BAR();
      BAR();
    } else {
      v4u a, uk[8], qf[4];
      {
        __builtin_amdgcn_s_sleep(H1_DELAY);
        asm volatile(LD16(0, 1, 2) " sc1" : "=&v"(a) : "v"(o), "s"(ws) : "memory");
        BAR();
      }
#pragma unroll
      for (int s = 0; s < 4; ++s) {
        uk[s] = *(LAS v4u*)(sm + (pu ^ (64 * s)));
        uk[4 + s] = *(LAS v4u*)(sm + (pu ^ (64 * s)) + 4096);
      }
      VMWAIT(0);
      PIN(a);
      for (unsigned spin = 0;; ++spin) {
        if (!__builtin_amdgcn_ballot_w64((T & a.y & a.w) != (T | a.y | a.w)) || spin > SPIN_CAP) break;
        asm volatile(LD16(0, 1, 2) " sc1" WAIT0 : "+v"(a) : "v"(o), "s"(ws) : "memory");
      }
      asm volatile("ds_write2_b32 %0, %1, %2 offset0:0 offset1:1" ::"v"(pqw), "v"(a.x), "v"(a.z) : "memory");
      STAMP(4);
      BAR();
#pragma unroll
      for (int s = 0; s < 4; ++s) qf[s] = *(LAS v4u*)(sm + pqr + 64 * s);
      __builtin_amdgcn_sched_barrier(0);
      f4 a0 = z, a1 = z;
#pragma unroll
      for (int s = 0; s < 4; ++s) {
        a0 = mfma32(uk[s], qf[s], a0);
        a1 = mfma32(uk[4 + s], qf[s], a1);
      }
      float q[4];
#pragma unroll
      for (int e = 0; e < 4; ++e) {
        const float mine = (col & 2) ? a1[e] : a0[e], other = (col & 2) ? a0[e] : a1[e];
        q[e] = mine + dpp<QP_2301>(other);
        q[e] += dpp<ROR4>(q[e]);
      }
      if (col < 4) st_sc1(rs, oqa, v4u{pk(q[0], q[1]), T, pk(q[2], q[3]), T});
    }
    }
    STAMP(5);
    PROBE_T(T);
    BAR();
    HOLD_UNTIL(T_START);
    {
      const unsigned vv = (unsigned)((((256 * ca + g4) * NH + h) * DH + 8 * (col ^ ((g4 | ((w & 2) << 1)) << 1))) * 2);
      const unsigned vo = (unsigned)(((h * DH + (l >> 5)) * DM + 256 * mb) * 2) + 16u * ((l & 31) ^ (((l >> 5) | ((w & 1) << 1) | (w & 4)) << 1));
      int kv = w + 32 * mb, ko = w + 32 * ca;
#pragma unroll 1
      for (int hf = 0; hf < 2; ++hf) {
#pragma unroll 1
        for (int i = 0; i < 4; ++i, kv = (kv + 8) & 63) dma16<NT_T>(wuv, vv + (unsigned)kv * 32768u, smb + L_WUV + kv * 1024);
#pragma unroll 1
        for (int i = 0; i < 4; ++i, ko = (ko + 8) & 63) dma16<NT_T>(wo, vo + (unsigned)ko * 2048u, smb + L_WO + ko * 1024);
        if (hf == 0) asm volatile("s_barrier" ::: "memory");
      }
    }
    const int tb = w >> 2, nq = w & 3;
    o = WS_PART + (((h * 2 + ca) * 2 + tb) * 32 + 8 * nq + g4) * 512 + 16 * col;
    unsigned pw = L_W + (tb * 32 + 8 * nq + g4) * 4;
    unsigned pv = L_WUV + 256 * (8 * g4 + tq) + 16 * ((2 * w + (tp >> 1)) ^ (fl << 1)) + 8 * (tp & 1);
    unsigned po = L_WO + 512 * (8 * g4 + tq) + 16 * ((4 * w + (tp >> 1)) ^ (fl << 1)) + 8 * (tp & 1);
    unsigned pcw = L_CC + (nq * 2 + tb) * SP2 + 16 * col + 8 * g4, pcr = L_CC + (col & 7) * SP2 + 16 * g4;
    unsigned ptw = L_CT + col * SP + (16 * w + 4 * g4) * 2, ptr = L_CT + (col & 1) * SP + 16 * g4;
    unsigned oo = WS_OPT + ((16 * mb + 2 * w + (col >> 1)) * 64 + ((2 * h + ca + ROT * 4 * (2 * w + (col >> 1))) & 63)) * 128 + ((col & 1) * 4 + g4) * 16;
    v4u mk;
#pragma unroll
    for (int d = 0; d < 4; ++d) mk[d] = (col >> 1) == d ? ((col & 1) ? 0xffff0000u : 0xffffu) : 0u;
    PIN(o); PIN(pw); PIN(pv); PIN(po); PIN(pcw); PIN(pcr); PIN(ptw); PIN(ptr); PIN(oo); PIN(mk);
    STAMP(11);
        if (w == 7) {
      const unsigned ol = WS_LSE + (HMAP ? ((l * 2 + (r >> 3)) * 8 + ((xcd + ROT * l) & 7)) * 128 + ((r >> 2) & 1) * 8 : (l * 8 + xcd) * 128 + (r >> 2) * 8);
      v2u la;
      UNDEF(la);
      for (unsigned spin = 0;; ++spin) {
        asm volatile("global_load_dwordx2 %0, %1, %2 sc1" WAIT0 : "+v"(la) : "v"(ol), "s"(ws) : "memory");
        if (!__builtin_amdgcn_ballot_w64(la.y != T) || spin > SPIN_CAP) break;
      }
      float M = asf(la.x);
      M = fmax2(M, dpp<ROR8>(M));
      M = fmax2(M, dpp<ROR4>(M));
      M = fmax2(M, dpp<ROR2>(M));
      M = fmax2(M, dpp<ROR1>(M));
      M = sw16_max(M);
      const float e = __builtin_amdgcn_exp2f(asf(la.x) - M);
      float L = e + dpp<ROR8>(e);
      L += dpp<ROR4>(L);
      L += dpp<ROR2>(L);
      L += dpp<ROR1>(L);
      L = sw16_add(L, L);
      const float wt = e * __builtin_amdgcn_rcpf(L);
      *(LAS unsigned*)(sm + L_W + 4 * ((l & 32) | ((l + ROT * 4 * (h & 7)) & 31))) = pk(wt, wt);
      STAMP(7);
    }
    v4u a[4];
    {
      if (LSE_PIPE && w == 7) VMWAIT(2);
      else VMWAIT(0);
      STAMP(6);
      BAR();
      SWEEP_4(a, o, 0, 256, 2048, 2304, "");
    }
    v4u A0 = mk & *(LAS unsigned*)(sm + pw), A1 = mk & *(LAS unsigned*)(sm + pw + 16);
    v4u av[8], ao[8];
#pragma unroll
    for (int s = 0; s < 8; ++s) av[s] = trrd2(sm + pv + 256 * 32 * s, sm + pv + 256 * (32 * s + 4));
#pragma unroll
    for (int t = 0; t < 2; ++t)
#pragma unroll
      for (int s = 0; s < 4; ++s) ao[4 * t + s] = trrd2(sm + (po ^ (32 * t)) + 512 * 32 * s, sm + (po ^ (32 * t)) + 512 * (32 * s + 4));
    VMWAIT(0);
    asm volatile("" : "+v"(a[0]), "+v"(a[1]), "+v"(a[2]), "+v"(a[3]), "+v"(A0), "+v"(A1)::"memory");
    for (unsigned spin = 0;; ++spin) {
      unsigned mx = 0u;
#pragma unroll
      for (int i = 0; i < 4; ++i) mx = max(max(mx, max(a[i].x, a[i].y)), max(a[i].z, a[i].w));
      if (!__builtin_amdgcn_ballot_w64(mx == ~0u) || spin > SPIN_CAP) break;
      if (HOLE(a[0])) RELOAD(a[0], o, 0);
      if (HOLE(a[1])) RELOAD(a[1], o, 256);
      if (HOLE(a[2])) RELOAD(a[2], o, 2048);
      if (HOLE(a[3])) RELOAD(a[3], o, 2304);
      PIN4(a);
    }
    STAMP(8);
    {
      f4 c0 = mfma32(A0, a[0], z), c1 = mfma32(A0, a[1], z);
      c0 = mfma32(A1, a[2], c0);
      c1 = mfma32(A1, a[3], c1);
      if (g4 < 2) {
        *(LAS v2u*)(sm + pcw) = v2u{pk(c0[0], c0[1]), pk(c0[2], c0[3])};
        *(LAS v2u*)(sm + pcw + 256) = v2u{pk(c1[0], c1[1]), pk(c1[2], c1[3])};
      }
    }
    BAR();
    {
      f4 acc = z;
      v4u bf[8];
#pragma unroll
      for (int s = 0; s < 8; ++s) bf[s] = *(LAS v4u*)(sm + pcr + 64 * s);
      __builtin_amdgcn_sched_barrier(0);
#pragma unroll
      for (int s = 0; s < 8; ++s) acc = mfma32(av[s], bf[s], acc);
#pragma unroll
      for (int e = 0; e < 4; ++e) {
        acc[e] += dpp<QP_2301>(acc[e]);
        acc[e] += dpp<ROR4>(acc[e]);
      }
      if (col < 2) *(LAS v2u*)(sm + ptw) = v2u{pk(acc[0], acc[1]), pk(acc[2], acc[3])};
    }
    BAR();
    {
      f4 c0 = z, c1 = z;
      v4u bf[4];
#pragma unroll
      for (int s = 0; s < 4; ++s) bf[s] = *(LAS v4u*)(sm + ptr + 64 * s);
      __builtin_amdgcn_sched_barrier(0);
#pragma unroll
      for (int s = 0; s < 4; ++s) {
        c0 = mfma32(ao[s], bf[s], c0);
        c1 = mfma32(ao[4 + s], bf[s], c1);
      }
      if (col & 2) c0 = c1;
      if (col < 4) st_sc1(rs, oo, v4u{pk(c0[0], c0[1]), T, pk(c0[2], c0[3]), T});
    }
    MARK(2);
    STAMP(9);
  }
  STAMP_FLUSH();
}

#define WS_END 0
#define WS_BASE (WS_END ? ((ws_size - (size_t)WS_END * 1048576) & ~(size_t)4095) : 0)
extern "C" void kernel_launch(void* const* d_in, const int* in_sizes, int n_in, void* d_out, int out_size, void* d_ws, size_t ws_size,
                              hipStream_t stream) {
  mla_glu_decode<<<256, 512, 0, stream>>>((const u16*)d_in[0], (const u16*)d_in[1], (const u16*)d_in[2], (const u16*)d_in[3],
                                          (const u16*)d_in[4], (const u16*)d_in[5], (const u16*)d_in[7], (const u16*)d_in[8],
                                          (const u16*)d_in[9], (u16*)d_out, (char*)d_ws + WS_BASE);
}
```

```cpp
#define OUT_SC1 1
#include <hip/hip_runtime.h>
#include <stdint.h>

#define GAS __attribute__((address_space(1)))
#define LAS __attribute__((address_space(3)))
typedef unsigned short u16;
typedef unsigned v4u __attribute__((ext_vector_type(4)));
typedef unsigned v2u __attribute__((ext_vector_type(2)));
typedef float f4 __attribute__((ext_vector_type(4)));
typedef float f2 __attribute__((ext_vector_type(2)));
typedef __bf16 bf8 __attribute__((ext_vector_type(8)));
typedef __bf16 bf2 __attribute__((ext_vector_type(2)));
typedef short s4 __attribute__((ext_vector_type(4)));

#define ROT 1
namespace {
constexpr int NH = 32, SK = 4096, DH = 128, DM = 512, DC = 512, FF = 1376;
constexpr int NSL = FF / 16;
constexpr int WS_E = 0;
constexpr int WS_QP = 4096;
constexpr int WS_QA = WS_QP + NH * 4 * 1024;
constexpr int WS_LSE = WS_QA + 2 * NH * 2048;
constexpr int WS_GO = WS_LSE + 2 * 32 * 16 * 128;
constexpr int WS_OPT = WS_GO + 2 * 32 * 16 * 128;
constexpr int WS_OPG = WS_OPT + 32 * 64 * 128;
constexpr int OGP = 16384 + ROT * 384;
constexpr int WS_PART = (WS_OPG + 32 * OGP + 4095) / 4096 * 4096;
constexpr int WS_BYTES = WS_PART + NH * 2 * 2 * 32 * 512;
constexpr int WS_STAMP = 7 << 19;
constexpr int CP = 1056;
constexpr int XP = 1088, SP = 288, SP2 = 544;
constexpr int L_C = 0, L_Q = 128 * CP, L_P = L_Q + 16 * CP, L_X = L_P + 4096, L_MX = L_X + 2 * XP, L_RR = L_MX + 256,
              L_JUNK = L_RR + 1024, L_FL = L_JUNK + 256, L_END = L_FL + 128;
static_assert(L_END <= 163840, "LDS");
constexpr int L_GG = 0, L_GD = 32768, L_GR = 65536;
constexpr int L_WUV = 0, L_WO = 65536, L_WQ = 0, L_WUK = 32768, L_CC = 131072, L_CT = L_CC + 8 * SP2, L_W = L_CT + 2 * SP;
#define UK_DELAY 4
#define H1_DELAY 12
#define T_START 340
#define CODE_TOUCH 1
#define NT_CKV 0
#define NT_WQ 0
#define NT_WUK 0
#define NT_T 0
#define NT_G 0
#define A_START 70
#define GLU_EARLY 0
#define G_START 0
#define C_START 0
#define LSE_LATE 0
#define LSE_WAVE 3
#define DATA_DELAY 0
#define NEWRED 1
#define R_START 0
#define G_TOUCH 4
#define LSE_PIPE 0
#define PV_MIX 4
#define LSE_QK 1
#define HMAP 1
#define GQ 1
#define DOWN_FIRST 1
#define EARLY_SWEEP 0
#define GO_DELAY 0
#define OUT_TOUCH 1
#define BUMP_EARLY 1
#define PIECEWISE 1
#define PF_GLU 1
constexpr unsigned SPIN_CAP = 1u << 20;
constexpr int AUX_SC1 = 16;

#define STAMP_W 0
#define STAMP(i)
#define STAMP_FLUSH()
#define PROBE_T(T_)

__device__ __forceinline__ f4 mfma32(v4u a, v4u b, f4 c) {
  return __builtin_amdgcn_mfma_f32_16x16x32_bf16(__builtin_bit_cast(bf8, a), __builtin_bit_cast(bf8, b), c, 0, 0, 0);
}
__device__ __forceinline__ f4 mfma16(v2u a, v2u b, f4 c) {
  return __builtin_amdgcn_mfma_f32_16x16x16bf16_1k(__builtin_bit_cast(s4, a), __builtin_bit_cast(s4, b), c, 0, 0, 0);
}
__device__ __forceinline__ unsigned pk(float lo, float hi) {
  f2 x = {lo, hi};
  return __builtin_bit_cast(unsigned, __builtin_convertvector(x, bf2));
}
__device__ __forceinline__ float asf(unsigned u) { return __builtin_bit_cast(float, u); }
__device__ __forceinline__ float blo(unsigned u) { return __builtin_bit_cast(float, u << 16); }
__device__ __forceinline__ float bhi(unsigned u) { return __builtin_bit_cast(float, u & 0xffff0000u); }
template <int CTRL> __device__ __forceinline__ float dpp(float v) {
  return __builtin_bit_cast(float, __builtin_amdgcn_update_dpp(0, __builtin_bit_cast(int, v), CTRL, 0xf, 0xf, true));
}
__device__ __forceinline__ float fmax2(float a, float b) {
#pragma float_control(precise, off)
  return __builtin_fmaxf(a, b);
}
typedef unsigned short h2 __attribute__((ext_vector_type(2)));
__device__ __forceinline__ unsigned scale_dn(unsigned p, unsigned kk) {
  return __builtin_bit_cast(unsigned, __builtin_elementwise_sub_sat(__builtin_bit_cast(h2, p), __builtin_bit_cast(h2, kk)));
}
#define PIN(x) asm volatile("" : "+v"(x))
constexpr int ROR1 = 0x121, ROR2 = 0x122, ROR4 = 0x124, ROR8 = 0x128, QP_2301 = 0x4e, QP_2222 = 0xaa;
__device__ __forceinline__ float sw32_add(float a, float b) {
  auto r = __builtin_amdgcn_permlane32_swap(__builtin_bit_cast(unsigned, a), __builtin_bit_cast(unsigned, b), false, false);
  return asf(r[0]) + asf(r[1]);
}
__device__ __forceinline__ float sw16_add(float a, float b) {
  auto r = __builtin_amdgcn_permlane16_swap(__builtin_bit_cast(unsigned, a), __builtin_bit_cast(unsigned, b), false, false);
  return asf(r[0]) + asf(r[1]);
}
__device__ __forceinline__ float vmax(unsigned a, unsigned b) {
  float r;
  asm("v_max_f32 %0, %1, %2" : "=v"(r) : "v"(a), "v"(b));
  return r;
}
__device__ __forceinline__ float vmax3(float a, float b, float c) {
  float r;
  asm("v_max3_f32 %0, %1, %2, %3" : "=v"(r) : "v"(a), "v"(b), "v"(c));
  return r;
}
__device__ __forceinline__ float sw32_max(float a) {
  auto r = __builtin_amdgcn_permlane32_swap(__builtin_bit_cast(unsigned, a), __builtin_bit_cast(unsigned, a), false, false);
  return vmax(r[0], r[1]);
}
__device__ __forceinline__ float sw16_max(float a) {
  auto r = __builtin_amdgcn_permlane16_swap(__builtin_bit_cast(unsigned, a), __builtin_bit_cast(unsigned, a), false, false);
  return vmax(r[0], r[1]);
}
__device__ __forceinline__ float wave_sum(float v) {
  v += dpp<ROR8>(v);
  v += dpp<ROR4>(v);
  v += dpp<ROR2>(v);
  v += dpp<ROR1>(v);
  v = sw16_add(v, v);
  return sw32_add(v, v);
}
__device__ __forceinline__ v2u trrd(LAS char* p) {
  return __builtin_bit_cast(v2u, __builtin_amdgcn_ds_read_tr16_b64_v4i16((LAS s4*)p));
}
__device__ __forceinline__ v4u trrd2(LAS char* p0, LAS char* p1) {
  v2u a = trrd(p0), b = trrd(p1);
  return v4u{a.x, a.y, b.x, b.y};
}
#define BAR() asm volatile("s_waitcnt lgkmcnt(0)\n\ts_barrier" ::: "memory")
#define VMWAIT(k) asm volatile("s_waitcnt vmcnt(%0)" ::"n"(k) : "memory")
__device__ __forceinline__ void gld16(v4u& d, const void* base, unsigned off) {
  asm volatile("global_load_dwordx4 %0, %1, %2" : "=v"(d) : "v"(off), "s"(base) : "memory");
}
__device__ __forceinline__ void gld4_sc1(unsigned& d, const void* base, unsigned off) {
  asm volatile("global_load_dword %0, %1, %2 sc1" : "=v"(d) : "v"(off), "s"(base) : "memory");
}
template <int NT = 0> __device__ __forceinline__ void touch(const void* base, unsigned off, unsigned lds) {
  if (NT) asm volatile("s_mov_b32 m0, %2\n\ts_nop 0\n\tglobal_load_lds_dword %0, %1 nt" ::"v"(off), "s"(base), "s"(lds) : "memory");
  else asm volatile("s_mov_b32 m0, %2\n\ts_nop 0\n\tglobal_load_lds_dword %0, %1" ::"v"(off), "s"(base), "s"(lds) : "memory");
}
__device__ __forceinline__ void touch_sc1(const void* base, unsigned off, unsigned lds) {
  asm volatile("s_mov_b32 m0, %2\n\ts_nop 0\n\tglobal_load_lds_dword %0, %1 sc1" ::"v"(off), "s"(base), "s"(lds) : "memory");
}
#define LD16(i, a, o) "global_load_dwordx4 %" #i ", %" #a ", %" #o
#define SWEEP_4(q, o, o0, o1, o2, o3, W)                                                                                     \
  asm volatile(LD16(0, 4, 5) " offset:" #o0 " sc1\n\t" LD16(1, 4, 5) " offset:" #o1 " sc1\n\t" LD16(2, 4, 5) " offset:" #o2         \
               " sc1\n\t" LD16(3, 4, 5) " offset:" #o3 " sc1" W                                                               \
               : "=&v"((q)[0]), "=&v"((q)[1]), "=&v"((q)[2]), "=&v"((q)[3])                                                  \
               : "v"(o), "s"(ws)                                                                                             \
               : "memory")
#define RESWEEP_4(q, o, o0, o1, o2, o3)                                                                                      \
  asm volatile(LD16(0, 4, 5) " offset:" #o0 " sc1\n\t" LD16(1, 4, 5) " offset:" #o1 " sc1\n\t" LD16(2, 4, 5) " offset:" #o2         \
               " sc1\n\t" LD16(3, 4, 5) " offset:" #o3 " sc1\n\ts_waitcnt vmcnt(0)"                                            \
               : "+v"((q)[0]), "+v"((q)[1]), "+v"((q)[2]), "+v"((q)[3])                                                      \
               : "v"(o), "s"(ws)                                                                                             \
               : "memory")
#define RELOAD(q, o, off) asm volatile(LD16(0, 1, 2) " offset:" #off " sc1" : "+v"(q) : "v"(o), "s"(ws) : "memory")
#define PIN4(q) asm volatile("s_waitcnt vmcnt(0)" : "+v"((q)[0]), "+v"((q)[1]), "+v"((q)[2]), "+v"((q)[3])::"memory")
#define TAG_BAD(q) __builtin_amdgcn_ballot_w64((T & (q).y & (q).w) != (T | (q).y | (q).w))
#define HOLE(q) __builtin_amdgcn_ballot_w64(max(max((q).x, (q).y), max((q).z, (q).w)) == ~0u)
#define SWEEP_L(W)                                                                                                          \
  asm volatile("global_load_dwordx2 %0, %3, %5 sc1\n\tglobal_load_dwordx2 %1, %4, %5 sc1\n\t"                              \
               "global_load_dwordx2 %2, %4, %5 offset:512 sc1" W                                                            \
               : "=&v"(la), "=&v"(l0), "=&v"(l1)                                                                            \
               : "v"(ola), "v"(olo), "s"(ws)                                                                                \
               : "memory")
#define SWEEP_2(p, q, oa, ob, W) \
  asm volatile(LD16(0, 2, 4) " sc1\n\t" LD16(1, 3, 4) " sc1" W : "=&v"(p), "=&v"(q) : "v"(oa), "v"(ob), "s"(ws) : "memory")
#define TAGS_BAD4(q) \
  __builtin_amdgcn_ballot_w64((T & (q)[0].y & (q)[0].w & (q)[1].y & (q)[1].w & (q)[2].y & (q)[2].w & (q)[3].y & (q)[3].w) != \
                              (T | (q)[0].y | (q)[0].w | (q)[1].y | (q)[1].w | (q)[2].y | (q)[2].w | (q)[3].y | (q)[3].w))
#define FLAG_SET(i) asm volatile("ds_write_b32 %0, %1" ::"v"(smb + L_FL + 4 * (i)), "v"(1u) : "memory")
#define FLAG_WAIT4(i)                                                                                     \
  for (unsigned spin_ = 0; spin_ < SPIN_CAP; ++spin_) {                                                   \
    v4u f_;                                                                                               \
    asm volatile("ds_read_b128 %0, %1\n\ts_waitcnt lgkmcnt(0)" : "=v"(f_) : "v"(smb + L_FL + 16 * (i)) : "memory"); \
    if (__builtin_amdgcn_readfirstlane(f_.x & f_.y & f_.z & f_.w)) break;                                 \
  }
#define HOLD_UNTIL(ticks) \
  while ((unsigned)__builtin_amdgcn_s_memrealtime() - (unsigned)tstart < (ticks)) __builtin_amdgcn_s_sleep(1)
#define MARK(i) asm volatile(".Lmla_e" #i ":")
#define UNDEF(x) asm volatile("" : "=v"(x))
#define EPOCH() asm volatile("v_add_u32 %0, 1, %1" : "=v"(T) : "v"(ep) : "memory")
#define WAIT0 "\n\ts_waitcnt vmcnt(0)"
template <int NT = 0> __device__ __forceinline__ void dma16(const void* base, unsigned off, unsigned lds) {
  if (NT) asm volatile("s_mov_b32 m0, %2\n\ts_nop 0\n\tglobal_load_lds_dwordx4 %0, %1 nt" ::"v"(off), "s"(base), "s"(lds) : "memory");
  else asm volatile("s_mov_b32 m0, %2\n\ts_nop 0\n\tglobal_load_lds_dwordx4 %0, %1" ::"v"(off), "s"(base), "s"(lds) : "memory");
}
__device__ __forceinline__ v4u ld_sc1(__amdgpu_buffer_rsrc_t rs, int off) {
  return __builtin_amdgcn_raw_buffer_load_b128(rs, off, 0, AUX_SC1);
}
__device__ __forceinline__ v2u ld8_sc1(__amdgpu_buffer_rsrc_t rs, int off) {
  return __builtin_amdgcn_raw_buffer_load_b64(rs, off, 0, AUX_SC1);
}
__device__ __forceinline__ void st_sc1(__amdgpu_buffer_rsrc_t rs, int off, v4u v) {
  __builtin_amdgcn_raw_buffer_store_b128(v, rs, off, 0, AUX_SC1);
}
__device__ __forceinline__ void ln_stats(v4u r, float& mean, float& rstd) {
  float e[8] = {blo(r.x), bhi(r.x), blo(r.y), bhi(r.y), blo(r.z), bhi(r.z), blo(r.w), bhi(r.w)};
  float s = ((e[0] + e[1]) + (e[2] + e[3])) + ((e[4] + e[5]) + (e[6] + e[7]));
  mean = wave_sum(s) * (1.f / DM);
  float q = 0.f;
#pragma unroll
  for (int i = 0; i < 8; ++i) {
    float d = e[i] - mean;
    q = fmaf(d, d, q);
  }
  rstd = __builtin_amdgcn_rsqf(wave_sum(q) * (1.f / DM) + 1e-5f);
}
}

__global__ void __launch_bounds__(512, 2)
mla_glu_decode(const u16* __restrict__ x, const u16* __restrict__ ckv, const u16* __restrict__ wq, const u16* __restrict__ wuk,
               const u16* __restrict__ wuv, const u16* __restrict__ wo, const u16* __restrict__ wg, const u16* __restrict__ wu,
               const u16* __restrict__ wd, u16* __restrict__ out, char* __restrict__ ws) {
  const unsigned long long tstart = __builtin_amdgcn_s_memrealtime();
  __builtin_amdgcn_sched_barrier(0);
  __shared__ __attribute__((aligned(16))) char smem_[L_END];
  LAS char* sm = (LAS char*)smem_;
  const unsigned smb = (unsigned)(size_t)sm;
  const int tid = threadIdx.x, l = tid & 63, w = __builtin_amdgcn_readfirstlane(tid >> 6);
  const int col = l & 15, g4 = l >> 4, tq = (l & 15) >> 2, tp = l & 3;
  const int xcd = blockIdx.x & 7, r = blockIdx.x >> 3;
  const __amdgpu_buffer_rsrc_t rs = __builtin_amdgcn_make_buffer_rsrc(ws, 0, WS_BYTES, 0x00020000);
  int bw = (blockIdx.x >> 7) * w;
  asm volatile("" : "+s"(bw));
  if (bw >= 5) {
    const char* pc0;
    unsigned len;
    asm volatile("s_getpc_b64 %0\n.Lmla_pc:\n\ts_mov_b32 %1, .Lmla_e0-.Lmla_pc\n\ts_max_i32 %1, %1, .Lmla_e1-.Lmla_pc\n\t"
                 "s_max_i32 %1, %1, .Lmla_e2-.Lmla_pc\n\ts_max_i32 %1, %1, .Lmla_e3-.Lmla_pc\n\ts_max_i32 %1, %1, .Lmla_e4-.Lmla_pc\n\t"
                 "s_max_i32 %1, %1, .Lmla_e5-.Lmla_pc"
                 : "=s"(pc0), "=s"(len)::"scc");
    touch(pc0, min((unsigned)(64 * (bw - 5) + l) * 128u, len), smb + L_JUNK);
  }
  __builtin_amdgcn_sched_barrier(0);
  asm volatile("" ::"s"(x), "s"(ckv), "s"(wq), "s"(wuk), "s"(wuv), "s"(wo), "s"(wg), "s"(wu), "s"(wd), "s"(out), "s"(ws));
  STAMP(0);

  unsigned ep, T;
  LAS char* const px = sm + L_X + (col & 1) * XP + 16 * g4;
  if ((w >> 1) == 2) dma16(x, (unsigned)((w & 1) * 1024 + l * 16), smb + L_X + (w & 1) * XP);
  const f4 z = {0.f, 0.f, 0.f, 0.f};
#define LAYERNORM()                                                                                                              \
  {                                                                                                                              \
    const v4u xr = *(LAS v4u*)(sm + L_X + (w & 1) * XP + l * 16);                                                                \
    float e[8] = {blo(xr.x), bhi(xr.x), blo(xr.y), bhi(xr.y), blo(xr.z), bhi(xr.z), blo(xr.w), bhi(xr.w)};                       \
    const float mean = wave_sum(((e[0] + e[1]) + (e[2] + e[3])) + ((e[4] + e[5]) + (e[6] + e[7]))) * (1.f / DM);                 \
    float q = 0.f;                                                                                                               \
    _Pragma("unroll") for (int i = 0; i < 8; ++i) {                                                                              \
      e[i] -= mean;                                                                                                              \
      q = fmaf(e[i], e[i], q);                                                                                                   \
    }                                                                                                                            \
    const float rstd = __builtin_amdgcn_rsqf(wave_sum(q) * (1.f / DM) + 1e-5f);                                                  \
    *(LAS v4u*)(sm + L_X + (w & 1) * XP + l * 16) =                                                                              \
        v4u{pk(e[0] * rstd, e[1] * rstd), pk(e[2] * rstd, e[3] * rstd), pk(e[4] * rstd, e[5] * rstd), pk(e[6] * rstd, e[7] * rstd)}; \
  }

  if (__builtin_expect(r >= 16, 0)) {
    const int a = r - 16, b = xcd >> 2, n = 8 * (xcd & 3) + (a >> 1), g = a & 1;
    const int sl = 12 * xcd + a, rd = (4 * xcd + a) & 31;
    const bool glu = a < 12 && sl < NSL;
    const int v = w & 3;
    unsigned op = WS_PART + ((((16 * g + (col & 7)) * 2 + (v >> 1)) * 2 + b) * 32 + ((n + ROT * 4 * (col & 7)) & 31)) * 512 + 256 * (v & 1) +
                  (col >> 3) * 64 + (g4 & 1) * 32 + (g4 >> 1) * 16;
    gld4_sc1(ep, ws, 0u);
    unsigned o = WS_QA + (b * NH + 16 * g) * 2048 + w * 4096 + l * 16, pqs = smb + L_Q + 2 * w * CP + l * 8;
    PIN(o); PIN(pqs);
    {
    {
      const unsigned vc = (unsigned)(((b * SK + 128 * n) * DC + 8 * l) * 2);
      STAMP(10);
      HOLD_UNTIL(C_START);
    if (w >= 4) {
#pragma unroll
      for (int q = 0; q < 4; ++q) st_sc1(rs, op + 128 * (q >> 1) + (q & 1) * 8 * 65536, v4u{~0u, ~0u, ~0u, ~0u});
      if (w == 7 && l < 32) *(LAS unsigned*)(sm + L_FL + 4 * l) = 0u;
    }
      HOLD_UNTIL(A_START);
#pragma unroll 1
      for (int q = 0; q < 4; ++q) {
        int row = w + ((64 * g + 32 * q) & 127);
        asm volatile("s_barrier" ::: "memory");
#pragma unroll 1
        for (int i = 0; i < 4; ++i, row += 8) dma16<NT_CKV>(ckv, vc + row * 1024u, smb + L_C + row * CP);
      }
    }
#define CLAND()                          \
  if ((w >> 1) == 2) {                   \
    VMWAIT(21 + 4 * EARLY_SWEEP);        \
    LAYERNORM();                         \
  }                                      \
  VMWAIT(4 * EARLY_SWEEP);               \
  EPOCH();                               \
  PROBE_T(T);                            \
  BAR();                                 \
  STAMP(1)
#define QISSUE() \
  CLAND();       \
  SWEEP_4(qg, o, 0, 1024, 2048, 3072, "")
#define QRETRY()                              \
  if (TAG_BAD(qg[0])) RELOAD(qg[0], o, 0);    \
  if (TAG_BAD(qg[1])) RELOAD(qg[1], o, 1024); \
  if (TAG_BAD(qg[2])) RELOAD(qg[2], o, 2048); \
  if (TAG_BAD(qg[3])) RELOAD(qg[3], o, 3072); \
  PIN4(qg)
#define QGATHER()                                                                                                           \
  VMWAIT(0);                                                                                                      \
  asm volatile("" : "+v"(qg[0]), "+v"(qg[1]), "+v"(qg[2]), "+v"(qg[3])::"memory");                                          \
  for (unsigned spin = 0;; ++spin) {                                                                                        \
    if (!TAGS_BAD4(qg) || spin > SPIN_CAP) break;                                                                           \
    QRETRY();                                                                                                               \
  }                                                                                                                         \
  STAMP(3);                                                                                                                 \
  asm volatile("ds_write2_b32 %0, %2, %3 offset0:0 offset1:1\n\tds_write2_b32 %0, %4, %5 offset0:128 offset1:129\n\t"        \
               "ds_write2_b32 %1, %6, %7 offset0:0 offset1:1\n\tds_write2_b32 %1, %8, %9 offset0:128 offset1:129" ::"v"(pqs), \
               "v"(pqs + CP), "v"(qg[0].x), "v"(qg[0].z), "v"(qg[1].x), "v"(qg[1].z), "v"(qg[2].x), "v"(qg[2].z), "v"(qg[3].x), \
               "v"(qg[3].z)                                                                                                 \
               : "memory")
    if (__builtin_expect(w < 4, 1)) {
      v4u fr[32], qg[4];
      QISSUE();
      LAS char* pc = sm + L_C + (32 * w + col) * CP + 16 * g4;
#pragma unroll
      for (int kt = 0; kt < 2; ++kt)
#pragma unroll
        for (int s = 0; s < 16; ++s) fr[16 * kt + s] = *(LAS v4u*)(pc + 16 * kt * CP + 64 * s);
      unsigned p = L_Q + col * CP + 16 * g4, pp = L_P + w * 1024 + l * 16, pm = L_MX + col * 16 + w * 4;
      unsigned ol = WS_LSE + (HMAP ? (((b * 32 + n) * 2 + g) * 8 + ((col + ROT * n) & 7)) * 128 + (col >> 3) * 8 : ((b * 32 + n) * 8 + 4 * g + (col >> 2)) * 128 + (col & 3) * 8);
      PIN(p); PIN(pp); PIN(pm); PIN(ol);
      STAMP(2);
      QGATHER();
      BAR();
      STAMP(4);
      f4 s0 = z, s1 = z;
      v4u qf[16];
#pragma unroll
      for (int s = 0; s < 6; ++s) qf[s] = *(LAS v4u*)(sm + p + 64 * s);
      __builtin_amdgcn_sched_barrier(0);
#pragma unroll
      for (int s = 0; s < 16; ++s) {
        if (s + 6 < 16) qf[s + 6] = *(LAS v4u*)(sm + p + 64 * (s + 6));
        s0 = mfma32(fr[s], qf[s], s0);
        s1 = mfma32(fr[16 + s], qf[s], s1);
        __builtin_amdgcn_sched_barrier(0);
      }
      float mx = fmax2(fmax2(fmax2(s0[0], s0[1]), fmax2(s0[2], s0[3])), fmax2(fmax2(s1[0], s1[1]), fmax2(s1[2], s1[3])));
      mx = sw32_max(sw16_max(mx));
      constexpr float SC = 0.08838834764831845f * 1.4426950408889634f;
      const float mi = __builtin_ceilf(mx * SC);
#pragma unroll
      for (int e = 0; e < 4; ++e) {
        s0[e] = __builtin_amdgcn_exp2f(fmaf(s0[e], SC, -mi));
        s1[e] = __builtin_amdgcn_exp2f(fmaf(s1[e], SC, -mi));
      }
      *(LAS float*)(sm + pm) = mi;
      *(LAS v4u*)(sm + pp) = v4u{pk(s0[0], s0[1]), pk(s0[2], s0[3]), pk(s1[0], s1[1]), pk(s1[2], s1[3])};
      BAR();
      STAMP(5);
      if (w == LSE_QK - 1) {
        const f4 m4 = *(LAS f4*)(sm + pm - 4 * w);
        v4u pf[4];
#pragma unroll
        for (int u = 0; u < 4; ++u) pf[u] = *(LAS v4u*)(sm + pp + (u - w) * 1024);
        __builtin_amdgcn_sched_barrier(0);
        const float M = vmax3(vmax3(m4[0], m4[1], m4[2]), m4[3], m4[3]);
        const v4u one = {0x3f803f80u, 0x3f803f80u, 0x3f803f80u, 0x3f803f80u};
        f4 ls = z;
#pragma unroll
        for (int u = 0; u < 4; ++u) {
          const unsigned kk = min((unsigned)(M - m4[u]), 255u) * 0x00800080u;
#pragma unroll
          for (int e = 0; e < 4; ++e) pf[u][e] = scale_dn(pf[u][e], kk);
          ls = mfma32(one, pf[u], ls);
        }
        if (g4 == 0)
          __builtin_amdgcn_raw_buffer_store_b64(v2u{__builtin_bit_cast(unsigned, M + __builtin_amdgcn_logf(ls[0])), T}, rs, ol, 0, AUX_SC1);
      }
    } else {
      v4u fr[32], qg[4];
      QISSUE();
      LAS char* p0 = sm + L_C + (4 * g4 + tq) * CP + 256 * v + 8 * tp;
      unsigned p1 = L_C + (64 + 4 * g4 + tq) * CP + 256 * v + 8 * tp;
      PIN(p1);
#pragma unroll
      for (int ct = 0; ct < 8; ++ct)
#pragma unroll
        for (int u = 0; u < 4; ++u) {
          LAS char* p = (u < 2 ? p0 : sm + p1) + 32 * (u & 1) * CP + 32 * ct;
          fr[4 * ct + u] = trrd2(p, p + 16 * CP);
        }
      unsigned ol = WS_LSE + (HMAP ? (((b * 32 + n) * 2 + g) * 8 + ((col + ROT * n) & 7)) * 128 + (col >> 3) * 8 : ((b * 32 + n) * 8 + 4 * g + (col >> 2)) * 128 + (col & 3) * 8), pp = L_P + l * 16, pm = L_MX + col * 16;
      v4u one = {0x3f803f80u, 0x3f803f80u, 0x3f803f80u, 0x3f803f80u};
      unsigned op2 = op + 8 * 65536;
      PIN(ol); PIN(pp); PIN(pm); PIN(one); PIN(op); PIN(op2);
      STAMP(2);
      QGATHER();
      BAR();
      STAMP(4);
      if (GQ && glu) {
        const int k = 4 * (a & 3) + v;
        const unsigned og = (unsigned)(((32 * k + (l >> 1)) * FF + 16 * sl + 8 * (l & 1)) * 2);
        dma16(wg, og, smb + L_GG + k * 1024);
        dma16(wu, og, smb + L_GG + 16384 + k * 1024);
      } else if (v < G_TOUCH) {
        const int ln = 128 * a + 64 * (v >> 1) + l;
        touch((v & 1) ? wu : wg, (unsigned)((ln >> 2) * (FF * 2)) + (unsigned)min(384 * xcd + 128 * (ln & 3), FF * 2 - 4), smb + L_JUNK);
      }
      BAR();
      STAMP(5);
      const f4 m4 = *(LAS f4*)(sm + pm);
      v4u pf[4];
#pragma unroll
      for (int u = 0; u < 4; ++u) pf[u] = *(LAS v4u*)(sm + pp + u * 1024);
      __builtin_amdgcn_sched_barrier(0);
      const float M = vmax3(vmax3(m4[0], m4[1], m4[2]), m4[3], m4[3]);
      f4 ls = z, c[4] = {z, z, z, z};
#pragma unroll
      for (int u = 0; u < 4; ++u) {
        const unsigned kk = min((unsigned)(M - m4[u]), 255u) * 0x00800080u;
#pragma unroll
        for (int e = 0; e < 4; ++e) pf[u][e] = scale_dn(pf[u][e], kk);
        ls = mfma32(one, pf[u], ls);
        if (u < 3) {
#pragma unroll
          for (int k = 0; k < 4; ++k) c[k] = mfma32(fr[4 * k + u], pf[u], c[k]);
        }
      }
      __builtin_amdgcn_sched_barrier(0);
      c[0] = mfma32(fr[3], pf[3], c[0]);
      c[1] = mfma32(fr[7], pf[3], c[1]);
      __builtin_amdgcn_sched_barrier(0);
      float il = __builtin_amdgcn_rcpf(ls[0]);
      unsigned lse = __builtin_bit_cast(unsigned, M + __builtin_amdgcn_logf(ls[0]));
      c[2] = mfma32(fr[11], pf[3], c[2]);
      c[3] = mfma32(fr[15], pf[3], c[3]);
#define PV_OUT(c, q)                                                                                          \
  {                                                                                                           \
    v4u R[2];                                                                                                 \
    _Pragma("unroll") for (int k = 0; k < 2; ++k) {                                                           \
      const f4 c0 = c[2 * k] * il, c1 = c[2 * k + 1] * il;                                                    \
      const unsigned x0 = min(pk(c0[0], c0[1]), 0xfffffffeu), x1 = min(pk(c0[2], c0[3]), 0xfffffffeu);        \
      const unsigned y0 = min(pk(c1[0], c1[1]), 0xfffffffeu), y1 = min(pk(c1[2], c1[3]), 0xfffffffeu);        \
      auto r0 = __builtin_amdgcn_permlane16_swap(x0, y0, false, false);                                       \
      auto r1 = __builtin_amdgcn_permlane16_swap(x1, y1, false, false);                                       \
      R[k] = v4u{r0[0], r1[0], r0[1], r1[1]};                                                                 \
    }                                                                                                         \
    v4u A, B;                                                                                                 \
    _Pragma("unroll") for (int e = 0; e < 4; ++e) {                                                           \
      A[e] = (unsigned)__builtin_amdgcn_update_dpp((int)R[0][e], (int)R[1][e], ROR8, 0xf, 0xc, false);        \
      B[e] = (unsigned)__builtin_amdgcn_update_dpp((int)R[1][e], (int)R[0][e], ROR8, 0xf, 0x3, false);        \
    }                                                                                                         \
    st_sc1(rs, op + 128 * q, A);                                                                              \
    st_sc1(rs, op2 + 128 * q, B);                                                                             \
  }
      __builtin_amdgcn_sched_barrier(0);
      f4 d[4] = {z, z, z, z};
#pragma unroll
      for (int u = 0; u < 4; ++u)
#pragma unroll
        for (int k = 0; k < 4; ++k) d[k] = mfma32(fr[16 + 4 * k + u], pf[u], d[k]);
      PV_OUT(c, 0);
#pragma unroll
      for (int i = 0; i < 16; ++i) {
        __builtin_amdgcn_sched_group_barrier(0x8, 1, 0);
        __builtin_amdgcn_sched_group_barrier(0x2, PV_MIX, 0);
      }
      __builtin_amdgcn_sched_barrier(0);
      PV_OUT(d, 1);
      FLAG_SET(v);
      if (GQ && glu) {
        VMWAIT(4);
        FLAG_SET(12 + v);
      }
      MARK(3);
      STAMP(6);
    }
    }
      if (glu && w < 4) {
        v4u af[8], xf[8];
#pragma unroll
        for (int s = 0; s < 8; ++s) xf[s] = *(LAS v4u*)(px + 512 * (w >> 1) + 64 * s);
        unsigned oog = WS_OPG + (8 * w + (col >> 1)) * OGP + sl * 128 + ((col & 1) * 4 + g4) * 16;
        PIN(oog);
        FLAG_WAIT4(0);
#pragma unroll 1
        for (int i = 0; i < 4; ++i) dma16<NT_G>(wd, (unsigned)(((16 * sl + 4 * w + i) * DM + 8 * l) * 2), smb + L_GD + (4 * w + i) * CP);
#pragma unroll 1
        for (int i = GQ; i < 4; ++i) {
          const int k = 4 * ((a + i) & 3) + w;
          const unsigned og = (unsigned)(((32 * k + (l >> 1)) * FF + 16 * sl + 8 * (l & 1)) * 2);
          dma16(wg, og, smb + L_GG + k * 1024);
          dma16(wu, og, smb + L_GG + 16384 + k * 1024);
        }
        unsigned pg = L_GG + (w & 1) * 16384 + 32 * (256 * (w >> 1) + 8 * g4 + tq) + 8 * tp;
        unsigned pd = L_GD + (4 * g4 + tq) * CP + 256 * w + 8 * tp, pgw = L_GR + ((w & 1) * 2 + (w >> 1)) * 1024 + l * 16;
        PIN(pg); PIN(pd); PIN(pgw);
        VMWAIT(0);
        FLAG_SET(4 + w);
        FLAG_WAIT4(1);
        if (GQ) FLAG_WAIT4(3);
        STAMP(7);
        f4 acc = z;
#pragma unroll
        for (int s = 0; s < 8; ++s) af[s] = trrd2(sm + pg + 32 * 32 * s, sm + pg + 32 * (32 * s + 4));
        __builtin_amdgcn_sched_barrier(0);
        v2u dn[8];
#pragma unroll
        for (int s = 0; s < 8; ++s) {
          acc = mfma32(af[s], xf[s], acc);
          dn[s] = trrd(sm + pd + 32 * s);
          __builtin_amdgcn_sched_barrier(0);
        }
        *(LAS f4*)(sm + pgw) = acc;
        FLAG_SET(8 + w);
        FLAG_WAIT4(2);
        LAS char* pr = sm + L_GR + l * 16;
        const f4 gx = *(LAS f4*)(pr) + *(LAS f4*)(pr + 1024), ux = *(LAS f4*)(pr + 2048) + *(LAS f4*)(pr + 3072);
        float hh[4];
#pragma unroll
        for (int e = 0; e < 4; ++e) hh[e] = ux[e] * __builtin_amdgcn_rcpf(1.f + __builtin_amdgcn_exp2f(-1.4426950408889634f * gx[e]));
        const v2u hb = {pk(hh[0], hh[1]), pk(hh[2], hh[3])};
        f4 d = z;
#pragma unroll
        for (int t = 0; t < 8; ++t) d = mfma16(dn[t], (col >> 1) == t ? hb : v2u{0u, 0u}, d);
        v4u dv = {pk(d[0], d[1]), T, pk(d[2], d[3]), T};
        PIN(dv);
        st_sc1(rs, oog, dv);
        MARK(4);
        STAMP(9);
      }
    if (a >= 12) {
      const unsigned m = (col & 1) ? 0x3f800000u : 0x3f80u;
      v4u sel = {col < 2 ? m : 0u, (col >> 1) == 1 ? m : 0u, col < 2 ? m : 0u, (col >> 1) == 1 ? m : 0u};
      PIN(sel);
      if (w == 0) {
        unsigned ot = WS_OPT + rd * 8192 + 4096 + l * 16, oy = (l >> 2) * 1024 + rd * 32 + (l & 3) * 8, pr = smb + L_RR + (l & 7) * 16;
        PIN(ot); PIN(oy); PIN(pr);
        v4u p[8];
        touch(out, (unsigned)((l & 1) * 1024 + rd * 32), smb + L_JUNK);
        HOLD_UNTIL(R_START);
        SWEEP_4(p, ot, -4096, -3072, -2048, -1024, "");
        SWEEP_4(p + 4, ot, 0, 1024, 2048, 3072, "");
        PIN4(p);
        PIN4(p + 4);
        for (unsigned spin = 0;; ++spin) {
          if (!(TAGS_BAD4(p) | TAGS_BAD4(p + 4)) || spin > SPIN_CAP) break;
          if (TAG_BAD(p[0])) RELOAD(p[0], ot, -4096);
          if (TAG_BAD(p[1])) RELOAD(p[1], ot, -3072);
          if (TAG_BAD(p[2])) RELOAD(p[2], ot, -2048);
          if (TAG_BAD(p[3])) RELOAD(p[3], ot, -1024);
          if (TAG_BAD(p[4])) RELOAD(p[4], ot, 0);
          if (TAG_BAD(p[5])) RELOAD(p[5], ot, 1024);
          if (TAG_BAD(p[6])) RELOAD(p[6], ot, 2048);
          if (TAG_BAD(p[7])) RELOAD(p[7], ot, 3072);
          PIN4(p);
          PIN4(p + 4);
        }
        STAMP(8);
        f4 acc = z;
#pragma unroll
        for (int i = 0; i < 4; ++i) acc = mfma32(sel, v4u{p[2 * i].x, p[2 * i].z, p[2 * i + 1].x, p[2 * i + 1].z}, acc);
        f4 rest[6];
        for (unsigned spin = 0; spin < SPIN_CAP; ++spin) {
          v4u f0;
          v2u f1;
          asm volatile("ds_read_b128 %0, %8\n\tds_read_b64 %1, %8 offset:16\n\tds_read_b128 %2, %9\n\tds_read_b128 %3, %9 offset:128\n\t"
                       "ds_read_b128 %4, %9 offset:256\n\tds_read_b128 %5, %9 offset:384\n\tds_read_b128 %6, %9 offset:512\n\t"
                       "ds_read_b128 %7, %9 offset:640\n\ts_waitcnt lgkmcnt(0)"
                       : "=&v"(f0), "=&v"(f1), "=&v"(rest[0]), "=&v"(rest[1]), "=&v"(rest[2]), "=&v"(rest[3]), "=&v"(rest[4]), "=&v"(rest[5])
                       : "v"(smb + L_FL + 64), "v"(pr)
                       : "memory");
          if (__builtin_amdgcn_readfirstlane(f0.x & f0.y & f0.z & f0.w & f1.x & f1.y)) break;
        }
        const f4 gl = ((rest[0] + rest[1]) + (rest[2] + rest[3])) + (rest[4] + rest[5]);
#pragma unroll
        for (int e = 0; e < 4; ++e) acc[e] = (acc[e] + dpp<ROR8>(acc[e])) + gl[e];
        if (l < 8) asm volatile("global_store_dwordx2 %0, %1, %2 sc1" ::"v"(oy), "v"(v2u{pk(acc[0], acc[1]), pk(acc[2], acc[3])}), "s"(out) : "memory");
      } else if (w < 7) {
        const bool last = w == 6;
        unsigned og = WS_OPG + rd * OGP + (w - 1) * 2048 + (last ? min(l, 47) : l) * 16, og2 = last ? og : og + 1024;
        const v2u xr = *(const v2u*)((const char*)x + ((l >> 2) & 1) * 1024 + rd * 32 + (l & 3) * 8);
        unsigned m0 = (last && l >= 48) ? 0u : ~0u, m1 = last ? 0u : ~0u;
        f4 xv = {blo(xr.x), bhi(xr.x), blo(xr.y), bhi(xr.y)};
        if (!last) xv = z;
        PIN(og); PIN(og2); PIN(m0); PIN(m1); PIN(xv);
        v4u p0, p1;
        HOLD_UNTIL(R_START);
        for (unsigned spin = 0;; ++spin) {
          SWEEP_2(p0, p1, og, og2, WAIT0);
          if (!(TAG_BAD(p0) | TAG_BAD(p1)) || spin > SPIN_CAP) break;
        }
        STAMP(8);
        f4 acc = mfma32(sel, v4u{p0.x & m0, p0.z & m0, p1.x & m1, p1.z & m1}, z);
#pragma unroll
        for (int e = 0; e < 4; ++e) acc[e] = (acc[e] + dpp<ROR8>(acc[e])) + xv[e];
        if (l < 8) *(LAS f4*)(sm + L_RR + (w - 1) * 128 + l * 16) = acc;
        FLAG_SET(15 + w);
      }
      else if (rd == 0) {
        const unsigned ol0 = WS_LSE + (l * 16 + ((ROT * l) & 7)) * 128 + 4;
        unsigned t0, t1;
        for (unsigned spin = 0;; ++spin) {
          __builtin_amdgcn_s_sleep(32);
          asm volatile("global_load_dword %0, %2, %3 sc1\n\tglobal_load_dword %1, %2, %3 offset:1024 sc1" WAIT0
                       : "=&v"(t0), "=&v"(t1)
                       : "v"(ol0), "s"(ws)
                       : "memory");
          if (!__builtin_amdgcn_ballot_w64((t0 & t1 & T) != (t0 | t1 | T)) || spin > SPIN_CAP) break;
        }
        if (l == 0) __builtin_amdgcn_raw_buffer_store_b32(T, rs, WS_E, 0, AUX_SC1);
      }
      MARK(5);
      STAMP(9);
    }
    STAMP_FLUSH();
    __builtin_amdgcn_endpgm();
  }
  {
    const int h = HMAP ? xcd + 8 * (r >> 2) : 4 * xcd + (r >> 2), j = r & 3, ca = j >> 1, mb = j & 1;
    const int fl = tq | ((g4 & 1) << 2);
    gld4_sc1(ep, ws, 0u);
    if (__builtin_expect(w < 4, 1)) {
      const unsigned vq = (unsigned)(((128 * j + g4) * 4096 + h * DH + 8 * (col ^ ((g4 | ((w & 2) << 1)) << 1))) * 2);
#pragma unroll 1
      for (int k = w; k < 32; k += 4) dma16<NT_WQ>(wq, vq + (unsigned)k * 32768u, smb + L_WQ + k * 1024);
    } else if (w < 6) {
      VMWAIT(1);
      LAYERNORM();
      MARK(0);
    } else {
      __builtin_amdgcn_s_sleep(UK_DELAY);
      const unsigned vu = (unsigned)((((128 * j + g4) * NH + h) * DH) * 2);
#pragma unroll 1
      for (int k = w - 6; k < 32; k += 2) dma16<NT_WUK>(wuk, vu + (unsigned)k * 32768u + 16u * (col ^ ((4 * k + g4) & 15)), smb + L_WUK + k * 1024);
      MARK(1);
    }
    unsigned p = L_WQ + 256 * (8 * g4 + tq) + 16 * ((2 * w + (tp >> 1)) ^ (fl << 1)) + 8 * (tp & 1);
    unsigned oqp = WS_QP + ((h * 4 + j) * 8 + w) * 128 + col * 64 + g4 * 16;
    unsigned pxj = L_X + (col & 1) * XP + 16 * g4 + 256 * j;
    unsigned o = WS_QP + (h * 4 + (w & 3)) * 1024 + l * 16;
    unsigned pqw = smb + L_CC + (2 * (w & 3) + ((l >> 2) & 1)) * SP + (l >> 3) * 32 + (l & 3) * 8, pqr = L_CC + (col & 7) * SP + 16 * g4;
    unsigned oqa = WS_QA + ((col & 1) * NH + h) * 2048 + (64 * j + 16 * w + 8 * (col >> 1) + 2 * g4) * 8;
    unsigned pu = L_WUK + 256 * (32 * w + col) + 16 * (g4 ^ col);
    PIN(p); PIN(oqp); PIN(pxj); PIN(o); PIN(oqa); PIN(pu); PIN(pqw); PIN(pqr);
    {
    {
      if (w < 6) VMWAIT(0);
      else VMWAIT(16);
      EPOCH();
      STAMP(1);
      BAR();
      STAMP(2);
    }
    {
      v4u af[4], xf[4];
#pragma unroll
      for (int s = 0; s < 4; ++s) {
        af[s] = trrd2(sm + p + 256 * 32 * s, sm + p + 256 * (32 * s + 4));
        xf[s] = *(LAS v4u*)(sm + pxj + 64 * s);
      }
      __builtin_amdgcn_sched_barrier(0);
      f4 acc = z;
#pragma unroll
      for (int s = 0; s < 4; ++s) acc = mfma32(af[s], xf[s], acc);
      if (col < 2) st_sc1(rs, oqp, v4u{pk(acc[0], acc[1]), T, pk(acc[2], acc[3]), T});
    }
    STAMP(3);
    if (w >= 6) VMWAIT(1);
    if (w >= 4) {
      BAR();
      BAR();
    } else {
      v4u a, uk[8], qf[4];
      {
        __builtin_amdgcn_s_sleep(H1_DELAY);
        asm volatile(LD16(0, 1, 2) " sc1" : "=&v"(a) : "v"(o), "s"(ws) : "memory");
        BAR();
      }
#pragma unroll
      for (int s = 0; s < 4; ++s) {
        uk[s] = *(LAS v4u*)(sm + (pu ^ (64 * s)));
        uk[4 + s] = *(LAS v4u*)(sm + (pu ^ (64 * s)) + 4096);
      }
      VMWAIT(0);
      PIN(a);
      for (unsigned spin = 0;; ++spin) {
        if (!__builtin_amdgcn_ballot_w64((T & a.y & a.w) != (T | a.y | a.w)) || spin > SPIN_CAP) break;
        asm volatile(LD16(0, 1, 2) " sc1" WAIT0 : "+v"(a) : "v"(o), "s"(ws) : "memory");
      }
      asm volatile("ds_write2_b32 %0, %1, %2 offset0:0 offset1:1" ::"v"(pqw), "v"(a.x), "v"(a.z) : "memory");
      STAMP(4);
      BAR();
#pragma unroll
      for (int s = 0; s < 4; ++s) qf[s] = *(LAS v4u*)(sm + pqr + 64 * s);
      __builtin_amdgcn_sched_barrier(0);
      f4 a0 = z, a1 = z;
#pragma unroll
      for (int s = 0; s < 4; ++s) {
        a0 = mfma32(uk[s], qf[s], a0);
        a1 = mfma32(uk[4 + s], qf[s], a1);
      }
      float q[4];
#pragma unroll
      for (int e = 0; e < 4; ++e) {
        const float mine = (col & 2) ? a1[e] : a0[e], other = (col & 2) ? a0[e] : a1[e];
        q[e] = mine + dpp<QP_2301>(other);
        q[e] += dpp<ROR4>(q[e]);
      }
      if (col < 4) st_sc1(rs, oqa, v4u{pk(q[0], q[1]), T, pk(q[2], q[3]), T});
    }
    }
    STAMP(5);
    PROBE_T(T);
    BAR();
    HOLD_UNTIL(T_START);
    {
      const unsigned vv = (unsigned)((((256 * ca + g4) * NH + h) * DH + 8 * (col ^ ((g4 | ((w & 2) << 1)) << 1))) * 2);
      const unsigned vo = (unsigned)(((h * DH + (l >> 5)) * DM + 256 * mb) * 2) + 16u * ((l & 31) ^ (((l >> 5) | ((w & 1) << 1) | (w & 4)) << 1));
      int kv = w + 32 * mb, ko = w + 32 * ca;
#pragma unroll 1
      for (int hf = 0; hf < 2; ++hf) {
#pragma unroll 1
        for (int i = 0; i < 4; ++i, kv = (kv + 8) & 63) dma16<NT_T>(wuv, vv + (unsigned)kv * 32768u, smb + L_WUV + kv * 1024);
#pragma unroll 1
        for (int i = 0; i < 4; ++i, ko = (ko + 8) & 63) dma16<NT_T>(wo, vo + (unsigned)ko * 2048u, smb + L_WO + ko * 1024);
        if (hf == 0) asm volatile("s_barrier" ::: "memory");
      }
    }
    const int tb = w >> 2, nq = w & 3;
    o = WS_PART + (((h * 2 + ca) * 2 + tb) * 32 + 8 * nq + g4) * 512 + 16 * col;
    unsigned pw = L_W + (tb * 32 + 8 * nq + g4) * 4;
    unsigned pv = L_WUV + 256 * (8 * g4 + tq) + 16 * ((2 * w + (tp >> 1)) ^ (fl << 1)) + 8 * (tp & 1);
    unsigned po = L_WO + 512 * (8 * g4 + tq) + 16 * ((4 * w + (tp >> 1)) ^ (fl << 1)) + 8 * (tp & 1);
    unsigned pcw = L_CC + (nq * 2 + tb) * SP2 + 16 * col + 8 * g4, pcr = L_CC + (col & 7) * SP2 + 16 * g4;
    unsigned ptw = L_CT + col * SP + (16 * w + 4 * g4) * 2, ptr = L_CT + (col & 1) * SP + 16 * g4;
    unsigned oo = WS_OPT + ((16 * mb + 2 * w + (col >> 1)) * 64 + ((2 * h + ca + ROT * 4 * (2 * w + (col >> 1))) & 63)) * 128 + ((col & 1) * 4 + g4) * 16;
    v4u mk;
#pragma unroll
    for (int d = 0; d < 4; ++d) mk[d] = (col >> 1) == d ? ((col & 1) ? 0xffff0000u : 0xffffu) : 0u;
    PIN(o); PIN(pw); PIN(pv); PIN(po); PIN(pcw); PIN(pcr); PIN(ptw); PIN(ptr); PIN(oo); PIN(mk);
    STAMP(11);
        if (w == 7) {
      const unsigned ol = WS_LSE + (HMAP ? ((l * 2 + (r >> 3)) * 8 + ((xcd + ROT * l) & 7)) * 128 + ((r >> 2) & 1) * 8 : (l * 8 + xcd) * 128 + (r >> 2) * 8);
      v2u la;
      UNDEF(la);
      for (unsigned spin = 0;; ++spin) {
        asm volatile("global_load_dwordx2 %0, %1, %2 sc1" WAIT0 : "+v"(la) : "v"(ol), "s"(ws) : "memory");
        if (!__builtin_amdgcn_ballot_w64(la.y != T) || spin > SPIN_CAP) break;
      }
      float M = asf(la.x);
      M = fmax2(M, dpp<ROR8>(M));
      M = fmax2(M, dpp<ROR4>(M));
      M = fmax2(M, dpp<ROR2>(M));
      M = fmax2(M, dpp<ROR1>(M));
      M = sw16_max(M);
      const float e = __builtin_amdgcn_exp2f(asf(la.x) - M);
      float L = e + dpp<ROR8>(e);
      L += dpp<ROR4>(L);
      L += dpp<ROR2>(L);
      L += dpp<ROR1>(L);
      L = sw16_add(L, L);
      const float wt = e * __builtin_amdgcn_rcpf(L);
      *(LAS unsigned*)(sm + L_W + 4 * ((l & 32) | ((l + ROT * 4 * (h & 7)) & 31))) = pk(wt, wt);
      STAMP(7);
    }
    v4u a[4];
    {
      if (LSE_PIPE && w == 7) VMWAIT(2);
      else VMWAIT(0);
      STAMP(6);
      BAR();
      SWEEP_4(a, o, 0, 256, 2048, 2304, "");
    }
    v4u A0 = mk & *(LAS unsigned*)(sm + pw), A1 = mk & *(LAS unsigned*)(sm + pw + 16);
    v4u av[8], ao[8];
#pragma unroll
    for (int s = 0; s < 8; ++s) av[s] = trrd2(sm + pv + 256 * 32 * s, sm + pv + 256 * (32 * s + 4));
#pragma unroll
    for (int t = 0; t < 2; ++t)
#pragma unroll
      for (int s = 0; s < 4; ++s) ao[4 * t + s] = trrd2(sm + (po ^ (32 * t)) + 512 * 32 * s, sm + (po ^ (32 * t)) + 512 * (32 * s + 4));
    VMWAIT(0);
    asm volatile("" : "+v"(a[0]), "+v"(a[1]), "+v"(a[2]), "+v"(a[3]), "+v"(A0), "+v"(A1)::"memory");
    for (unsigned spin = 0;; ++spin) {
      unsigned mx = 0u;
#pragma unroll
      for (int i = 0; i < 4; ++i) mx = max(max(mx, max(a[i].x, a[i].y)), max(a[i].z, a[i].w));
      if (!__builtin_amdgcn_ballot_w64(mx == ~0u) || spin > SPIN_CAP) break;
      if (HOLE(a[0])) RELOAD(a[0], o, 0);
      if (HOLE(a[1])) RELOAD(a[1], o, 256);
      if (HOLE(a[2])) RELOAD(a[2], o, 2048);
      if (HOLE(a[3])) RELOAD(a[3], o, 2304);
      PIN4(a);
    }
    STAMP(8);
    {
      f4 c0 = mfma32(A0, a[0], z), c1 = mfma32(A0, a[1], z);
      c0 = mfma32(A1, a[2], c0);
      c1 = mfma32(A1, a[3], c1);
      if (g4 < 2) {
        *(LAS v2u*)(sm + pcw) = v2u{pk(c0[0], c0[1]), pk(c0[2], c0[3])};
        *(LAS v2u*)(sm + pcw + 256) = v2u{pk(c1[0], c1[1]), pk(c1[2], c1[3])};
      }
    }
    BAR();
    {
      f4 acc = z;
      v4u bf[8];
#pragma unroll
      for (int s = 0; s < 8; ++s) bf[s] = *(LAS v4u*)(sm + pcr + 64 * s);
      __builtin_amdgcn_sched_barrier(0);
#pragma unroll
      for (int s = 0; s < 8; ++s) acc = mfma32(av[s], bf[s], acc);
#pragma unroll
      for (int e = 0; e < 4; ++e) {
        acc[e] += dpp<QP_2301>(acc[e]);
        acc[e] += dpp<ROR4>(acc[e]);
      }
      if (col < 2) *(LAS v2u*)(sm + ptw) = v2u{pk(acc[0], acc[1]), pk(acc[2], acc[3])};
    }
    BAR();
    {
      f4 c0 = z, c1 = z;
      v4u bf[4];
#pragma unroll
      for (int s = 0; s < 4; ++s) bf[s] = *(LAS v4u*)(sm + ptr + 64 * s);
      __builtin_amdgcn_sched_barrier(0);
#pragma unroll
      for (int s = 0; s < 4; ++s) {
        c0 = mfma32(ao[s], bf[s], c0);
        c1 = mfma32(ao[4 + s], bf[s], c1);
      }
      if (col & 2) c0 = c1;
      if (col < 4) st_sc1(rs, oo, v4u{pk(c0[0], c0[1]), T, pk(c0[2], c0[3]), T});
    }
    MARK(2);
    STAMP(9);
  }
  STAMP_FLUSH();
}

#define WS_END 0
#define WS_BASE (WS_END ? ((ws_size - (size_t)WS_END * 1048576) & ~(size_t)4095) : 0)
extern "C" void kernel_launch(void* const* d_in, const int* in_sizes, int n_in, void* d_out, int out_size, void* d_ws, size_t ws_size,
                              hipStream_t stream) {
  mla_glu_decode<<<256, 512, 0, stream>>>((const u16*)d_in[0], (const u16*)d_in[1], (const u16*)d_in[2], (const u16*)d_in[3],
                                          (const u16*)d_in[4], (const u16*)d_in[5], (const u16*)d_in[7], (const u16*)d_in[8],
                                          (const u16*)d_in[9], (u16*)d_out, (char*)d_ws + WS_BASE);
}
```

```cpp
#include <hip/hip_runtime.h>
#include <stdint.h>

#define GAS __attribute__((address_space(1)))
#define LAS __attribute__((address_space(3)))
typedef unsigned short u16;
typedef unsigned v4u __attribute__((ext_vector_type(4)));
typedef unsigned v2u __attribute__((ext_vector_type(2)));
typedef float f4 __attribute__((ext_vector_type(4)));
typedef float f2 __attribute__((ext_vector_type(2)));
typedef __bf16 bf8 __attribute__((ext_vector_type(8)));
typedef __bf16 bf2 __attribute__((ext_vector_type(2)));
typedef short s4 __attribute__((ext_vector_type(4)));

#define ROT 1
namespace {
constexpr unsigned NOWHERE = 0x7ff00000u;
constexpr int NH = 32, SK = 4096, DH = 128, DM = 512, DC = 512, FF = 1376;
constexpr int NSL = FF / 16;
constexpr int WS_E = 0;
constexpr int WS_QP = 4096;
constexpr int WS_QA = WS_QP + NH * 4 * 1024;
constexpr int WS_LSE = WS_QA + 2 * NH * 2048;
constexpr int WS_GO = WS_LSE + 2 * 32 * 16 * 128;
constexpr int WS_OPT = WS_GO + 2 * 32 * 16 * 128;
constexpr int WS_OPG = WS_OPT + 32 * 64 * 128;
constexpr int OGP = 16384 + ROT * 384;
constexpr int WS_PART = (WS_OPG + 32 * OGP + 4095) / 4096 * 4096;
constexpr int WS_BYTES = WS_PART + NH * 2 * 2 * 32 * 512;
constexpr int WS_STAMP = 7 << 19;
constexpr int CP = 1056;
constexpr int XP = 1088, SP = 288, SP2 = 544;
constexpr int L_C = 0, L_Q = 128 * CP, L_P = L_Q + 16 * CP, L_X = L_P + 4096, L_MX = L_X + 2 * XP, L_RR = L_MX + 256,
              L_JUNK = L_RR + 1024, L_FL = L_JUNK + 256, L_END = L_FL + 128;
static_assert(L_END <= 163840, "LDS");
constexpr int L_GG = 0, L_GD = 32768, L_GR = 65536;
constexpr int L_WUV = 0, L_WO = 65536, L_WQ = 0, L_WUK = 32768, L_CC = 131072, L_CT = L_CC + 8 * SP2, L_W = L_CT + 2 * SP;
#define UK_DELAY 20
#define H1_DELAY 12
#define T_START 370
#define CODE_TOUCH 1
#define NT_CKV 0
#define NT_WQ 0
#define NT_WUK 0
#define NT_T 0
#define NT_G 0
#define A_START 70
#define GLU_EARLY 0
#define G_START 0
#define C_START 0
#define LSE_LATE 0
#define LSE_WAVE 3
#define DATA_DELAY 0
#define NEWRED 1
#define R_START 0
#define G_TOUCH 4
#define LSE_PIPE 0
#define PV_MIX 4
#define LSE_QK 1
#define HMAP 1
#define GQ 1
#define DOWN_FIRST 1
#define EARLY_SWEEP 1
#define GO_DELAY 0
#define OUT_TOUCH 1
#define OUT_SC1 1
#define BUMP_EARLY 1
#define G2_DELAY 0
#define UK_FIRST 6
#define UK_TOUCH 1
#define PIECEWISE 1
#define PF_GLU 1
constexpr unsigned SPIN_CAP = 1u << 20;
constexpr int AUX_SC1 = 16;

#define STAMP_W 0
#define STAMP(i)
#define STAMP_FLUSH()
#define PROBE_T(T_)

__device__ __forceinline__ f4 mfma32(v4u a, v4u b, f4 c) {
  return __builtin_amdgcn_mfma_f32_16x16x32_bf16(__builtin_bit_cast(bf8, a), __builtin_bit_cast(bf8, b), c, 0, 0, 0);
}
__device__ __forceinline__ f4 mfma16(v2u a, v2u b, f4 c) {
  return __builtin_amdgcn_mfma_f32_16x16x16bf16_1k(__builtin_bit_cast(s4, a), __builtin_bit_cast(s4, b), c, 0, 0, 0);
}
__device__ __forceinline__ unsigned pk(float lo, float hi) {
  f2 x = {lo, hi};
  return __builtin_bit_cast(unsigned, __builtin_convertvector(x, bf2));
}
__device__ __forceinline__ float asf(unsigned u) { return __builtin_bit_cast(float, u); }
__device__ __forceinline__ float blo(unsigned u) { return __builtin_bit_cast(float, u << 16); }
__device__ __forceinline__ float bhi(unsigned u) { return __builtin_bit_cast(float, u & 0xffff0000u); }
template <int CTRL> __device__ __forceinline__ float dpp(float v) {
  return __builtin_bit_cast(float, __builtin_amdgcn_update_dpp(0, __builtin_bit_cast(int, v), CTRL, 0xf, 0xf, true));
}
__device__ __forceinline__ float fmax2(float a, float b) {
#pragma float_control(precise, off)
  return __builtin_fmaxf(a, b);
}
typedef unsigned short h2 __attribute__((ext_vector_type(2)));
__device__ __forceinline__ unsigned scale_dn(unsigned p, unsigned kk) {
  return __builtin_bit_cast(unsigned, __builtin_elementwise_sub_sat(__builtin_bit_cast(h2, p), __builtin_bit_cast(h2, kk)));
}
#define PIN(x) asm volatile("" : "+v"(x))
constexpr int ROR1 = 0x121, ROR2 = 0x122, ROR4 = 0x124, ROR8 = 0x128, QP_2301 = 0x4e, QP_2222 = 0xaa;
__device__ __forceinline__ float sw32_add(float a, float b) {
  auto r = __builtin_amdgcn_permlane32_swap(__builtin_bit_cast(unsigned, a), __builtin_bit_cast(unsigned, b), false, false);
  return asf(r[0]) + asf(r[1]);
}
__device__ __forceinline__ float sw16_add(float a, float b) {
  auto r = __builtin_amdgcn_permlane16_swap(__builtin_bit_cast(unsigned, a), __builtin_bit_cast(unsigned, b), false, false);
  return asf(r[0]) + asf(r[1]);
}
__device__ __forceinline__ float vmax(unsigned a, unsigned b) {
  float r;
  asm("v_max_f32 %0, %1, %2" : "=v"(r) : "v"(a), "v"(b));
  return r;
}
__device__ __forceinline__ float vmax3(float a, float b, float c) {
  float r;
  asm("v_max3_f32 %0, %1, %2, %3" : "=v"(r) : "v"(a), "v"(b), "v"(c));
  return r;
}
__device__ __forceinline__ float sw32_max(float a) {
  auto r = __builtin_amdgcn_permlane32_swap(__builtin_bit_cast(unsigned, a), __builtin_bit_cast(unsigned, a), false, false);
  return vmax(r[0], r[1]);
}
__device__ __forceinline__ float sw16_max(float a) {
  auto r = __builtin_amdgcn_permlane16_swap(__builtin_bit_cast(unsigned, a), __builtin_bit_cast(unsigned, a), false, false);
  return vmax(r[0], r[1]);
}
__device__ __forceinline__ float wave_sum(float v) {
  v += dpp<ROR8>(v);
  v += dpp<ROR4>(v);
  v += dpp<ROR2>(v);
  v += dpp<ROR1>(v);
  v = sw16_add(v, v);
  return sw32_add(v, v);
}
__device__ __forceinline__ v2u trrd(LAS char* p) {
  return __builtin_bit_cast(v2u, __builtin_amdgcn_ds_read_tr16_b64_v4i16((LAS s4*)p));
}
__device__ __forceinline__ v4u trrd2(LAS char* p0, LAS char* p1) {
  v2u a = trrd(p0), b = trrd(p1);
  return v4u{a.x, a.y, b.x, b.y};
}
#define BAR() asm volatile("s_waitcnt lgkmcnt(0)\n\ts_barrier" ::: "memory")
#define VMWAIT(k) asm volatile("s_waitcnt vmcnt(%0)" ::"n"(k) : "memory")
__device__ __forceinline__ void gld16(v4u& d, const void* base, unsigned off) {
  asm volatile("global_load_dwordx4 %0, %1, %2" : "=v"(d) : "v"(off), "s"(base) : "memory");
}
__device__ __forceinline__ void gld4_sc1(unsigned& d, const void* base, unsigned off) {
  asm volatile("global_load_dword %0, %1, %2 sc1" : "=v"(d) : "v"(off), "s"(base) : "memory");
}
template <int NT = 0> __device__ __forceinline__ void touch(const void* base, unsigned off, unsigned lds) {
  if (NT) asm volatile("s_mov_b32 m0, %2\n\ts_nop 0\n\tglobal_load_lds_dword %0, %1 nt" ::"v"(off), "s"(base), "s"(lds) : "memory");
  else asm volatile("s_mov_b32 m0, %2\n\ts_nop 0\n\tglobal_load_lds_dword %0, %1" ::"v"(off), "s"(base), "s"(lds) : "memory");
}
__device__ __forceinline__ void touch_sc1(const void* base, unsigned off, unsigned lds) {
  asm volatile("s_mov_b32 m0, %2\n\ts_nop 0\n\tglobal_load_lds_dword %0, %1 sc1" ::"v"(off), "s"(base), "s"(lds) : "memory");
}
#define LD16(i, a, o) "global_load_dwordx4 %" #i ", %" #a ", %" #o
#define SWEEP_4(q, o, o0, o1, o2, o3, W)                                                                                     \
  asm volatile(LD16(0, 4, 5) " offset:" #o0 " sc1\n\t" LD16(1, 4, 5) " offset:" #o1 " sc1\n\t" LD16(2, 4, 5) " offset:" #o2         \
               " sc1\n\t" LD16(3, 4, 5) " offset:" #o3 " sc1" W                                                               \
               : "=&v"((q)[0]), "=&v"((q)[1]), "=&v"((q)[2]), "=&v"((q)[3])                                                  \
               : "v"(o), "s"(ws)                                                                                             \
               : "memory")
#define RESWEEP_4(q, o, o0, o1, o2, o3)                                                                                      \
  asm volatile(LD16(0, 4, 5) " offset:" #o0 " sc1\n\t" LD16(1, 4, 5) " offset:" #o1 " sc1\n\t" LD16(2, 4, 5) " offset:" #o2         \
               " sc1\n\t" LD16(3, 4, 5) " offset:" #o3 " sc1\n\ts_waitcnt vmcnt(0)"                                            \
               : "+v"((q)[0]), "+v"((q)[1]), "+v"((q)[2]), "+v"((q)[3])                                                      \
               : "v"(o), "s"(ws)                                                                                             \
               : "memory")
#define RELOAD(q, o, off) asm volatile(LD16(0, 1, 2) " offset:" #off " sc1" : "+v"(q) : "v"(o), "s"(ws) : "memory")
#define PIN4(q) asm volatile("s_waitcnt vmcnt(0)" : "+v"((q)[0]), "+v"((q)[1]), "+v"((q)[2]), "+v"((q)[3])::"memory")
#define TAG_BAD(q) __builtin_amdgcn_ballot_w64((T & (q).y & (q).w) != (T | (q).y | (q).w))
#define HOLE(q) __builtin_amdgcn_ballot_w64(max(max((q).x, (q).y), max((q).z, (q).w)) == ~0u)
#define MAX4(q) max(max((q).x, (q).y), max((q).z, (q).w))
#define HOLES4(q) __builtin_amdgcn_ballot_w64(max(max(MAX4((q)[0]), MAX4((q)[1])), max(MAX4((q)[2]), MAX4((q)[3]))) == ~0u)
#define TRETRY()                          \
  if (HOLE(a[0])) RELOAD(a[0], o, 0);     \
  if (HOLE(a[1])) RELOAD(a[1], o, 256);   \
  if (HOLE(a[2])) RELOAD(a[2], o, 2048);  \
  if (HOLE(a[3])) RELOAD(a[3], o, 2304);  \
  PIN4(a)
#define SWEEP_L(W)                                                                                                          \
  asm volatile("global_load_dwordx2 %0, %3, %5 sc1\n\tglobal_load_dwordx2 %1, %4, %5 sc1\n\t"                              \
               "global_load_dwordx2 %2, %4, %5 offset:512 sc1" W                                                            \
               : "=&v"(la), "=&v"(l0), "=&v"(l1)                                                                            \
               : "v"(ola), "v"(olo), "s"(ws)                                                                                \
               : "memory")
#define SWEEP_2(p, q, oa, ob, W) \
  asm volatile(LD16(0, 2, 4) " sc1\n\t" LD16(1, 3, 4) " sc1" W : "=&v"(p), "=&v"(q) : "v"(oa), "v"(ob), "s"(ws) : "memory")
#define TAGS_BAD4(q) \
  __builtin_amdgcn_ballot_w64((T & (q)[0].y & (q)[0].w & (q)[1].y & (q)[1].w & (q)[2].y & (q)[2].w & (q)[3].y & (q)[3].w) != \
                              (T | (q)[0].y | (q)[0].w | (q)[1].y | (q)[1].w | (q)[2].y | (q)[2].w | (q)[3].y | (q)[3].w))
#define FLAG_SET(i) asm volatile("ds_write_b32 %0, %1" ::"v"(smb + L_FL + 4 * (i)), "v"(1u) : "memory")
#define FLAG_WAIT4(i)                                                                                     \
  for (unsigned spin_ = 0; spin_ < SPIN_CAP; ++spin_) {                                                   \
    v4u f_;                                                                                               \
    asm volatile("ds_read_b128 %0, %1\n\ts_waitcnt lgkmcnt(0)" : "=v"(f_) : "v"(smb + L_FL + 16 * (i)) : "memory"); \
    if (__builtin_amdgcn_readfirstlane(f_.x & f_.y & f_.z & f_.w)) break;                                 \
  }
#define HOLD_UNTIL(ticks)              \
  asm volatile("" : "+s"(tstart));     \
  while ((unsigned)__builtin_amdgcn_s_memrealtime() - (unsigned)tstart < (ticks)) __builtin_amdgcn_s_sleep(1)
#define MARK(i) asm volatile(".Lmla_e" #i ":")
#define RETRY(bad, again)                                   \
  if (bad) {                                                \
    for (unsigned spin = SPIN_CAP; spin; --spin) {          \
      again;                                                \
      if (!(bad)) break;                                    \
    }                                                       \
  }
#define UNDEF(x) asm volatile("" : "=v"(x))
#define EPOCH() asm volatile("v_add_u32 %0, 1, %1" : "=v"(T) : "v"(ep) : "memory")
#define WAIT0 "\n\ts_waitcnt vmcnt(0)"
template <int NT = 0> __device__ __forceinline__ void dma16(const void* base, unsigned off, unsigned lds) {
  if (NT) asm volatile("s_mov_b32 m0, %2\n\ts_nop 0\n\tglobal_load_lds_dwordx4 %0, %1 nt" ::"v"(off), "s"(base), "s"(lds) : "memory");
  else asm volatile("s_mov_b32 m0, %2\n\ts_nop 0\n\tglobal_load_lds_dwordx4 %0, %1" ::"v"(off), "s"(base), "s"(lds) : "memory");
}
__device__ __forceinline__ v4u ld_sc1(__amdgpu_buffer_rsrc_t rs, int off) {
  return __builtin_amdgcn_raw_buffer_load_b128(rs, off, 0, AUX_SC1);
}
__device__ __forceinline__ v2u ld8_sc1(__amdgpu_buffer_rsrc_t rs, int off) {
  return __builtin_amdgcn_raw_buffer_load_b64(rs, off, 0, AUX_SC1);
}
__device__ __forceinline__ void st_sc1(__amdgpu_buffer_rsrc_t rs, int off, v4u v) {
  __builtin_amdgcn_raw_buffer_store_b128(v, rs, off, 0, AUX_SC1);
}
__device__ __forceinline__ void ln_stats(v4u r, float& mean, float& rstd) {
  float e[8] = {blo(r.x), bhi(r.x), blo(r.y), bhi(r.y), blo(r.z), bhi(r.z), blo(r.w), bhi(r.w)};
  float s = ((e[0] + e[1]) + (e[2] + e[3])) + ((e[4] + e[5]) + (e[6] + e[7]));
  mean = wave_sum(s) * (1.f / DM);
  float q = 0.f;
#pragma unroll
  for (int i = 0; i < 8; ++i) {
    float d = e[i] - mean;
    q = fmaf(d, d, q);
  }
  rstd = __builtin_amdgcn_rsqf(wave_sum(q) * (1.f / DM) + 1e-5f);
}
}

__global__ void __launch_bounds__(512, 2)
mla_glu_decode(const u16* __restrict__ x, const u16* __restrict__ ckv, const u16* __restrict__ wq, const u16* __restrict__ wuk,
               const u16* __restrict__ wuv, const u16* __restrict__ wo, const u16* __restrict__ wg, const u16* __restrict__ wu,
               const u16* __restrict__ wd, u16* __restrict__ out, char* __restrict__ ws) {
  unsigned long long tstart = __builtin_amdgcn_s_memrealtime();
  __builtin_amdgcn_sched_barrier(0);
  __shared__ __attribute__((aligned(16))) char smem_[L_END];
  LAS char* sm = (LAS char*)smem_;
  const unsigned smb = (unsigned)(size_t)sm;
  const int tid = threadIdx.x, l = tid & 63, w = __builtin_amdgcn_readfirstlane(tid >> 6);
  const int col = l & 15, g4 = l >> 4, tq = (l & 15) >> 2, tp = l & 3;
  const int xcd = blockIdx.x & 7, r = blockIdx.x >> 3;
  const __amdgpu_buffer_rsrc_t rs = __builtin_amdgcn_make_buffer_rsrc(ws, 0, WS_BYTES, 0x00020000);
  int bw = (blockIdx.x >> 7) * w;
  asm volatile("" : "+s"(bw));
  if (bw >= 5) {
    const char* pc0;
    unsigned len;
    asm volatile("s_getpc_b64 %0\n.Lmla_pc:\n\ts_mov_b32 %1, .Lmla_e0-.Lmla_pc\n\ts_max_i32 %1, %1, .Lmla_e1-.Lmla_pc\n\t"
                 "s_max_i32 %1, %1, .Lmla_e2-.Lmla_pc\n\ts_max_i32 %1, %1, .Lmla_e3-.Lmla_pc\n\ts_max_i32 %1, %1, .Lmla_e4-.Lmla_pc\n\t"
                 "s_max_i32 %1, %1, .Lmla_e5-.Lmla_pc"
                 : "=s"(pc0), "=s"(len)::"scc");
    touch(pc0, min((unsigned)(64 * (bw - 5) + l) * 128u, len), smb + L_JUNK);
  }
  __builtin_amdgcn_sched_barrier(0);
  const int h = HMAP ? xcd + 8 * ((r >> 2) & 3) : 4 * xcd + ((r >> 2) & 3), j = r & 3;
  unsigned vq = (unsigned)(((128 * j + g4 + 4 * w) * 4096 + h * DH + 8 * (col ^ ((g4 | ((w & 2) << 1)) << 1))) * 2), vx = (w & 1) * 1024 + l * 16;
  unsigned vzero = 0u;
  int lq = smb + L_WQ + w * 1024, lx = smb + L_X + (w & 1) * XP;
  asm volatile("" : "+v"(vq), "+v"(vx), "+v"(vzero), "+s"(lq), "+s"(lx));
  asm volatile("" ::"s"(x), "s"(ckv), "s"(wq), "s"(wuk), "s"(wuv), "s"(wo), "s"(wg), "s"(wu), "s"(wd), "s"(out), "s"(ws));
  STAMP(0);

  unsigned ep, T;
  LAS char* const px = sm + L_X + (col & 1) * XP + 16 * g4;
  if ((w >> 1) == 2) dma16(x, vx, lx);
  const f4 z = {0.f, 0.f, 0.f, 0.f};
#define LAYERNORM()                                                                                                              \
  {                                                                                                                              \
    const v4u xr = *(LAS v4u*)(sm + L_X + (w & 1) * XP + l * 16);                                                                \
    float e[8] = {blo(xr.x), bhi(xr.x), blo(xr.y), bhi(xr.y), blo(xr.z), bhi(xr.z), blo(xr.w), bhi(xr.w)};                       \
    const float mean = wave_sum(((e[0] + e[1]) + (e[2] + e[3])) + ((e[4] + e[5]) + (e[6] + e[7]))) * (1.f / DM);                 \
    float q = 0.f;                                                                                                               \
    _Pragma("unroll") for (int i = 0; i < 8; ++i) {                                                                              \
      e[i] -= mean;                                                                                                              \
      q = fmaf(e[i], e[i], q);                                                                                                   \
    }                                                                                                                            \
    const float rstd = __builtin_amdgcn_rsqf(wave_sum(q) * (1.f / DM) + 1e-5f);                                                  \
    *(LAS v4u*)(sm + L_X + (w & 1) * XP + l * 16) =                                                                              \
        v4u{pk(e[0] * rstd, e[1] * rstd), pk(e[2] * rstd, e[3] * rstd), pk(e[4] * rstd, e[5] * rstd), pk(e[6] * rstd, e[7] * rstd)}; \
  }

  if (__builtin_expect(r >= 16, 0)) {
    const int a = r - 16, b = xcd >> 2, n = 8 * (xcd & 3) + (a >> 1), g = a & 1;
    const int sl = 12 * xcd + a, rd = (4 * xcd + a) & 31;
    const bool glu = a < 12 && sl < NSL;
    const int v = w & 3;
    unsigned op = WS_PART + ((((16 * g + (col & 7)) * 2 + (v >> 1)) * 2 + b) * 32 + ((n + ROT * 4 * (col & 7)) & 31)) * 512 + 256 * (v & 1) +
                  (col >> 3) * 64 + (g4 & 1) * 32 + (g4 >> 1) * 16;
    gld4_sc1(ep, ws, vzero);
    unsigned o = WS_QA + (b * NH + 16 * g) * 2048 + w * 4096 + l * 16, pqs = smb + L_Q + 2 * w * CP + l * 8;
    PIN(o); PIN(pqs);
    {
    {
      const unsigned vc = (unsigned)(((b * SK + 128 * n) * DC + 8 * l) * 2);
      STAMP(10);
      if (w < 4) touch(wuk, (unsigned)((32 * a + 8 * w + (l >> 3)) * 8192 + (xcd + 8 * ((l >> 1) & 3)) * 256 + (l & 1) * 128), smb + L_JUNK);
      HOLD_UNTIL(C_START);
    if (w >= 4) {
#pragma unroll
      for (int q = 0; q < 4; ++q) st_sc1(rs, op + 128 * (q >> 1) + (q & 1) * 8 * 65536, v4u{~0u, ~0u, ~0u, ~0u});
      if (w == 7 && l < 32) *(LAS unsigned*)(sm + L_FL + 4 * l) = 0u;
    }
      HOLD_UNTIL(A_START);
#pragma unroll 1
      for (int q = 0; q < 4; ++q) {
        int row = w + ((64 * g + 32 * q) & 127);
        asm volatile("s_barrier" ::: "memory");
#pragma unroll 1
        for (int i = 0; i < 4; ++i, row += 8) dma16<NT_CKV>(ckv, vc + row * 1024u, smb + L_C + row * CP);
      }
    }
#define CHALF()                               \
  asm volatile("s_barrier" ::: "memory");     \
  SWEEP_4(qg, o, 0, 1024, 2048, 3072, "");    \
  VMWAIT(12);                                 \
  asm volatile("s_barrier" ::: "memory")
#define CLAND() \
  VMWAIT(4);    \
  EPOCH();      \
  PROBE_T(T);   \
  BAR();        \
  STAMP(1)
#define QRETRY()                              \
  if (TAG_BAD(qg[0])) RELOAD(qg[0], o, 0);    \
  if (TAG_BAD(qg[1])) RELOAD(qg[1], o, 1024); \
  if (TAG_BAD(qg[2])) RELOAD(qg[2], o, 2048); \
  if (TAG_BAD(qg[3])) RELOAD(qg[3], o, 3072); \
  PIN4(qg)
#define QGATHER()                                                                                                           \
  VMWAIT(0);                                                                                                      \
  asm volatile("" : "+v"(qg[0]), "+v"(qg[1]), "+v"(qg[2]), "+v"(qg[3])::"memory");                                          \
  RETRY(TAGS_BAD4(qg), QRETRY());                                                                                           \
  STAMP(3);                                                                                                                 \
  asm volatile("ds_write2_b32 %0, %2, %3 offset0:0 offset1:1\n\tds_write2_b32 %0, %4, %5 offset0:128 offset1:129\n\t"        \
               "ds_write2_b32 %1, %6, %7 offset0:0 offset1:1\n\tds_write2_b32 %1, %8, %9 offset0:128 offset1:129" ::"v"(pqs), \
               "v"(pqs + CP), "v"(qg[0].x), "v"(qg[0].z), "v"(qg[1].x), "v"(qg[1].z), "v"(qg[2].x), "v"(qg[2].z), "v"(qg[3].x), \
               "v"(qg[3].z)                                                                                                 \
               : "memory")
    if (__builtin_expect(w < 4, 1)) {
      v4u fr[32], qg[4];
      unsigned pc = L_C + (32 * w + col) * CP + 16 * g4;
      PIN(pc);
      CHALF();
      const bool early = (w >> 1) == g;
      if (!early) {
        CLAND();
      }
#pragma unroll
      for (int kt = 0; kt < 2; ++kt)
#pragma unroll
        for (int s = 0; s < 16; ++s) fr[16 * kt + s] = *(LAS v4u*)(sm + pc + 16 * kt * CP + 64 * s);
      if (early) {
        CLAND();
      }
      const int u = w ^ (2 * g);
      unsigned p = L_Q + col * CP + 16 * g4, pp = L_P + u * 1024 + l * 16, pm = L_MX + col * 16 + u * 4;
      unsigned ol = WS_LSE + (HMAP ? (((b * 32 + n) * 2 + g) * 8 + ((col + ROT * n) & 7)) * 128 + (col >> 3) * 8 : ((b * 32 + n) * 8 + 4 * g + (col >> 2)) * 128 + (col & 3) * 8);
      if (g4) ol = NOWHERE;
      PIN(p); PIN(pp); PIN(pm); PIN(ol);
      STAMP(2);
      QGATHER();
      BAR();
      STAMP(4);
      f4 s0 = z, s1 = z;
      v4u qf[16];
#pragma unroll
      for (int s = 0; s < 6; ++s) qf[s] = *(LAS v4u*)(sm + p + 64 * s);
      __builtin_amdgcn_sched_barrier(0);
#pragma unroll
      for (int s = 0; s < 16; ++s) {
        if (s + 6 < 16) qf[s + 6] = *(LAS v4u*)(sm + p + 64 * (s + 6));
        s0 = mfma32(fr[s], qf[s], s0);
        s1 = mfma32(fr[16 + s], qf[s], s1);
        __builtin_amdgcn_sched_barrier(0);
      }
      float mx = fmax2(fmax2(fmax2(s0[0], s0[1]), fmax2(s0[2], s0[3])), fmax2(fmax2(s1[0], s1[1]), fmax2(s1[2], s1[3])));
      mx = sw32_max(sw16_max(mx));
      constexpr float SC = 0.08838834764831845f * 1.4426950408889634f;
      const float mi = __builtin_ceilf(mx * SC);
#pragma unroll
      for (int e = 0; e < 4; ++e) {
        s0[e] = __builtin_amdgcn_exp2f(fmaf(s0[e], SC, -mi));
        s1[e] = __builtin_amdgcn_exp2f(fmaf(s1[e], SC, -mi));
      }
      *(LAS float*)(sm + pm) = mi;
      *(LAS v4u*)(sm + pp) = v4u{pk(s0[0], s0[1]), pk(s0[2], s0[3]), pk(s1[0], s1[1]), pk(s1[2], s1[3])};
      BAR();
      STAMP(5);
      if (w == LSE_QK - 1) {
        const f4 m4 = *(LAS f4*)(sm + pm - 4 * u);
        v4u pf[4];
#pragma unroll
        for (int k = 0; k < 4; ++k) pf[k] = *(LAS v4u*)(sm + pp + (k - u) * 1024);
        __builtin_amdgcn_sched_barrier(0);
        const float M = vmax3(vmax3(m4[0], m4[1], m4[2]), m4[3], m4[3]);
        const v4u one = {0x3f803f80u, 0x3f803f80u, 0x3f803f80u, 0x3f803f80u};
        f4 ls = z;
#pragma unroll
        for (int u = 0; u < 4; ++u) {
          const unsigned kk = min((unsigned)(M - m4[u]), 255u) * 0x00800080u;
#pragma unroll
          for (int e = 0; e < 4; ++e) pf[u][e] = scale_dn(pf[u][e], kk);
          ls = mfma32(one, pf[u], ls);
        }
        __builtin_amdgcn_raw_buffer_store_b64(v2u{__builtin_bit_cast(unsigned, M + __builtin_amdgcn_logf(ls[0])), T}, rs, ol, 0, AUX_SC1);
      }
    } else {
      v4u fr[32], qg[4];
      unsigned p0 = L_C + (64 * g + 4 * g4 + tq) * CP + 256 * v + 8 * tp, p1 = L_C + (64 * (g ^ 1) + 4 * g4 + tq) * CP + 256 * v + 8 * tp;
      PIN(p0); PIN(p1);
      CHALF();
#pragma unroll
      for (int ct = 0; ct < 8; ++ct)
#pragma unroll
        for (int u = 0; u < 2; ++u) fr[4 * ct + u] = trrd2(sm + p0 + 32 * u * CP + 32 * ct, sm + p0 + (32 * u + 16) * CP + 32 * ct);
      CLAND();
#pragma unroll
      for (int ct = 0; ct < 8; ++ct)
#pragma unroll
        for (int u = 0; u < 2; ++u) fr[4 * ct + 2 + u] = trrd2(sm + p1 + 32 * u * CP + 32 * ct, sm + p1 + (32 * u + 16) * CP + 32 * ct);
      unsigned ol = WS_LSE + (HMAP ? (((b * 32 + n) * 2 + g) * 8 + ((col + ROT * n) & 7)) * 128 + (col >> 3) * 8 : ((b * 32 + n) * 8 + 4 * g + (col >> 2)) * 128 + (col & 3) * 8), pp = L_P + l * 16, pm = L_MX + col * 16;
      v4u one = {0x3f803f80u, 0x3f803f80u, 0x3f803f80u, 0x3f803f80u};
      unsigned op2 = op + 8 * 65536;
      PIN(ol); PIN(pp); PIN(pm); PIN(one); PIN(op); PIN(op2);
      STAMP(2);
      QGATHER();
      BAR();
      STAMP(4);
      if (GQ && glu) {
        const int k = 4 * (a & 3) + v;
        const unsigned og = (unsigned)(((32 * k + (l >> 1)) * FF + 16 * sl + 8 * (l & 1)) * 2);
        dma16(wg, og, smb + L_GG + k * 1024);
        dma16(wu, og, smb + L_GG + 16384 + k * 1024);
      } else if (v < G_TOUCH) {
        const int ln = 128 * a + 64 * (v >> 1) + l;
        touch((v & 1) ? wu : wg, (unsigned)((ln >> 2) * (FF * 2)) + (unsigned)min(384 * xcd + 128 * (ln & 3), FF * 2 - 4), smb + L_JUNK);
      }
      if (glu && w < 6) LAYERNORM();
      BAR();
      STAMP(5);
      const f4 m4 = *(LAS f4*)(sm + pm);
      v4u pf[4];
#pragma unroll
      for (int u = 0; u < 4; ++u) pf[u] = *(LAS v4u*)(sm + pp + u * 1024);
      __builtin_amdgcn_sched_barrier(0);
      const float M = vmax3(vmax3(m4[0], m4[1], m4[2]), m4[3], m4[3]);
      f4 ls = z, c[4] = {z, z, z, z};
#pragma unroll
      for (int u = 0; u < 4; ++u) {
        const unsigned kk = min((unsigned)(M - m4[u]), 255u) * 0x00800080u;
#pragma unroll
        for (int e = 0; e < 4; ++e) pf[u][e] = scale_dn(pf[u][e], kk);
        ls = mfma32(one, pf[u], ls);
        if (u < 3) {
#pragma unroll
          for (int k = 0; k < 4; ++k) c[k] = mfma32(fr[4 * k + u], pf[u], c[k]);
        }
      }
      __builtin_amdgcn_sched_barrier(0);
      c[0] = mfma32(fr[3], pf[3], c[0]);
      c[1] = mfma32(fr[7], pf[3], c[1]);
      __builtin_amdgcn_sched_barrier(0);
      float il = __builtin_amdgcn_rcpf(ls[0]);
      unsigned lse = __builtin_bit_cast(unsigned, M + __builtin_amdgcn_logf(ls[0]));
      c[2] = mfma32(fr[11], pf[3], c[2]);
      c[3] = mfma32(fr[15], pf[3], c[3]);
#define PV_OUT(c, q)                                                                                          \
  {                                                                                                           \
    v4u R[2];                                                                                                 \
    _Pragma("unroll") for (int k = 0; k < 2; ++k) {                                                           \
      const f4 c0 = c[2 * k] * il, c1 = c[2 * k + 1] * il;                                                    \
      const unsigned x0 = min(pk(c0[0], c0[1]), 0xfffffffeu), x1 = min(pk(c0[2], c0[3]), 0xfffffffeu);        \
      const unsigned y0 = min(pk(c1[0], c1[1]), 0xfffffffeu), y1 = min(pk(c1[2], c1[3]), 0xfffffffeu);        \
      auto r0 = __builtin_amdgcn_permlane16_swap(x0, y0, false, false);                                       \
      auto r1 = __builtin_amdgcn_permlane16_swap(x1, y1, false, false);                                       \
      R[k] = v4u{r0[0], r1[0], r0[1], r1[1]};                                                                 \
    }                                                                                                         \
    v4u A, B;                                                                                                 \
    _Pragma("unroll") for (int e = 0; e < 4; ++e) {                                                           \
      A[e] = (unsigned)__builtin_amdgcn_update_dpp((int)R[0][e], (int)R[1][e], ROR8, 0xf, 0xc, false);        \
      B[e] = (unsigned)__builtin_amdgcn_update_dpp((int)R[1][e], (int)R[0][e], ROR8, 0xf, 0x3, false);        \
    }                                                                                                         \
    st_sc1(rs, op + 128 * q, A);                                                                              \
    st_sc1(rs, op2 + 128 * q, B);                                                                             \
  }
      __builtin_amdgcn_sched_barrier(0);
      f4 d[4] = {z, z, z, z};
#pragma unroll
      for (int u = 0; u < 4; ++u)
#pragma unroll
        for (int k = 0; k < 4; ++k) d[k] = mfma32(fr[16 + 4 * k + u], pf[u], d[k]);
      PV_OUT(c, 0);
#pragma unroll
      for (int i = 0; i < 16; ++i) {
        __builtin_amdgcn_sched_group_barrier(0x8, 1, 0);
        __builtin_amdgcn_sched_group_barrier(0x2, PV_MIX, 0);
      }
      __builtin_amdgcn_sched_barrier(0);
      PV_OUT(d, 1);
      FLAG_SET(v);
      if (GQ && glu) {
        VMWAIT(4);
        FLAG_SET(12 + v);
      }
      MARK(3);
      STAMP(6);
    }
    }
      if (glu && w < 4) {
        v4u af[8], xf[8];
        unsigned oog = WS_OPG + (8 * w + (col >> 1)) * OGP + sl * 128 + ((col & 1) * 4 + g4) * 16;
        PIN(oog);
        FLAG_WAIT4(0);
#pragma unroll 1
        for (int i = 0; i < 4; ++i) dma16<NT_G>(wd, (unsigned)(((16 * sl + 4 * w + i) * DM + 8 * l) * 2), smb + L_GD + (4 * w + i) * CP);
#pragma unroll 1
        for (int i = GQ; i < 4; ++i) {
          const int k = 4 * ((a + i) & 3) + w;
          const unsigned og = (unsigned)(((32 * k + (l >> 1)) * FF + 16 * sl + 8 * (l & 1)) * 2);
          dma16(wg, og, smb + L_GG + k * 1024);
          dma16(wu, og, smb + L_GG + 16384 + k * 1024);
        }
        unsigned pg = L_GG + (w & 1) * 16384 + 32 * (256 * (w >> 1) + 8 * g4 + tq) + 8 * tp;
        unsigned pd = L_GD + (4 * g4 + tq) * CP + 256 * w + 8 * tp, pgw = L_GR + ((w & 1) * 2 + (w >> 1)) * 1024 + l * 16;
        PIN(pg); PIN(pd); PIN(pgw);
#pragma unroll
        for (int s = 0; s < 8; ++s) xf[s] = *(LAS v4u*)(px + 512 * (w >> 1) + 64 * s);
        VMWAIT(0);
        FLAG_SET(4 + w);
        FLAG_WAIT4(1);
        if (GQ) FLAG_WAIT4(3);
        STAMP(7);
        f4 acc = z;
#pragma unroll
        for (int s = 0; s < 8; ++s) af[s] = trrd2(sm + pg + 32 * 32 * s, sm + pg + 32 * (32 * s + 4));
        __builtin_amdgcn_sched_barrier(0);
        v2u dn[8];
#pragma unroll
        for (int s = 0; s < 8; ++s) {
          acc = mfma32(af[s], xf[s], acc);
          dn[s] = trrd(sm + pd + 32 * s);
          __builtin_amdgcn_sched_barrier(0);
        }
        *(LAS f4*)(sm + pgw) = acc;
        FLAG_SET(8 + w);
        FLAG_WAIT4(2);
        LAS char* pr = sm + L_GR + l * 16;
        const f4 gx = *(LAS f4*)(pr) + *(LAS f4*)(pr + 1024), ux = *(LAS f4*)(pr + 2048) + *(LAS f4*)(pr + 3072);
        float hh[4];
#pragma unroll
        for (int e = 0; e < 4; ++e) hh[e] = ux[e] * __builtin_amdgcn_rcpf(1.f + __builtin_amdgcn_exp2f(-1.4426950408889634f * gx[e]));
        const v2u hb = {pk(hh[0], hh[1]), pk(hh[2], hh[3])};
        f4 d = z;
#pragma unroll
        for (int t = 0; t < 8; ++t) d = mfma16(dn[t], (col >> 1) == t ? hb : v2u{0u, 0u}, d);
        v4u dv = {pk(d[0], d[1]), T, pk(d[2], d[3]), T};
        PIN(dv);
        st_sc1(rs, oog, dv);
        MARK(4);
        STAMP(9);
      }
    if (a >= 12) {
      const unsigned m = (col & 1) ? 0x3f800000u : 0x3f80u;
      v4u sel = {col < 2 ? m : 0u, (col >> 1) == 1 ? m : 0u, col < 2 ? m : 0u, (col >> 1) == 1 ? m : 0u};
      PIN(sel);
      if (w == 0) {
        unsigned ot = WS_OPT + rd * 8192 + 4096 + l * 16, oy = (l >> 2) * 1024 + rd * 32 + (l & 3) * 8, pr = smb + L_RR + (l & 7) * 16;
        PIN(ot); PIN(oy); PIN(pr);
        v4u p[8];
        touch(out, (unsigned)((l & 1) * 1024 + rd * 32), smb + L_JUNK);
        HOLD_UNTIL(R_START);
        SWEEP_4(p, ot, -4096, -3072, -2048, -1024, "");
        SWEEP_4(p + 4, ot, 0, 1024, 2048, 3072, "");
        PIN4(p);
        PIN4(p + 4);
#define RRETRY()                                \
  if (TAG_BAD(p[0])) RELOAD(p[0], ot, -4096);   \
  if (TAG_BAD(p[1])) RELOAD(p[1], ot, -3072);   \
  if (TAG_BAD(p[2])) RELOAD(p[2], ot, -2048);   \
  if (TAG_BAD(p[3])) RELOAD(p[3], ot, -1024);   \
  if (TAG_BAD(p[4])) RELOAD(p[4], ot, 0);       \
  if (TAG_BAD(p[5])) RELOAD(p[5], ot, 1024);    \
  if (TAG_BAD(p[6])) RELOAD(p[6], ot, 2048);    \
  if (TAG_BAD(p[7])) RELOAD(p[7], ot, 3072);    \
  PIN4(p);                                      \
  PIN4(p + 4)
        RETRY(TAGS_BAD4(p) | TAGS_BAD4(p + 4), RRETRY());
        STAMP(8);
        f4 acc = z;
#pragma unroll
        for (int i = 0; i < 4; ++i) acc = mfma32(sel, v4u{p[2 * i].x, p[2 * i].z, p[2 * i + 1].x, p[2 * i + 1].z}, acc);
        f4 rest[6];
        for (unsigned spin = 0; spin < SPIN_CAP; ++spin) {
          v4u f0;
          v2u f1;
          asm volatile("ds_read_b128 %0, %8\n\tds_read_b64 %1, %8 offset:16\n\tds_read_b128 %2, %9\n\tds_read_b128 %3, %9 offset:128\n\t"
                       "ds_read_b128 %4, %9 offset:256\n\tds_read_b128 %5, %9 offset:384\n\tds_read_b128 %6, %9 offset:512\n\t"
                       "ds_read_b128 %7, %9 offset:640\n\ts_waitcnt lgkmcnt(0)"
                       : "=&v"(f0), "=&v"(f1), "=&v"(rest[0]), "=&v"(rest[1]), "=&v"(rest[2]), "=&v"(rest[3]), "=&v"(rest[4]), "=&v"(rest[5])
                       : "v"(smb + L_FL + 64), "v"(pr)
                       : "memory");
          if (__builtin_amdgcn_readfirstlane(f0.x & f0.y & f0.z & f0.w & f1.x & f1.y)) break;
        }
        const f4 gl = ((rest[0] + rest[1]) + (rest[2] + rest[3])) + (rest[4] + rest[5]);
#pragma unroll
        for (int e = 0; e < 4; ++e) acc[e] = (acc[e] + dpp<ROR8>(acc[e])) + gl[e];
        if (l < 8) asm volatile("global_store_dwordx2 %0, %1, %2 sc1" ::"v"(oy), "v"(v2u{pk(acc[0], acc[1]), pk(acc[2], acc[3])}), "s"(out) : "memory");
      } else if (w < 7) {
        const bool last = w == 6;
        unsigned og = WS_OPG + rd * OGP + (w - 1) * 2048 + (last ? min(l, 47) : l) * 16, og2 = last ? og : og + 1024;
        const v2u xr = *(const v2u*)((const char*)x + ((l >> 2) & 1) * 1024 + rd * 32 + (l & 3) * 8);
        unsigned m0 = (last && l >= 48) ? 0u : ~0u, m1 = last ? 0u : ~0u;
        f4 xv = {blo(xr.x), bhi(xr.x), blo(xr.y), bhi(xr.y)};
        if (!last) xv = z;
        PIN(og); PIN(og2); PIN(m0); PIN(m1); PIN(xv);
        v4u p0, p1;
        HOLD_UNTIL(R_START);
        UNDEF(p0); UNDEF(p1);
        for (unsigned spin = SPIN_CAP; spin; --spin) {
          SWEEP_2(p0, p1, og, og2, WAIT0);
          if (!(TAG_BAD(p0) | TAG_BAD(p1))) break;
        }
        STAMP(8);
        f4 acc = mfma32(sel, v4u{p0.x & m0, p0.z & m0, p1.x & m1, p1.z & m1}, z);
#pragma unroll
        for (int e = 0; e < 4; ++e) acc[e] = (acc[e] + dpp<ROR8>(acc[e])) + xv[e];
        if (l < 8) *(LAS f4*)(sm + L_RR + (w - 1) * 128 + l * 16) = acc;
        FLAG_SET(15 + w);
      }
      else if (rd == 0) {
        const unsigned ol0 = WS_LSE + (l * 16 + ((ROT * l) & 7)) * 128 + 4;
        unsigned t0, t1;
        for (unsigned spin = 0;; ++spin) {
          __builtin_amdgcn_s_sleep(32);
          asm volatile("global_load_dword %0, %2, %3 sc1\n\tglobal_load_dword %1, %2, %3 offset:1024 sc1" WAIT0
                       : "=&v"(t0), "=&v"(t1)
                       : "v"(ol0), "s"(ws)
                       : "memory");
          if (!__builtin_amdgcn_ballot_w64((t0 & t1 & T) != (t0 | t1 | T)) || spin > SPIN_CAP) break;
        }
        if (l == 0) __builtin_amdgcn_raw_buffer_store_b32(T, rs, WS_E, 0, AUX_SC1);
      }
      MARK(5);
      STAMP(9);
    }
    STAMP_FLUSH();
    __builtin_amdgcn_endpgm();
  }
  {
    const int ca = j >> 1, mb = j & 1;
    const int fl = tq | ((g4 & 1) << 2);
    if (__builtin_expect(w < 4, 1)) {
#pragma unroll 1
      for (int i = 0; i < 8; ++i, vq += 131072u, lq += 4096) dma16<NT_WQ>(wq, vq, lq);
      gld4_sc1(ep, ws, vzero);
      unsigned p = L_WQ + 256 * (8 * g4 + tq) + 16 * ((4 * w + (tp >> 1)) ^ (fl << 1)) + 8 * (tp & 1), p2 = p ^ 32;
      unsigned oqp = col < 4 ? WS_QP + ((h * 4 + j) * 8 + 2 * w) * 128 + col * 64 + g4 * 16 : NOWHERE;
      unsigned pxj = L_X + (col & 1) * XP + 16 * g4 + 256 * j;
      PIN(p); PIN(p2); PIN(oqp); PIN(pxj);
      unsigned o = WS_QP + (h * 4 + w) * 1024 + l * 16;
      unsigned pqw = smb + L_CC + (2 * w + ((l >> 2) & 1)) * SP + (l >> 3) * 32 + (l & 3) * 8, pqr = L_CC + (col & 7) * SP + 16 * g4;
      unsigned oqa = col < 4 ? WS_QA + ((col & 1) * NH + h) * 2048 + (64 * j + 16 * w + 8 * (col >> 1) + 2 * g4) * 8 : NOWHERE;
      unsigned pu = L_WUK + 256 * (32 * w + col) + 16 * (g4 ^ col);
      PIN(o); PIN(oqa); PIN(pu); PIN(pqw); PIN(pqr);
      VMWAIT(0);
      EPOCH();
      STAMP(1);
      BAR();
      STAMP(2);
      {
        v4u af[8], xf[4];
#pragma unroll
        for (int s = 0; s < 4; ++s) {
          xf[s] = *(LAS v4u*)(sm + pxj + 64 * s);
          af[s] = trrd2(sm + p + 256 * 32 * s, sm + p + 256 * (32 * s + 4));
          af[4 + s] = trrd2(sm + p2 + 256 * 32 * s, sm + p2 + 256 * (32 * s + 4));
        }
        __builtin_amdgcn_sched_barrier(0);
        f4 c0 = z, c1 = z;
#pragma unroll
        for (int s = 0; s < 4; ++s) {
          c0 = mfma32(af[s], xf[s], c0);
          c1 = mfma32(af[4 + s], xf[s], c1);
        }
        if (col & 2) c0 = c1;
        st_sc1(rs, oqp, v4u{pk(c0[0], c0[1]), T, pk(c0[2], c0[3]), T});
      }
      STAMP(3);
      v4u a, uk[8], qf[4];
      __builtin_amdgcn_s_sleep(H1_DELAY);
      asm volatile(LD16(0, 1, 2) " sc1" : "=&v"(a) : "v"(o), "s"(ws) : "memory");
      BAR();
#pragma unroll
      for (int s = 0; s < 4; ++s) {
        uk[s] = *(LAS v4u*)(sm + (pu ^ (64 * s)));
        uk[4 + s] = *(LAS v4u*)(sm + (pu ^ (64 * s)) + 4096);
      }
      VMWAIT(0);
      PIN(a);
      RETRY(TAG_BAD(a), asm volatile(LD16(0, 1, 2) " sc1" WAIT0 : "+v"(a) : "v"(o), "s"(ws) : "memory"));
      asm volatile("ds_write2_b32 %0, %1, %2 offset0:0 offset1:1" ::"v"(pqw), "v"(a.x), "v"(a.z) : "memory");
      STAMP(4);
      BAR();
#pragma unroll
      for (int s = 0; s < 4; ++s) qf[s] = *(LAS v4u*)(sm + pqr + 64 * s);
      __builtin_amdgcn_sched_barrier(0);
      f4 a0 = z, a1 = z;
#pragma unroll
      for (int s = 0; s < 4; ++s) {
        a0 = mfma32(uk[s], qf[s], a0);
        a1 = mfma32(uk[4 + s], qf[s], a1);
      }
      float q[4];
#pragma unroll
      for (int e = 0; e < 4; ++e) {
        const float mine = (col & 2) ? a1[e] : a0[e], other = (col & 2) ? a0[e] : a1[e];
        q[e] = mine + dpp<QP_2301>(other);
        q[e] += dpp<ROR4>(q[e]);
      }
      st_sc1(rs, oqa, v4u{pk(q[0], q[1]), T, pk(q[2], q[3]), T});
    } else {
      gld4_sc1(ep, ws, vzero);
      if (w < 6) {
        VMWAIT(1);
        LAYERNORM();
        STAMP(1);
        BAR();
        MARK(0);
      } else {
        __builtin_amdgcn_s_sleep(UK_DELAY);
        const unsigned vu = (unsigned)((((128 * j + g4) * NH + h) * DH) * 2);
#pragma unroll 1
        for (int k = w - 6; k < 32; k += 2) {
          if (k >> 1 == UK_FIRST) asm volatile("s_barrier" ::: "memory");
          dma16<NT_WUK>(wuk, vu + (unsigned)k * 32768u + 16u * (col ^ ((4 * k + g4) & 15)), smb + L_WUK + k * 1024);
        }
        MARK(1);
      }
      VMWAIT(0);
      EPOCH();
      STAMP(3);
      BAR();
      BAR();
    }
    STAMP(5);
    PROBE_T(T);
    BAR();
    HOLD_UNTIL(T_START);
    {
      const unsigned vv = (unsigned)((((256 * ca + g4) * NH + h) * DH + 8 * (col ^ ((g4 | ((w & 2) << 1)) << 1))) * 2);
      const unsigned vo = (unsigned)(((h * DH + (l >> 5)) * DM + 256 * mb) * 2) + 16u * ((l & 31) ^ (((l >> 5) | ((w & 1) << 1) | (w & 4)) << 1));
      int kv = w + 32 * mb, ko = w + 32 * ca;
#pragma unroll 1
      for (int hf = 0; hf < 2; ++hf) {
#pragma unroll 1
        for (int i = 0; i < 4; ++i, kv = (kv + 8) & 63) dma16<NT_T>(wuv, vv + (unsigned)kv * 32768u, smb + L_WUV + kv * 1024);
#pragma unroll 1
        for (int i = 0; i < 4; ++i, ko = (ko + 8) & 63) dma16<NT_T>(wo, vo + (unsigned)ko * 2048u, smb + L_WO + ko * 1024);
        if (hf == 0) asm volatile("s_barrier" ::: "memory");
      }
    }
    const int tb = w >> 2, nq = w & 3;
    unsigned o = WS_PART + (((h * 2 + ca) * 2 + tb) * 32 + 8 * nq + g4) * 512 + 16 * col;
    unsigned pw = L_W + (tb * 32 + 8 * nq + g4) * 4;
    unsigned pv = L_WUV + 256 * (8 * g4 + tq) + 16 * ((2 * w + (tp >> 1)) ^ (fl << 1)) + 8 * (tp & 1);
    unsigned po = L_WO + 512 * (8 * g4 + tq) + 16 * ((4 * w + (tp >> 1)) ^ (fl << 1)) + 8 * (tp & 1);
    unsigned pcw = L_CC + (nq * 2 + tb) * SP2 + 16 * col + 8 * g4, pcr = L_CC + (col & 7) * SP2 + 16 * g4;
    unsigned ptw = L_CT + col * SP + (16 * w + 4 * g4) * 2, ptr = L_CT + (col & 1) * SP + 16 * g4;
    unsigned oo = col >= 4 ? NOWHERE : WS_OPT + ((16 * mb + 2 * w + (col >> 1)) * 64 + ((2 * h + ca + ROT * 4 * (2 * w + (col >> 1))) & 63)) * 128 + ((col & 1) * 4 + g4) * 16;
    v4u mk;
#pragma unroll
    for (int d = 0; d < 4; ++d) mk[d] = (col >> 1) == d ? ((col & 1) ? 0xffff0000u : 0xffffu) : 0u;
    PIN(o); PIN(pw); PIN(pv); PIN(po); PIN(pcw); PIN(pcr); PIN(ptw); PIN(ptr); PIN(oo); PIN(mk);
    STAMP(11);
        if (w == 7) {
      const unsigned ol = WS_LSE + (HMAP ? ((l * 2 + (r >> 3)) * 8 + ((xcd + ROT * l) & 7)) * 128 + ((r >> 2) & 1) * 8 : (l * 8 + xcd) * 128 + (r >> 2) * 8);
      v2u la;
      UNDEF(la);
      for (unsigned spin = SPIN_CAP; spin; --spin) {
        asm volatile("global_load_dwordx2 %0, %1, %2 sc1" WAIT0 : "+v"(la) : "v"(ol), "s"(ws) : "memory");
        if (!__builtin_amdgcn_ballot_w64(la.y != T)) break;
      }
      float M = asf(la.x);
      M = fmax2(M, dpp<ROR8>(M));
      M = fmax2(M, dpp<ROR4>(M));
      M = fmax2(M, dpp<ROR2>(M));
      M = fmax2(M, dpp<ROR1>(M));
      M = sw16_max(M);
      const float e = __builtin_amdgcn_exp2f(asf(la.x) - M);
      float L = e + dpp<ROR8>(e);
      L += dpp<ROR4>(L);
      L += dpp<ROR2>(L);
      L += dpp<ROR1>(L);
      L = sw16_add(L, L);
      const float wt = e * __builtin_amdgcn_rcpf(L);
      *(LAS unsigned*)(sm + L_W + 4 * ((l & 32) | ((l + ROT * 4 * (h & 7)) & 31))) = pk(wt, wt);
      STAMP(7);
    }
    v4u a[4];
    {
      if (LSE_PIPE && w == 7) VMWAIT(2);
      else VMWAIT(0);
      STAMP(6);
      BAR();
      SWEEP_4(a, o, 0, 256, 2048, 2304, "");
    }
    v4u A0 = mk & *(LAS unsigned*)(sm + pw), A1 = mk & *(LAS unsigned*)(sm + pw + 16);
    v4u av[8], ao[8];
#pragma unroll
    for (int s = 0; s < 8; ++s) av[s] = trrd2(sm + pv + 256 * 32 * s, sm + pv + 256 * (32 * s + 4));
#pragma unroll
    for (int t = 0; t < 2; ++t)
#pragma unroll
      for (int s = 0; s < 4; ++s) ao[4 * t + s] = trrd2(sm + (po ^ (32 * t)) + 512 * 32 * s, sm + (po ^ (32 * t)) + 512 * (32 * s + 4));
    VMWAIT(0);
    asm volatile("" : "+v"(a[0]), "+v"(a[1]), "+v"(a[2]), "+v"(a[3]), "+v"(A0), "+v"(A1)::"memory");
    RETRY(HOLES4(a), TRETRY());
    STAMP(8);
    {
      f4 c0 = mfma32(A0, a[0], z), c1 = mfma32(A0, a[1], z);
      c0 = mfma32(A1, a[2], c0);
      c1 = mfma32(A1, a[3], c1);
      if (g4 < 2) {
        *(LAS v2u*)(sm + pcw) = v2u{pk(c0[0], c0[1]), pk(c0[2], c0[3])};
        *(LAS v2u*)(sm + pcw + 256) = v2u{pk(c1[0], c1[1]), pk(c1[2], c1[3])};
      }
    }
    BAR();
    {
      f4 acc = z;
      v4u bf[8];
#pragma unroll
      for (int s = 0; s < 8; ++s) bf[s] = *(LAS v4u*)(sm + pcr + 64 * s);
      __builtin_amdgcn_sched_barrier(0);
#pragma unroll
      for (int s = 0; s < 8; ++s) acc = mfma32(av[s], bf[s], acc);
#pragma unroll
      for (int e = 0; e < 4; ++e) {
        acc[e] += dpp<QP_2301>(acc[e]);
        acc[e] += dpp<ROR4>(acc[e]);
      }
      if (col < 2) *(LAS v2u*)(sm + ptw) = v2u{pk(acc[0], acc[1]), pk(acc[2], acc[3])};
    }
    BAR();
    {
      f4 c0 = z, c1 = z;
      v4u bf[4];
#pragma unroll
      for (int s = 0; s < 4; ++s) bf[s] = *(LAS v4u*)(sm + ptr + 64 * s);
      __builtin_amdgcn_sched_barrier(0);
#pragma unroll
      for (int s = 0; s < 4; ++s) {
        c0 = mfma32(ao[s], bf[s], c0);
        c1 = mfma32(ao[4 + s], bf[s], c1);
      }
      if (col & 2) c0 = c1;
      st_sc1(rs, oo, v4u{pk(c0[0], c0[1]), T, pk(c0[2], c0[3]), T});
    }
    MARK(2);
    STAMP(9);
  }
  STAMP_FLUSH();
}

#define WS_END 0
#define WS_BASE (WS_END ? ((ws_size - (size_t)WS_END * 1048576) & ~(size_t)4095) : 0)
extern "C" void kernel_launch(void* const* d_in, const int* in_sizes, int n_in, void* d_out, int out_size, void* d_ws, size_t ws_size,
                              hipStream_t stream) {
  mla_glu_decode<<<256, 512, 0, stream>>>((const u16*)d_in[0], (const u16*)d_in[1], (const u16*)d_in[2], (const u16*)d_in[3],
                                          (const u16*)d_in[4], (const u16*)d_in[5], (const u16*)d_in[7], (const u16*)d_in[8],
                                          (const u16*)d_in[9], (u16*)d_out, (char*)d_ws + WS_BASE);
}
```

```cpp
#define T_START 340
#define TLB_LATE 150
#include <hip/hip_runtime.h>
#include <stdint.h>

#define GAS __attribute__((address_space(1)))
#define LAS __attribute__((address_space(3)))
typedef unsigned short u16;
typedef unsigned v4u __attribute__((ext_vector_type(4)));
typedef unsigned v2u __attribute__((ext_vector_type(2)));
typedef float f4 __attribute__((ext_vector_type(4)));
typedef float f2 __attribute__((ext_vector_type(2)));
typedef __bf16 bf8 __attribute__((ext_vector_type(8)));
typedef __bf16 bf2 __attribute__((ext_vector_type(2)));
typedef short s4 __attribute__((ext_vector_type(4)));

#define ROT 1
namespace {
constexpr unsigned NOWHERE = 0x7ff00000u;
constexpr int NH = 32, SK = 4096, DH = 128, DM = 512, DC = 512, FF = 1376;
constexpr int NSL = FF / 16;
constexpr int WS_E = 0;
constexpr int WS_QP = 4096;
constexpr int WS_QA = WS_QP + NH * 4 * 1024;
constexpr int WS_LSE = WS_QA + 2 * NH * 2048;
constexpr int WS_GO = WS_LSE + 2 * 32 * 16 * 128;
constexpr int WS_OPT = WS_GO + 2 * 32 * 16 * 128;
constexpr int WS_OPG = WS_OPT + 32 * 64 * 128;
constexpr int OGP = 16384 + ROT * 384;
constexpr int WS_PART = (WS_OPG + 32 * OGP + 4095) / 4096 * 4096;
constexpr int WS_BYTES = WS_PART + NH * 2 * 2 * 32 * 512;
constexpr int WS_STAMP = 7 << 19;
constexpr int CP = 1056;
constexpr int XP = 1088, SP = 288, SP2 = 544;
constexpr int L_C = 0, L_Q = 128 * CP, L_P = L_Q + 16 * CP, L_X = L_P + 4096, L_MX = L_X + 2 * XP, L_RR = L_MX + 256,
              L_JUNK = L_RR + 1024, L_FL = L_JUNK + 256, L_END = L_FL + 128;
static_assert(L_END <= 163840, "LDS");
constexpr int L_GG = 0, L_GD = 32768, L_GR = 65536;
constexpr int L_WUV = 0, L_WO = 65536, L_WQ = 0, L_WUK = 32768, L_CC = 131072, L_CT = L_CC + 8 * SP2, L_W = L_CT + 2 * SP;
#define UK_DELAY 20
#define H1_DELAY 12
#define CODE_TOUCH 1
#define NT_CKV 0
#define NT_WQ 0
#define NT_WUK 0
#define NT_T 0
#define NT_G 0
#define A_START 70
#define GLU_EARLY 0
#define G_START 0
#define C_START 0
#define LSE_LATE 0
#define LSE_WAVE 3
#define DATA_DELAY 0
#define NEWRED 1
#define R_START 0
#define G_TOUCH 4
#define LSE_PIPE 0
#define PV_MIX 4
#define LSE_QK 1
#define HMAP 1
#define GQ 1
#define DOWN_FIRST 1
#define EARLY_SWEEP 1
#define GO_DELAY 0
#define OUT_TOUCH 1
#define OUT_SC1 1
#define BUMP_EARLY 1
#define G2_DELAY 0
#define UK_FIRST 6
#define UK_TOUCH 1
#define PIECEWISE 1
#define PF_GLU 1
constexpr unsigned SPIN_CAP = 1u << 20;
constexpr int AUX_SC1 = 16;

#define STAMP_W 0
#define STAMP(i)
#define STAMP_FLUSH()
#define PROBE_T(T_)

__device__ __forceinline__ f4 mfma32(v4u a, v4u b, f4 c) {
  return __builtin_amdgcn_mfma_f32_16x16x32_bf16(__builtin_bit_cast(bf8, a), __builtin_bit_cast(bf8, b), c, 0, 0, 0);
}
__device__ __forceinline__ f4 mfma16(v2u a, v2u b, f4 c) {
  return __builtin_amdgcn_mfma_f32_16x16x16bf16_1k(__builtin_bit_cast(s4, a), __builtin_bit_cast(s4, b), c, 0, 0, 0);
}
__device__ __forceinline__ unsigned pk(float lo, float hi) {
  f2 x = {lo, hi};
  return __builtin_bit_cast(unsigned, __builtin_convertvector(x, bf2));
}
__device__ __forceinline__ float asf(unsigned u) { return __builtin_bit_cast(float, u); }
__device__ __forceinline__ float blo(unsigned u) { return __builtin_bit_cast(float, u << 16); }
__device__ __forceinline__ float bhi(unsigned u) { return __builtin_bit_cast(float, u & 0xffff0000u); }
template <int CTRL> __device__ __forceinline__ float dpp(float v) {
  return __builtin_bit_cast(float, __builtin_amdgcn_update_dpp(0, __builtin_bit_cast(int, v), CTRL, 0xf, 0xf, true));
}
__device__ __forceinline__ float fmax2(float a, float b) {
#pragma float_control(precise, off)
  return __builtin_fmaxf(a, b);
}
typedef unsigned short h2 __attribute__((ext_vector_type(2)));
__device__ __forceinline__ unsigned scale_dn(unsigned p, unsigned kk) {
  return __builtin_bit_cast(unsigned, __builtin_elementwise_sub_sat(__builtin_bit_cast(h2, p), __builtin_bit_cast(h2, kk)));
}
#define PIN(x) asm volatile("" : "+v"(x))
constexpr int ROR1 = 0x121, ROR2 = 0x122, ROR4 = 0x124, ROR8 = 0x128, QP_2301 = 0x4e, QP_2222 = 0xaa;
__device__ __forceinline__ float sw32_add(float a, float b) {
  auto r = __builtin_amdgcn_permlane32_swap(__builtin_bit_cast(unsigned, a), __builtin_bit_cast(unsigned, b), false, false);
  return asf(r[0]) + asf(r[1]);
}
__device__ __forceinline__ float sw16_add(float a, float b) {
  auto r = __builtin_amdgcn_permlane16_swap(__builtin_bit_cast(unsigned, a), __builtin_bit_cast(unsigned, b), false, false);
  return asf(r[0]) + asf(r[1]);
}
__device__ __forceinline__ float vmax(unsigned a, unsigned b) {
  float r;
  asm("v_max_f32 %0, %1, %2" : "=v"(r) : "v"(a), "v"(b));
  return r;
}
__device__ __forceinline__ float vmax3(float a, float b, float c) {
  float r;
  asm("v_max3_f32 %0, %1, %2, %3" : "=v"(r) : "v"(a), "v"(b), "v"(c));
  return r;
}
__device__ __forceinline__ float sw32_max(float a) {
  auto r = __builtin_amdgcn_permlane32_swap(__builtin_bit_cast(unsigned, a), __builtin_bit_cast(unsigned, a), false, false);
  return vmax(r[0], r[1]);
}
__device__ __forceinline__ float sw16_max(float a) {
  auto r = __builtin_amdgcn_permlane16_swap(__builtin_bit_cast(unsigned, a), __builtin_bit_cast(unsigned, a), false, false);
  return vmax(r[0], r[1]);
}
__device__ __forceinline__ float wave_sum(float v) {
  v += dpp<ROR8>(v);
  v += dpp<ROR4>(v);
  v += dpp<ROR2>(v);
  v += dpp<ROR1>(v);
  v = sw16_add(v, v);
  return sw32_add(v, v);
}
__device__ __forceinline__ v2u trrd(LAS char* p) {
  return __builtin_bit_cast(v2u, __builtin_amdgcn_ds_read_tr16_b64_v4i16((LAS s4*)p));
}
__device__ __forceinline__ v4u trrd2(LAS char* p0, LAS char* p1) {
  v2u a = trrd(p0), b = trrd(p1);
  return v4u{a.x, a.y, b.x, b.y};
}
#define BAR() asm volatile("s_waitcnt lgkmcnt(0)\n\ts_barrier" ::: "memory")
#define VMWAIT(k) asm volatile("s_waitcnt vmcnt(%0)" ::"n"(k) : "memory")
__device__ __forceinline__ void gld16(v4u& d, const void* base, unsigned off) {
  asm volatile("global_load_dwordx4 %0, %1, %2" : "=v"(d) : "v"(off), "s"(base) : "memory");
}
__device__ __forceinline__ void gld4_sc1(unsigned& d, const void* base, unsigned off) {
  asm volatile("global_load_dword %0, %1, %2 sc1" : "=v"(d) : "v"(off), "s"(base) : "memory");
}
template <int NT = 0> __device__ __forceinline__ void touch(const void* base, unsigned off, unsigned lds) {
  if (NT) asm volatile("s_mov_b32 m0, %2\n\ts_nop 0\n\tglobal_load_lds_dword %0, %1 nt" ::"v"(off), "s"(base), "s"(lds) : "memory");
  else asm volatile("s_mov_b32 m0, %2\n\ts_nop 0\n\tglobal_load_lds_dword %0, %1" ::"v"(off), "s"(base), "s"(lds) : "memory");
}
__device__ __forceinline__ void touch_sc1(const void* base, unsigned off, unsigned lds) {
  asm volatile("s_mov_b32 m0, %2\n\ts_nop 0\n\tglobal_load_lds_dword %0, %1 sc1" ::"v"(off), "s"(base), "s"(lds) : "memory");
}
#define LD16(i, a, o) "global_load_dwordx4 %" #i ", %" #a ", %" #o
#define SWEEP_4(q, o, o0, o1, o2, o3, W)                                                                                     \
  asm volatile(LD16(0, 4, 5) " offset:" #o0 " sc1\n\t" LD16(1, 4, 5) " offset:" #o1 " sc1\n\t" LD16(2, 4, 5) " offset:" #o2         \
               " sc1\n\t" LD16(3, 4, 5) " offset:" #o3 " sc1" W                                                               \
               : "=&v"((q)[0]), "=&v"((q)[1]), "=&v"((q)[2]), "=&v"((q)[3])                                                  \
               : "v"(o), "s"(ws)                                                                                             \
               : "memory")
#define RESWEEP_4(q, o, o0, o1, o2, o3)                                                                                      \
  asm volatile(LD16(0, 4, 5) " offset:" #o0 " sc1\n\t" LD16(1, 4, 5) " offset:" #o1 " sc1\n\t" LD16(2, 4, 5) " offset:" #o2         \
               " sc1\n\t" LD16(3, 4, 5) " offset:" #o3 " sc1\n\ts_waitcnt vmcnt(0)"                                            \
               : "+v"((q)[0]), "+v"((q)[1]), "+v"((q)[2]), "+v"((q)[3])                                                      \
               : "v"(o), "s"(ws)                                                                                             \
               : "memory")
#define RELOAD(q, o, off) asm volatile(LD16(0, 1, 2) " offset:" #off " sc1" : "+v"(q) : "v"(o), "s"(ws) : "memory")
#define PIN4(q) asm volatile("s_waitcnt vmcnt(0)" : "+v"((q)[0]), "+v"((q)[1]), "+v"((q)[2]), "+v"((q)[3])::"memory")
#define TAG_BAD(q) __builtin_amdgcn_ballot_w64((T & (q).y & (q).w) != (T | (q).y | (q).w))
#define HOLE(q) __builtin_amdgcn_ballot_w64(max(max((q).x, (q).y), max((q).z, (q).w)) == ~0u)
#define MAX4(q) max(max((q).x, (q).y), max((q).z, (q).w))
#define HOLES4(q) __builtin_amdgcn_ballot_w64(max(max(MAX4((q)[0]), MAX4((q)[1])), max(MAX4((q)[2]), MAX4((q)[3]))) == ~0u)
#define TRETRY()                          \
  if (HOLE(a[0])) RELOAD(a[0], o, 0);     \
  if (HOLE(a[1])) RELOAD(a[1], o, 256);   \
  if (HOLE(a[2])) RELOAD(a[2], o, 2048);  \
  if (HOLE(a[3])) RELOAD(a[3], o, 2304);  \
  PIN4(a)
#define SWEEP_L(W)                                                                                                          \
  asm volatile("global_load_dwordx2 %0, %3, %5 sc1\n\tglobal_load_dwordx2 %1, %4, %5 sc1\n\t"                              \
               "global_load_dwordx2 %2, %4, %5 offset:512 sc1" W                                                            \
               : "=&v"(la), "=&v"(l0), "=&v"(l1)                                                                            \
               : "v"(ola), "v"(olo), "s"(ws)                                                                                \
               : "memory")
#define SWEEP_2(p, q, oa, ob, W) \
  asm volatile(LD16(0, 2, 4) " sc1\n\t" LD16(1, 3, 4) " sc1" W : "=&v"(p), "=&v"(q) : "v"(oa), "v"(ob), "s"(ws) : "memory")
#define TAGS_BAD4(q) \
  __builtin_amdgcn_ballot_w64((T & (q)[0].y & (q)[0].w & (q)[1].y & (q)[1].w & (q)[2].y & (q)[2].w & (q)[3].y & (q)[3].w) != \
                              (T | (q)[0].y | (q)[0].w | (q)[1].y | (q)[1].w | (q)[2].y | (q)[2].w | (q)[3].y | (q)[3].w))
#define FLAG_SET(i) asm volatile("ds_write_b32 %0, %1" ::"v"(smb + L_FL + 4 * (i)), "v"(1u) : "memory")
#define FLAG_WAIT4(i)                                                                                     \
  for (unsigned spin_ = 0; spin_ < SPIN_CAP; ++spin_) {                                                   \
    v4u f_;                                                                                               \
    asm volatile("ds_read_b128 %0, %1\n\ts_waitcnt lgkmcnt(0)" : "=v"(f_) : "v"(smb + L_FL + 16 * (i)) : "memory"); \
    if (__builtin_amdgcn_readfirstlane(f_.x & f_.y & f_.z & f_.w)) break;                                 \
  }
#define HOLD_UNTIL(ticks)              \
  asm volatile("" : "+s"(tstart));     \
  while ((unsigned)__builtin_amdgcn_s_memrealtime() - (unsigned)tstart < (ticks)) __builtin_amdgcn_s_sleep(1)
#define MARK(i) asm volatile(".Lmla_e" #i ":")
#define RETRY(bad, again)                                   \
  if (bad) {                                                \
    for (unsigned spin = SPIN_CAP; spin; --spin) {          \
      again;                                                \
      if (!(bad)) break;                                    \
    }                                                       \
  }
#define UNDEF(x) asm volatile("" : "=v"(x))
#define EPOCH() asm volatile("v_add_u32 %0, 1, %1" : "=v"(T) : "v"(ep) : "memory")
#define WAIT0 "\n\ts_waitcnt vmcnt(0)"
template <int NT = 0> __device__ __forceinline__ void dma16(const void* base, unsigned off, unsigned lds) {
  if (NT) asm volatile("s_mov_b32 m0, %2\n\ts_nop 0\n\tglobal_load_lds_dwordx4 %0, %1 nt" ::"v"(off), "s"(base), "s"(lds) : "memory");
  else asm volatile("s_mov_b32 m0, %2\n\ts_nop 0\n\tglobal_load_lds_dwordx4 %0, %1" ::"v"(off), "s"(base), "s"(lds) : "memory");
}
__device__ __forceinline__ v4u ld_sc1(__amdgpu_buffer_rsrc_t rs, int off) {
  return __builtin_amdgcn_raw_buffer_load_b128(rs, off, 0, AUX_SC1);
}
__device__ __forceinline__ v2u ld8_sc1(__amdgpu_buffer_rsrc_t rs, int off) {
  return __builtin_amdgcn_raw_buffer_load_b64(rs, off, 0, AUX_SC1);
}
__device__ __forceinline__ void st_sc1(__amdgpu_buffer_rsrc_t rs, int off, v4u v) {
  __builtin_amdgcn_raw_buffer_store_b128(v, rs, off, 0, AUX_SC1);
}
__device__ __forceinline__ void ln_stats(v4u r, float& mean, float& rstd) {
  float e[8] = {blo(r.x), bhi(r.x), blo(r.y), bhi(r.y), blo(r.z), bhi(r.z), blo(r.w), bhi(r.w)};
  float s = ((e[0] + e[1]) + (e[2] + e[3])) + ((e[4] + e[5]) + (e[6] + e[7]));
  mean = wave_sum(s) * (1.f / DM);
  float q = 0.f;
#pragma unroll
  for (int i = 0; i < 8; ++i) {
    float d = e[i] - mean;
    q = fmaf(d, d, q);
  }
  rstd = __builtin_amdgcn_rsqf(wave_sum(q) * (1.f / DM) + 1e-5f);
}
}

__global__ void __launch_bounds__(512, 2)
mla_glu_decode(const u16* __restrict__ x, const u16* __restrict__ ckv, const u16* __restrict__ wq, const u16* __restrict__ wuk,
               const u16* __restrict__ wuv, const u16* __restrict__ wo, const u16* __restrict__ wg, const u16* __restrict__ wu,
               const u16* __restrict__ wd, u16* __restrict__ out, char* __restrict__ ws) {
  unsigned long long tstart = __builtin_amdgcn_s_memrealtime();
  __builtin_amdgcn_sched_barrier(0);
  __shared__ __attribute__((aligned(16))) char smem_[L_END];
  LAS char* sm = (LAS char*)smem_;
  const unsigned smb = (unsigned)(size_t)sm;
  const int tid = threadIdx.x, l = tid & 63, w = __builtin_amdgcn_readfirstlane(tid >> 6);
  const int col = l & 15, g4 = l >> 4, tq = (l & 15) >> 2, tp = l & 3;
  const int xcd = blockIdx.x & 7, r = blockIdx.x >> 3;
  const __amdgpu_buffer_rsrc_t rs = __builtin_amdgcn_make_buffer_rsrc(ws, 0, WS_BYTES, 0x00020000);
  int bw = (blockIdx.x >> 7) * w;
  asm volatile("" : "+s"(bw));
  if (bw >= 5) {
    const char* pc0;
    unsigned len;
    asm volatile("s_getpc_b64 %0\n.Lmla_pc:\n\ts_mov_b32 %1, .Lmla_e0-.Lmla_pc\n\ts_max_i32 %1, %1, .Lmla_e1-.Lmla_pc\n\t"
                 "s_max_i32 %1, %1, .Lmla_e2-.Lmla_pc\n\ts_max_i32 %1, %1, .Lmla_e3-.Lmla_pc\n\ts_max_i32 %1, %1, .Lmla_e4-.Lmla_pc\n\t"
                 "s_max_i32 %1, %1, .Lmla_e5-.Lmla_pc"
                 : "=s"(pc0), "=s"(len)::"scc");
    touch(pc0, min((unsigned)(64 * (bw - 5) + l) * 128u, len), smb + L_JUNK);
  }
  __builtin_amdgcn_sched_barrier(0);
  const int h = HMAP ? xcd + 8 * ((r >> 2) & 3) : 4 * xcd + ((r >> 2) & 3), j = r & 3;
  unsigned vq = (unsigned)(((128 * j + g4 + 4 * w) * 4096 + h * DH + 8 * (col ^ ((g4 | ((w & 2) << 1)) << 1))) * 2), vx = (w & 1) * 1024 + l * 16;
  unsigned vzero = 0u;
  int lq = smb + L_WQ + w * 1024, lx = smb + L_X + (w & 1) * XP;
  asm volatile("" : "+v"(vq), "+v"(vx), "+v"(vzero), "+s"(lq), "+s"(lx));
  asm volatile("" ::"s"(x), "s"(ckv), "s"(wq), "s"(wuk), "s"(wuv), "s"(wo), "s"(wg), "s"(wu), "s"(wd), "s"(out), "s"(ws));
  STAMP(0);

  unsigned ep, T;
  LAS char* const px = sm + L_X + (col & 1) * XP + 16 * g4;
  if ((w >> 1) == 2) dma16(x, vx, lx);
  const f4 z = {0.f, 0.f, 0.f, 0.f};
#define LAYERNORM()                                                                                                              \
  {                                                                                                                              \
    const v4u xr = *(LAS v4u*)(sm + L_X + (w & 1) * XP + l * 16);                                                                \
    float e[8] = {blo(xr.x), bhi(xr.x), blo(xr.y), bhi(xr.y), blo(xr.z), bhi(xr.z), blo(xr.w), bhi(xr.w)};                       \
    const float mean = wave_sum(((e[0] + e[1]) + (e[2] + e[3])) + ((e[4] + e[5]) + (e[6] + e[7]))) * (1.f / DM);                 \
    float q = 0.f;                                                                                                               \
    _Pragma("unroll") for (int i = 0; i < 8; ++i) {                                                                              \
      e[i] -= mean;                                                                                                              \
      q = fmaf(e[i], e[i], q);                                                                                                   \
    }                                                                                                                            \
    const float rstd = __builtin_amdgcn_rsqf(wave_sum(q) * (1.f / DM) + 1e-5f);                                                  \
    *(LAS v4u*)(sm + L_X + (w & 1) * XP + l * 16) =                                                                              \
        v4u{pk(e[0] * rstd, e[1] * rstd), pk(e[2] * rstd, e[3] * rstd), pk(e[4] * rstd, e[5] * rstd), pk(e[6] * rstd, e[7] * rstd)}; \
  }

  if (__builtin_expect(r >= 16, 0)) {
    const int a = r - 16, b = xcd >> 2, n = 8 * (xcd & 3) + (a >> 1), g = a & 1;
    const int sl = 12 * xcd + a, rd = (4 * xcd + a) & 31;
    const bool glu = a < 12 && sl < NSL;
    const int v = w & 3;
    unsigned op = WS_PART + ((((16 * g + (col & 7)) * 2 + (v >> 1)) * 2 + b) * 32 + ((n + ROT * 4 * (col & 7)) & 31)) * 512 + 256 * (v & 1) +
                  (col >> 3) * 64 + (g4 & 1) * 32 + (g4 >> 1) * 16;
    gld4_sc1(ep, ws, vzero);
    unsigned o = WS_QA + (b * NH + 16 * g) * 2048 + w * 4096 + l * 16, pqs = smb + L_Q + 2 * w * CP + l * 8;
    PIN(o); PIN(pqs);
    {
    {
      const unsigned vc = (unsigned)(((b * SK + 128 * n) * DC + 8 * l) * 2);
      STAMP(10);
      if (w < 4) touch(wuk, (unsigned)((32 * a + 8 * w + (l >> 3)) * 8192 + (xcd + 8 * ((l >> 1) & 3)) * 256 + (l & 1) * 128), smb + L_JUNK);
      HOLD_UNTIL(C_START);
    if (w >= 4) {
#pragma unroll
      for (int q = 0; q < 4; ++q) st_sc1(rs, op + 128 * (q >> 1) + (q & 1) * 8 * 65536, v4u{~0u, ~0u, ~0u, ~0u});
      if (w == 7 && l < 32) *(LAS unsigned*)(sm + L_FL + 4 * l) = 0u;
    }
      HOLD_UNTIL(A_START);
#pragma unroll 1
      for (int q = 0; q < 4; ++q) {
        int row = w + ((64 * g + 32 * q) & 127);
        asm volatile("s_barrier" ::: "memory");
#pragma unroll 1
        for (int i = 0; i < 4; ++i, row += 8) dma16<NT_CKV>(ckv, vc + row * 1024u, smb + L_C + row * CP);
      }
    }
#define CHALF()                               \
  asm volatile("s_barrier" ::: "memory");     \
  SWEEP_4(qg, o, 0, 1024, 2048, 3072, "");    \
  VMWAIT(12);                                 \
  asm volatile("s_barrier" ::: "memory")
#define CLAND() \
  VMWAIT(4);    \
  EPOCH();      \
  PROBE_T(T);   \
  BAR();        \
  STAMP(1)
#define QRETRY()                              \
  if (TAG_BAD(qg[0])) RELOAD(qg[0], o, 0);    \
  if (TAG_BAD(qg[1])) RELOAD(qg[1], o, 1024); \
  if (TAG_BAD(qg[2])) RELOAD(qg[2], o, 2048); \
  if (TAG_BAD(qg[3])) RELOAD(qg[3], o, 3072); \
  PIN4(qg)
#define QGATHER()                                                                                                           \
  VMWAIT(0);                                                                                                      \
  asm volatile("" : "+v"(qg[0]), "+v"(qg[1]), "+v"(qg[2]), "+v"(qg[3])::"memory");                                          \
  RETRY(TAGS_BAD4(qg), QRETRY());                                                                                           \
  STAMP(3);                                                                                                                 \
  asm volatile("ds_write2_b32 %0, %2, %3 offset0:0 offset1:1\n\tds_write2_b32 %0, %4, %5 offset0:128 offset1:129\n\t"        \
               "ds_write2_b32 %1, %6, %7 offset0:0 offset1:1\n\tds_write2_b32 %1, %8, %9 offset0:128 offset1:129" ::"v"(pqs), \
               "v"(pqs + CP), "v"(qg[0].x), "v"(qg[0].z), "v"(qg[1].x), "v"(qg[1].z), "v"(qg[2].x), "v"(qg[2].z), "v"(qg[3].x), \
               "v"(qg[3].z)                                                                                                 \
               : "memory")
    if (__builtin_expect(w < 4, 1)) {
      v4u fr[32], qg[4];
      unsigned pc = L_C + (32 * w + col) * CP + 16 * g4;
      PIN(pc);
      CHALF();
      const bool early = (w >> 1) == g;
      if (!early) {
        CLAND();
      }
#pragma unroll
      for (int kt = 0; kt < 2; ++kt)
#pragma unroll
        for (int s = 0; s < 16; ++s) fr[16 * kt + s] = *(LAS v4u*)(sm + pc + 16 * kt * CP + 64 * s);
      if (early) {
        CLAND();
      }
      const int u = w ^ (2 * g);
      unsigned p = L_Q + col * CP + 16 * g4, pp = L_P + u * 1024 + l * 16, pm = L_MX + col * 16 + u * 4;
      unsigned ol = WS_LSE + (HMAP ? (((b * 32 + n) * 2 + g) * 8 + ((col + ROT * n) & 7)) * 128 + (col >> 3) * 8 : ((b * 32 + n) * 8 + 4 * g + (col >> 2)) * 128 + (col & 3) * 8);
      if (g4) ol = NOWHERE;
      PIN(p); PIN(pp); PIN(pm); PIN(ol);
      STAMP(2);
      QGATHER();
      BAR();
      STAMP(4);
      f4 s0 = z, s1 = z;
      v4u qf[16];
#pragma unroll
      for (int s = 0; s < 6; ++s) qf[s] = *(LAS v4u*)(sm + p + 64 * s);
      __builtin_amdgcn_sched_barrier(0);
#pragma unroll
      for (int s = 0; s < 16; ++s) {
        if (s + 6 < 16) qf[s + 6] = *(LAS v4u*)(sm + p + 64 * (s + 6));
        s0 = mfma32(fr[s], qf[s], s0);
        s1 = mfma32(fr[16 + s], qf[s], s1);
        __builtin_amdgcn_sched_barrier(0);
      }
      float mx = fmax2(fmax2(fmax2(s0[0], s0[1]), fmax2(s0[2], s0[3])), fmax2(fmax2(s1[0], s1[1]), fmax2(s1[2], s1[3])));
      mx = sw32_max(sw16_max(mx));
      constexpr float SC = 0.08838834764831845f * 1.4426950408889634f;
      const float mi = __builtin_ceilf(mx * SC);
#pragma unroll
      for (int e = 0; e < 4; ++e) {
        s0[e] = __builtin_amdgcn_exp2f(fmaf(s0[e], SC, -mi));
        s1[e] = __builtin_amdgcn_exp2f(fmaf(s1[e], SC, -mi));
      }
      *(LAS float*)(sm + pm) = mi;
      *(LAS v4u*)(sm + pp) = v4u{pk(s0[0], s0[1]), pk(s0[2], s0[3]), pk(s1[0], s1[1]), pk(s1[2], s1[3])};
      BAR();
      STAMP(5);
      if (w == LSE_QK - 1) {
        const f4 m4 = *(LAS f4*)(sm + pm - 4 * u);
        v4u pf[4];
#pragma unroll
        for (int k = 0; k < 4; ++k) pf[k] = *(LAS v4u*)(sm + pp + (k - u) * 1024);
        __builtin_amdgcn_sched_barrier(0);
        const float M = vmax3(vmax3(m4[0], m4[1], m4[2]), m4[3], m4[3]);
        const v4u one = {0x3f803f80u, 0x3f803f80u, 0x3f803f80u, 0x3f803f80u};
        f4 ls = z;
#pragma unroll
        for (int u = 0; u < 4; ++u) {
          const unsigned kk = min((unsigned)(M - m4[u]), 255u) * 0x00800080u;
#pragma unroll
          for (int e = 0; e < 4; ++e) pf[u][e] = scale_dn(pf[u][e], kk);
          ls = mfma32(one, pf[u], ls);
        }
        __builtin_amdgcn_raw_buffer_store_b64(v2u{__builtin_bit_cast(unsigned, M + __builtin_amdgcn_logf(ls[0])), T}, rs, ol, 0, AUX_SC1);
      }
    } else {
      v4u fr[32], qg[4];
      unsigned p0 = L_C + (64 * g + 4 * g4 + tq) * CP + 256 * v + 8 * tp, p1 = L_C + (64 * (g ^ 1) + 4 * g4 + tq) * CP + 256 * v + 8 * tp;
      PIN(p0); PIN(p1);
      CHALF();
#pragma unroll
      for (int ct = 0; ct < 8; ++ct)
#pragma unroll
        for (int u = 0; u < 2; ++u) fr[4 * ct + u] = trrd2(sm + p0 + 32 * u * CP + 32 * ct, sm + p0 + (32 * u + 16) * CP + 32 * ct);
      CLAND();
#pragma unroll
      for (int ct = 0; ct < 8; ++ct)
#pragma unroll
        for (int u = 0; u < 2; ++u) fr[4 * ct + 2 + u] = trrd2(sm + p1 + 32 * u * CP + 32 * ct, sm + p1 + (32 * u + 16) * CP + 32 * ct);
      unsigned ol = WS_LSE + (HMAP ? (((b * 32 + n) * 2 + g) * 8 + ((col + ROT * n) & 7)) * 128 + (col >> 3) * 8 : ((b * 32 + n) * 8 + 4 * g + (col >> 2)) * 128 + (col & 3) * 8), pp = L_P + l * 16, pm = L_MX + col * 16;
      v4u one = {0x3f803f80u, 0x3f803f80u, 0x3f803f80u, 0x3f803f80u};
      unsigned op2 = op + 8 * 65536;
      PIN(ol); PIN(pp); PIN(pm); PIN(one); PIN(op); PIN(op2);
      STAMP(2);
      QGATHER();
      BAR();
      STAMP(4);
      if (GQ && glu) {
        const int k = 4 * (a & 3) + v;
        const unsigned og = (unsigned)(((32 * k + (l >> 1)) * FF + 16 * sl + 8 * (l & 1)) * 2);
        dma16(wg, og, smb + L_GG + k * 1024);
        dma16(wu, og, smb + L_GG + 16384 + k * 1024);
      } else if (v < G_TOUCH) {
        const int ln = 128 * a + 64 * (v >> 1) + l;
        touch((v & 1) ? wu : wg, (unsigned)((ln >> 2) * (FF * 2)) + (unsigned)min(384 * xcd + 128 * (ln & 3), FF * 2 - 4), smb + L_JUNK);
      }
      if (glu && w < 6) LAYERNORM();
      BAR();
      STAMP(5);
      const f4 m4 = *(LAS f4*)(sm + pm);
      v4u pf[4];
#pragma unroll
      for (int u = 0; u < 4; ++u) pf[u] = *(LAS v4u*)(sm + pp + u * 1024);
      __builtin_amdgcn_sched_barrier(0);
      const float M = vmax3(vmax3(m4[0], m4[1], m4[2]), m4[3], m4[3]);
      f4 ls = z, c[4] = {z, z, z, z};
#pragma unroll
      for (int u = 0; u < 4; ++u) {
        const unsigned kk = min((unsigned)(M - m4[u]), 255u) * 0x00800080u;
#pragma unroll
        for (int e = 0; e < 4; ++e) pf[u][e] = scale_dn(pf[u][e], kk);
        ls = mfma32(one, pf[u], ls);
        if (u < 3) {
#pragma unroll
          for (int k = 0; k < 4; ++k) c[k] = mfma32(fr[4 * k + u], pf[u], c[k]);
        }
      }
      __builtin_amdgcn_sched_barrier(0);
      c[0] = mfma32(fr[3], pf[3], c[0]);
      c[1] = mfma32(fr[7], pf[3], c[1]);
      __builtin_amdgcn_sched_barrier(0);
      float il = __builtin_amdgcn_rcpf(ls[0]);
      unsigned lse = __builtin_bit_cast(unsigned, M + __builtin_amdgcn_logf(ls[0]));
      c[2] = mfma32(fr[11], pf[3], c[2]);
      c[3] = mfma32(fr[15], pf[3], c[3]);
#define PV_OUT(c, q)                                                                                          \
  {                                                                                                           \
    v4u R[2];                                                                                                 \
    _Pragma("unroll") for (int k = 0; k < 2; ++k) {                                                           \
      const f4 c0 = c[2 * k] * il, c1 = c[2 * k + 1] * il;                                                    \
      const unsigned x0 = min(pk(c0[0], c0[1]), 0xfffffffeu), x1 = min(pk(c0[2], c0[3]), 0xfffffffeu);        \
      const unsigned y0 = min(pk(c1[0], c1[1]), 0xfffffffeu), y1 = min(pk(c1[2], c1[3]), 0xfffffffeu);        \
      auto r0 = __builtin_amdgcn_permlane16_swap(x0, y0, false, false);                                       \
      auto r1 = __builtin_amdgcn_permlane16_swap(x1, y1, false, false);                                       \
      R[k] = v4u{r0[0], r1[0], r0[1], r1[1]};                                                                 \
    }                                                                                                         \
    v4u A, B;                                                                                                 \
    _Pragma("unroll") for (int e = 0; e < 4; ++e) {                                                           \
      A[e] = (unsigned)__builtin_amdgcn_update_dpp((int)R[0][e], (int)R[1][e], ROR8, 0xf, 0xc, false);        \
      B[e] = (unsigned)__builtin_amdgcn_update_dpp((int)R[1][e], (int)R[0][e], ROR8, 0xf, 0x3, false);        \
    }                                                                                                         \
    st_sc1(rs, op + 128 * q, A);                                                                              \
    st_sc1(rs, op2 + 128 * q, B);                                                                             \
  }
      __builtin_amdgcn_sched_barrier(0);
      f4 d[4] = {z, z, z, z};
#pragma unroll
      for (int u = 0; u < 4; ++u)
#pragma unroll
        for (int k = 0; k < 4; ++k) d[k] = mfma32(fr[16 + 4 * k + u], pf[u], d[k]);
      PV_OUT(c, 0);
#pragma unroll
      for (int i = 0; i < 16; ++i) {
        __builtin_amdgcn_sched_group_barrier(0x8, 1, 0);
        __builtin_amdgcn_sched_group_barrier(0x2, PV_MIX, 0);
      }
      __builtin_amdgcn_sched_barrier(0);
      PV_OUT(d, 1);
      FLAG_SET(v);
      if (GQ && glu) {
        VMWAIT(4);
        FLAG_SET(12 + v);
      }
      MARK(3);
      STAMP(6);
    }
    }
      if (glu && w < 4) {
        v4u af[8], xf[8];
        unsigned oog = WS_OPG + (8 * w + (col >> 1)) * OGP + sl * 128 + ((col & 1) * 4 + g4) * 16;
        PIN(oog);
        FLAG_WAIT4(0);
#pragma unroll 1
        for (int i = 0; i < 4; ++i) dma16<NT_G>(wd, (unsigned)(((16 * sl + 4 * w + i) * DM + 8 * l) * 2), smb + L_GD + (4 * w + i) * CP);
#pragma unroll 1
        for (int i = GQ; i < 4; ++i) {
          const int k = 4 * ((a + i) & 3) + w;
          const unsigned og = (unsigned)(((32 * k + (l >> 1)) * FF + 16 * sl + 8 * (l & 1)) * 2);
          dma16(wg, og, smb + L_GG + k * 1024);
          dma16(wu, og, smb + L_GG + 16384 + k * 1024);
        }
        unsigned pg = L_GG + (w & 1) * 16384 + 32 * (256 * (w >> 1) + 8 * g4 + tq) + 8 * tp;
        unsigned pd = L_GD + (4 * g4 + tq) * CP + 256 * w + 8 * tp, pgw = L_GR + ((w & 1) * 2 + (w >> 1)) * 1024 + l * 16;
        PIN(pg); PIN(pd); PIN(pgw);
#pragma unroll
        for (int s = 0; s < 8; ++s) xf[s] = *(LAS v4u*)(px + 512 * (w >> 1) + 64 * s);
        VMWAIT(0);
        FLAG_SET(4 + w);
        FLAG_WAIT4(1);
        if (GQ) FLAG_WAIT4(3);
        STAMP(7);
        f4 acc = z;
#pragma unroll
        for (int s = 0; s < 8; ++s) af[s] = trrd2(sm + pg + 32 * 32 * s, sm + pg + 32 * (32 * s + 4));
        __builtin_amdgcn_sched_barrier(0);
        v2u dn[8];
#pragma unroll
        for (int s = 0; s < 8; ++s) {
          acc = mfma32(af[s], xf[s], acc);
          dn[s] = trrd(sm + pd + 32 * s);
          __builtin_amdgcn_sched_barrier(0);
        }
        *(LAS f4*)(sm + pgw) = acc;
        FLAG_SET(8 + w);
        FLAG_WAIT4(2);
        LAS char* pr = sm + L_GR + l * 16;
        const f4 gx = *(LAS f4*)(pr) + *(LAS f4*)(pr + 1024), ux = *(LAS f4*)(pr + 2048) + *(LAS f4*)(pr + 3072);
        float hh[4];
#pragma unroll
        for (int e = 0; e < 4; ++e) hh[e] = ux[e] * __builtin_amdgcn_rcpf(1.f + __builtin_amdgcn_exp2f(-1.4426950408889634f * gx[e]));
        const v2u hb = {pk(hh[0], hh[1]), pk(hh[2], hh[3])};
        f4 d = z;
#pragma unroll
        for (int t = 0; t < 8; ++t) d = mfma16(dn[t], (col >> 1) == t ? hb : v2u{0u, 0u}, d);
        v4u dv = {pk(d[0], d[1]), T, pk(d[2], d[3]), T};
        PIN(dv);
        st_sc1(rs, oog, dv);
        MARK(4);
        STAMP(9);
      }
    if (a >= 12) {
      const unsigned m = (col & 1) ? 0x3f800000u : 0x3f80u;
      v4u sel = {col < 2 ? m : 0u, (col >> 1) == 1 ? m : 0u, col < 2 ? m : 0u, (col >> 1) == 1 ? m : 0u};
      PIN(sel);
      if (w == 0) {
        unsigned ot = WS_OPT + rd * 8192 + 4096 + l * 16, oy = (l >> 2) * 1024 + rd * 32 + (l & 3) * 8, pr = smb + L_RR + (l & 7) * 16;
        PIN(ot); PIN(oy); PIN(pr);
        v4u p[8];
        touch(out, (unsigned)((l & 1) * 1024 + rd * 32), smb + L_JUNK);
        HOLD_UNTIL(R_START);
        SWEEP_4(p, ot, -4096, -3072, -2048, -1024, "");
        SWEEP_4(p + 4, ot, 0, 1024, 2048, 3072, "");
        PIN4(p);
        PIN4(p + 4);
#define RRETRY()                                \
  if (TAG_BAD(p[0])) RELOAD(p[0], ot, -4096);   \
  if (TAG_BAD(p[1])) RELOAD(p[1], ot, -3072);   \
  if (TAG_BAD(p[2])) RELOAD(p[2], ot, -2048);   \
  if (TAG_BAD(p[3])) RELOAD(p[3], ot, -1024);   \
  if (TAG_BAD(p[4])) RELOAD(p[4], ot, 0);       \
  if (TAG_BAD(p[5])) RELOAD(p[5], ot, 1024);    \
  if (TAG_BAD(p[6])) RELOAD(p[6], ot, 2048);    \
  if (TAG_BAD(p[7])) RELOAD(p[7], ot, 3072);    \
  PIN4(p);                                      \
  PIN4(p + 4)
        RETRY(TAGS_BAD4(p) | TAGS_BAD4(p + 4), RRETRY());
        STAMP(8);
        f4 acc = z;
#pragma unroll
        for (int i = 0; i < 4; ++i) acc = mfma32(sel, v4u{p[2 * i].x, p[2 * i].z, p[2 * i + 1].x, p[2 * i + 1].z}, acc);
        f4 rest[6];
        for (unsigned spin = 0; spin < SPIN_CAP; ++spin) {
          v4u f0;
          v2u f1;
          asm volatile("ds_read_b128 %0, %8\n\tds_read_b64 %1, %8 offset:16\n\tds_read_b128 %2, %9\n\tds_read_b128 %3, %9 offset:128\n\t"
                       "ds_read_b128 %4, %9 offset:256\n\tds_read_b128 %5, %9 offset:384\n\tds_read_b128 %6, %9 offset:512\n\t"
                       "ds_read_b128 %7, %9 offset:640\n\ts_waitcnt lgkmcnt(0)"
                       : "=&v"(f0), "=&v"(f1), "=&v"(rest[0]), "=&v"(rest[1]), "=&v"(rest[2]), "=&v"(rest[3]), "=&v"(rest[4]), "=&v"(rest[5])
                       : "v"(smb + L_FL + 64), "v"(pr)
                       : "memory");
          if (__builtin_amdgcn_readfirstlane(f0.x & f0.y & f0.z & f0.w & f1.x & f1.y)) break;
        }
        const f4 gl = ((rest[0] + rest[1]) + (rest[2] + rest[3])) + (rest[4] + rest[5]);
#pragma unroll
        for (int e = 0; e < 4; ++e) acc[e] = (acc[e] + dpp<ROR8>(acc[e])) + gl[e];
        if (l < 8) asm volatile("global_store_dwordx2 %0, %1, %2 sc1" ::"v"(oy), "v"(v2u{pk(acc[0], acc[1]), pk(acc[2], acc[3])}), "s"(out) : "memory");
      } else if (w < 7) {
        const bool last = w == 6;
        unsigned og = WS_OPG + rd * OGP + (w - 1) * 2048 + (last ? min(l, 47) : l) * 16, og2 = last ? og : og + 1024;
        const v2u xr = *(const v2u*)((const char*)x + ((l >> 2) & 1) * 1024 + rd * 32 + (l & 3) * 8);
        unsigned m0 = (last && l >= 48) ? 0u : ~0u, m1 = last ? 0u : ~0u;
        f4 xv = {blo(xr.x), bhi(xr.x), blo(xr.y), bhi(xr.y)};
        if (!last) xv = z;
        PIN(og); PIN(og2); PIN(m0); PIN(m1); PIN(xv);
        v4u p0, p1;
        HOLD_UNTIL(R_START);
        UNDEF(p0); UNDEF(p1);
        for (unsigned spin = SPIN_CAP; spin; --spin) {
          SWEEP_2(p0, p1, og, og2, WAIT0);
          if (!(TAG_BAD(p0) | TAG_BAD(p1))) break;
        }
        STAMP(8);
        f4 acc = mfma32(sel, v4u{p0.x & m0, p0.z & m0, p1.x & m1, p1.z & m1}, z);
#pragma unroll
        for (int e = 0; e < 4; ++e) acc[e] = (acc[e] + dpp<ROR8>(acc[e])) + xv[e];
        if (l < 8) *(LAS f4*)(sm + L_RR + (w - 1) * 128 + l * 16) = acc;
        FLAG_SET(15 + w);
      }
      else if (rd == 0) {
        const unsigned ol0 = WS_LSE + (l * 16 + ((ROT * l) & 7)) * 128 + 4;
        unsigned t0, t1;
        for (unsigned spin = 0;; ++spin) {
          __builtin_amdgcn_s_sleep(32);
          asm volatile("global_load_dword %0, %2, %3 sc1\n\tglobal_load_dword %1, %2, %3 offset:1024 sc1" WAIT0
                       : "=&v"(t0), "=&v"(t1)
                       : "v"(ol0), "s"(ws)
                       : "memory");
          if (!__builtin_amdgcn_ballot_w64((t0 & t1 & T) != (t0 | t1 | T)) || spin > SPIN_CAP) break;
        }
        if (l == 0) __builtin_amdgcn_raw_buffer_store_b32(T, rs, WS_E, 0, AUX_SC1);
      }
      MARK(5);
      STAMP(9);
    }
    STAMP_FLUSH();
    __builtin_amdgcn_endpgm();
  }
  {
    const int ca = j >> 1, mb = j & 1;
    const int fl = tq | ((g4 & 1) << 2);
    if (__builtin_expect(w < 4, 1)) {
#pragma unroll 1
      for (int i = 0; i < 8; ++i, vq += 131072u, lq += 4096) dma16<NT_WQ>(wq, vq, lq);
      gld4_sc1(ep, ws, vzero);
      unsigned p = L_WQ + 256 * (8 * g4 + tq) + 16 * ((4 * w + (tp >> 1)) ^ (fl << 1)) + 8 * (tp & 1), p2 = p ^ 32;
      unsigned oqp = col < 4 ? WS_QP + ((h * 4 + j) * 8 + 2 * w) * 128 + col * 64 + g4 * 16 : NOWHERE;
      unsigned pxj = L_X + (col & 1) * XP + 16 * g4 + 256 * j;
      PIN(p); PIN(p2); PIN(oqp); PIN(pxj);
      unsigned o = WS_QP + (h * 4 + w) * 1024 + l * 16;
      unsigned pqw = smb + L_CC + (2 * w + ((l >> 2) & 1)) * SP + (l >> 3) * 32 + (l & 3) * 8, pqr = L_CC + (col & 7) * SP + 16 * g4;
      unsigned oqa = col < 4 ? WS_QA + ((col & 1) * NH + h) * 2048 + (64 * j + 16 * w + 8 * (col >> 1) + 2 * g4) * 8 : NOWHERE;
      unsigned pu = L_WUK + 256 * (32 * w + col) + 16 * (g4 ^ col);
      PIN(o); PIN(oqa); PIN(pu); PIN(pqw); PIN(pqr);
      VMWAIT(0);
      EPOCH();
      STAMP(1);
      BAR();
      STAMP(2);
      {
        v4u af[8], xf[4];
#pragma unroll
        for (int s = 0; s < 4; ++s) {
          xf[s] = *(LAS v4u*)(sm + pxj + 64 * s);
          af[s] = trrd2(sm + p + 256 * 32 * s, sm + p + 256 * (32 * s + 4));
          af[4 + s] = trrd2(sm + p2 + 256 * 32 * s, sm + p2 + 256 * (32 * s + 4));
        }
        __builtin_amdgcn_sched_barrier(0);
        f4 c0 = z, c1 = z;
#pragma unroll
        for (int s = 0; s < 4; ++s) {
          c0 = mfma32(af[s], xf[s], c0);
          c1 = mfma32(af[4 + s], xf[s], c1);
        }
        if (col & 2) c0 = c1;
        st_sc1(rs, oqp, v4u{pk(c0[0], c0[1]), T, pk(c0[2], c0[3]), T});
      }
      STAMP(3);
      v4u a, uk[8], qf[4];
      __builtin_amdgcn_s_sleep(H1_DELAY);
      asm volatile(LD16(0, 1, 2) " sc1" : "=&v"(a) : "v"(o), "s"(ws) : "memory");
      BAR();
#pragma unroll
      for (int s = 0; s < 4; ++s) {
        uk[s] = *(LAS v4u*)(sm + (pu ^ (64 * s)));
        uk[4 + s] = *(LAS v4u*)(sm + (pu ^ (64 * s)) + 4096);
      }
      VMWAIT(0);
      PIN(a);
      RETRY(TAG_BAD(a), asm volatile(LD16(0, 1, 2) " sc1" WAIT0 : "+v"(a) : "v"(o), "s"(ws) : "memory"));
      asm volatile("ds_write2_b32 %0, %1, %2 offset0:0 offset1:1" ::"v"(pqw), "v"(a.x), "v"(a.z) : "memory");
      STAMP(4);
      BAR();
#pragma unroll
      for (int s = 0; s < 4; ++s) qf[s] = *(LAS v4u*)(sm + pqr + 64 * s);
      __builtin_amdgcn_sched_barrier(0);
      f4 a0 = z, a1 = z;
#pragma unroll
      for (int s = 0; s < 4; ++s) {
        a0 = mfma32(uk[s], qf[s], a0);
        a1 = mfma32(uk[4 + s], qf[s], a1);
      }
      float q[4];
#pragma unroll
      for (int e = 0; e < 4; ++e) {
        const float mine = (col & 2) ? a1[e] : a0[e], other = (col & 2) ? a0[e] : a1[e];
        q[e] = mine + dpp<QP_2301>(other);
        q[e] += dpp<ROR4>(q[e]);
      }
      st_sc1(rs, oqa, v4u{pk(q[0], q[1]), T, pk(q[2], q[3]), T});
      STAMP(5);
      BAR();
    } else {
      unsigned sdummy = 0u;
      gld4_sc1(ep, ws, vzero);
      if (w < 6) {
        VMWAIT(1);
        LAYERNORM();
        STAMP(1);
        BAR();
#define STOUCH(base, off) asm volatile("s_load_dword %0, %1, %2" : "+s"(sdummy) : "s"(base), "s"((unsigned)(off)) : "memory")
        HOLD_UNTIL(TLB_LATE);
        {
          const int i = 2 * r + (w - 4);
          STOUCH(wuv, i * 131072);
          STOUCH(wo, h * 131072 + (w - 4) * 65536);
          STOUCH(wg, min(i, 10) * 131072);
          STOUCH(wu, min(i, 10) * 131072);
          STOUCH(wd, min(i, 10) * 131072);
          STOUCH(out, 0);
        }
        MARK(0);
      } else {
        __builtin_amdgcn_s_sleep(UK_DELAY);
        const unsigned vu = (unsigned)((((128 * j + g4) * NH + h) * DH) * 2);
#pragma unroll 1
        for (int k = w - 6; k < 32; k += 2) {
          if (k >> 1 == UK_FIRST) asm volatile("s_barrier" ::: "memory");
          dma16<NT_WUK>(wuk, vu + (unsigned)k * 32768u + 16u * (col ^ ((4 * k + g4) & 15)), smb + L_WUK + k * 1024);
        }
        MARK(1);
      }
      VMWAIT(0);
      EPOCH();
      asm volatile("s_barrier\n\ts_barrier\n\ts_barrier\n\ts_waitcnt lgkmcnt(0)" : "+s"(sdummy)::"memory");
    }
    PROBE_T(T);
    HOLD_UNTIL(T_START);
    {
      const unsigned vv = (unsigned)((((256 * ca + g4) * NH + h) * DH + 8 * (col ^ ((g4 | ((w & 2) << 1)) << 1))) * 2);
      const unsigned vo = (unsigned)(((h * DH + (l >> 5)) * DM + 256 * mb) * 2) + 16u * ((l & 31) ^ (((l >> 5) | ((w & 1) << 1) | (w & 4)) << 1));
      int kv = w + 32 * mb, ko = w + 32 * ca;
#pragma unroll 1
      for (int hf = 0; hf < 2; ++hf) {
#pragma unroll 1
        for (int i = 0; i < 4; ++i, kv = (kv + 8) & 63) dma16<NT_T>(wuv, vv + (unsigned)kv * 32768u, smb + L_WUV + kv * 1024);
#pragma unroll 1
        for (int i = 0; i < 4; ++i, ko = (ko + 8) & 63) dma16<NT_T>(wo, vo + (unsigned)ko * 2048u, smb + L_WO + ko * 1024);
        if (hf == 0) asm volatile("s_barrier" ::: "memory");
      }
    }
    const int tb = w >> 2, nq = w & 3;
    unsigned o = WS_PART + (((h * 2 + ca) * 2 + tb) * 32 + 8 * nq + g4) * 512 + 16 * col;
    unsigned pw = L_W + (tb * 32 + 8 * nq + g4) * 4;
    unsigned pv = L_WUV + 256 * (8 * g4 + tq) + 16 * ((2 * w + (tp >> 1)) ^ (fl << 1)) + 8 * (tp & 1);
    unsigned po = L_WO + 512 * (8 * g4 + tq) + 16 * ((4 * w + (tp >> 1)) ^ (fl << 1)) + 8 * (tp & 1);
    unsigned pcw = L_CC + (nq * 2 + tb) * SP2 + 16 * col + 8 * g4, pcr = L_CC + (col & 7) * SP2 + 16 * g4;
    unsigned ptw = L_CT + col * SP + (16 * w + 4 * g4) * 2, ptr = L_CT + (col & 1) * SP + 16 * g4;
    unsigned oo = col >= 4 ? NOWHERE : WS_OPT + ((16 * mb + 2 * w + (col >> 1)) * 64 + ((2 * h + ca + ROT * 4 * (2 * w + (col >> 1))) & 63)) * 128 + ((col & 1) * 4 + g4) * 16;
    v4u mk;
#pragma unroll
    for (int d = 0; d < 4; ++d) mk[d] = (col >> 1) == d ? ((col & 1) ? 0xffff0000u : 0xffffu) : 0u;
    PIN(o); PIN(pw); PIN(pv); PIN(po); PIN(pcw); PIN(pcr); PIN(ptw); PIN(ptr); PIN(oo); PIN(mk);
    STAMP(11);
        if (w == 7) {
      const unsigned ol = WS_LSE + (HMAP ? ((l * 2 + (r >> 3)) * 8 + ((xcd + ROT * l) & 7)) * 128 + ((r >> 2) & 1) * 8 : (l * 8 + xcd) * 128 + (r >> 2) * 8);
      v2u la;
      UNDEF(la);
      for (unsigned spin = SPIN_CAP; spin; --spin) {
        asm volatile("global_load_dwordx2 %0, %1, %2 sc1" WAIT0 : "+v"(la) : "v"(ol), "s"(ws) : "memory");
        if (!__builtin_amdgcn_ballot_w64(la.y != T)) break;
      }
      float M = asf(la.x);
      M = fmax2(M, dpp<ROR8>(M));
      M = fmax2(M, dpp<ROR4>(M));
      M = fmax2(M, dpp<ROR2>(M));
      M = fmax2(M, dpp<ROR1>(M));
      M = sw16_max(M);
      const float e = __builtin_amdgcn_exp2f(asf(la.x) - M);
      float L = e + dpp<ROR8>(e);
      L += dpp<ROR4>(L);
      L += dpp<ROR2>(L);
      L += dpp<ROR1>(L);
      L = sw16_add(L, L);
      const float wt = e * __builtin_amdgcn_rcpf(L);
      *(LAS unsigned*)(sm + L_W + 4 * ((l & 32) | ((l + ROT * 4 * (h & 7)) & 31))) = pk(wt, wt);
      STAMP(7);
    }
    v4u a[4];
    {
      if (LSE_PIPE && w == 7) VMWAIT(2);
      else VMWAIT(0);
      STAMP(6);
      BAR();
      SWEEP_4(a, o, 0, 256, 2048, 2304, "");
    }
    v4u A0 = mk & *(LAS unsigned*)(sm + pw), A1 = mk & *(LAS unsigned*)(sm + pw + 16);
    v4u av[8], ao[8];
#pragma unroll
    for (int s = 0; s < 8; ++s) av[s] = trrd2(sm + pv + 256 * 32 * s, sm + pv + 256 * (32 * s + 4));
#pragma unroll
    for (int t = 0; t < 2; ++t)
#pragma unroll
      for (int s = 0; s < 4; ++s) ao[4 * t + s] = trrd2(sm + (po ^ (32 * t)) + 512 * 32 * s, sm + (po ^ (32 * t)) + 512 * (32 * s + 4));
    VMWAIT(0);
    asm volatile("" : "+v"(a[0]), "+v"(a[1]), "+v"(a[2]), "+v"(a[3]), "+v"(A0), "+v"(A1)::"memory");
    RETRY(HOLES4(a), TRETRY());
    STAMP(8);
    {
      f4 c0 = mfma32(A0, a[0], z), c1 = mfma32(A0, a[1], z);
      c0 = mfma32(A1, a[2], c0);
      c1 = mfma32(A1, a[3], c1);
      if (g4 < 2) {
        *(LAS v2u*)(sm + pcw) = v2u{pk(c0[0], c0[1]), pk(c0[2], c0[3])};
        *(LAS v2u*)(sm + pcw + 256) = v2u{pk(c1[0], c1[1]), pk(c1[2], c1[3])};
      }
    }
    BAR();
    {
      f4 acc = z;
      v4u bf[8];
#pragma unroll
      for (int s = 0; s < 8; ++s) bf[s] = *(LAS v4u*)(sm + pcr + 64 * s);
      __builtin_amdgcn_sched_barrier(0);
#pragma unroll
      for (int s = 0; s < 8; ++s) acc = mfma32(av[s], bf[s], acc);
#pragma unroll
      for (int e = 0; e < 4; ++e) {
        acc[e] += dpp<QP_2301>(acc[e]);
        acc[e] += dpp<ROR4>(acc[e]);
      }
      if (col < 2) *(LAS v2u*)(sm + ptw) = v2u{pk(acc[0], acc[1]), pk(acc[2], acc[3])};
    }
    BAR();
    {
      f4 c0 = z, c1 = z;
      v4u bf[4];
#pragma unroll
      for (int s = 0; s < 4; ++s) bf[s] = *(LAS v4u*)(sm + ptr + 64 * s);
      __builtin_amdgcn_sched_barrier(0);
#pragma unroll
      for (int s = 0; s < 4; ++s) {
        c0 = mfma32(ao[s], bf[s], c0);
        c1 = mfma32(ao[4 + s], bf[s], c1);
      }
      if (col & 2) c0 = c1;
      st_sc1(rs, oo, v4u{pk(c0[0], c0[1]), T, pk(c0[2], c0[3]), T});
    }
    MARK(2);
    STAMP(9);
  }
  STAMP_FLUSH();
}

#define WS_END 0
#define WS_BASE (WS_END ? ((ws_size - (size_t)WS_END * 1048576) & ~(size_t)4095) : 0)
extern "C" void kernel_launch(void* const* d_in, const int* in_sizes, int n_in, void* d_out, int out_size, void* d_ws, size_t ws_size,
                              hipStream_t stream) {
  mla_glu_decode<<<256, 512, 0, stream>>>((const u16*)d_in[0], (const u16*)d_in[1], (const u16*)d_in[2], (const u16*)d_in[3],
                                          (const u16*)d_in[4], (const u16*)d_in[5], (const u16*)d_in[7], (const u16*)d_in[8],
                                          (const u16*)d_in[9], (u16*)d_out, (char*)d_ws + WS_BASE);
}
```

```cpp
#define U_START 65
#define C_START 75
#define A_START 90
#include <hip/hip_runtime.h>
#include <stdint.h>

#define GAS __attribute__((address_space(1)))
#define LAS __attribute__((address_space(3)))
typedef unsigned short u16;
typedef unsigned v4u __attribute__((ext_vector_type(4)));
typedef unsigned v2u __attribute__((ext_vector_type(2)));
typedef float f4 __attribute__((ext_vector_type(4)));
typedef float f2 __attribute__((ext_vector_type(2)));
typedef __bf16 bf8 __attribute__((ext_vector_type(8)));
typedef __bf16 bf2 __attribute__((ext_vector_type(2)));
typedef short s4 __attribute__((ext_vector_type(4)));

#define ROT 1
namespace {
constexpr unsigned NOWHERE = 0x7ff00000u;
constexpr int NH = 32, SK = 4096, DH = 128, DM = 512, DC = 512, FF = 1376;
constexpr int NSL = FF / 16;
constexpr int WS_E = 0;
constexpr int WS_QP = 4096;
constexpr int WS_QA = WS_QP + NH * 4 * 1024;
constexpr int WS_LSE = WS_QA + 2 * NH * 2048;
constexpr int WS_GO = WS_LSE + 2 * 32 * 16 * 128;
constexpr int WS_OPT = WS_GO + 2 * 32 * 16 * 128;
constexpr int WS_OPG = WS_OPT + 32 * 64 * 128;
constexpr int OGP = 16384 + ROT * 384;
constexpr int WS_PART = (WS_OPG + 32 * OGP + 4095) / 4096 * 4096;
constexpr int WS_BYTES = WS_PART + NH * 2 * 2 * 32 * 512;
constexpr int WS_STAMP = 7 << 19;
constexpr int CP = 1056;
constexpr int XP = 1088, SP = 288, SP2 = 544;
constexpr int L_C = 0, L_Q = 128 * CP, L_P = L_Q + 16 * CP, L_X = L_P + 4096, L_MX = L_X + 2 * XP, L_RR = L_MX + 256,
              L_JUNK = L_RR + 1024, L_FL = L_JUNK + 256, L_END = L_FL + 128;
static_assert(L_END <= 163840, "LDS");
constexpr int L_GG = 0, L_GD = 32768, L_GR = 65536;
constexpr int L_WUV = 0, L_WO = 65536, L_WQ = 0, L_WUK = 32768, L_CC = 131072, L_CT = L_CC + 8 * SP2, L_W = L_CT + 2 * SP;
#define UK_DELAY 20
#define H1_DELAY 12
#define T_START 340
#define CODE_TOUCH 1
#define NT_CKV 0
#define NT_WQ 0
#define NT_WUK 0
#define NT_T 0
#define NT_G 0
#define GLU_EARLY 0
#define G_START 0
#define LSE_LATE 0
#define LSE_WAVE 3
#define DATA_DELAY 0
#define NEWRED 1
#define R_START 0
#define G_TOUCH 4
#define LSE_PIPE 0
#define PV_MIX 4
#define LSE_QK 1
#define HMAP 1
#define GQ 1
#define DOWN_FIRST 1
#define EARLY_SWEEP 1
#define GO_DELAY 0
#define OUT_TOUCH 1
#define OUT_SC1 1
#define BUMP_EARLY 1
#define G2_DELAY 0
#define UK_FIRST 6
#define UK_TOUCH 1
#define TLB_LATE 150
#define TLB_FINE 0
#define PIECEWISE 1
#define PF_GLU 1
constexpr unsigned SPIN_CAP = 1u << 20;
constexpr int AUX_SC1 = 16;

#define STAMP_W 0
#define STAMP(i)
#define STAMP_FLUSH()
#define PROBE_T(T_)

__device__ __forceinline__ f4 mfma32(v4u a, v4u b, f4 c) {
  return __builtin_amdgcn_mfma_f32_16x16x32_bf16(__builtin_bit_cast(bf8, a), __builtin_bit_cast(bf8, b), c, 0, 0, 0);
}
__device__ __forceinline__ f4 mfma16(v2u a, v2u b, f4 c) {
  return __builtin_amdgcn_mfma_f32_16x16x16bf16_1k(__builtin_bit_cast(s4, a), __builtin_bit_cast(s4, b), c, 0, 0, 0);
}
__device__ __forceinline__ unsigned pk(float lo, float hi) {
  f2 x = {lo, hi};
  return __builtin_bit_cast(unsigned, __builtin_convertvector(x, bf2));
}
__device__ __forceinline__ float asf(unsigned u) { return __builtin_bit_cast(float, u); }
__device__ __forceinline__ float blo(unsigned u) { return __builtin_bit_cast(float, u << 16); }
__device__ __forceinline__ float bhi(unsigned u) { return __builtin_bit_cast(float, u & 0xffff0000u); }
template <int CTRL> __device__ __forceinline__ float dpp(float v) {
  return __builtin_bit_cast(float, __builtin_amdgcn_update_dpp(0, __builtin_bit_cast(int, v), CTRL, 0xf, 0xf, true));
}
__device__ __forceinline__ float fmax2(float a, float b) {
#pragma float_control(precise, off)
  return __builtin_fmaxf(a, b);
}
typedef unsigned short h2 __attribute__((ext_vector_type(2)));
__device__ __forceinline__ unsigned scale_dn(unsigned p, unsigned kk) {
  return __builtin_bit_cast(unsigned, __builtin_elementwise_sub_sat(__builtin_bit_cast(h2, p), __builtin_bit_cast(h2, kk)));
}
#define PIN(x) asm volatile("" : "+v"(x))
constexpr int ROR1 = 0x121, ROR2 = 0x122, ROR4 = 0x124, ROR8 = 0x128, QP_2301 = 0x4e, QP_2222 = 0xaa;
__device__ __forceinline__ float sw32_add(float a, float b) {
  auto r = __builtin_amdgcn_permlane32_swap(__builtin_bit_cast(unsigned, a), __builtin_bit_cast(unsigned, b), false, false);
  return asf(r[0]) + asf(r[1]);
}
__device__ __forceinline__ float sw16_add(float a, float b) {
  auto r = __builtin_amdgcn_permlane16_swap(__builtin_bit_cast(unsigned, a), __builtin_bit_cast(unsigned, b), false, false);
  return asf(r[0]) + asf(r[1]);
}
__device__ __forceinline__ float vmax(unsigned a, unsigned b) {
  float r;
  asm("v_max_f32 %0, %1, %2" : "=v"(r) : "v"(a), "v"(b));
  return r;
}
__device__ __forceinline__ float vmax3(float a, float b, float c) {
  float r;
  asm("v_max3_f32 %0, %1, %2, %3" : "=v"(r) : "v"(a), "v"(b), "v"(c));
  return r;
}
__device__ __forceinline__ float sw32_max(float a) {
  auto r = __builtin_amdgcn_permlane32_swap(__builtin_bit_cast(unsigned, a), __builtin_bit_cast(unsigned, a), false, false);
  return vmax(r[0], r[1]);
}
__device__ __forceinline__ float sw16_max(float a) {
  auto r = __builtin_amdgcn_permlane16_swap(__builtin_bit_cast(unsigned, a), __builtin_bit_cast(unsigned, a), false, false);
  return vmax(r[0], r[1]);
}
__device__ __forceinline__ float wave_sum(float v) {
  v += dpp<ROR8>(v);
  v += dpp<ROR4>(v);
  v += dpp<ROR2>(v);
  v += dpp<ROR1>(v);
  v = sw16_add(v, v);
  return sw32_add(v, v);
}
__device__ __forceinline__ v2u trrd(LAS char* p) {
  return __builtin_bit_cast(v2u, __builtin_amdgcn_ds_read_tr16_b64_v4i16((LAS s4*)p));
}
__device__ __forceinline__ v4u trrd2(LAS char* p0, LAS char* p1) {
  v2u a = trrd(p0), b = trrd(p1);
  return v4u{a.x, a.y, b.x, b.y};
}
#define BAR() asm volatile("s_waitcnt lgkmcnt(0)\n\ts_barrier" ::: "memory")
#define VMWAIT(k) asm volatile("s_waitcnt vmcnt(%0)" ::"n"(k) : "memory")
__device__ __forceinline__ void gld16(v4u& d, const void* base, unsigned off) {
  asm volatile("global_load_dwordx4 %0, %1, %2" : "=v"(d) : "v"(off), "s"(base) : "memory");
}
__device__ __forceinline__ void gld4_sc1(unsigned& d, const void* base, unsigned off) {
  asm volatile("global_load_dword %0, %1, %2 sc1" : "=v"(d) : "v"(off), "s"(base) : "memory");
}
template <int NT = 0> __device__ __forceinline__ void touch(const void* base, unsigned off, unsigned lds) {
  if (NT) asm volatile("s_mov_b32 m0, %2\n\ts_nop 0\n\tglobal_load_lds_dword %0, %1 nt" ::"v"(off), "s"(base), "s"(lds) : "memory");
  else asm volatile("s_mov_b32 m0, %2\n\ts_nop 0\n\tglobal_load_lds_dword %0, %1" ::"v"(off), "s"(base), "s"(lds) : "memory");
}
__device__ __forceinline__ void touch_sc1(const void* base, unsigned off, unsigned lds) {
  asm volatile("s_mov_b32 m0, %2\n\ts_nop 0\n\tglobal_load_lds_dword %0, %1 sc1" ::"v"(off), "s"(base), "s"(lds) : "memory");
}
#define LD16(i, a, o) "global_load_dwordx4 %" #i ", %" #a ", %" #o
#define SWEEP_4(q, o, o0, o1, o2, o3, W)                                                                                     \
  asm volatile(LD16(0, 4, 5) " offset:" #o0 " sc1\n\t" LD16(1, 4, 5) " offset:" #o1 " sc1\n\t" LD16(2, 4, 5) " offset:" #o2         \
               " sc1\n\t" LD16(3, 4, 5) " offset:" #o3 " sc1" W                                                               \
               : "=&v"((q)[0]), "=&v"((q)[1]), "=&v"((q)[2]), "=&v"((q)[3])                                                  \
               : "v"(o), "s"(ws)                                                                                             \
               : "memory")
#define RESWEEP_4(q, o, o0, o1, o2, o3)                                                                                      \
  asm volatile(LD16(0, 4, 5) " offset:" #o0 " sc1\n\t" LD16(1, 4, 5) " offset:" #o1 " sc1\n\t" LD16(2, 4, 5) " offset:" #o2         \
               " sc1\n\t" LD16(3, 4, 5) " offset:" #o3 " sc1\n\ts_waitcnt vmcnt(0)"                                            \
               : "+v"((q)[0]), "+v"((q)[1]), "+v"((q)[2]), "+v"((q)[3])                                                      \
               : "v"(o), "s"(ws)                                                                                             \
               : "memory")
#define RELOAD(q, o, off) asm volatile(LD16(0, 1, 2) " offset:" #off " sc1" : "+v"(q) : "v"(o), "s"(ws) : "memory")
#define PIN4(q) asm volatile("s_waitcnt vmcnt(0)" : "+v"((q)[0]), "+v"((q)[1]), "+v"((q)[2]), "+v"((q)[3])::"memory")
#define TAG_BAD(q) __builtin_amdgcn_ballot_w64((T & (q).y & (q).w) != (T | (q).y | (q).w))
#define HOLE(q) __builtin_amdgcn_ballot_w64(max(max((q).x, (q).y), max((q).z, (q).w)) == ~0u)
#define MAX4(q) max(max((q).x, (q).y), max((q).z, (q).w))
#define HOLES4(q) __builtin_amdgcn_ballot_w64(max(max(MAX4((q)[0]), MAX4((q)[1])), max(MAX4((q)[2]), MAX4((q)[3]))) == ~0u)
#define TRETRY()                          \
  if (HOLE(a[0])) RELOAD(a[0], o, 0);     \
  if (HOLE(a[1])) RELOAD(a[1], o, 256);   \
  if (HOLE(a[2])) RELOAD(a[2], o, 2048);  \
  if (HOLE(a[3])) RELOAD(a[3], o, 2304);  \
  PIN4(a)
#define SWEEP_L(W)                                                                                                          \
  asm volatile("global_load_dwordx2 %0, %3, %5 sc1\n\tglobal_load_dwordx2 %1, %4, %5 sc1\n\t"                              \
               "global_load_dwordx2 %2, %4, %5 offset:512 sc1" W                                                            \
               : "=&v"(la), "=&v"(l0), "=&v"(l1)                                                                            \
               : "v"(ola), "v"(olo), "s"(ws)                                                                                \
               : "memory")
#define SWEEP_2(p, q, oa, ob, W) \
  asm volatile(LD16(0, 2, 4) " sc1\n\t" LD16(1, 3, 4) " sc1" W : "=&v"(p), "=&v"(q) : "v"(oa), "v"(ob), "s"(ws) : "memory")
#define TAGS_BAD4(q) \
  __builtin_amdgcn_ballot_w64((T & (q)[0].y & (q)[0].w & (q)[1].y & (q)[1].w & (q)[2].y & (q)[2].w & (q)[3].y & (q)[3].w) != \
                              (T | (q)[0].y | (q)[0].w | (q)[1].y | (q)[1].w | (q)[2].y | (q)[2].w | (q)[3].y | (q)[3].w))
#define FLAG_SET(i) asm volatile("ds_write_b32 %0, %1" ::"v"(smb + L_FL + 4 * (i)), "v"(1u) : "memory")
#define FLAG_WAIT4(i)                                                                                     \
  for (unsigned spin_ = 0; spin_ < SPIN_CAP; ++spin_) {                                                   \
    v4u f_;                                                                                               \
    asm volatile("ds_read_b128 %0, %1\n\ts_waitcnt lgkmcnt(0)" : "=v"(f_) : "v"(smb + L_FL + 16 * (i)) : "memory"); \
    if (__builtin_amdgcn_readfirstlane(f_.x & f_.y & f_.z & f_.w)) break;                                 \
  }
#define HOLD_UNTIL(ticks)              \
  asm volatile("" : "+s"(tstart));     \
  while ((unsigned)__builtin_amdgcn_s_memrealtime() - (unsigned)tstart < (ticks)) __builtin_amdgcn_s_sleep(1)
#define MARK(i) asm volatile(".Lmla_e" #i ":")
#define RETRY(bad, again)                                   \
  if (bad) {                                                \
    for (unsigned spin = SPIN_CAP; spin; --spin) {          \
      again;                                                \
      if (!(bad)) break;                                    \
    }                                                       \
  }
#define UNDEF(x) asm volatile("" : "=v"(x))
#define EPOCH() asm volatile("v_add_u32 %0, 1, %1" : "=v"(T) : "v"(ep) : "memory")
#define WAIT0 "\n\ts_waitcnt vmcnt(0)"
template <int NT = 0> __device__ __forceinline__ void dma16(const void* base, unsigned off, unsigned lds) {
  if (NT) asm volatile("s_mov_b32 m0, %2\n\ts_nop 0\n\tglobal_load_lds_dwordx4 %0, %1 nt" ::"v"(off), "s"(base), "s"(lds) : "memory");
  else asm volatile("s_mov_b32 m0, %2\n\ts_nop 0\n\tglobal_load_lds_dwordx4 %0, %1" ::"v"(off), "s"(base), "s"(lds) : "memory");
}
__device__ __forceinline__ v4u ld_sc1(__amdgpu_buffer_rsrc_t rs, int off) {
  return __builtin_amdgcn_raw_buffer_load_b128(rs, off, 0, AUX_SC1);
}
__device__ __forceinline__ v2u ld8_sc1(__amdgpu_buffer_rsrc_t rs, int off) {
  return __builtin_amdgcn_raw_buffer_load_b64(rs, off, 0, AUX_SC1);
}
__device__ __forceinline__ void st_sc1(__amdgpu_buffer_rsrc_t rs, int off, v4u v) {
  __builtin_amdgcn_raw_buffer_store_b128(v, rs, off, 0, AUX_SC1);
}
__device__ __forceinline__ void ln_stats(v4u r, float& mean, float& rstd) {
  float e[8] = {blo(r.x), bhi(r.x), blo(r.y), bhi(r.y), blo(r.z), bhi(r.z), blo(r.w), bhi(r.w)};
  float s = ((e[0] + e[1]) + (e[2] + e[3])) + ((e[4] + e[5]) + (e[6] + e[7]));
  mean = wave_sum(s) * (1.f / DM);
  float q = 0.f;
#pragma unroll
  for (int i = 0; i < 8; ++i) {
    float d = e[i] - mean;
    q = fmaf(d, d, q);
  }
  rstd = __builtin_amdgcn_rsqf(wave_sum(q) * (1.f / DM) + 1e-5f);
}
}

__global__ void __launch_bounds__(512, 2)
mla_glu_decode(const u16* __restrict__ x, const u16* __restrict__ ckv, const u16* __restrict__ wq, const u16* __restrict__ wuk,
               const u16* __restrict__ wuv, const u16* __restrict__ wo, const u16* __restrict__ wg, const u16* __restrict__ wu,
               const u16* __restrict__ wd, u16* __restrict__ out, char* __restrict__ ws) {
  unsigned long long tstart = __builtin_amdgcn_s_memrealtime();
  __builtin_amdgcn_sched_barrier(0);
  __shared__ __attribute__((aligned(16))) char smem_[L_END];
  LAS char* sm = (LAS char*)smem_;
  const unsigned smb = (unsigned)(size_t)sm;
  const int tid = threadIdx.x, l = tid & 63, w = __builtin_amdgcn_readfirstlane(tid >> 6);
  const int col = l & 15, g4 = l >> 4, tq = (l & 15) >> 2, tp = l & 3;
  const int xcd = blockIdx.x & 7, r = blockIdx.x >> 3;
  const __amdgpu_buffer_rsrc_t rs = __builtin_amdgcn_make_buffer_rsrc(ws, 0, WS_BYTES, 0x00020000);
  int bw = (blockIdx.x >> 7) * w;
  asm volatile("" : "+s"(bw));
  if (bw >= 5) {
    const char* pc0;
    unsigned len;
    asm volatile("s_getpc_b64 %0\n.Lmla_pc:\n\ts_mov_b32 %1, .Lmla_e0-.Lmla_pc\n\ts_max_i32 %1, %1, .Lmla_e1-.Lmla_pc\n\t"
                 "s_max_i32 %1, %1, .Lmla_e2-.Lmla_pc\n\ts_max_i32 %1, %1, .Lmla_e3-.Lmla_pc\n\ts_max_i32 %1, %1, .Lmla_e4-.Lmla_pc\n\t"
                 "s_max_i32 %1, %1, .Lmla_e5-.Lmla_pc"
                 : "=s"(pc0), "=s"(len)::"scc");
    touch(pc0, min((unsigned)(64 * (bw - 5) + l) * 128u, len), smb + L_JUNK);
  }
  __builtin_amdgcn_sched_barrier(0);
  const int h = HMAP ? xcd + 8 * ((r >> 2) & 3) : 4 * xcd + ((r >> 2) & 3), j = r & 3;
  unsigned vq = (unsigned)(((128 * j + g4 + 4 * w) * 4096 + h * DH + 8 * (col ^ ((g4 | ((w & 2) << 1)) << 1))) * 2), vx = (w & 1) * 1024 + l * 16;
  unsigned vzero = 0u;
  int lq = smb + L_WQ + w * 1024, lx = smb + L_X + (w & 1) * XP;
  asm volatile("" : "+v"(vq), "+v"(vx), "+v"(vzero), "+s"(lq), "+s"(lx));
  asm volatile("" ::"s"(x), "s"(ckv), "s"(wq), "s"(wuk), "s"(wuv), "s"(wo), "s"(wg), "s"(wu), "s"(wd), "s"(out), "s"(ws));
  STAMP(0);

  unsigned ep, T;
  LAS char* const px = sm + L_X + (col & 1) * XP + 16 * g4;
  if ((w >> 1) == 2) dma16(x, vx, lx);
  const f4 z = {0.f, 0.f, 0.f, 0.f};
#define LAYERNORM()                                                                                                              \
  {                                                                                                                              \
    const v4u xr = *(LAS v4u*)(sm + L_X + (w & 1) * XP + l * 16);                                                                \
    float e[8] = {blo(xr.x), bhi(xr.x), blo(xr.y), bhi(xr.y), blo(xr.z), bhi(xr.z), blo(xr.w), bhi(xr.w)};                       \
    const float mean = wave_sum(((e[0] + e[1]) + (e[2] + e[3])) + ((e[4] + e[5]) + (e[6] + e[7]))) * (1.f / DM);                 \
    float q = 0.f;                                                                                                               \
    _Pragma("unroll") for (int i = 0; i < 8; ++i) {                                                                              \
      e[i] -= mean;                                                                                                              \
      q = fmaf(e[i], e[i], q);                                                                                                   \
    }                                                                                                                            \
    const float rstd = __builtin_amdgcn_rsqf(wave_sum(q) * (1.f / DM) + 1e-5f);                                                  \
    *(LAS v4u*)(sm + L_X + (w & 1) * XP + l * 16) =                                                                              \
        v4u{pk(e[0] * rstd, e[1] * rstd), pk(e[2] * rstd, e[3] * rstd), pk(e[4] * rstd, e[5] * rstd), pk(e[6] * rstd, e[7] * rstd)}; \
  }

  if (__builtin_expect(r >= 16, 0)) {
    const int a = r - 16, b = xcd >> 2, n = 8 * (xcd & 3) + (a >> 1), g = a & 1;
    const int sl = 12 * xcd + a, rd = (4 * xcd + a) & 31;
    const bool glu = a < 12 && sl < NSL;
    const int v = w & 3;
    unsigned op = WS_PART + ((((16 * g + (col & 7)) * 2 + (v >> 1)) * 2 + b) * 32 + ((n + ROT * 4 * (col & 7)) & 31)) * 512 + 256 * (v & 1) +
                  (col >> 3) * 64 + (g4 & 1) * 32 + (g4 >> 1) * 16;
    gld4_sc1(ep, ws, vzero);
    unsigned o = WS_QA + (b * NH + 16 * g) * 2048 + w * 4096 + l * 16, pqs = smb + L_Q + 2 * w * CP + l * 8;
    PIN(o); PIN(pqs);
    {
    {
      const unsigned vc = (unsigned)(((b * SK + 128 * n) * DC + 8 * l) * 2);
      STAMP(10);
      HOLD_UNTIL(U_START);
      if (w < 4) touch(wuk, (unsigned)((32 * a + 8 * w + (l >> 3)) * 8192 + (xcd + 8 * ((l >> 1) & 3)) * 256 + (l & 1) * 128), smb + L_JUNK);
      HOLD_UNTIL(C_START);
    if (w >= 4) {
#pragma unroll
      for (int q = 0; q < 4; ++q) st_sc1(rs, op + 128 * (q >> 1) + (q & 1) * 8 * 65536, v4u{~0u, ~0u, ~0u, ~0u});
      if (w == 7 && l < 32) *(LAS unsigned*)(sm + L_FL + 4 * l) = 0u;
    }
      HOLD_UNTIL(A_START);
#pragma unroll 1
      for (int q = 0; q < 4; ++q) {
        int row = w + ((64 * g + 32 * q) & 127);
        asm volatile("s_barrier" ::: "memory");
#pragma unroll 1
        for (int i = 0; i < 4; ++i, row += 8) dma16<NT_CKV>(ckv, vc + row * 1024u, smb + L_C + row * CP);
      }
    }
#define CHALF()                               \
  asm volatile("s_barrier" ::: "memory");     \
  SWEEP_4(qg, o, 0, 1024, 2048, 3072, "");    \
  VMWAIT(12);                                 \
  asm volatile("s_barrier" ::: "memory")
#define CLAND() \
  VMWAIT(4);    \
  EPOCH();      \
  PROBE_T(T);   \
  BAR();        \
  STAMP(1)
#define QRETRY()                              \
  if (TAG_BAD(qg[0])) RELOAD(qg[0], o, 0);    \
  if (TAG_BAD(qg[1])) RELOAD(qg[1], o, 1024); \
  if (TAG_BAD(qg[2])) RELOAD(qg[2], o, 2048); \
  if (TAG_BAD(qg[3])) RELOAD(qg[3], o, 3072); \
  PIN4(qg)
#define QGATHER()                                                                                                           \
  VMWAIT(0);                                                                                                      \
  asm volatile("" : "+v"(qg[0]), "+v"(qg[1]), "+v"(qg[2]), "+v"(qg[3])::"memory");                                          \
  RETRY(TAGS_BAD4(qg), QRETRY());                                                                                           \
  STAMP(3);                                                                                                                 \
  asm volatile("ds_write2_b32 %0, %2, %3 offset0:0 offset1:1\n\tds_write2_b32 %0, %4, %5 offset0:128 offset1:129\n\t"        \
               "ds_write2_b32 %1, %6, %7 offset0:0 offset1:1\n\tds_write2_b32 %1, %8, %9 offset0:128 offset1:129" ::"v"(pqs), \
               "v"(pqs + CP), "v"(qg[0].x), "v"(qg[0].z), "v"(qg[1].x), "v"(qg[1].z), "v"(qg[2].x), "v"(qg[2].z), "v"(qg[3].x), \
               "v"(qg[3].z)                                                                                                 \
               : "memory")
    if (__builtin_expect(w < 4, 1)) {
      v4u fr[32], qg[4];
      unsigned pc = L_C + (32 * w + col) * CP + 16 * g4;
      PIN(pc);
      CHALF();
      const bool early = (w >> 1) == g;
      if (!early) {
        CLAND();
      }
#pragma unroll
      for (int kt = 0; kt < 2; ++kt)
#pragma unroll
        for (int s = 0; s < 16; ++s) fr[16 * kt + s] = *(LAS v4u*)(sm + pc + 16 * kt * CP + 64 * s);
      if (early) {
        CLAND();
      }
      const int u = w ^ (2 * g);
      unsigned p = L_Q + col * CP + 16 * g4, pp = L_P + u * 1024 + l * 16, pm = L_MX + col * 16 + u * 4;
      unsigned ol = WS_LSE + (HMAP ? (((b * 32 + n) * 2 + g) * 8 + ((col + ROT * n) & 7)) * 128 + (col >> 3) * 8 : ((b * 32 + n) * 8 + 4 * g + (col >> 2)) * 128 + (col & 3) * 8);
      if (g4) ol = NOWHERE;
      PIN(p); PIN(pp); PIN(pm); PIN(ol);
      STAMP(2);
      QGATHER();
      BAR();
      STAMP(4);
      f4 s0 = z, s1 = z;
      v4u qf[16];
#pragma unroll
      for (int s = 0; s < 6; ++s) qf[s] = *(LAS v4u*)(sm + p + 64 * s);
      __builtin_amdgcn_sched_barrier(0);
#pragma unroll
      for (int s = 0; s < 16; ++s) {
        if (s + 6 < 16) qf[s + 6] = *(LAS v4u*)(sm + p + 64 * (s + 6));
        s0 = mfma32(fr[s], qf[s], s0);
        s1 = mfma32(fr[16 + s], qf[s], s1);
        __builtin_amdgcn_sched_barrier(0);
      }
      float mx = fmax2(fmax2(fmax2(s0[0], s0[1]), fmax2(s0[2], s0[3])), fmax2(fmax2(s1[0], s1[1]), fmax2(s1[2], s1[3])));
      mx = sw32_max(sw16_max(mx));
      constexpr float SC = 0.08838834764831845f * 1.4426950408889634f;
      const float mi = __builtin_ceilf(mx * SC);
#pragma unroll
      for (int e = 0; e < 4; ++e) {
        s0[e] = __builtin_amdgcn_exp2f(fmaf(s0[e], SC, -mi));
        s1[e] = __builtin_amdgcn_exp2f(fmaf(s1[e], SC, -mi));
      }
      *(LAS float*)(sm + pm) = mi;
      *(LAS v4u*)(sm + pp) = v4u{pk(s0[0], s0[1]), pk(s0[2], s0[3]), pk(s1[0], s1[1]), pk(s1[2], s1[3])};
      BAR();
      STAMP(5);
      if (w == LSE_QK - 1) {
        const f4 m4 = *(LAS f4*)(sm + pm - 4 * u);
        v4u pf[4];
#pragma unroll
        for (int k = 0; k < 4; ++k) pf[k] = *(LAS v4u*)(sm + pp + (k - u) * 1024);
        __builtin_amdgcn_sched_barrier(0);
        const float M = vmax3(vmax3(m4[0], m4[1], m4[2]), m4[3], m4[3]);
        const v4u one = {0x3f803f80u, 0x3f803f80u, 0x3f803f80u, 0x3f803f80u};
        f4 ls = z;
#pragma unroll
        for (int u = 0; u < 4; ++u) {
          const unsigned kk = min((unsigned)(M - m4[u]), 255u) * 0x00800080u;
#pragma unroll
          for (int e = 0; e < 4; ++e) pf[u][e] = scale_dn(pf[u][e], kk);
          ls = mfma32(one, pf[u], ls);
        }
        __builtin_amdgcn_raw_buffer_store_b64(v2u{__builtin_bit_cast(unsigned, M + __builtin_amdgcn_logf(ls[0])), T}, rs, ol, 0, AUX_SC1);
      }
    } else {
      v4u fr[32], qg[4];
      unsigned p0 = L_C + (64 * g + 4 * g4 + tq) * CP + 256 * v + 8 * tp, p1 = L_C + (64 * (g ^ 1) + 4 * g4 + tq) * CP + 256 * v + 8 * tp;
      PIN(p0); PIN(p1);
      CHALF();
#pragma unroll
      for (int ct = 0; ct < 8; ++ct)
#pragma unroll
        for (int u = 0; u < 2; ++u) fr[4 * ct + u] = trrd2(sm + p0 + 32 * u * CP + 32 * ct, sm + p0 + (32 * u + 16) * CP + 32 * ct);
      CLAND();
#pragma unroll
      for (int ct = 0; ct < 8; ++ct)
#pragma unroll
        for (int u = 0; u < 2; ++u) fr[4 * ct + 2 + u] = trrd2(sm + p1 + 32 * u * CP + 32 * ct, sm + p1 + (32 * u + 16) * CP + 32 * ct);
      unsigned ol = WS_LSE + (HMAP ? (((b * 32 + n) * 2 + g) * 8 + ((col + ROT * n) & 7)) * 128 + (col >> 3) * 8 : ((b * 32 + n) * 8 + 4 * g + (col >> 2)) * 128 + (col & 3) * 8), pp = L_P + l * 16, pm = L_MX + col * 16;
      v4u one = {0x3f803f80u, 0x3f803f80u, 0x3f803f80u, 0x3f803f80u};
      unsigned op2 = op + 8 * 65536;
      PIN(ol); PIN(pp); PIN(pm); PIN(one); PIN(op); PIN(op2);
      STAMP(2);
      QGATHER();
      BAR();
      STAMP(4);
      if (GQ && glu) {
        const int k = 4 * (a & 3) + v;
        const unsigned og = (unsigned)(((32 * k + (l >> 1)) * FF + 16 * sl + 8 * (l & 1)) * 2);
        dma16(wg, og, smb + L_GG + k * 1024);
        dma16(wu, og, smb + L_GG + 16384 + k * 1024);
      } else if (v < G_TOUCH) {
        const int ln = 128 * a + 64 * (v >> 1) + l;
        touch((v & 1) ? wu : wg, (unsigned)((ln >> 2) * (FF * 2)) + (unsigned)min(384 * xcd + 128 * (ln & 3), FF * 2 - 4), smb + L_JUNK);
      }
      if (glu && w < 6) LAYERNORM();
      BAR();
      STAMP(5);
      const f4 m4 = *(LAS f4*)(sm + pm);
      v4u pf[4];
#pragma unroll
      for (int u = 0; u < 4; ++u) pf[u] = *(LAS v4u*)(sm + pp + u * 1024);
      __builtin_amdgcn_sched_barrier(0);
      const float M = vmax3(vmax3(m4[0], m4[1], m4[2]), m4[3], m4[3]);
      f4 ls = z, c[4] = {z, z, z, z};
#pragma unroll
      for (int u = 0; u < 4; ++u) {
        const unsigned kk = min((unsigned)(M - m4[u]), 255u) * 0x00800080u;
#pragma unroll
        for (int e = 0; e < 4; ++e) pf[u][e] = scale_dn(pf[u][e], kk);
        ls = mfma32(one, pf[u], ls);
        if (u < 3) {
#pragma unroll
          for (int k = 0; k < 4; ++k) c[k] = mfma32(fr[4 * k + u], pf[u], c[k]);
        }
      }
      __builtin_amdgcn_sched_barrier(0);
      c[0] = mfma32(fr[3], pf[3], c[0]);
      c[1] = mfma32(fr[7], pf[3], c[1]);
      __builtin_amdgcn_sched_barrier(0);
      float il = __builtin_amdgcn_rcpf(ls[0]);
      unsigned lse = __builtin_bit_cast(unsigned, M + __builtin_amdgcn_logf(ls[0]));
      c[2] = mfma32(fr[11], pf[3], c[2]);
      c[3] = mfma32(fr[15], pf[3], c[3]);
#define PV_OUT(c, q)                                                                                          \
  {                                                                                                           \
    v4u R[2];                                                                                                 \
    _Pragma("unroll") for (int k = 0; k < 2; ++k) {                                                           \
      const f4 c0 = c[2 * k] * il, c1 = c[2 * k + 1] * il;                                                    \
      const unsigned x0 = min(pk(c0[0], c0[1]), 0xfffffffeu), x1 = min(pk(c0[2], c0[3]), 0xfffffffeu);        \
      const unsigned y0 = min(pk(c1[0], c1[1]), 0xfffffffeu), y1 = min(pk(c1[2], c1[3]), 0xfffffffeu);        \
      auto r0 = __builtin_amdgcn_permlane16_swap(x0, y0, false, false);                                       \
      auto r1 = __builtin_amdgcn_permlane16_swap(x1, y1, false, false);                                       \
      R[k] = v4u{r0[0], r1[0], r0[1], r1[1]};                                                                 \
    }                                                                                                         \
    v4u A, B;                                                                                                 \
    _Pragma("unroll") for (int e = 0; e < 4; ++e) {                                                           \
      A[e] = (unsigned)__builtin_amdgcn_update_dpp((int)R[0][e], (int)R[1][e], ROR8, 0xf, 0xc, false);        \
      B[e] = (unsigned)__builtin_amdgcn_update_dpp((int)R[1][e], (int)R[0][e], ROR8, 0xf, 0x3, false);        \
    }                                                                                                         \
    st_sc1(rs, op + 128 * q, A);                                                                              \
    st_sc1(rs, op2 + 128 * q, B);                                                                             \
  }
      __builtin_amdgcn_sched_barrier(0);
      f4 d[4] = {z, z, z, z};
#pragma unroll
      for (int u = 0; u < 4; ++u)
#pragma unroll
        for (int k = 0; k < 4; ++k) d[k] = mfma32(fr[16 + 4 * k + u], pf[u], d[k]);
      PV_OUT(c, 0);
#pragma unroll
      for (int i = 0; i < 16; ++i) {
        __builtin_amdgcn_sched_group_barrier(0x8, 1, 0);
        __builtin_amdgcn_sched_group_barrier(0x2, PV_MIX, 0);
      }
      __builtin_amdgcn_sched_barrier(0);
      PV_OUT(d, 1);
      FLAG_SET(v);
      if (GQ && glu) {
        VMWAIT(4);
        FLAG_SET(12 + v);
      }
      MARK(3);
      STAMP(6);
    }
    }
      if (glu && w < 4) {
        v4u af[8], xf[8];
        unsigned oog = WS_OPG + (8 * w + (col >> 1)) * OGP + sl * 128 + ((col & 1) * 4 + g4) * 16;
        PIN(oog);
        FLAG_WAIT4(0);
#pragma unroll 1
        for (int i = 0; i < 4; ++i) dma16<NT_G>(wd, (unsigned)(((16 * sl + 4 * w + i) * DM + 8 * l) * 2), smb + L_GD + (4 * w + i) * CP);
#pragma unroll 1
        for (int i = GQ; i < 4; ++i) {
          const int k = 4 * ((a + i) & 3) + w;
          const unsigned og = (unsigned)(((32 * k + (l >> 1)) * FF + 16 * sl + 8 * (l & 1)) * 2);
          dma16(wg, og, smb + L_GG + k * 1024);
          dma16(wu, og, smb + L_GG + 16384 + k * 1024);
        }
        unsigned pg = L_GG + (w & 1) * 16384 + 32 * (256 * (w >> 1) + 8 * g4 + tq) + 8 * tp;
        unsigned pd = L_GD + (4 * g4 + tq) * CP + 256 * w + 8 * tp, pgw = L_GR + ((w & 1) * 2 + (w >> 1)) * 1024 + l * 16;
        PIN(pg); PIN(pd); PIN(pgw);
#pragma unroll
        for (int s = 0; s < 8; ++s) xf[s] = *(LAS v4u*)(px + 512 * (w >> 1) + 64 * s);
        VMWAIT(0);
        FLAG_SET(4 + w);
        FLAG_WAIT4(1);
        if (GQ) FLAG_WAIT4(3);
        STAMP(7);
        f4 acc = z;
#pragma unroll
        for (int s = 0; s < 8; ++s) af[s] = trrd2(sm + pg + 32 * 32 * s, sm + pg + 32 * (32 * s + 4));
        __builtin_amdgcn_sched_barrier(0);
        v2u dn[8];
#pragma unroll
        for (int s = 0; s < 8; ++s) {
          acc = mfma32(af[s], xf[s], acc);
          dn[s] = trrd(sm + pd + 32 * s);
          __builtin_amdgcn_sched_barrier(0);
        }
        *(LAS f4*)(sm + pgw) = acc;
        FLAG_SET(8 + w);
        FLAG_WAIT4(2);
        LAS char* pr = sm + L_GR + l * 16;
        const f4 gx = *(LAS f4*)(pr) + *(LAS f4*)(pr + 1024), ux = *(LAS f4*)(pr + 2048) + *(LAS f4*)(pr + 3072);
        float hh[4];
#pragma unroll
        for (int e = 0; e < 4; ++e) hh[e] = ux[e] * __builtin_amdgcn_rcpf(1.f + __builtin_amdgcn_exp2f(-1.4426950408889634f * gx[e]));
        const v2u hb = {pk(hh[0], hh[1]), pk(hh[2], hh[3])};
        f4 d = z;
#pragma unroll
        for (int t = 0; t < 8; ++t) d = mfma16(dn[t], (col >> 1) == t ? hb : v2u{0u, 0u}, d);
        v4u dv = {pk(d[0], d[1]), T, pk(d[2], d[3]), T};
        PIN(dv);
        st_sc1(rs, oog, dv);
        MARK(4);
        STAMP(9);
      }
    if (a >= 12) {
      const unsigned m = (col & 1) ? 0x3f800000u : 0x3f80u;
      v4u sel = {col < 2 ? m : 0u, (col >> 1) == 1 ? m : 0u, col < 2 ? m : 0u, (col >> 1) == 1 ? m : 0u};
      PIN(sel);
      if (w == 0) {
        unsigned ot = WS_OPT + rd * 8192 + 4096 + l * 16, oy = (l >> 2) * 1024 + rd * 32 + (l & 3) * 8, pr = smb + L_RR + (l & 7) * 16;
        PIN(ot); PIN(oy); PIN(pr);
        v4u p[8];
        touch(out, (unsigned)((l & 1) * 1024 + rd * 32), smb + L_JUNK);
        HOLD_UNTIL(R_START);
        SWEEP_4(p, ot, -4096, -3072, -2048, -1024, "");
        SWEEP_4(p + 4, ot, 0, 1024, 2048, 3072, "");
        PIN4(p);
        PIN4(p + 4);
#define RRETRY()                                \
  if (TAG_BAD(p[0])) RELOAD(p[0], ot, -4096);   \
  if (TAG_BAD(p[1])) RELOAD(p[1], ot, -3072);   \
  if (TAG_BAD(p[2])) RELOAD(p[2], ot, -2048);   \
  if (TAG_BAD(p[3])) RELOAD(p[3], ot, -1024);   \
  if (TAG_BAD(p[4])) RELOAD(p[4], ot, 0);       \
  if (TAG_BAD(p[5])) RELOAD(p[5], ot, 1024);    \
  if (TAG_BAD(p[6])) RELOAD(p[6], ot, 2048);    \
  if (TAG_BAD(p[7])) RELOAD(p[7], ot, 3072);    \
  PIN4(p);                                      \
  PIN4(p + 4)
        RETRY(TAGS_BAD4(p) | TAGS_BAD4(p + 4), RRETRY());
        STAMP(8);
        f4 acc = z;
#pragma unroll
        for (int i = 0; i < 4; ++i) acc = mfma32(sel, v4u{p[2 * i].x, p[2 * i].z, p[2 * i + 1].x, p[2 * i + 1].z}, acc);
        f4 rest[6];
        for (unsigned spin = 0; spin < SPIN_CAP; ++spin) {
          v4u f0;
          v2u f1;
          asm volatile("ds_read_b128 %0, %8\n\tds_read_b64 %1, %8 offset:16\n\tds_read_b128 %2, %9\n\tds_read_b128 %3, %9 offset:128\n\t"
                       "ds_read_b128 %4, %9 offset:256\n\tds_read_b128 %5, %9 offset:384\n\tds_read_b128 %6, %9 offset:512\n\t"
                       "ds_read_b128 %7, %9 offset:640\n\ts_waitcnt lgkmcnt(0)"
                       : "=&v"(f0), "=&v"(f1), "=&v"(rest[0]), "=&v"(rest[1]), "=&v"(rest[2]), "=&v"(rest[3]), "=&v"(rest[4]), "=&v"(rest[5])
                       : "v"(smb + L_FL + 64), "v"(pr)
                       : "memory");
          if (__builtin_amdgcn_readfirstlane(f0.x & f0.y & f0.z & f0.w & f1.x & f1.y)) break;
        }
        const f4 gl = ((rest[0] + rest[1]) + (rest[2] + rest[3])) + (rest[4] + rest[5]);
#pragma unroll
        for (int e = 0; e < 4; ++e) acc[e] = (acc[e] + dpp<ROR8>(acc[e])) + gl[e];
        if (l < 8) asm volatile("global_store_dwordx2 %0, %1, %2 sc1" ::"v"(oy), "v"(v2u{pk(acc[0], acc[1]), pk(acc[2], acc[3])}), "s"(out) : "memory");
      } else if (w < 7) {
        const bool last = w == 6;
        unsigned og = WS_OPG + rd * OGP + (w - 1) * 2048 + (last ? min(l, 47) : l) * 16, og2 = last ? og : og + 1024;
        const v2u xr = *(const v2u*)((const char*)x + ((l >> 2) & 1) * 1024 + rd * 32 + (l & 3) * 8);
        unsigned m0 = (last && l >= 48) ? 0u : ~0u, m1 = last ? 0u : ~0u;
        f4 xv = {blo(xr.x), bhi(xr.x), blo(xr.y), bhi(xr.y)};
        if (!last) xv = z;
        PIN(og); PIN(og2); PIN(m0); PIN(m1); PIN(xv);
        v4u p0, p1;
        HOLD_UNTIL(R_START);
        UNDEF(p0); UNDEF(p1);
        for (unsigned spin = SPIN_CAP; spin; --spin) {
          SWEEP_2(p0, p1, og, og2, WAIT0);
          if (!(TAG_BAD(p0) | TAG_BAD(p1))) break;
        }
        STAMP(8);
        f4 acc = mfma32(sel, v4u{p0.x & m0, p0.z & m0, p1.x & m1, p1.z & m1}, z);
#pragma unroll
        for (int e = 0; e < 4; ++e) acc[e] = (acc[e] + dpp<ROR8>(acc[e])) + xv[e];
        if (l < 8) *(LAS f4*)(sm + L_RR + (w - 1) * 128 + l * 16) = acc;
        FLAG_SET(15 + w);
      }
      else if (rd == 0) {
        const unsigned ol0 = WS_LSE + (l * 16 + ((ROT * l) & 7)) * 128 + 4;
        unsigned t0, t1;
        for (unsigned spin = 0;; ++spin) {
          __builtin_amdgcn_s_sleep(32);
          asm volatile("global_load_dword %0, %2, %3 sc1\n\tglobal_load_dword %1, %2, %3 offset:1024 sc1" WAIT0
                       : "=&v"(t0), "=&v"(t1)
                       : "v"(ol0), "s"(ws)
                       : "memory");
          if (!__builtin_amdgcn_ballot_w64((t0 & t1 & T) != (t0 | t1 | T)) || spin > SPIN_CAP) break;
        }
        if (l == 0) __builtin_amdgcn_raw_buffer_store_b32(T, rs, WS_E, 0, AUX_SC1);
      }
      MARK(5);
      STAMP(9);
    }
    STAMP_FLUSH();
    __builtin_amdgcn_endpgm();
  }
  {
    const int ca = j >> 1, mb = j & 1;
    const int fl = tq | ((g4 & 1) << 2);
    if (__builtin_expect(w < 4, 1)) {
#pragma unroll 1
      for (int i = 0; i < 8; ++i, vq += 131072u, lq += 4096) dma16<NT_WQ>(wq, vq, lq);
      gld4_sc1(ep, ws, vzero);
      unsigned p = L_WQ + 256 * (8 * g4 + tq) + 16 * ((4 * w + (tp >> 1)) ^ (fl << 1)) + 8 * (tp & 1), p2 = p ^ 32;
      unsigned oqp = col < 4 ? WS_QP + ((h * 4 + j) * 8 + 2 * w) * 128 + col * 64 + g4 * 16 : NOWHERE;
      unsigned pxj = L_X + (col & 1) * XP + 16 * g4 + 256 * j;
      PIN(p); PIN(p2); PIN(oqp); PIN(pxj);
      unsigned o = WS_QP + (h * 4 + w) * 1024 + l * 16;
      unsigned pqw = smb + L_CC + (2 * w + ((l >> 2) & 1)) * SP + (l >> 3) * 32 + (l & 3) * 8, pqr = L_CC + (col & 7) * SP + 16 * g4;
      unsigned oqa = col < 4 ? WS_QA + ((col & 1) * NH + h) * 2048 + (64 * j + 16 * w + 8 * (col >> 1) + 2 * g4) * 8 : NOWHERE;
      unsigned pu = L_WUK + 256 * (32 * w + col) + 16 * (g4 ^ col);
      PIN(o); PIN(oqa); PIN(pu); PIN(pqw); PIN(pqr);
      VMWAIT(0);
      EPOCH();
      STAMP(1);
      BAR();
      STAMP(2);
      {
        v4u af[8], xf[4];
#pragma unroll
        for (int s = 0; s < 4; ++s) {
          xf[s] = *(LAS v4u*)(sm + pxj + 64 * s);
          af[s] = trrd2(sm + p + 256 * 32 * s, sm + p + 256 * (32 * s + 4));
          af[4 + s] = trrd2(sm + p2 + 256 * 32 * s, sm + p2 + 256 * (32 * s + 4));
        }
        __builtin_amdgcn_sched_barrier(0);
        f4 c0 = z, c1 = z;
#pragma unroll
        for (int s = 0; s < 4; ++s) {
          c0 = mfma32(af[s], xf[s], c0);
          c1 = mfma32(af[4 + s], xf[s], c1);
        }
        if (col & 2) c0 = c1;
        st_sc1(rs, oqp, v4u{pk(c0[0], c0[1]), T, pk(c0[2], c0[3]), T});
      }
      STAMP(3);
      v4u a, uk[8], qf[4];
      __builtin_amdgcn_s_sleep(H1_DELAY);
      asm volatile(LD16(0, 1, 2) " sc1" : "=&v"(a) : "v"(o), "s"(ws) : "memory");
      BAR();
#pragma unroll
      for (int s = 0; s < 4; ++s) {
        uk[s] = *(LAS v4u*)(sm + (pu ^ (64 * s)));
        uk[4 + s] = *(LAS v4u*)(sm + (pu ^ (64 * s)) + 4096);
      }
      VMWAIT(0);
      PIN(a);
      RETRY(TAG_BAD(a), asm volatile(LD16(0, 1, 2) " sc1" WAIT0 : "+v"(a) : "v"(o), "s"(ws) : "memory"));
      asm volatile("ds_write2_b32 %0, %1, %2 offset0:0 offset1:1" ::"v"(pqw), "v"(a.x), "v"(a.z) : "memory");
      STAMP(4);
      BAR();
#pragma unroll
      for (int s = 0; s < 4; ++s) qf[s] = *(LAS v4u*)(sm + pqr + 64 * s);
      __builtin_amdgcn_sched_barrier(0);
      f4 a0 = z, a1 = z;
#pragma unroll
      for (int s = 0; s < 4; ++s) {
        a0 = mfma32(uk[s], qf[s], a0);
        a1 = mfma32(uk[4 + s], qf[s], a1);
      }
      float q[4];
#pragma unroll
      for (int e = 0; e < 4; ++e) {
        const float mine = (col & 2) ? a1[e] : a0[e], other = (col & 2) ? a0[e] : a1[e];
        q[e] = mine + dpp<QP_2301>(other);
        q[e] += dpp<ROR4>(q[e]);
      }
      st_sc1(rs, oqa, v4u{pk(q[0], q[1]), T, pk(q[2], q[3]), T});
      STAMP(5);
      BAR();
    } else {
      unsigned sdummy = 0u;
      gld4_sc1(ep, ws, vzero);
      if (w < 6) {
        VMWAIT(1);
        LAYERNORM();
        STAMP(1);
        BAR();
#define STOUCH(base, off) asm volatile("s_load_dword %0, %1, %2" : "+s"(sdummy) : "s"(base), "s"((unsigned)(off)) : "memory")
        HOLD_UNTIL(TLB_LATE);
        {
          const int i = 2 * r + (w - 4);
          STOUCH(wuv, i * 131072);
          STOUCH(wo, h * 131072 + (w - 4) * 65536);
          STOUCH(wg, TLB_FINE ? min(i, 21) * 65536 : min(i, 10) * 131072);
          STOUCH(wu, TLB_FINE ? min(i, 21) * 65536 : min(i, 10) * 131072);
          STOUCH(wd, TLB_FINE ? min(i, 21) * 65536 : min(i, 10) * 131072);
          if (TLB_FINE) STOUCH(wuv, i * 131072 + 65536);
          STOUCH(out, 0);
        }
        MARK(0);
      } else {
        __builtin_amdgcn_s_sleep(UK_DELAY);
        const unsigned vu = (unsigned)((((128 * j + g4) * NH + h) * DH) * 2);
#pragma unroll 1
        for (int k = w - 6; k < 32; k += 2) {
          if (k >> 1 == UK_FIRST) asm volatile("s_barrier" ::: "memory");
          dma16<NT_WUK>(wuk, vu + (unsigned)k * 32768u + 16u * (col ^ ((4 * k + g4) & 15)), smb + L_WUK + k * 1024);
        }
        MARK(1);
      }
      VMWAIT(0);
      EPOCH();
      asm volatile("s_barrier\n\ts_barrier\n\ts_barrier\n\ts_waitcnt lgkmcnt(0)" : "+s"(sdummy)::"memory");
    }
    PROBE_T(T);
    HOLD_UNTIL(T_START);
    {
      const unsigned vv = (unsigned)((((256 * ca + g4) * NH + h) * DH + 8 * (col ^ ((g4 | ((w & 2) << 1)) << 1))) * 2);
      const unsigned vo = (unsigned)(((h * DH + (l >> 5)) * DM + 256 * mb) * 2) + 16u * ((l & 31) ^ (((l >> 5) | ((w & 1) << 1) | (w & 4)) << 1));
      int kv = w + 32 * mb, ko = w + 32 * ca;
#pragma unroll 1
      for (int hf = 0; hf < 2; ++hf) {
#pragma unroll 1
        for (int i = 0; i < 4; ++i, kv = (kv + 8) & 63) dma16<NT_T>(wuv, vv + (unsigned)kv * 32768u, smb + L_WUV + kv * 1024);
#pragma unroll 1
        for (int i = 0; i < 4; ++i, ko = (ko + 8) & 63) dma16<NT_T>(wo, vo + (unsigned)ko * 2048u, smb + L_WO + ko * 1024);
        if (hf == 0) asm volatile("s_barrier" ::: "memory");
      }
    }
    const int tb = w >> 2, nq = w & 3;
    unsigned o = WS_PART + (((h * 2 + ca) * 2 + tb) * 32 + 8 * nq + g4) * 512 + 16 * col;
    unsigned pw = L_W + (tb * 32 + 8 * nq + g4) * 4;
    unsigned pv = L_WUV + 256 * (8 * g4 + tq) + 16 * ((2 * w + (tp >> 1)) ^ (fl << 1)) + 8 * (tp & 1);
    unsigned po = L_WO + 512 * (8 * g4 + tq) + 16 * ((4 * w + (tp >> 1)) ^ (fl << 1)) + 8 * (tp & 1);
    unsigned pcw = L_CC + (nq * 2 + tb) * SP2 + 16 * col + 8 * g4, pcr = L_CC + (col & 7) * SP2 + 16 * g4;
    unsigned ptw = L_CT + col * SP + (16 * w + 4 * g4) * 2, ptr = L_CT + (col & 1) * SP + 16 * g4;
    unsigned oo = col >= 4 ? NOWHERE : WS_OPT + ((16 * mb + 2 * w + (col >> 1)) * 64 + ((2 * h + ca + ROT * 4 * (2 * w + (col >> 1))) & 63)) * 128 + ((col & 1) * 4 + g4) * 16;
    v4u mk;
#pragma unroll
    for (int d = 0; d < 4; ++d) mk[d] = (col >> 1) == d ? ((col & 1) ? 0xffff0000u : 0xffffu) : 0u;
    PIN(o); PIN(pw); PIN(pv); PIN(po); PIN(pcw); PIN(pcr); PIN(ptw); PIN(ptr); PIN(oo); PIN(mk);
    STAMP(11);
        if (w == 7) {
      const unsigned ol = WS_LSE + (HMAP ? ((l * 2 + (r >> 3)) * 8 + ((xcd + ROT * l) & 7)) * 128 + ((r >> 2) & 1) * 8 : (l * 8 + xcd) * 128 + (r >> 2) * 8);
      v2u la;
      UNDEF(la);
      for (unsigned spin = SPIN_CAP; spin; --spin) {
        asm volatile("global_load_dwordx2 %0, %1, %2 sc1" WAIT0 : "+v"(la) : "v"(ol), "s"(ws) : "memory");
        if (!__builtin_amdgcn_ballot_w64(la.y != T)) break;
      }
      float M = asf(la.x);
      M = fmax2(M, dpp<ROR8>(M));
      M = fmax2(M, dpp<ROR4>(M));
      M = fmax2(M, dpp<ROR2>(M));
      M = fmax2(M, dpp<ROR1>(M));
      M = sw16_max(M);
      const float e = __builtin_amdgcn_exp2f(asf(la.x) - M);
      float L = e + dpp<ROR8>(e);
      L += dpp<ROR4>(L);
      L += dpp<ROR2>(L);
      L += dpp<ROR1>(L);
      L = sw16_add(L, L);
      const float wt = e * __builtin_amdgcn_rcpf(L);
      *(LAS unsigned*)(sm + L_W + 4 * ((l & 32) | ((l + ROT * 4 * (h & 7)) & 31))) = pk(wt, wt);
      STAMP(7);
    }
    v4u a[4];
    {
      if (LSE_PIPE && w == 7) VMWAIT(2);
      else VMWAIT(0);
      STAMP(6);
      BAR();
      SWEEP_4(a, o, 0, 256, 2048, 2304, "");
    }
    v4u A0 = mk & *(LAS unsigned*)(sm + pw), A1 = mk & *(LAS unsigned*)(sm + pw + 16);
    v4u av[8], ao[8];
#pragma unroll
    for (int s = 0; s < 8; ++s) av[s] = trrd2(sm + pv + 256 * 32 * s, sm + pv + 256 * (32 * s + 4));
#pragma unroll
    for (int t = 0; t < 2; ++t)
#pragma unroll
      for (int s = 0; s < 4; ++s) ao[4 * t + s] = trrd2(sm + (po ^ (32 * t)) + 512 * 32 * s, sm + (po ^ (32 * t)) + 512 * (32 * s + 4));
    VMWAIT(0);
    asm volatile("" : "+v"(a[0]), "+v"(a[1]), "+v"(a[2]), "+v"(a[3]), "+v"(A0), "+v"(A1)::"memory");
    RETRY(HOLES4(a), TRETRY());
    STAMP(8);
    {
      f4 c0 = mfma32(A0, a[0], z), c1 = mfma32(A0, a[1], z);
      c0 = mfma32(A1, a[2], c0);
      c1 = mfma32(A1, a[3], c1);
      if (g4 < 2) {
        *(LAS v2u*)(sm + pcw) = v2u{pk(c0[0], c0[1]), pk(c0[2], c0[3])};
        *(LAS v2u*)(sm + pcw + 256) = v2u{pk(c1[0], c1[1]), pk(c1[2], c1[3])};
      }
    }
    BAR();
    {
      f4 acc = z;
      v4u bf[8];
#pragma unroll
      for (int s = 0; s < 8; ++s) bf[s] = *(LAS v4u*)(sm + pcr + 64 * s);
      __builtin_amdgcn_sched_barrier(0);
#pragma unroll
      for (int s = 0; s < 8; ++s) acc = mfma32(av[s], bf[s], acc);
#pragma unroll
      for (int e = 0; e < 4; ++e) {
        acc[e] += dpp<QP_2301>(acc[e]);
        acc[e] += dpp<ROR4>(acc[e]);
      }
      if (col < 2) *(LAS v2u*)(sm + ptw) = v2u{pk(acc[0], acc[1]), pk(acc[2], acc[3])};
    }
    BAR();
    {
      f4 c0 = z, c1 = z;
      v4u bf[4];
#pragma unroll
      for (int s = 0; s < 4; ++s) bf[s] = *(LAS v4u*)(sm + ptr + 64 * s);
      __builtin_amdgcn_sched_barrier(0);
#pragma unroll
      for (int s = 0; s < 4; ++s) {
        c0 = mfma32(ao[s], bf[s], c0);
        c1 = mfma32(ao[4 + s], bf[s], c1);
      }
      if (col & 2) c0 = c1;
      st_sc1(rs, oo, v4u{pk(c0[0], c0[1]), T, pk(c0[2], c0[3]), T});
    }
    MARK(2);
    STAMP(9);
  }
  STAMP_FLUSH();
}

#define WS_END 0
#define WS_BASE (WS_END ? ((ws_size - (size_t)WS_END * 1048576) & ~(size_t)4095) : 0)
extern "C" void kernel_launch(void* const* d_in, const int* in_sizes, int n_in, void* d_out, int out_size, void* d_ws, size_t ws_size,
                              hipStream_t stream) {
  mla_glu_decode<<<256, 512, 0, stream>>>((const u16*)d_in[0], (const u16*)d_in[1], (const u16*)d_in[2], (const u16*)d_in[3],
                                          (const u16*)d_in[4], (const u16*)d_in[5], (const u16*)d_in[7], (const u16*)d_in[8],
                                          (const u16*)d_in[9], (u16*)d_out, (char*)d_ws + WS_BASE);
}
```

```cpp
#define T_START 370
#define G_TOUCH 0
#include <hip/hip_runtime.h>
#include <stdint.h>

#define GAS __attribute__((address_space(1)))
#define LAS __attribute__((address_space(3)))
typedef unsigned short u16;
typedef unsigned v4u __attribute__((ext_vector_type(4)));
typedef unsigned v2u __attribute__((ext_vector_type(2)));
typedef float f4 __attribute__((ext_vector_type(4)));
typedef float f2 __attribute__((ext_vector_type(2)));
typedef __bf16 bf8 __attribute__((ext_vector_type(8)));
typedef __bf16 bf2 __attribute__((ext_vector_type(2)));
typedef short s4 __attribute__((ext_vector_type(4)));

#define ROT 1
namespace {
constexpr unsigned NOWHERE = 0x7ff00000u;
constexpr int NH = 32, SK = 4096, DH = 128, DM = 512, DC = 512, FF = 1376;
constexpr int NSL = FF / 16;
constexpr int WS_E = 0;
constexpr int WS_QP = 4096;
constexpr int WS_QA = WS_QP + NH * 4 * 1024;
constexpr int WS_LSE = WS_QA + 2 * NH * 2048;
constexpr int WS_GO = WS_LSE + 2 * 32 * 16 * 128;
constexpr int WS_OPT = WS_GO + 2 * 32 * 16 * 128;
constexpr int WS_OPG = WS_OPT + 32 * 64 * 128;
constexpr int OGP = 16384 + ROT * 384;
constexpr int WS_PART = (WS_OPG + 32 * OGP + 4095) / 4096 * 4096;
constexpr int WS_BYTES = WS_PART + NH * 2 * 2 * 32 * 512;
constexpr int WS_STAMP = 7 << 19;
constexpr int CP = 1056;
constexpr int XP = 1088, SP = 288, SP2 = 544;
constexpr int L_C = 0, L_Q = 128 * CP, L_P = L_Q + 16 * CP, L_X = L_P + 4096, L_MX = L_X + 2 * XP, L_RR = L_MX + 256,
              L_JUNK = L_RR + 1024, L_FL = L_JUNK + 256, L_END = L_FL + 128;
static_assert(L_END <= 163840, "LDS");
constexpr int L_GG = 0, L_GD = 32768, L_GR = 65536;
constexpr int L_WUV = 0, L_WO = 65536, L_WQ = 0, L_WUK = 32768, L_CC = 131072, L_CT = L_CC + 8 * SP2, L_W = L_CT + 2 * SP;
#define UK_DELAY 20
#define H1_DELAY 12
#define CODE_TOUCH 1
#define NT_CKV 0
#define NT_WQ 0
#define NT_WUK 0
#define NT_T 0
#define NT_G 0
#define A_START 70
#define GLU_EARLY 0
#define G_START 0
#define C_START 0
#define LSE_LATE 0
#define LSE_WAVE 3
#define DATA_DELAY 0
#define NEWRED 1
#define R_START 0
#define LSE_PIPE 0
#define PV_MIX 4
#define LSE_QK 1
#define HMAP 1
#define GQ 1
#define DOWN_FIRST 1
#define EARLY_SWEEP 1
#define GO_DELAY 0
#define OUT_TOUCH 1
#define OUT_SC1 1
#define BUMP_EARLY 1
#define G2_DELAY 0
#define UK_FIRST 6
#define UK_TOUCH 1
#define TLB_LATE 150
#define U_START 0
#define TLB_FINE 0
#define CK_TLB 1
#define PIECEWISE 1
#define PF_GLU 1
constexpr unsigned SPIN_CAP = 1u << 20;
constexpr int AUX_SC1 = 16;

#define STAMP_W 0
#define STAMP(i)
#define STAMP_FLUSH()
#define PROBE_T(T_)

__device__ __forceinline__ f4 mfma32(v4u a, v4u b, f4 c) {
  return __builtin_amdgcn_mfma_f32_16x16x32_bf16(__builtin_bit_cast(bf8, a), __builtin_bit_cast(bf8, b), c, 0, 0, 0);
}
__device__ __forceinline__ f4 mfma16(v2u a, v2u b, f4 c) {
  return __builtin_amdgcn_mfma_f32_16x16x16bf16_1k(__builtin_bit_cast(s4, a), __builtin_bit_cast(s4, b), c, 0, 0, 0);
}
__device__ __forceinline__ unsigned pk(float lo, float hi) {
  f2 x = {lo, hi};
  return __builtin_bit_cast(unsigned, __builtin_convertvector(x, bf2));
}
__device__ __forceinline__ float asf(unsigned u) { return __builtin_bit_cast(float, u); }
__device__ __forceinline__ float blo(unsigned u) { return __builtin_bit_cast(float, u << 16); }
__device__ __forceinline__ float bhi(unsigned u) { return __builtin_bit_cast(float, u & 0xffff0000u); }
template <int CTRL> __device__ __forceinline__ float dpp(float v) {
  return __builtin_bit_cast(float, __builtin_amdgcn_update_dpp(0, __builtin_bit_cast(int, v), CTRL, 0xf, 0xf, true));
}
__device__ __forceinline__ float fmax2(float a, float b) {
#pragma float_control(precise, off)
  return __builtin_fmaxf(a, b);
}
typedef unsigned short h2 __attribute__((ext_vector_type(2)));
__device__ __forceinline__ unsigned scale_dn(unsigned p, unsigned kk) {
  return __builtin_bit_cast(unsigned, __builtin_elementwise_sub_sat(__builtin_bit_cast(h2, p), __builtin_bit_cast(h2, kk)));
}
#define PIN(x) asm volatile("" : "+v"(x))
constexpr int ROR1 = 0x121, ROR2 = 0x122, ROR4 = 0x124, ROR8 = 0x128, QP_2301 = 0x4e, QP_2222 = 0xaa;
__device__ __forceinline__ float sw32_add(float a, float b) {
  auto r = __builtin_amdgcn_permlane32_swap(__builtin_bit_cast(unsigned, a), __builtin_bit_cast(unsigned, b), false, false);
  return asf(r[0]) + asf(r[1]);
}
__device__ __forceinline__ float sw16_add(float a, float b) {
  auto r = __builtin_amdgcn_permlane16_swap(__builtin_bit_cast(unsigned, a), __builtin_bit_cast(unsigned, b), false, false);
  return asf(r[0]) + asf(r[1]);
}
__device__ __forceinline__ float vmax(unsigned a, unsigned b) {
  float r;
  asm("v_max_f32 %0, %1, %2" : "=v"(r) : "v"(a), "v"(b));
  return r;
}
__device__ __forceinline__ float vmax3(float a, float b, float c) {
  float r;
  asm("v_max3_f32 %0, %1, %2, %3" : "=v"(r) : "v"(a), "v"(b), "v"(c));
  return r;
}
__device__ __forceinline__ float sw32_max(float a) {
  auto r = __builtin_amdgcn_permlane32_swap(__builtin_bit_cast(unsigned, a), __builtin_bit_cast(unsigned, a), false, false);
  return vmax(r[0], r[1]);
}
__device__ __forceinline__ float sw16_max(float a) {
  auto r = __builtin_amdgcn_permlane16_swap(__builtin_bit_cast(unsigned, a), __builtin_bit_cast(unsigned, a), false, false);
  return vmax(r[0], r[1]);
}
__device__ __forceinline__ float wave_sum(float v) {
  v += dpp<ROR8>(v);
  v += dpp<ROR4>(v);
  v += dpp<ROR2>(v);
  v += dpp<ROR1>(v);
  v = sw16_add(v, v);
  return sw32_add(v, v);
}
__device__ __forceinline__ v2u trrd(LAS char* p) {
  return __builtin_bit_cast(v2u, __builtin_amdgcn_ds_read_tr16_b64_v4i16((LAS s4*)p));
}
__device__ __forceinline__ v4u trrd2(LAS char* p0, LAS char* p1) {
  v2u a = trrd(p0), b = trrd(p1);
  return v4u{a.x, a.y, b.x, b.y};
}
#define BAR() asm volatile("s_waitcnt lgkmcnt(0)\n\ts_barrier" ::: "memory")
#define VMWAIT(k) asm volatile("s_waitcnt vmcnt(%0)" ::"n"(k) : "memory")
__device__ __forceinline__ void gld16(v4u& d, const void* base, unsigned off) {
  asm volatile("global_load_dwordx4 %0, %1, %2" : "=v"(d) : "v"(off), "s"(base) : "memory");
}
__device__ __forceinline__ void gld4_sc1(unsigned& d, const void* base, unsigned off) {
  asm volatile("global_load_dword %0, %1, %2 sc1" : "=v"(d) : "v"(off), "s"(base) : "memory");
}
template <int NT = 0> __device__ __forceinline__ void touch(const void* base, unsigned off, unsigned lds) {
  if (NT) asm volatile("s_mov_b32 m0, %2\n\ts_nop 0\n\tglobal_load_lds_dword %0, %1 nt" ::"v"(off), "s"(base), "s"(lds) : "memory");
  else asm volatile("s_mov_b32 m0, %2\n\ts_nop 0\n\tglobal_load_lds_dword %0, %1" ::"v"(off), "s"(base), "s"(lds) : "memory");
}
__device__ __forceinline__ void touch_sc1(const void* base, unsigned off, unsigned lds) {
  asm volatile("s_mov_b32 m0, %2\n\ts_nop 0\n\tglobal_load_lds_dword %0, %1 sc1" ::"v"(off), "s"(base), "s"(lds) : "memory");
}
#define LD16(i, a, o) "global_load_dwordx4 %" #i ", %" #a ", %" #o
#define SWEEP_4(q, o, o0, o1, o2, o3, W)                                                                                     \
  asm volatile(LD16(0, 4, 5) " offset:" #o0 " sc1\n\t" LD16(1, 4, 5) " offset:" #o1 " sc1\n\t" LD16(2, 4, 5) " offset:" #o2         \
               " sc1\n\t" LD16(3, 4, 5) " offset:" #o3 " sc1" W                                                               \
               : "=&v"((q)[0]), "=&v"((q)[1]), "=&v"((q)[2]), "=&v"((q)[3])                                                  \
               : "v"(o), "s"(ws)                                                                                             \
               : "memory")
#define RESWEEP_4(q, o, o0, o1, o2, o3)                                                                                      \
  asm volatile(LD16(0, 4, 5) " offset:" #o0 " sc1\n\t" LD16(1, 4, 5) " offset:" #o1 " sc1\n\t" LD16(2, 4, 5) " offset:" #o2         \
               " sc1\n\t" LD16(3, 4, 5) " offset:" #o3 " sc1\n\ts_waitcnt vmcnt(0)"                                            \
               : "+v"((q)[0]), "+v"((q)[1]), "+v"((q)[2]), "+v"((q)[3])                                                      \
               : "v"(o), "s"(ws)                                                                                             \
               : "memory")
#define RELOAD(q, o, off) asm volatile(LD16(0, 1, 2) " offset:" #off " sc1" : "+v"(q) : "v"(o), "s"(ws) : "memory")
#define PIN4(q) asm volatile("s_waitcnt vmcnt(0)" : "+v"((q)[0]), "+v"((q)[1]), "+v"((q)[2]), "+v"((q)[3])::"memory")
#define TAG_BAD(q) __builtin_amdgcn_ballot_w64((T & (q).y & (q).w) != (T | (q).y | (q).w))
#define HOLE(q) __builtin_amdgcn_ballot_w64(max(max((q).x, (q).y), max((q).z, (q).w)) == ~0u)
#define MAX4(q) max(max((q).x, (q).y), max((q).z, (q).w))
#define HOLES4(q) __builtin_amdgcn_ballot_w64(max(max(MAX4((q)[0]), MAX4((q)[1])), max(MAX4((q)[2]), MAX4((q)[3]))) == ~0u)
#define TRETRY()                          \
  if (HOLE(a[0])) RELOAD(a[0], o, 0);     \
  if (HOLE(a[1])) RELOAD(a[1], o, 256);   \
  if (HOLE(a[2])) RELOAD(a[2], o, 2048);  \
  if (HOLE(a[3])) RELOAD(a[3], o, 2304);  \
  PIN4(a)
#define SWEEP_L(W)                                                                                                          \
  asm volatile("global_load_dwordx2 %0, %3, %5 sc1\n\tglobal_load_dwordx2 %1, %4, %5 sc1\n\t"                              \
               "global_load_dwordx2 %2, %4, %5 offset:512 sc1" W                                                            \
               : "=&v"(la), "=&v"(l0), "=&v"(l1)                                                                            \
               : "v"(ola), "v"(olo), "s"(ws)                                                                                \
               : "memory")
#define SWEEP_2(p, q, oa, ob, W) \
  asm volatile(LD16(0, 2, 4) " sc1\n\t" LD16(1, 3, 4) " sc1" W : "=&v"(p), "=&v"(q) : "v"(oa), "v"(ob), "s"(ws) : "memory")
#define TAGS_BAD4(q) \
  __builtin_amdgcn_ballot_w64((T & (q)[0].y & (q)[0].w & (q)[1].y & (q)[1].w & (q)[2].y & (q)[2].w & (q)[3].y & (q)[3].w) != \
                              (T | (q)[0].y | (q)[0].w | (q)[1].y | (q)[1].w | (q)[2].y | (q)[2].w | (q)[3].y | (q)[3].w))
#define FLAG_SET(i) asm volatile("ds_write_b32 %0, %1" ::"v"(smb + L_FL + 4 * (i)), "v"(1u) : "memory")
#define FLAG_WAIT4(i)                                                                                     \
  for (unsigned spin_ = 0; spin_ < SPIN_CAP; ++spin_) {                                                   \
    v4u f_;                                                                                               \
    asm volatile("ds_read_b128 %0, %1\n\ts_waitcnt lgkmcnt(0)" : "=v"(f_) : "v"(smb + L_FL + 16 * (i)) : "memory"); \
    if (__builtin_amdgcn_readfirstlane(f_.x & f_.y & f_.z & f_.w)) break;                                 \
  }
#define HOLD_UNTIL(ticks)              \
  asm volatile("" : "+s"(tstart));     \
  while ((unsigned)__builtin_amdgcn_s_memrealtime() - (unsigned)tstart < (ticks)) __builtin_amdgcn_s_sleep(1)
#define MARK(i) asm volatile(".Lmla_e" #i ":")
#define RETRY(bad, again)                                   \
  if (bad) {                                                \
    for (unsigned spin = SPIN_CAP; spin; --spin) {          \
      again;                                                \
      if (!(bad)) break;                                    \
    }                                                       \
  }
#define UNDEF(x) asm volatile("" : "=v"(x))
#define EPOCH() asm volatile("v_add_u32 %0, 1, %1" : "=v"(T) : "v"(ep) : "memory")
#define WAIT0 "\n\ts_waitcnt vmcnt(0)"
template <int NT = 0> __device__ __forceinline__ void dma16(const void* base, unsigned off, unsigned lds) {
  if (NT) asm volatile("s_mov_b32 m0, %2\n\ts_nop 0\n\tglobal_load_lds_dwordx4 %0, %1 nt" ::"v"(off), "s"(base), "s"(lds) : "memory");
  else asm volatile("s_mov_b32 m0, %2\n\ts_nop 0\n\tglobal_load_lds_dwordx4 %0, %1" ::"v"(off), "s"(base), "s"(lds) : "memory");
}
__device__ __forceinline__ v4u ld_sc1(__amdgpu_buffer_rsrc_t rs, int off) {
  return __builtin_amdgcn_raw_buffer_load_b128(rs, off, 0, AUX_SC1);
}
__device__ __forceinline__ v2u ld8_sc1(__amdgpu_buffer_rsrc_t rs, int off) {
  return __builtin_amdgcn_raw_buffer_load_b64(rs, off, 0, AUX_SC1);
}
__device__ __forceinline__ void st_sc1(__amdgpu_buffer_rsrc_t rs, int off, v4u v) {
  __builtin_amdgcn_raw_buffer_store_b128(v, rs, off, 0, AUX_SC1);
}
__device__ __forceinline__ void ln_stats(v4u r, float& mean, float& rstd) {
  float e[8] = {blo(r.x), bhi(r.x), blo(r.y), bhi(r.y), blo(r.z), bhi(r.z), blo(r.w), bhi(r.w)};
  float s = ((e[0] + e[1]) + (e[2] + e[3])) + ((e[4] + e[5]) + (e[6] + e[7]));
  mean = wave_sum(s) * (1.f / DM);
  float q = 0.f;
#pragma unroll
  for (int i = 0; i < 8; ++i) {
    float d = e[i] - mean;
    q = fmaf(d, d, q);
  }
  rstd = __builtin_amdgcn_rsqf(wave_sum(q) * (1.f / DM) + 1e-5f);
}
}

__global__ void __launch_bounds__(512, 2)
mla_glu_decode(const u16* __restrict__ x, const u16* __restrict__ ckv, const u16* __restrict__ wq, const u16* __restrict__ wuk,
               const u16* __restrict__ wuv, const u16* __restrict__ wo, const u16* __restrict__ wg, const u16* __restrict__ wu,
               const u16* __restrict__ wd, u16* __restrict__ out, char* __restrict__ ws) {
  unsigned long long tstart = __builtin_amdgcn_s_memrealtime();
  __builtin_amdgcn_sched_barrier(0);
  __shared__ __attribute__((aligned(16))) char smem_[L_END];
  LAS char* sm = (LAS char*)smem_;
  const unsigned smb = (unsigned)(size_t)sm;
  const int tid = threadIdx.x, l = tid & 63, w = __builtin_amdgcn_readfirstlane(tid >> 6);
  const int col = l & 15, g4 = l >> 4, tq = (l & 15) >> 2, tp = l & 3;
  const int xcd = blockIdx.x & 7, r = blockIdx.x >> 3;
  const __amdgpu_buffer_rsrc_t rs = __builtin_amdgcn_make_buffer_rsrc(ws, 0, WS_BYTES, 0x00020000);
  int bw = (blockIdx.x >> 7) * w;
  asm volatile("" : "+s"(bw));
  if (bw >= 5) {
    const char* pc0;
    unsigned len;
    asm volatile("s_getpc_b64 %0\n.Lmla_pc:\n\ts_mov_b32 %1, .Lmla_e0-.Lmla_pc\n\ts_max_i32 %1, %1, .Lmla_e1-.Lmla_pc\n\t"
                 "s_max_i32 %1, %1, .Lmla_e2-.Lmla_pc\n\ts_max_i32 %1, %1, .Lmla_e3-.Lmla_pc\n\ts_max_i32 %1, %1, .Lmla_e4-.Lmla_pc\n\t"
                 "s_max_i32 %1, %1, .Lmla_e5-.Lmla_pc"
                 : "=s"(pc0), "=s"(len)::"scc");
    touch(pc0, min((unsigned)(64 * (bw - 5) + l) * 128u, len), smb + L_JUNK);
  }
  __builtin_amdgcn_sched_barrier(0);
  const int h = HMAP ? xcd + 8 * ((r >> 2) & 3) : 4 * xcd + ((r >> 2) & 3), j = r & 3;
  unsigned vq = (unsigned)(((128 * j + g4 + 4 * w) * 4096 + h * DH + 8 * (col ^ ((g4 | ((w & 2) << 1)) << 1))) * 2), vx = (w & 1) * 1024 + l * 16;
  unsigned vzero = 0u;
  int lq = smb + L_WQ + w * 1024, lx = smb + L_X + (w & 1) * XP;
  asm volatile("" : "+v"(vq), "+v"(vx), "+v"(vzero), "+s"(lq), "+s"(lx));
  asm volatile("" ::"s"(x), "s"(ckv), "s"(wq), "s"(wuk), "s"(wuv), "s"(wo), "s"(wg), "s"(wu), "s"(wd), "s"(out), "s"(ws));
  STAMP(0);

  unsigned ep, T;
  LAS char* const px = sm + L_X + (col & 1) * XP + 16 * g4;
  if ((w >> 1) == 2) dma16(x, vx, lx);
  const f4 z = {0.f, 0.f, 0.f, 0.f};
#define LAYERNORM()                                                                                                              \
  {                                                                                                                              \
    const v4u xr = *(LAS v4u*)(sm + L_X + (w & 1) * XP + l * 16);                                                                \
    float e[8] = {blo(xr.x), bhi(xr.x), blo(xr.y), bhi(xr.y), blo(xr.z), bhi(xr.z), blo(xr.w), bhi(xr.w)};                       \
    const float mean = wave_sum(((e[0] + e[1]) + (e[2] + e[3])) + ((e[4] + e[5]) + (e[6] + e[7]))) * (1.f / DM);                 \
    float q = 0.f;                                                                                                               \
    _Pragma("unroll") for (int i = 0; i < 8; ++i) {                                                                              \
      e[i] -= mean;                                                                                                              \
      q = fmaf(e[i], e[i], q);                                                                                                   \
    }                                                                                                                            \
    const float rstd = __builtin_amdgcn_rsqf(wave_sum(q) * (1.f / DM) + 1e-5f);                                                  \
    *(LAS v4u*)(sm + L_X + (w & 1) * XP + l * 16) =                                                                              \
        v4u{pk(e[0] * rstd, e[1] * rstd), pk(e[2] * rstd, e[3] * rstd), pk(e[4] * rstd, e[5] * rstd), pk(e[6] * rstd, e[7] * rstd)}; \
  }

  if (__builtin_expect(r >= 16, 0)) {
    const int a = r - 16, b = xcd >> 2, n = 8 * (xcd & 3) + (a >> 1), g = a & 1;
    const int sl = 12 * xcd + a, rd = (4 * xcd + a) & 31;
    const bool glu = a < 12 && sl < NSL;
    const int v = w & 3;
    unsigned op = WS_PART + ((((16 * g + (col & 7)) * 2 + (v >> 1)) * 2 + b) * 32 + ((n + ROT * 4 * (col & 7)) & 31)) * 512 + 256 * (v & 1) +
                  (col >> 3) * 64 + (g4 & 1) * 32 + (g4 >> 1) * 16;
    gld4_sc1(ep, ws, vzero);
    unsigned o = WS_QA + (b * NH + 16 * g) * 2048 + w * 4096 + l * 16, pqs = smb + L_Q + 2 * w * CP + l * 8;
    PIN(o); PIN(pqs);
    {
    {
      const unsigned vc = (unsigned)(((b * SK + 128 * n) * DC + 8 * l) * 2);
      STAMP(10);
      if (w == 4) touch(ckv, (unsigned)((b * SK + 128 * n) * DC * 2), smb + L_JUNK);
      HOLD_UNTIL(U_START);
      if (w < 4) touch(wuk, (unsigned)((32 * a + 8 * w + (l >> 3)) * 8192 + (xcd + 8 * ((l >> 1) & 3)) * 256 + (l & 1) * 128), smb + L_JUNK);
      HOLD_UNTIL(C_START);
    if (w >= 4) {
#pragma unroll
      for (int q = 0; q < 4; ++q) st_sc1(rs, op + 128 * (q >> 1) + (q & 1) * 8 * 65536, v4u{~0u, ~0u, ~0u, ~0u});
      if (w == 7 && l < 32) *(LAS unsigned*)(sm + L_FL + 4 * l) = 0u;
    }
      HOLD_UNTIL(A_START);
#pragma unroll 1
      for (int q = 0; q < 4; ++q) {
        int row = w + ((64 * g + 32 * q) & 127);
        asm volatile("s_barrier" ::: "memory");
#pragma unroll 1
        for (int i = 0; i < 4; ++i, row += 8) dma16<NT_CKV>(ckv, vc + row * 1024u, smb + L_C + row * CP);
      }
    }
#define CHALF()                               \
  asm volatile("s_barrier" ::: "memory");     \
  SWEEP_4(qg, o, 0, 1024, 2048, 3072, "");    \
  VMWAIT(12);                                 \
  asm volatile("s_barrier" ::: "memory")
#define CLAND() \
  VMWAIT(4);    \
  EPOCH();      \
  PROBE_T(T);   \
  BAR();        \
  STAMP(1)
#define QRETRY()                              \
  if (TAG_BAD(qg[0])) RELOAD(qg[0], o, 0);    \
  if (TAG_BAD(qg[1])) RELOAD(qg[1], o, 1024); \
  if (TAG_BAD(qg[2])) RELOAD(qg[2], o, 2048); \
  if (TAG_BAD(qg[3])) RELOAD(qg[3], o, 3072); \
  PIN4(qg)
#define QGATHER()                                                                                                           \
  VMWAIT(0);                                                                                                      \
  asm volatile("" : "+v"(qg[0]), "+v"(qg[1]), "+v"(qg[2]), "+v"(qg[3])::"memory");                                          \
  RETRY(TAGS_BAD4(qg), QRETRY());                                                                                           \
  STAMP(3);                                                                                                                 \
  asm volatile("ds_write2_b32 %0, %2, %3 offset0:0 offset1:1\n\tds_write2_b32 %0, %4, %5 offset0:128 offset1:129\n\t"        \
               "ds_write2_b32 %1, %6, %7 offset0:0 offset1:1\n\tds_write2_b32 %1, %8, %9 offset0:128 offset1:129" ::"v"(pqs), \
               "v"(pqs + CP), "v"(qg[0].x), "v"(qg[0].z), "v"(qg[1].x), "v"(qg[1].z), "v"(qg[2].x), "v"(qg[2].z), "v"(qg[3].x), \
               "v"(qg[3].z)                                                                                                 \
               : "memory")
    if (__builtin_expect(w < 4, 1)) {
      v4u fr[32], qg[4];
      unsigned pc = L_C + (32 * w + col) * CP + 16 * g4;
      PIN(pc);
      CHALF();
      const bool early = (w >> 1) == g;
      if (!early) {
        CLAND();
      }
#pragma unroll
      for (int kt = 0; kt < 2; ++kt)
#pragma unroll
        for (int s = 0; s < 16; ++s) fr[16 * kt + s] = *(LAS v4u*)(sm + pc + 16 * kt * CP + 64 * s);
      if (early) {
        CLAND();
      }
      const int u = w ^ (2 * g);
      unsigned p = L_Q + col * CP + 16 * g4, pp = L_P + u * 1024 + l * 16, pm = L_MX + col * 16 + u * 4;
      unsigned ol = WS_LSE + (HMAP ? (((b * 32 + n) * 2 + g) * 8 + ((col + ROT * n) & 7)) * 128 + (col >> 3) * 8 : ((b * 32 + n) * 8 + 4 * g + (col >> 2)) * 128 + (col & 3) * 8);
      if (g4) ol = NOWHERE;
      PIN(p); PIN(pp); PIN(pm); PIN(ol);
      STAMP(2);
      QGATHER();
      BAR();
      STAMP(4);
      f4 s0 = z, s1 = z;
      v4u qf[16];
#pragma unroll
      for (int s = 0; s < 6; ++s) qf[s] = *(LAS v4u*)(sm + p + 64 * s);
      __builtin_amdgcn_sched_barrier(0);
#pragma unroll
      for (int s = 0; s < 16; ++s) {
        if (s + 6 < 16) qf[s + 6] = *(LAS v4u*)(sm + p + 64 * (s + 6));
        s0 = mfma32(fr[s], qf[s], s0);
        s1 = mfma32(fr[16 + s], qf[s], s1);
        __builtin_amdgcn_sched_barrier(0);
      }
      float mx = fmax2(fmax2(fmax2(s0[0], s0[1]), fmax2(s0[2], s0[3])), fmax2(fmax2(s1[0], s1[1]), fmax2(s1[2], s1[3])));
      mx = sw32_max(sw16_max(mx));
      constexpr float SC = 0.08838834764831845f * 1.4426950408889634f;
      const float mi = __builtin_ceilf(mx * SC);
#pragma unroll
      for (int e = 0; e < 4; ++e) {
        s0[e] = __builtin_amdgcn_exp2f(fmaf(s0[e], SC, -mi));
        s1[e] = __builtin_amdgcn_exp2f(fmaf(s1[e], SC, -mi));
      }
      *(LAS float*)(sm + pm) = mi;
      *(LAS v4u*)(sm + pp) = v4u{pk(s0[0], s0[1]), pk(s0[2], s0[3]), pk(s1[0], s1[1]), pk(s1[2], s1[3])};
      BAR();
      STAMP(5);
      if (w == LSE_QK - 1) {
        const f4 m4 = *(LAS f4*)(sm + pm - 4 * u);
        v4u pf[4];
#pragma unroll
        for (int k = 0; k < 4; ++k) pf[k] = *(LAS v4u*)(sm + pp + (k - u) * 1024);
        __builtin_amdgcn_sched_barrier(0);
        const float M = vmax3(vmax3(m4[0], m4[1], m4[2]), m4[3], m4[3]);
        const v4u one = {0x3f803f80u, 0x3f803f80u, 0x3f803f80u, 0x3f803f80u};
        f4 ls = z;
#pragma unroll
        for (int u = 0; u < 4; ++u) {
          const unsigned kk = min((unsigned)(M - m4[u]), 255u) * 0x00800080u;
#pragma unroll
          for (int e = 0; e < 4; ++e) pf[u][e] = scale_dn(pf[u][e], kk);
          ls = mfma32(one, pf[u], ls);
        }
        __builtin_amdgcn_raw_buffer_store_b64(v2u{__builtin_bit_cast(unsigned, M + __builtin_amdgcn_logf(ls[0])), T}, rs, ol, 0, AUX_SC1);
      }
    } else {
      v4u fr[32], qg[4];
      unsigned p0 = L_C + (64 * g + 4 * g4 + tq) * CP + 256 * v + 8 * tp, p1 = L_C + (64 * (g ^ 1) + 4 * g4 + tq) * CP + 256 * v + 8 * tp;
      PIN(p0); PIN(p1);
      CHALF();
#pragma unroll
      for (int ct = 0; ct < 8; ++ct)
#pragma unroll
        for (int u = 0; u < 2; ++u) fr[4 * ct + u] = trrd2(sm + p0 + 32 * u * CP + 32 * ct, sm + p0 + (32 * u + 16) * CP + 32 * ct);
      CLAND();
#pragma unroll
      for (int ct = 0; ct < 8; ++ct)
#pragma unroll
        for (int u = 0; u < 2; ++u) fr[4 * ct + 2 + u] = trrd2(sm + p1 + 32 * u * CP + 32 * ct, sm + p1 + (32 * u + 16) * CP + 32 * ct);
      unsigned ol = WS_LSE + (HMAP ? (((b * 32 + n) * 2 + g) * 8 + ((col + ROT * n) & 7)) * 128 + (col >> 3) * 8 : ((b * 32 + n) * 8 + 4 * g + (col >> 2)) * 128 + (col & 3) * 8), pp = L_P + l * 16, pm = L_MX + col * 16;
      v4u one = {0x3f803f80u, 0x3f803f80u, 0x3f803f80u, 0x3f803f80u};
      unsigned op2 = op + 8 * 65536;
      PIN(ol); PIN(pp); PIN(pm); PIN(one); PIN(op); PIN(op2);
      STAMP(2);
      QGATHER();
      BAR();
      STAMP(4);
      if (glu) {
        const int k = 8 * (v >> 1) + 2 * (a & 3);
        const unsigned og = (unsigned)(((32 * k + (l >> 1)) * FF + 16 * sl + 8 * (l & 1)) * 2);
        dma16((v & 1) ? wu : wg, og, smb + L_GG + (v & 1) * 16384 + k * 1024);
        dma16((v & 1) ? wu : wg, og + 32 * FF * 2, smb + L_GG + (v & 1) * 16384 + k * 1024 + 1024);
      } else if (v < G_TOUCH) {
        const int ln = 128 * a + 64 * (v >> 1) + l;
        touch((v & 1) ? wu : wg, (unsigned)((ln >> 2) * (FF * 2)) + (unsigned)min(384 * xcd + 128 * (ln & 3), FF * 2 - 4), smb + L_JUNK);
      }
      if (glu && w < 6) LAYERNORM();
      BAR();
      STAMP(5);
      const f4 m4 = *(LAS f4*)(sm + pm);
      v4u pf[4];
#pragma unroll
      for (int u = 0; u < 4; ++u) pf[u] = *(LAS v4u*)(sm + pp + u * 1024);
      __builtin_amdgcn_sched_barrier(0);
      const float M = vmax3(vmax3(m4[0], m4[1], m4[2]), m4[3], m4[3]);
      f4 ls = z, c[4] = {z, z, z, z};
#pragma unroll
      for (int u = 0; u < 4; ++u) {
        const unsigned kk = min((unsigned)(M - m4[u]), 255u) * 0x00800080u;
#pragma unroll
        for (int e = 0; e < 4; ++e) pf[u][e] = scale_dn(pf[u][e], kk);
        ls = mfma32(one, pf[u], ls);
        if (u < 3) {
#pragma unroll
          for (int k = 0; k < 4; ++k) c[k] = mfma32(fr[4 * k + u], pf[u], c[k]);
        }
      }
      __builtin_amdgcn_sched_barrier(0);
      c[0] = mfma32(fr[3], pf[3], c[0]);
      c[1] = mfma32(fr[7], pf[3], c[1]);
      __builtin_amdgcn_sched_barrier(0);
      float il = __builtin_amdgcn_rcpf(ls[0]);
      unsigned lse = __builtin_bit_cast(unsigned, M + __builtin_amdgcn_logf(ls[0]));
      c[2] = mfma32(fr[11], pf[3], c[2]);
      c[3] = mfma32(fr[15], pf[3], c[3]);
#define PV_OUT(c, q)                                                                                          \
  {                                                                                                           \
    v4u R[2];                                                                                                 \
    _Pragma("unroll") for (int k = 0; k < 2; ++k) {                                                           \
      const f4 c0 = c[2 * k] * il, c1 = c[2 * k + 1] * il;                                                    \
      const unsigned x0 = min(pk(c0[0], c0[1]), 0xfffffffeu), x1 = min(pk(c0[2], c0[3]), 0xfffffffeu);        \
      const unsigned y0 = min(pk(c1[0], c1[1]), 0xfffffffeu), y1 = min(pk(c1[2], c1[3]), 0xfffffffeu);        \
      auto r0 = __builtin_amdgcn_permlane16_swap(x0, y0, false, false);                                       \
      auto r1 = __builtin_amdgcn_permlane16_swap(x1, y1, false, false);                                       \
      R[k] = v4u{r0[0], r1[0], r0[1], r1[1]};                                                                 \
    }                                                                                                         \
    v4u A, B;                                                                                                 \
    _Pragma("unroll") for (int e = 0; e < 4; ++e) {                                                           \
      A[e] = (unsigned)__builtin_amdgcn_update_dpp((int)R[0][e], (int)R[1][e], ROR8, 0xf, 0xc, false);        \
      B[e] = (unsigned)__builtin_amdgcn_update_dpp((int)R[1][e], (int)R[0][e], ROR8, 0xf, 0x3, false);        \
    }                                                                                                         \
    st_sc1(rs, op + 128 * q, A);                                                                              \
    st_sc1(rs, op2 + 128 * q, B);                                                                             \
  }
      __builtin_amdgcn_sched_barrier(0);
      f4 d[4] = {z, z, z, z};
#pragma unroll
      for (int u = 0; u < 4; ++u)
#pragma unroll
        for (int k = 0; k < 4; ++k) d[k] = mfma32(fr[16 + 4 * k + u], pf[u], d[k]);
      PV_OUT(c, 0);
#pragma unroll
      for (int i = 0; i < 16; ++i) {
        __builtin_amdgcn_sched_group_barrier(0x8, 1, 0);
        __builtin_amdgcn_sched_group_barrier(0x2, PV_MIX, 0);
      }
      __builtin_amdgcn_sched_barrier(0);
      PV_OUT(d, 1);
      FLAG_SET(v);
      if (glu) {
        VMWAIT(4);
        FLAG_SET(12 + v);
      }
      MARK(3);
      STAMP(6);
    }
    }
      if (glu && w < 4) {
        v4u af[8], xf[8];
        unsigned oog = WS_OPG + (8 * w + (col >> 1)) * OGP + sl * 128 + ((col & 1) * 4 + g4) * 16;
        PIN(oog);
        FLAG_WAIT4(0);
#pragma unroll 1
        for (int i = 2; i < 8; ++i) {
          const int k = 8 * (w >> 1) + ((2 * a + i) & 7);
          dma16((w & 1) ? wu : wg, (unsigned)(((32 * k + (l >> 1)) * FF + 16 * sl + 8 * (l & 1)) * 2), smb + L_GG + (w & 1) * 16384 + k * 1024);
        }
#pragma unroll 1
        for (int i = 0; i < 4; ++i) dma16(wd, (unsigned)(((16 * sl + 4 * w + i) * DM + 8 * l) * 2), smb + L_GD + (4 * w + i) * CP);
        unsigned pg = L_GG + (w & 1) * 16384 + 32 * (256 * (w >> 1) + 8 * g4 + tq) + 8 * tp;
        unsigned pd = L_GD + (4 * g4 + tq) * CP + 256 * w + 8 * tp, pgw = L_GR + ((w & 1) * 2 + (w >> 1)) * 1024 + l * 16;
        PIN(pg); PIN(pd); PIN(pgw);
#pragma unroll
        for (int s = 0; s < 8; ++s) xf[s] = *(LAS v4u*)(px + 512 * (w >> 1) + 64 * s);
        FLAG_WAIT4(3);
        VMWAIT(4);
        STAMP(7);
        f4 acc = z;
#pragma unroll
        for (int s = 0; s < 8; ++s) af[s] = trrd2(sm + pg + 32 * 32 * s, sm + pg + 32 * (32 * s + 4));
        __builtin_amdgcn_sched_barrier(0);
#pragma unroll
        for (int s = 0; s < 8; ++s) acc = mfma32(af[s], xf[s], acc);
        *(LAS f4*)(sm + pgw) = acc;
        VMWAIT(0);
        FLAG_SET(8 + w);
        FLAG_WAIT4(2);
        v2u dn[8];
        LAS char* pr = sm + L_GR + l * 16;
        const f4 gx = *(LAS f4*)(pr) + *(LAS f4*)(pr + 1024), ux = *(LAS f4*)(pr + 2048) + *(LAS f4*)(pr + 3072);
#pragma unroll
        for (int t = 0; t < 8; ++t) dn[t] = trrd(sm + pd + 32 * t);
        float hh[4];
#pragma unroll
        for (int e = 0; e < 4; ++e) hh[e] = ux[e] * __builtin_amdgcn_rcpf(1.f + __builtin_amdgcn_exp2f(-1.4426950408889634f * gx[e]));
        const v2u hb = {pk(hh[0], hh[1]), pk(hh[2], hh[3])};
        f4 d = z;
#pragma unroll
        for (int t = 0; t < 8; ++t) d = mfma16(dn[t], (col >> 1) == t ? hb : v2u{0u, 0u}, d);
        v4u dv = {pk(d[0], d[1]), T, pk(d[2], d[3]), T};
        PIN(dv);
        st_sc1(rs, oog, dv);
        MARK(4);
        STAMP(9);
      }
    if (a >= 12) {
      const unsigned m = (col & 1) ? 0x3f800000u : 0x3f80u;
      v4u sel = {col < 2 ? m : 0u, (col >> 1) == 1 ? m : 0u, col < 2 ? m : 0u, (col >> 1) == 1 ? m : 0u};
      PIN(sel);
      if (w == 0) {
        unsigned ot = WS_OPT + rd * 8192 + 4096 + l * 16, oy = (l >> 2) * 1024 + rd * 32 + (l & 3) * 8, pr = smb + L_RR + (l & 7) * 16;
        PIN(ot); PIN(oy); PIN(pr);
        v4u p[8];
        touch(out, (unsigned)((l & 1) * 1024 + rd * 32), smb + L_JUNK);
        HOLD_UNTIL(R_START);
        SWEEP_4(p, ot, -4096, -3072, -2048, -1024, "");
        SWEEP_4(p + 4, ot, 0, 1024, 2048, 3072, "");
        PIN4(p);
        PIN4(p + 4);
#define RRETRY()                                \
  if (TAG_BAD(p[0])) RELOAD(p[0], ot, -4096);   \
  if (TAG_BAD(p[1])) RELOAD(p[1], ot, -3072);   \
  if (TAG_BAD(p[2])) RELOAD(p[2], ot, -2048);   \
  if (TAG_BAD(p[3])) RELOAD(p[3], ot, -1024);   \
  if (TAG_BAD(p[4])) RELOAD(p[4], ot, 0);       \
  if (TAG_BAD(p[5])) RELOAD(p[5], ot, 1024);    \
  if (TAG_BAD(p[6])) RELOAD(p[6], ot, 2048);    \
  if (TAG_BAD(p[7])) RELOAD(p[7], ot, 3072);    \
  PIN4(p);                                      \
  PIN4(p + 4)
        RETRY(TAGS_BAD4(p) | TAGS_BAD4(p + 4), RRETRY());
        STAMP(8);
        f4 acc = z;
#pragma unroll
        for (int i = 0; i < 4; ++i) acc = mfma32(sel, v4u{p[2 * i].x, p[2 * i].z, p[2 * i + 1].x, p[2 * i + 1].z}, acc);
        f4 rest[6];
        for (unsigned spin = 0; spin < SPIN_CAP; ++spin) {
          v4u f0;
          v2u f1;
          asm volatile("ds_read_b128 %0, %8\n\tds_read_b64 %1, %8 offset:16\n\tds_read_b128 %2, %9\n\tds_read_b128 %3, %9 offset:128\n\t"
                       "ds_read_b128 %4, %9 offset:256\n\tds_read_b128 %5, %9 offset:384\n\tds_read_b128 %6, %9 offset:512\n\t"
                       "ds_read_b128 %7, %9 offset:640\n\ts_waitcnt lgkmcnt(0)"
                       : "=&v"(f0), "=&v"(f1), "=&v"(rest[0]), "=&v"(rest[1]), "=&v"(rest[2]), "=&v"(rest[3]), "=&v"(rest[4]), "=&v"(rest[5])
                       : "v"(smb + L_FL + 64), "v"(pr)
                       : "memory");
          if (__builtin_amdgcn_readfirstlane(f0.x & f0.y & f0.z & f0.w & f1.x & f1.y)) break;
        }
        const f4 gl = ((rest[0] + rest[1]) + (rest[2] + rest[3])) + (rest[4] + rest[5]);
#pragma unroll
        for (int e = 0; e < 4; ++e) acc[e] = (acc[e] + dpp<ROR8>(acc[e])) + gl[e];
        if (l < 8) asm volatile("global_store_dwordx2 %0, %1, %2 sc1" ::"v"(oy), "v"(v2u{pk(acc[0], acc[1]), pk(acc[2], acc[3])}), "s"(out) : "memory");
      } else if (w < 7) {
        const bool last = w == 6;
        unsigned og = WS_OPG + rd * OGP + (w - 1) * 2048 + (last ? min(l, 47) : l) * 16, og2 = last ? og : og + 1024;
        const v2u xr = *(const v2u*)((const char*)x + ((l >> 2) & 1) * 1024 + rd * 32 + (l & 3) * 8);
        unsigned m0 = (last && l >= 48) ? 0u : ~0u, m1 = last ? 0u : ~0u;
        f4 xv = {blo(xr.x), bhi(xr.x), blo(xr.y), bhi(xr.y)};
        if (!last) xv = z;
        PIN(og); PIN(og2); PIN(m0); PIN(m1); PIN(xv);
        v4u p0, p1;
        HOLD_UNTIL(R_START);
        UNDEF(p0); UNDEF(p1);
        for (unsigned spin = SPIN_CAP; spin; --spin) {
          SWEEP_2(p0, p1, og, og2, WAIT0);
          if (!(TAG_BAD(p0) | TAG_BAD(p1))) break;
        }
        STAMP(8);
        f4 acc = mfma32(sel, v4u{p0.x & m0, p0.z & m0, p1.x & m1, p1.z & m1}, z);
#pragma unroll
        for (int e = 0; e < 4; ++e) acc[e] = (acc[e] + dpp<ROR8>(acc[e])) + xv[e];
        if (l < 8) *(LAS f4*)(sm + L_RR + (w - 1) * 128 + l * 16) = acc;
        FLAG_SET(15 + w);
      }
      else if (rd == 0) {
        const unsigned ol0 = WS_LSE + (l * 16 + ((ROT * l) & 7)) * 128 + 4;
        unsigned t0, t1;
        for (unsigned spin = 0;; ++spin) {
          __builtin_amdgcn_s_sleep(32);
          asm volatile("global_load_dword %0, %2, %3 sc1\n\tglobal_load_dword %1, %2, %3 offset:1024 sc1" WAIT0
                       : "=&v"(t0), "=&v"(t1)
                       : "v"(ol0), "s"(ws)
                       : "memory");
          if (!__builtin_amdgcn_ballot_w64((t0 & t1 & T) != (t0 | t1 | T)) || spin > SPIN_CAP) break;
        }
        if (l == 0) __builtin_amdgcn_raw_buffer_store_b32(T, rs, WS_E, 0, AUX_SC1);
      }
      MARK(5);
      STAMP(9);
    }
    STAMP_FLUSH();
    __builtin_amdgcn_endpgm();
  }
  {
    const int ca = j >> 1, mb = j & 1;
    const int fl = tq | ((g4 & 1) << 2);
    if (__builtin_expect(w < 4, 1)) {
#pragma unroll 1
      for (int i = 0; i < 8; ++i, vq += 131072u, lq += 4096) dma16<NT_WQ>(wq, vq, lq);
      gld4_sc1(ep, ws, vzero);
      unsigned p = L_WQ + 256 * (8 * g4 + tq) + 16 * ((4 * w + (tp >> 1)) ^ (fl << 1)) + 8 * (tp & 1), p2 = p ^ 32;
      unsigned oqp = col < 4 ? WS_QP + ((h * 4 + j) * 8 + 2 * w) * 128 + col * 64 + g4 * 16 : NOWHERE;
      unsigned pxj = L_X + (col & 1) * XP + 16 * g4 + 256 * j;
      PIN(p); PIN(p2); PIN(oqp); PIN(pxj);
      unsigned o = WS_QP + (h * 4 + w) * 1024 + l * 16;
      unsigned pqw = smb + L_CC + (2 * w + ((l >> 2) & 1)) * SP + (l >> 3) * 32 + (l & 3) * 8, pqr = L_CC + (col & 7) * SP + 16 * g4;
      unsigned oqa = col < 4 ? WS_QA + ((col & 1) * NH + h) * 2048 + (64 * j + 16 * w + 8 * (col >> 1) + 2 * g4) * 8 : NOWHERE;
      unsigned pu = L_WUK + 256 * (32 * w + col) + 16 * (g4 ^ col);
      PIN(o); PIN(oqa); PIN(pu); PIN(pqw); PIN(pqr);
      VMWAIT(0);
      EPOCH();
      STAMP(1);
      BAR();
      STAMP(2);
      {
        v4u af[8], xf[4];
#pragma unroll
        for (int s = 0; s < 4; ++s) {
          xf[s] = *(LAS v4u*)(sm + pxj + 64 * s);
          af[s] = trrd2(sm + p + 256 * 32 * s, sm + p + 256 * (32 * s + 4));
          af[4 + s] = trrd2(sm + p2 + 256 * 32 * s, sm + p2 + 256 * (32 * s + 4));
        }
        __builtin_amdgcn_sched_barrier(0);
        f4 c0 = z, c1 = z;
#pragma unroll
        for (int s = 0; s < 4; ++s) {
          c0 = mfma32(af[s], xf[s], c0);
          c1 = mfma32(af[4 + s], xf[s], c1);
        }
        if (col & 2) c0 = c1;
        st_sc1(rs, oqp, v4u{pk(c0[0], c0[1]), T, pk(c0[2], c0[3]), T});
      }
      STAMP(3);
      v4u a, uk[8], qf[4];
      __builtin_amdgcn_s_sleep(H1_DELAY);
      asm volatile(LD16(0, 1, 2) " sc1" : "=&v"(a) : "v"(o), "s"(ws) : "memory");
      BAR();
#pragma unroll
      for (int s = 0; s < 4; ++s) {
        uk[s] = *(LAS v4u*)(sm + (pu ^ (64 * s)));
        uk[4 + s] = *(LAS v4u*)(sm + (pu ^ (64 * s)) + 4096);
      }
      VMWAIT(0);
      PIN(a);
      RETRY(TAG_BAD(a), asm volatile(LD16(0, 1, 2) " sc1" WAIT0 : "+v"(a) : "v"(o), "s"(ws) : "memory"));
      asm volatile("ds_write2_b32 %0, %1, %2 offset0:0 offset1:1" ::"v"(pqw), "v"(a.x), "v"(a.z) : "memory");
      STAMP(4);
      BAR();
#pragma unroll
      for (int s = 0; s < 4; ++s) qf[s] = *(LAS v4u*)(sm + pqr + 64 * s);
      __builtin_amdgcn_sched_barrier(0);
      f4 a0 = z, a1 = z;
#pragma unroll
      for (int s = 0; s < 4; ++s) {
        a0 = mfma32(uk[s], qf[s], a0);
        a1 = mfma32(uk[4 + s], qf[s], a1);
      }
      float q[4];
#pragma unroll
      for (int e = 0; e < 4; ++e) {
        const float mine = (col & 2) ? a1[e] : a0[e], other = (col & 2) ? a0[e] : a1[e];
        q[e] = mine + dpp<QP_2301>(other);
        q[e] += dpp<ROR4>(q[e]);
      }
      st_sc1(rs, oqa, v4u{pk(q[0], q[1]), T, pk(q[2], q[3]), T});
      STAMP(5);
      BAR();
    } else {
      unsigned sdummy = 0u;
      gld4_sc1(ep, ws, vzero);
      if (w < 6) {
        VMWAIT(1);
        LAYERNORM();
        STAMP(1);
        BAR();
#define STOUCH(base, off) asm volatile("s_load_dword %0, %1, %2" : "+s"(sdummy) : "s"(base), "s"((unsigned)(off)) : "memory")
        HOLD_UNTIL(TLB_LATE);
        {
          const int i = 2 * r + (w - 4);
          STOUCH(wuv, i * 131072);
          STOUCH(wo, h * 131072 + (w - 4) * 65536);
          STOUCH(wg, TLB_FINE ? min(i, 21) * 65536 : min(i, 10) * 131072);
          STOUCH(wu, TLB_FINE ? min(i, 21) * 65536 : min(i, 10) * 131072);
          STOUCH(wd, TLB_FINE ? min(i, 21) * 65536 : min(i, 10) * 131072);
          if (TLB_FINE) STOUCH(wuv, i * 131072 + 65536);
          STOUCH(out, 0);
        }
        MARK(0);
      } else {
        __builtin_amdgcn_s_sleep(UK_DELAY);
        const unsigned vu = (unsigned)((((128 * j + g4) * NH + h) * DH) * 2);
#pragma unroll 1
        for (int k = w - 6; k < 32; k += 2) {
          if (k >> 1 == UK_FIRST) asm volatile("s_barrier" ::: "memory");
          dma16<NT_WUK>(wuk, vu + (unsigned)k * 32768u + 16u * (col ^ ((4 * k + g4) & 15)), smb + L_WUK + k * 1024);
        }
        MARK(1);
      }
      VMWAIT(0);
      EPOCH();
      asm volatile("s_barrier\n\ts_barrier\n\ts_barrier\n\ts_waitcnt lgkmcnt(0)" : "+s"(sdummy)::"memory");
    }
    PROBE_T(T);
    HOLD_UNTIL(T_START);
    {
      const unsigned vv = (unsigned)((((256 * ca + g4) * NH + h) * DH + 8 * (col ^ ((g4 | ((w & 2) << 1)) << 1))) * 2);
      const unsigned vo = (unsigned)(((h * DH + (l >> 5)) * DM + 256 * mb) * 2) + 16u * ((l & 31) ^ (((l >> 5) | ((w & 1) << 1) | (w & 4)) << 1));
      int kv = w + 32 * mb, ko = w + 32 * ca;
#pragma unroll 1
      for (int hf = 0; hf < 2; ++hf) {
#pragma unroll 1
        for (int i = 0; i < 4; ++i, kv = (kv + 8) & 63) dma16<NT_T>(wuv, vv + (unsigned)kv * 32768u, smb + L_WUV + kv * 1024);
#pragma unroll 1
        for (int i = 0; i < 4; ++i, ko = (ko + 8) & 63) dma16<NT_T>(wo, vo + (unsigned)ko * 2048u, smb + L_WO + ko * 1024);
        if (hf == 0) asm volatile("s_barrier" ::: "memory");
      }
    }
    const int tb = w >> 2, nq = w & 3;
    unsigned o = WS_PART + (((h * 2 + ca) * 2 + tb) * 32 + 8 * nq + g4) * 512 + 16 * col;
    unsigned pw = L_W + (tb * 32 + 8 * nq + g4) * 4;
    unsigned pv = L_WUV + 256 * (8 * g4 + tq) + 16 * ((2 * w + (tp >> 1)) ^ (fl << 1)) + 8 * (tp & 1);
    unsigned po = L_WO + 512 * (8 * g4 + tq) + 16 * ((4 * w + (tp >> 1)) ^ (fl << 1)) + 8 * (tp & 1);
    unsigned pcw = L_CC + (nq * 2 + tb) * SP2 + 16 * col + 8 * g4, pcr = L_CC + (col & 7) * SP2 + 16 * g4;
    unsigned ptw = L_CT + col * SP + (16 * w + 4 * g4) * 2, ptr = L_CT + (col & 1) * SP + 16 * g4;
    unsigned oo = col >= 4 ? NOWHERE : WS_OPT + ((16 * mb + 2 * w + (col >> 1)) * 64 + ((2 * h + ca + ROT * 4 * (2 * w + (col >> 1))) & 63)) * 128 + ((col & 1) * 4 + g4) * 16;
    v4u mk;
#pragma unroll
    for (int d = 0; d < 4; ++d) mk[d] = (col >> 1) == d ? ((col & 1) ? 0xffff0000u : 0xffffu) : 0u;
    PIN(o); PIN(pw); PIN(pv); PIN(po); PIN(pcw); PIN(pcr); PIN(ptw); PIN(ptr); PIN(oo); PIN(mk);
    STAMP(11);
        if (w == 7) {
      const unsigned ol = WS_LSE + (HMAP ? ((l * 2 + (r >> 3)) * 8 + ((xcd + ROT * l) & 7)) * 128 + ((r >> 2) & 1) * 8 : (l * 8 + xcd) * 128 + (r >> 2) * 8);
      v2u la;
      UNDEF(la);
      for (unsigned spin = SPIN_CAP; spin; --spin) {
        asm volatile("global_load_dwordx2 %0, %1, %2 sc1" WAIT0 : "+v"(la) : "v"(ol), "s"(ws) : "memory");
        if (!__builtin_amdgcn_ballot_w64(la.y != T)) break;
      }
      float M = asf(la.x);
      M = fmax2(M, dpp<ROR8>(M));
      M = fmax2(M, dpp<ROR4>(M));
      M = fmax2(M, dpp<ROR2>(M));
      M = fmax2(M, dpp<ROR1>(M));
      M = sw16_max(M);
      const float e = __builtin_amdgcn_exp2f(asf(la.x) - M);
      float L = e + dpp<ROR8>(e);
      L += dpp<ROR4>(L);
      L += dpp<ROR2>(L);
      L += dpp<ROR1>(L);
      L = sw16_add(L, L);
      const float wt = e * __builtin_amdgcn_rcpf(L);
      *(LAS unsigned*)(sm + L_W + 4 * ((l & 32) | ((l + ROT * 4 * (h & 7)) & 31))) = pk(wt, wt);
      STAMP(7);
    }
    v4u a[4];
    {
      if (LSE_PIPE && w == 7) VMWAIT(2);
      else VMWAIT(0);
      STAMP(6);
      BAR();
      SWEEP_4(a, o, 0, 256, 2048, 2304, "");
    }
    v4u A0 = mk & *(LAS unsigned*)(sm + pw), A1 = mk & *(LAS unsigned*)(sm + pw + 16);
    v4u av[8], ao[8];
#pragma unroll
    for (int s = 0; s < 8; ++s) av[s] = trrd2(sm + pv + 256 * 32 * s, sm + pv + 256 * (32 * s + 4));
#pragma unroll
    for (int t = 0; t < 2; ++t)
#pragma unroll
      for (int s = 0; s < 4; ++s) ao[4 * t + s] = trrd2(sm + (po ^ (32 * t)) + 512 * 32 * s, sm + (po ^ (32 * t)) + 512 * (32 * s + 4));
    VMWAIT(0);
    asm volatile("" : "+v"(a[0]), "+v"(a[1]), "+v"(a[2]), "+v"(a[3]), "+v"(A0), "+v"(A1)::"memory");
    RETRY(HOLES4(a), TRETRY());
    STAMP(8);
    {
      f4 c0 = mfma32(A0, a[0], z), c1 = mfma32(A0, a[1], z);
      c0 = mfma32(A1, a[2], c0);
      c1 = mfma32(A1, a[3], c1);
      if (g4 < 2) {
        *(LAS v2u*)(sm + pcw) = v2u{pk(c0[0], c0[1]), pk(c0[2], c0[3])};
        *(LAS v2u*)(sm + pcw + 256) = v2u{pk(c1[0], c1[1]), pk(c1[2], c1[3])};
      }
    }
    BAR();
    {
      f4 acc = z;
      v4u bf[8];
#pragma unroll
      for (int s = 0; s < 8; ++s) bf[s] = *(LAS v4u*)(sm + pcr + 64 * s);
      __builtin_amdgcn_sched_barrier(0);
#pragma unroll
      for (int s = 0; s < 8; ++s) acc = mfma32(av[s], bf[s], acc);
#pragma unroll
      for (int e = 0; e < 4; ++e) {
        acc[e] += dpp<QP_2301>(acc[e]);
        acc[e] += dpp<ROR4>(acc[e]);
      }
      if (col < 2) *(LAS v2u*)(sm + ptw) = v2u{pk(acc[0], acc[1]), pk(acc[2], acc[3])};
    }
    BAR();
    {
      f4 c0 = z, c1 = z;
      v4u bf[4];
#pragma unroll
      for (int s = 0; s < 4; ++s) bf[s] = *(LAS v4u*)(sm + ptr + 64 * s);
      __builtin_amdgcn_sched_barrier(0);
#pragma unroll
      for (int s = 0; s < 4; ++s) {
        c0 = mfma32(ao[s], bf[s], c0);
        c1 = mfma32(ao[4 + s], bf[s], c1);
      }
      if (col & 2) c0 = c1;
      st_sc1(rs, oo, v4u{pk(c0[0], c0[1]), T, pk(c0[2], c0[3]), T});
    }
    MARK(2);
    STAMP(9);
  }
  STAMP_FLUSH();
}

#define WS_END 0
#define WS_BASE (WS_END ? ((ws_size - (size_t)WS_END * 1048576) & ~(size_t)4095) : 0)
extern "C" void kernel_launch(void* const* d_in, const int* in_sizes, int n_in, void* d_out, int out_size, void* d_ws, size_t ws_size,
                              hipStream_t stream) {
  mla_glu_decode<<<256, 512, 0, stream>>>((const u16*)d_in[0], (const u16*)d_in[1], (const u16*)d_in[2], (const u16*)d_in[3],
                                          (const u16*)d_in[4], (const u16*)d_in[5], (const u16*)d_in[7], (const u16*)d_in[8],
                                          (const u16*)d_in[9], (u16*)d_out, (char*)d_ws + WS_BASE);
}
```

```cpp
#define PROBE_CLASS 2
#define PROBE_IDX 9
#define PROBE_WAVE 0
#define PROBE_K 0
#define STAMPS_MIN
#include <hip/hip_runtime.h>
#include <stdint.h>

#define GAS __attribute__((address_space(1)))
#define LAS __attribute__((address_space(3)))
typedef unsigned short u16;
typedef unsigned v4u __attribute__((ext_vector_type(4)));
typedef unsigned v2u __attribute__((ext_vector_type(2)));
typedef float f4 __attribute__((ext_vector_type(4)));
typedef float f2 __attribute__((ext_vector_type(2)));
typedef __bf16 bf8 __attribute__((ext_vector_type(8)));
typedef __bf16 bf2 __attribute__((ext_vector_type(2)));
typedef short s4 __attribute__((ext_vector_type(4)));

#define ROT 1
namespace {
constexpr unsigned NOWHERE = 0x7ff00000u;
constexpr int NH = 32, SK = 4096, DH = 128, DM = 512, DC = 512, FF = 1376;
constexpr int NSL = FF / 16;
constexpr int WS_E = 0;
constexpr int WS_QP = 4096;
constexpr int WS_QA = WS_QP + NH * 4 * 1024;
constexpr int WS_LSE = WS_QA + 2 * NH * 2048;
constexpr int WS_GO = WS_LSE + 2 * 32 * 16 * 128;
constexpr int WS_OPT = WS_GO + 2 * 32 * 16 * 128;
constexpr int WS_OPG = WS_OPT + 32 * 64 * 128;
constexpr int OGP = 16384 + ROT * 384;
constexpr int WS_PART = (WS_OPG + 32 * OGP + 4095) / 4096 * 4096;
constexpr int WS_BYTES = WS_PART + NH * 2 * 2 * 32 * 512;
constexpr int WS_STAMP = 7 << 19;
constexpr int CP = 1056;
constexpr int XP = 1088, SP = 288, SP2 = 544;
constexpr int L_C = 0, L_Q = 128 * CP, L_P = L_Q + 16 * CP, L_X = L_P + 4096, L_MX = L_X + 2 * XP, L_RR = L_MX + 256,
              L_JUNK = L_RR + 1024, L_FL = L_JUNK + 256, L_END = L_FL + 128;
static_assert(L_END <= 163840, "LDS");
constexpr int L_GG = 0, L_GD = 32768, L_GR = 65536;
constexpr int L_WUV = 0, L_WO = 65536, L_WQ = 0, L_WUK = 32768, L_CC = 131072, L_CT = L_CC + 8 * SP2, L_W = L_CT + 2 * SP;
#define UK_DELAY 20
#define H1_DELAY 12
#define T_START 340
#define CODE_TOUCH 1
#define NT_CKV 0
#define NT_WQ 0
#define NT_WUK 0
#define NT_T 0
#define NT_G 0
#define A_START 70
#define GLU_EARLY 0
#define G_START 0
#define C_START 0
#define LSE_LATE 0
#define LSE_WAVE 3
#define DATA_DELAY 0
#define NEWRED 1
#define R_START 0
#define G_TOUCH 0
#define LSE_PIPE 0
#define PV_MIX 4
#define LSE_QK 1
#define HMAP 1
#define GQ 1
#define DOWN_FIRST 1
#define EARLY_SWEEP 1
#define GO_DELAY 0
#define OUT_TOUCH 1
#define OUT_SC1 1
#define BUMP_EARLY 1
#define G2_DELAY 0
#define UK_FIRST 6
#define UK_TOUCH 1
#define TLB_LATE 150
#define U_START 0
#define TLB_FINE 0
#define CK_TLB 1
#define CK2_START 0
#define UK_WAVES 4
#define PIECEWISE 1
#define PF_GLU 1
constexpr unsigned SPIN_CAP = 1u << 20;
constexpr int AUX_SC1 = 16;

#define STAMPS
#define STAMP_W 0
#define STAMP_ON(i) ((i) == 0 || (i) == 9)
#define STAMP(i)                                                                          \
  do {                                                                                    \
    if (!STAMP_ON(i)) break;                                                              \
    unsigned long long t__;                                                               \
    asm volatile("s_memrealtime %0\n\ts_waitcnt lgkmcnt(0)" : "=s"(t__)::"memory");       \
    asm volatile("v_writelane_b32 %0, %1, " #i : "+v"(tsv) : "s"((unsigned)t__));         \
  } while (0)
#define STAMP_FLUSH()                                                                      \
  do {                                                                                     \
    const int cls_ = r < 16 ? 0 : (r < 28 ? 1 : 2);                                        \
    if ((cls_ == PROBE_CLASS || PROBE_CLASS == 3) && w == PROBE_WAVE && __builtin_amdgcn_readlane(tsv, PROBE_IDX) != 0) {   \
      v4u g_;                                                                              \
      for (unsigned sp_ = 0; sp_ < SPIN_CAP; ++sp_) { asm volatile("" ::: "memory"); g_ = ld_sc1(rs, 2048); if (g_.z == Tg) break; } \
      const unsigned long long t0_ = ((unsigned long long)__builtin_amdgcn_readfirstlane(g_.y) << 32) | (unsigned)__builtin_amdgcn_readfirstlane(g_.x); \
      const long long d_ = (int)(__builtin_amdgcn_readlane(tsv, PROBE_IDX) - (unsigned)t0_);                               \
      const unsigned long long until_ = t0_ + 4000ull + PROBE_K * d_;                      \
      while (__builtin_amdgcn_s_memrealtime() < until_) __builtin_amdgcn_s_sleep(1);       \
    }                                                                                      \
  } while (0)
#define PROBE_T(T_) do { Tg = T_; if (blockIdx.x == 0 && tid == 0) st_sc1(rs, 2048, v4u{(unsigned)tstart, (unsigned)(tstart >> 32), T_, 0u}); } while (0)

__device__ __forceinline__ f4 mfma32(v4u a, v4u b, f4 c) {
  return __builtin_amdgcn_mfma_f32_16x16x32_bf16(__builtin_bit_cast(bf8, a), __builtin_bit_cast(bf8, b), c, 0, 0, 0);
}
__device__ __forceinline__ f4 mfma16(v2u a, v2u b, f4 c) {
  return __builtin_amdgcn_mfma_f32_16x16x16bf16_1k(__builtin_bit_cast(s4, a), __builtin_bit_cast(s4, b), c, 0, 0, 0);
}
__device__ __forceinline__ unsigned pk(float lo, float hi) {
  f2 x = {lo, hi};
  return __builtin_bit_cast(unsigned, __builtin_convertvector(x, bf2));
}
__device__ __forceinline__ float asf(unsigned u) { return __builtin_bit_cast(float, u); }
__device__ __forceinline__ float blo(unsigned u) { return __builtin_bit_cast(float, u << 16); }
__device__ __forceinline__ float bhi(unsigned u) { return __builtin_bit_cast(float, u & 0xffff0000u); }
template <int CTRL> __device__ __forceinline__ float dpp(float v) {
  return __builtin_bit_cast(float, __builtin_amdgcn_update_dpp(0, __builtin_bit_cast(int, v), CTRL, 0xf, 0xf, true));
}
__device__ __forceinline__ float fmax2(float a, float b) {
#pragma float_control(precise, off)
  return __builtin_fmaxf(a, b);
}
typedef unsigned short h2 __attribute__((ext_vector_type(2)));
__device__ __forceinline__ unsigned scale_dn(unsigned p, unsigned kk) {
  return __builtin_bit_cast(unsigned, __builtin_elementwise_sub_sat(__builtin_bit_cast(h2, p), __builtin_bit_cast(h2, kk)));
}
#define PIN(x) asm volatile("" : "+v"(x))
constexpr int ROR1 = 0x121, ROR2 = 0x122, ROR4 = 0x124, ROR8 = 0x128, QP_2301 = 0x4e, QP_2222 = 0xaa;
__device__ __forceinline__ float sw32_add(float a, float b) {
  auto r = __builtin_amdgcn_permlane32_swap(__builtin_bit_cast(unsigned, a), __builtin_bit_cast(unsigned, b), false, false);
  return asf(r[0]) + asf(r[1]);
}
__device__ __forceinline__ float sw16_add(float a, float b) {
  auto r = __builtin_amdgcn_permlane16_swap(__builtin_bit_cast(unsigned, a), __builtin_bit_cast(unsigned, b), false, false);
  return asf(r[0]) + asf(r[1]);
}
__device__ __forceinline__ float vmax(unsigned a, unsigned b) {
  float r;
  asm("v_max_f32 %0, %1, %2" : "=v"(r) : "v"(a), "v"(b));
  return r;
}
__device__ __forceinline__ float vmax3(float a, float b, float c) {
  float r;
  asm("v_max3_f32 %0, %1, %2, %3" : "=v"(r) : "v"(a), "v"(b), "v"(c));
  return r;
}
__device__ __forceinline__ float sw32_max(float a) {
  auto r = __builtin_amdgcn_permlane32_swap(__builtin_bit_cast(unsigned, a), __builtin_bit_cast(unsigned, a), false, false);
  return vmax(r[0], r[1]);
}
__device__ __forceinline__ float sw16_max(float a) {
  auto r = __builtin_amdgcn_permlane16_swap(__builtin_bit_cast(unsigned, a), __builtin_bit_cast(unsigned, a), false, false);
  return vmax(r[0], r[1]);
}
__device__ __forceinline__ float wave_sum(float v) {
  v += dpp<ROR8>(v);
  v += dpp<ROR4>(v);
  v += dpp<ROR2>(v);
  v += dpp<ROR1>(v);
  v = sw16_add(v, v);
  return sw32_add(v, v);
}
__device__ __forceinline__ v2u trrd(LAS char* p) {
  return __builtin_bit_cast(v2u, __builtin_amdgcn_ds_read_tr16_b64_v4i16((LAS s4*)p));
}
__device__ __forceinline__ v4u trrd2(LAS char* p0, LAS char* p1) {
  v2u a = trrd(p0), b = trrd(p1);
  return v4u{a.x, a.y, b.x, b.y};
}
#define BAR() asm volatile("s_waitcnt lgkmcnt(0)\n\ts_barrier" ::: "memory")
#define VMWAIT(k) asm volatile("s_waitcnt vmcnt(%0)" ::"n"(k) : "memory")
__device__ __forceinline__ void gld16(v4u& d, const void* base, unsigned off) {
  asm volatile("global_load_dwordx4 %0, %1, %2" : "=v"(d) : "v"(off), "s"(base) : "memory");
}
__device__ __forceinline__ void gld4_sc1(unsigned& d, const void* base, unsigned off) {
  asm volatile("global_load_dword %0, %1, %2 sc1" : "=v"(d) : "v"(off), "s"(base) : "memory");
}
template <int NT = 0> __device__ __forceinline__ void touch(const void* base, unsigned off, unsigned lds) {
  if (NT) asm volatile("s_mov_b32 m0, %2\n\ts_nop 0\n\tglobal_load_lds_dword %0, %1 nt" ::"v"(off), "s"(base), "s"(lds) : "memory");
  else asm volatile("s_mov_b32 m0, %2\n\ts_nop 0\n\tglobal_load_lds_dword %0, %1" ::"v"(off), "s"(base), "s"(lds) : "memory");
}
__device__ __forceinline__ void touch_sc1(const void* base, unsigned off, unsigned lds) {
  asm volatile("s_mov_b32 m0, %2\n\ts_nop 0\n\tglobal_load_lds_dword %0, %1 sc1" ::"v"(off), "s"(base), "s"(lds) : "memory");
}
#define LD16(i, a, o) "global_load_dwordx4 %" #i ", %" #a ", %" #o
#define SWEEP_4(q, o, o0, o1, o2, o3, W)                                                                                     \
  asm volatile(LD16(0, 4, 5) " offset:" #o0 " sc1\n\t" LD16(1, 4, 5) " offset:" #o1 " sc1\n\t" LD16(2, 4, 5) " offset:" #o2         \
               " sc1\n\t" LD16(3, 4, 5) " offset:" #o3 " sc1" W                                                               \
               : "=&v"((q)[0]), "=&v"((q)[1]), "=&v"((q)[2]), "=&v"((q)[3])                                                  \
               : "v"(o), "s"(ws)                                                                                             \
               : "memory")
#define RESWEEP_4(q, o, o0, o1, o2, o3)                                                                                      \
  asm volatile(LD16(0, 4, 5) " offset:" #o0 " sc1\n\t" LD16(1, 4, 5) " offset:" #o1 " sc1\n\t" LD16(2, 4, 5) " offset:" #o2         \
               " sc1\n\t" LD16(3, 4, 5) " offset:" #o3 " sc1\n\ts_waitcnt vmcnt(0)"                                            \
               : "+v"((q)[0]), "+v"((q)[1]), "+v"((q)[2]), "+v"((q)[3])                                                      \
               : "v"(o), "s"(ws)                                                                                             \
               : "memory")
#define RELOAD(q, o, off) asm volatile(LD16(0, 1, 2) " offset:" #off " sc1" : "+v"(q) : "v"(o), "s"(ws) : "memory")
#define PIN4(q) asm volatile("s_waitcnt vmcnt(0)" : "+v"((q)[0]), "+v"((q)[1]), "+v"((q)[2]), "+v"((q)[3])::"memory")
#define TAG_BAD(q) __builtin_amdgcn_ballot_w64((T & (q).y & (q).w) != (T | (q).y | (q).w))
#define HOLE(q) __builtin_amdgcn_ballot_w64(max(max((q).x, (q).y), max((q).z, (q).w)) == ~0u)
#define MAX4(q) max(max((q).x, (q).y), max((q).z, (q).w))
#define HOLES4(q) __builtin_amdgcn_ballot_w64(max(max(MAX4((q)[0]), MAX4((q)[1])), max(MAX4((q)[2]), MAX4((q)[3]))) == ~0u)
#define TRETRY()                          \
  if (HOLE(a[0])) RELOAD(a[0], o, 0);     \
  if (HOLE(a[1])) RELOAD(a[1], o, 256);   \
  if (HOLE(a[2])) RELOAD(a[2], o, 2048);  \
  if (HOLE(a[3])) RELOAD(a[3], o, 2304);  \
  PIN4(a)
#define SWEEP_L(W)                                                                                                          \
  asm volatile("global_load_dwordx2 %0, %3, %5 sc1\n\tglobal_load_dwordx2 %1, %4, %5 sc1\n\t"                              \
               "global_load_dwordx2 %2, %4, %5 offset:512 sc1" W                                                            \
               : "=&v"(la), "=&v"(l0), "=&v"(l1)                                                                            \
               : "v"(ola), "v"(olo), "s"(ws)                                                                                \
               : "memory")
#define SWEEP_2(p, q, oa, ob, W) \
  asm volatile(LD16(0, 2, 4) " sc1\n\t" LD16(1, 3, 4) " sc1" W : "=&v"(p), "=&v"(q) : "v"(oa), "v"(ob), "s"(ws) : "memory")
#define TAGS_BAD4(q) \
  __builtin_amdgcn_ballot_w64((T & (q)[0].y & (q)[0].w & (q)[1].y & (q)[1].w & (q)[2].y & (q)[2].w & (q)[3].y & (q)[3].w) != \
                              (T | (q)[0].y | (q)[0].w | (q)[1].y | (q)[1].w | (q)[2].y | (q)[2].w | (q)[3].y | (q)[3].w))
#define FLAG_SET(i) asm volatile("ds_write_b32 %0, %1" ::"v"(smb + L_FL + 4 * (i)), "v"(1u) : "memory")
#define FLAG_WAIT4(i)                                                                                     \
  for (unsigned spin_ = 0; spin_ < SPIN_CAP; ++spin_) {                                                   \
    v4u f_;                                                                                               \
    asm volatile("ds_read_b128 %0, %1\n\ts_waitcnt lgkmcnt(0)" : "=v"(f_) : "v"(smb + L_FL + 16 * (i)) : "memory"); \
    if (__builtin_amdgcn_readfirstlane(f_.x & f_.y & f_.z & f_.w)) break;                                 \
  }
#define HOLD_UNTIL(ticks)              \
  asm volatile("" : "+s"(tstart));     \
  while ((unsigned)__builtin_amdgcn_s_memrealtime() - (unsigned)tstart < (ticks)) __builtin_amdgcn_s_sleep(1)
#define MARK(i) asm volatile(".Lmla_e" #i ":")
#define RETRY(bad, again)                                   \
  if (bad) {                                                \
    for (unsigned spin = SPIN_CAP; spin; --spin) {          \
      again;                                                \
      if (!(bad)) break;                                    \
    }                                                       \
  }
#define UNDEF(x) asm volatile("" : "=v"(x))
#define EPOCH() asm volatile("v_add_u32 %0, 1, %1" : "=v"(T) : "v"(ep) : "memory")
#define WAIT0 "\n\ts_waitcnt vmcnt(0)"
template <int NT = 0> __device__ __forceinline__ void dma16(const void* base, unsigned off, unsigned lds) {
  if (NT) asm volatile("s_mov_b32 m0, %2\n\ts_nop 0\n\tglobal_load_lds_dwordx4 %0, %1 nt" ::"v"(off), "s"(base), "s"(lds) : "memory");
  else asm volatile("s_mov_b32 m0, %2\n\ts_nop 0\n\tglobal_load_lds_dwordx4 %0, %1" ::"v"(off), "s"(base), "s"(lds) : "memory");
}
__device__ __forceinline__ v4u ld_sc1(__amdgpu_buffer_rsrc_t rs, int off) {
  return __builtin_amdgcn_raw_buffer_load_b128(rs, off, 0, AUX_SC1);
}
__device__ __forceinline__ v2u ld8_sc1(__amdgpu_buffer_rsrc_t rs, int off) {
  return __builtin_amdgcn_raw_buffer_load_b64(rs, off, 0, AUX_SC1);
}
__device__ __forceinline__ void st_sc1(__amdgpu_buffer_rsrc_t rs, int off, v4u v) {
  __builtin_amdgcn_raw_buffer_store_b128(v, rs, off, 0, AUX_SC1);
}
__device__ __forceinline__ void ln_stats(v4u r, float& mean, float& rstd) {
  float e[8] = {blo(r.x), bhi(r.x), blo(r.y), bhi(r.y), blo(r.z), bhi(r.z), blo(r.w), bhi(r.w)};
  float s = ((e[0] + e[1]) + (e[2] + e[3])) + ((e[4] + e[5]) + (e[6] + e[7]));
  mean = wave_sum(s) * (1.f / DM);
  float q = 0.f;
#pragma unroll
  for (int i = 0; i < 8; ++i) {
    float d = e[i] - mean;
    q = fmaf(d, d, q);
  }
  rstd = __builtin_amdgcn_rsqf(wave_sum(q) * (1.f / DM) + 1e-5f);
}
}

__global__ void __launch_bounds__(512, 2)
mla_glu_decode(const u16* __restrict__ x, const u16* __restrict__ ckv, const u16* __restrict__ wq, const u16* __restrict__ wuk,
               const u16* __restrict__ wuv, const u16* __restrict__ wo, const u16* __restrict__ wg, const u16* __restrict__ wu,
               const u16* __restrict__ wd, u16* __restrict__ out, char* __restrict__ ws) {
  unsigned long long tstart = __builtin_amdgcn_s_memrealtime();
  __builtin_amdgcn_sched_barrier(0);
  __shared__ __attribute__((aligned(16))) char smem_[L_END];
  LAS char* sm = (LAS char*)smem_;
  const unsigned smb = (unsigned)(size_t)sm;
  const int tid = threadIdx.x, l = tid & 63, w = __builtin_amdgcn_readfirstlane(tid >> 6);
  const int col = l & 15, g4 = l >> 4, tq = (l & 15) >> 2, tp = l & 3;
  const int xcd = blockIdx.x & 7, r = blockIdx.x >> 3;
  const __amdgpu_buffer_rsrc_t rs = __builtin_amdgcn_make_buffer_rsrc(ws, 0, WS_BYTES, 0x00020000);
  int bw = (blockIdx.x >> 7) * w;
  asm volatile("" : "+s"(bw));
  if (bw >= 5) {
    const char* pc0;
    unsigned len;
    asm volatile("s_getpc_b64 %0\n.Lmla_pc:\n\ts_mov_b32 %1, .Lmla_e0-.Lmla_pc\n\ts_max_i32 %1, %1, .Lmla_e1-.Lmla_pc\n\t"
                 "s_max_i32 %1, %1, .Lmla_e2-.Lmla_pc\n\ts_max_i32 %1, %1, .Lmla_e3-.Lmla_pc\n\ts_max_i32 %1, %1, .Lmla_e4-.Lmla_pc\n\t"
                 "s_max_i32 %1, %1, .Lmla_e5-.Lmla_pc"
                 : "=s"(pc0), "=s"(len)::"scc");
    touch(pc0, min((unsigned)(64 * (bw - 5) + l) * 128u, len), smb + L_JUNK);
  }
  __builtin_amdgcn_sched_barrier(0);
  const int h = HMAP ? xcd + 8 * ((r >> 2) & 3) : 4 * xcd + ((r >> 2) & 3), j = r & 3;
  unsigned vq = (unsigned)(((128 * j + g4 + 4 * w) * 4096 + h * DH + 8 * (col ^ ((g4 | ((w & 2) << 1)) << 1))) * 2), vx = (w & 1) * 1024 + l * 16;
  unsigned vzero = 0u;
  int lq = smb + L_WQ + w * 1024, lx = smb + L_X + (w & 1) * XP;
  asm volatile("" : "+v"(vq), "+v"(vx), "+v"(vzero), "+s"(lq), "+s"(lx));
  asm volatile("" ::"s"(x), "s"(ckv), "s"(wq), "s"(wuk), "s"(wuv), "s"(wo), "s"(wg), "s"(wu), "s"(wd), "s"(out), "s"(ws));
  unsigned tsv = 0u;
  STAMP(0);
  unsigned Tg = 0;

  unsigned ep, T;
  LAS char* const px = sm + L_X + (col & 1) * XP + 16 * g4;
  if ((w >> 1) == 2) dma16(x, vx, lx);
  const f4 z = {0.f, 0.f, 0.f, 0.f};
#define LAYERNORM()                                                                                                              \
  {                                                                                                                              \
    const v4u xr = *(LAS v4u*)(sm + L_X + (w & 1) * XP + l * 16);                                                                \
    float e[8] = {blo(xr.x), bhi(xr.x), blo(xr.y), bhi(xr.y), blo(xr.z), bhi(xr.z), blo(xr.w), bhi(xr.w)};                       \
    const float mean = wave_sum(((e[0] + e[1]) + (e[2] + e[3])) + ((e[4] + e[5]) + (e[6] + e[7]))) * (1.f / DM);                 \
    float q = 0.f;                                                                                                               \
    _Pragma("unroll") for (int i = 0; i < 8; ++i) {                                                                              \
      e[i] -= mean;                                                                                                              \
      q = fmaf(e[i], e[i], q);                                                                                                   \
    }                                                                                                                            \
    const float rstd = __builtin_amdgcn_rsqf(wave_sum(q) * (1.f / DM) + 1e-5f);                                                  \
    *(LAS v4u*)(sm + L_X + (w & 1) * XP + l * 16) =                                                                              \
        v4u{pk(e[0] * rstd, e[1] * rstd), pk(e[2] * rstd, e[3] * rstd), pk(e[4] * rstd, e[5] * rstd), pk(e[6] * rstd, e[7] * rstd)}; \
  }

  if (__builtin_expect(r >= 16, 0)) {
    const int a = r - 16, b = xcd >> 2, n = 8 * (xcd & 3) + (a >> 1), g = a & 1;
    const int sl = 12 * xcd + a, rd = (4 * xcd + a) & 31;
    const bool glu = a < 12 && sl < NSL;
    const int v = w & 3;
    unsigned op = WS_PART + ((((16 * g + (col & 7)) * 2 + (v >> 1)) * 2 + b) * 32 + ((n + ROT * 4 * (col & 7)) & 31)) * 512 + 256 * (v & 1) +
                  (col >> 3) * 64 + (g4 & 1) * 32 + (g4 >> 1) * 16;
    gld4_sc1(ep, ws, vzero);
    unsigned o = WS_QA + (b * NH + 16 * g) * 2048 + w * 4096 + l * 16, pqs = smb + L_Q + 2 * w * CP + l * 8;
    PIN(o); PIN(pqs);
    {
    {
      const unsigned vc = (unsigned)(((b * SK + 128 * n) * DC + 8 * l) * 2);
      STAMP(10);
      if (w == 4) touch(ckv, (unsigned)((b * SK + 128 * n) * DC * 2), smb + L_JUNK);
      HOLD_UNTIL(U_START);
      if (w < UK_WAVES) touch(wuk, (unsigned)((32 * a + 8 * w + (l >> 3)) * 8192 + (xcd + 8 * ((l >> 1) & 3)) * 256 + (l & 1) * 128), smb + L_JUNK);
      HOLD_UNTIL(C_START);
    if (w >= 4) {
#pragma unroll
      for (int q = 0; q < 4; ++q) st_sc1(rs, op + 128 * (q >> 1) + (q & 1) * 8 * 65536, v4u{~0u, ~0u, ~0u, ~0u});
      if (w == 7 && l < 32) *(LAS unsigned*)(sm + L_FL + 4 * l) = 0u;
    }
      HOLD_UNTIL(A_START);
#pragma unroll 1
      for (int q = 0; q < 4; ++q) {
        int row = w + ((64 * g + 32 * q) & 127);
        asm volatile("s_barrier" ::: "memory");
#pragma unroll 1
        for (int i = 0; i < 4; ++i, row += 8) dma16<NT_CKV>(ckv, vc + row * 1024u, smb + L_C + row * CP);
      }
    }
#define CHALF()                               \
  asm volatile("s_barrier" ::: "memory");     \
  SWEEP_4(qg, o, 0, 1024, 2048, 3072, "");    \
  VMWAIT(12);                                 \
  asm volatile("s_barrier" ::: "memory")
#define CLAND() \
  VMWAIT(4);    \
  EPOCH();      \
  PROBE_T(T);   \
  BAR();        \
  STAMP(1)
#define QRETRY()                              \
  if (TAG_BAD(qg[0])) RELOAD(qg[0], o, 0);    \
  if (TAG_BAD(qg[1])) RELOAD(qg[1], o, 1024); \
  if (TAG_BAD(qg[2])) RELOAD(qg[2], o, 2048); \
  if (TAG_BAD(qg[3])) RELOAD(qg[3], o, 3072); \
  PIN4(qg)
#define QGATHER()                                                                                                           \
  VMWAIT(0);                                                                                                      \
  asm volatile("" : "+v"(qg[0]), "+v"(qg[1]), "+v"(qg[2]), "+v"(qg[3])::"memory");                                          \
  RETRY(TAGS_BAD4(qg), QRETRY());                                                                                           \
  STAMP(3);                                                                                                                 \
  asm volatile("ds_write2_b32 %0, %2, %3 offset0:0 offset1:1\n\tds_write2_b32 %0, %4, %5 offset0:128 offset1:129\n\t"        \
               "ds_write2_b32 %1, %6, %7 offset0:0 offset1:1\n\tds_write2_b32 %1, %8, %9 offset0:128 offset1:129" ::"v"(pqs), \
               "v"(pqs + CP), "v"(qg[0].x), "v"(qg[0].z), "v"(qg[1].x), "v"(qg[1].z), "v"(qg[2].x), "v"(qg[2].z), "v"(qg[3].x), \
               "v"(qg[3].z)                                                                                                 \
               : "memory")
    if (__builtin_expect(w < 4, 1)) {
      v4u fr[32], qg[4];
      unsigned pc = L_C + (32 * w + col) * CP + 16 * g4;
      PIN(pc);
      CHALF();
      const bool early = (w >> 1) == g;
      if (!early) {
        CLAND();
      }
#pragma unroll
      for (int kt = 0; kt < 2; ++kt)
#pragma unroll
        for (int s = 0; s < 16; ++s) fr[16 * kt + s] = *(LAS v4u*)(sm + pc + 16 * kt * CP + 64 * s);
      if (early) {
        CLAND();
      }
      const int u = w ^ (2 * g);
      unsigned p = L_Q + col * CP + 16 * g4, pp = L_P + u * 1024 + l * 16, pm = L_MX + col * 16 + u * 4;
      unsigned ol = WS_LSE + (HMAP ? (((b * 32 + n) * 2 + g) * 8 + ((col + ROT * n) & 7)) * 128 + (col >> 3) * 8 : ((b * 32 + n) * 8 + 4 * g + (col >> 2)) * 128 + (col & 3) * 8);
      if (g4) ol = NOWHERE;
      PIN(p); PIN(pp); PIN(pm); PIN(ol);
      STAMP(2);
      QGATHER();
      BAR();
      STAMP(4);
      f4 s0 = z, s1 = z;
      v4u qf[16];
#pragma unroll
      for (int s = 0; s < 6; ++s) qf[s] = *(LAS v4u*)(sm + p + 64 * s);
      __builtin_amdgcn_sched_barrier(0);
#pragma unroll
      for (int s = 0; s < 16; ++s) {
        if (s + 6 < 16) qf[s + 6] = *(LAS v4u*)(sm + p + 64 * (s + 6));
        s0 = mfma32(fr[s], qf[s], s0);
        s1 = mfma32(fr[16 + s], qf[s], s1);
        __builtin_amdgcn_sched_barrier(0);
      }
      float mx = fmax2(fmax2(fmax2(s0[0], s0[1]), fmax2(s0[2], s0[3])), fmax2(fmax2(s1[0], s1[1]), fmax2(s1[2], s1[3])));
      mx = sw32_max(sw16_max(mx));
      constexpr float SC = 0.08838834764831845f * 1.4426950408889634f;
      const float mi = __builtin_ceilf(mx * SC);
#pragma unroll
      for (int e = 0; e < 4; ++e) {
        s0[e] = __builtin_amdgcn_exp2f(fmaf(s0[e], SC, -mi));
        s1[e] = __builtin_amdgcn_exp2f(fmaf(s1[e], SC, -mi));
      }
      *(LAS float*)(sm + pm) = mi;
      *(LAS v4u*)(sm + pp) = v4u{pk(s0[0], s0[1]), pk(s0[2], s0[3]), pk(s1[0], s1[1]), pk(s1[2], s1[3])};
      BAR();
      STAMP(5);
      if (w == LSE_QK - 1) {
        const f4 m4 = *(LAS f4*)(sm + pm - 4 * u);
        v4u pf[4];
#pragma unroll
        for (int k = 0; k < 4; ++k) pf[k] = *(LAS v4u*)(sm + pp + (k - u) * 1024);
        __builtin_amdgcn_sched_barrier(0);
        const float M = vmax3(vmax3(m4[0], m4[1], m4[2]), m4[3], m4[3]);
        const v4u one = {0x3f803f80u, 0x3f803f80u, 0x3f803f80u, 0x3f803f80u};
        f4 ls = z;
#pragma unroll
        for (int u = 0; u < 4; ++u) {
          const unsigned kk = min((unsigned)(M - m4[u]), 255u) * 0x00800080u;
#pragma unroll
          for (int e = 0; e < 4; ++e) pf[u][e] = scale_dn(pf[u][e], kk);
          ls = mfma32(one, pf[u], ls);
        }
        __builtin_amdgcn_raw_buffer_store_b64(v2u{__builtin_bit_cast(unsigned, M + __builtin_amdgcn_logf(ls[0])), T}, rs, ol, 0, AUX_SC1);
      }
    } else {
      v4u fr[32], qg[4];
      unsigned p0 = L_C + (64 * g + 4 * g4 + tq) * CP + 256 * v + 8 * tp, p1 = L_C + (64 * (g ^ 1) + 4 * g4 + tq) * CP + 256 * v + 8 * tp;
      PIN(p0); PIN(p1);
      CHALF();
#pragma unroll
      for (int ct = 0; ct < 8; ++ct)
#pragma unroll
        for (int u = 0; u < 2; ++u) fr[4 * ct + u] = trrd2(sm + p0 + 32 * u * CP + 32 * ct, sm + p0 + (32 * u + 16) * CP + 32 * ct);
      CLAND();
#pragma unroll
      for (int ct = 0; ct < 8; ++ct)
#pragma unroll
        for (int u = 0; u < 2; ++u) fr[4 * ct + 2 + u] = trrd2(sm + p1 + 32 * u * CP + 32 * ct, sm + p1 + (32 * u + 16) * CP + 32 * ct);
      unsigned ol = WS_LSE + (HMAP ? (((b * 32 + n) * 2 + g) * 8 + ((col + ROT * n) & 7)) * 128 + (col >> 3) * 8 : ((b * 32 + n) * 8 + 4 * g + (col >> 2)) * 128 + (col & 3) * 8), pp = L_P + l * 16, pm = L_MX + col * 16;
      v4u one = {0x3f803f80u, 0x3f803f80u, 0x3f803f80u, 0x3f803f80u};
      unsigned op2 = op + 8 * 65536;
      PIN(ol); PIN(pp); PIN(pm); PIN(one); PIN(op); PIN(op2);
      STAMP(2);
      QGATHER();
      BAR();
      STAMP(4);
      if (glu) {
        const int k = 8 * (v >> 1) + 2 * (a & 3);
        const unsigned og = (unsigned)(((32 * k + (l >> 1)) * FF + 16 * sl + 8 * (l & 1)) * 2);
        dma16((v & 1) ? wu : wg, og, smb + L_GG + (v & 1) * 16384 + k * 1024);
        dma16((v & 1) ? wu : wg, og + 32 * FF * 2, smb + L_GG + (v & 1) * 16384 + k * 1024 + 1024);
      } else if (v < G_TOUCH) {
        const int ln = 128 * a + 64 * (v >> 1) + l;
        touch((v & 1) ? wu : wg, (unsigned)((ln >> 2) * (FF * 2)) + (unsigned)min(384 * xcd + 128 * (ln & 3), FF * 2 - 4), smb + L_JUNK);
      }
      if (glu && w < 6) LAYERNORM();
      BAR();
      STAMP(5);
      const f4 m4 = *(LAS f4*)(sm + pm);
      v4u pf[4];
#pragma unroll
      for (int u = 0; u < 4; ++u) pf[u] = *(LAS v4u*)(sm + pp + u * 1024);
      __builtin_amdgcn_sched_barrier(0);
      const float M = vmax3(vmax3(m4[0], m4[1], m4[2]), m4[3], m4[3]);
      f4 ls = z, c[4] = {z, z, z, z};
#pragma unroll
      for (int u = 0; u < 4; ++u) {
        const unsigned kk = min((unsigned)(M - m4[u]), 255u) * 0x00800080u;
#pragma unroll
        for (int e = 0; e < 4; ++e) pf[u][e] = scale_dn(pf[u][e], kk);
        ls = mfma32(one, pf[u], ls);
        if (u < 3) {
#pragma unroll
          for (int k = 0; k < 4; ++k) c[k] = mfma32(fr[4 * k + u], pf[u], c[k]);
        }
      }
      __builtin_amdgcn_sched_barrier(0);
      c[0] = mfma32(fr[3], pf[3], c[0]);
      c[1] = mfma32(fr[7], pf[3], c[1]);
      __builtin_amdgcn_sched_barrier(0);
      float il = __builtin_amdgcn_rcpf(ls[0]);
      unsigned lse = __builtin_bit_cast(unsigned, M + __builtin_amdgcn_logf(ls[0]));
      c[2] = mfma32(fr[11], pf[3], c[2]);
      c[3] = mfma32(fr[15], pf[3], c[3]);
#define PV_OUT(c, q)                                                                                          \
  {                                                                                                           \
    v4u R[2];                                                                                                 \
    _Pragma("unroll") for (int k = 0; k < 2; ++k) {                                                           \
      const f4 c0 = c[2 * k] * il, c1 = c[2 * k + 1] * il;                                                    \
      const unsigned x0 = min(pk(c0[0], c0[1]), 0xfffffffeu), x1 = min(pk(c0[2], c0[3]), 0xfffffffeu);        \
      const unsigned y0 = min(pk(c1[0], c1[1]), 0xfffffffeu), y1 = min(pk(c1[2], c1[3]), 0xfffffffeu);        \
      auto r0 = __builtin_amdgcn_permlane16_swap(x0, y0, false, false);                                       \
      auto r1 = __builtin_amdgcn_permlane16_swap(x1, y1, false, false);                                       \
      R[k] = v4u{r0[0], r1[0], r0[1], r1[1]};                                                                 \
    }                                                                                                         \
    v4u A, B;                                                                                                 \
    _Pragma("unroll") for (int e = 0; e < 4; ++e) {                                                           \
      A[e] = (unsigned)__builtin_amdgcn_update_dpp((int)R[0][e], (int)R[1][e], ROR8, 0xf, 0xc, false);        \
      B[e] = (unsigned)__builtin_amdgcn_update_dpp((int)R[1][e], (int)R[0][e], ROR8, 0xf, 0x3, false);        \
    }                                                                                                         \
    st_sc1(rs, op + 128 * q, A);                                                                              \
    st_sc1(rs, op2 + 128 * q, B);                                                                             \
  }
      __builtin_amdgcn_sched_barrier(0);
      f4 d[4] = {z, z, z, z};
#pragma unroll
      for (int u = 0; u < 4; ++u)
#pragma unroll
        for (int k = 0; k < 4; ++k) d[k] = mfma32(fr[16 + 4 * k + u], pf[u], d[k]);
      PV_OUT(c, 0);
#pragma unroll
      for (int i = 0; i < 16; ++i) {
        __builtin_amdgcn_sched_group_barrier(0x8, 1, 0);
        __builtin_amdgcn_sched_group_barrier(0x2, PV_MIX, 0);
      }
      __builtin_amdgcn_sched_barrier(0);
      PV_OUT(d, 1);
      FLAG_SET(v);
      if (glu) {
        VMWAIT(4);
        FLAG_SET(12 + v);
      }
      MARK(3);
      STAMP(6);
    }
    }
      if (glu && w < 4) {
        v4u af[8], xf[8];
        unsigned oog = WS_OPG + (8 * w + (col >> 1)) * OGP + sl * 128 + ((col & 1) * 4 + g4) * 16;
        PIN(oog);
        FLAG_WAIT4(0);
#pragma unroll 1
        for (int i = 2; i < 8; ++i) {
          const int k = 8 * (w >> 1) + ((2 * a + i) & 7);
          dma16((w & 1) ? wu : wg, (unsigned)(((32 * k + (l >> 1)) * FF + 16 * sl + 8 * (l & 1)) * 2), smb + L_GG + (w & 1) * 16384 + k * 1024);
        }
#pragma unroll 1
        for (int i = 0; i < 4; ++i) dma16(wd, (unsigned)(((16 * sl + 4 * w + i) * DM + 8 * l) * 2), smb + L_GD + (4 * w + i) * CP);
        unsigned pg = L_GG + (w & 1) * 16384 + 32 * (256 * (w >> 1) + 8 * g4 + tq) + 8 * tp;
        unsigned pd = L_GD + (4 * g4 + tq) * CP + 256 * w + 8 * tp, pgw = L_GR + ((w & 1) * 2 + (w >> 1)) * 1024 + l * 16;
        PIN(pg); PIN(pd); PIN(pgw);
#pragma unroll
        for (int s = 0; s < 8; ++s) xf[s] = *(LAS v4u*)(px + 512 * (w >> 1) + 64 * s);
        FLAG_WAIT4(3);
        VMWAIT(4);
        STAMP(7);
        f4 acc = z;
#pragma unroll
        for (int s = 0; s < 8; ++s) af[s] = trrd2(sm + pg + 32 * 32 * s, sm + pg + 32 * (32 * s + 4));
        __builtin_amdgcn_sched_barrier(0);
#pragma unroll
        for (int s = 0; s < 8; ++s) acc = mfma32(af[s], xf[s], acc);
        *(LAS f4*)(sm + pgw) = acc;
        VMWAIT(0);
        FLAG_SET(8 + w);
        FLAG_WAIT4(2);
        v2u dn[8];
        LAS char* pr = sm + L_GR + l * 16;
        const f4 gx = *(LAS f4*)(pr) + *(LAS f4*)(pr + 1024), ux = *(LAS f4*)(pr + 2048) + *(LAS f4*)(pr + 3072);
#pragma unroll
        for (int t = 0; t < 8; ++t) dn[t] = trrd(sm + pd + 32 * t);
        float hh[4];
#pragma unroll
        for (int e = 0; e < 4; ++e) hh[e] = ux[e] * __builtin_amdgcn_rcpf(1.f + __builtin_amdgcn_exp2f(-1.4426950408889634f * gx[e]));
        const v2u hb = {pk(hh[0], hh[1]), pk(hh[2], hh[3])};
        f4 d = z;
#pragma unroll
        for (int t = 0; t < 8; ++t) d = mfma16(dn[t], (col >> 1) == t ? hb : v2u{0u, 0u}, d);
        v4u dv = {pk(d[0], d[1]), T, pk(d[2], d[3]), T};
        PIN(dv);
        st_sc1(rs, oog, dv);
        MARK(4);
        STAMP(9);
      }
    if (a >= 12) {
      const unsigned m = (col & 1) ? 0x3f800000u : 0x3f80u;
      v4u sel = {col < 2 ? m : 0u, (col >> 1) == 1 ? m : 0u, col < 2 ? m : 0u, (col >> 1) == 1 ? m : 0u};
      PIN(sel);
      if (w == 0) {
        unsigned ot = WS_OPT + rd * 8192 + 4096 + l * 16, oy = (l >> 2) * 1024 + rd * 32 + (l & 3) * 8, pr = smb + L_RR + (l & 7) * 16;
        PIN(ot); PIN(oy); PIN(pr);
        v4u p[8];
        touch(out, (unsigned)((l & 1) * 1024 + rd * 32), smb + L_JUNK);
        HOLD_UNTIL(R_START);
        SWEEP_4(p, ot, -4096, -3072, -2048, -1024, "");
        SWEEP_4(p + 4, ot, 0, 1024, 2048, 3072, "");
        PIN4(p);
        PIN4(p + 4);
#define RRETRY()                                \
  if (TAG_BAD(p[0])) RELOAD(p[0], ot, -4096);   \
  if (TAG_BAD(p[1])) RELOAD(p[1], ot, -3072);   \
  if (TAG_BAD(p[2])) RELOAD(p[2], ot, -2048);   \
  if (TAG_BAD(p[3])) RELOAD(p[3], ot, -1024);   \
  if (TAG_BAD(p[4])) RELOAD(p[4], ot, 0);       \
  if (TAG_BAD(p[5])) RELOAD(p[5], ot, 1024);    \
  if (TAG_BAD(p[6])) RELOAD(p[6], ot, 2048);    \
  if (TAG_BAD(p[7])) RELOAD(p[7], ot, 3072);    \
  PIN4(p);                                      \
  PIN4(p + 4)
        RETRY(TAGS_BAD4(p) | TAGS_BAD4(p + 4), RRETRY());
        STAMP(8);
        f4 acc = z;
#pragma unroll
        for (int i = 0; i < 4; ++i) acc = mfma32(sel, v4u{p[2 * i].x, p[2 * i].z, p[2 * i + 1].x, p[2 * i + 1].z}, acc);
        f4 rest[6];
        for (unsigned spin = 0; spin < SPIN_CAP; ++spin) {
          v4u f0;
          v2u f1;
          asm volatile("ds_read_b128 %0, %8\n\tds_read_b64 %1, %8 offset:16\n\tds_read_b128 %2, %9\n\tds_read_b128 %3, %9 offset:128\n\t"
                       "ds_read_b128 %4, %9 offset:256\n\tds_read_b128 %5, %9 offset:384\n\tds_read_b128 %6, %9 offset:512\n\t"
                       "ds_read_b128 %7, %9 offset:640\n\ts_waitcnt lgkmcnt(0)"
                       : "=&v"(f0), "=&v"(f1), "=&v"(rest[0]), "=&v"(rest[1]), "=&v"(rest[2]), "=&v"(rest[3]), "=&v"(rest[4]), "=&v"(rest[5])
                       : "v"(smb + L_FL + 64), "v"(pr)
                       : "memory");
          if (__builtin_amdgcn_readfirstlane(f0.x & f0.y & f0.z & f0.w & f1.x & f1.y)) break;
        }
        const f4 gl = ((rest[0] + rest[1]) + (rest[2] + rest[3])) + (rest[4] + rest[5]);
#pragma unroll
        for (int e = 0; e < 4; ++e) acc[e] = (acc[e] + dpp<ROR8>(acc[e])) + gl[e];
        if (l < 8) asm volatile("global_store_dwordx2 %0, %1, %2 sc1" ::"v"(oy), "v"(v2u{pk(acc[0], acc[1]), pk(acc[2], acc[3])}), "s"(out) : "memory");
      } else if (w < 7) {
        const bool last = w == 6;
        unsigned og = WS_OPG + rd * OGP + (w - 1) * 2048 + (last ? min(l, 47) : l) * 16, og2 = last ? og : og + 1024;
        const v2u xr = *(const v2u*)((const char*)x + ((l >> 2) & 1) * 1024 + rd * 32 + (l & 3) * 8);
        unsigned m0 = (last && l >= 48) ? 0u : ~0u, m1 = last ? 0u : ~0u;
        f4 xv = {blo(xr.x), bhi(xr.x), blo(xr.y), bhi(xr.y)};
        if (!last) xv = z;
        PIN(og); PIN(og2); PIN(m0); PIN(m1); PIN(xv);
        v4u p0, p1;
        HOLD_UNTIL(R_START);
        UNDEF(p0); UNDEF(p1);
        for (unsigned spin = SPIN_CAP; spin; --spin) {
          SWEEP_2(p0, p1, og, og2, WAIT0);
          if (!(TAG_BAD(p0) | TAG_BAD(p1))) break;
        }
        STAMP(8);
        f4 acc = mfma32(sel, v4u{p0.x & m0, p0.z & m0, p1.x & m1, p1.z & m1}, z);
#pragma unroll
        for (int e = 0; e < 4; ++e) acc[e] = (acc[e] + dpp<ROR8>(acc[e])) + xv[e];
        if (l < 8) *(LAS f4*)(sm + L_RR + (w - 1) * 128 + l * 16) = acc;
        FLAG_SET(15 + w);
      }
      else if (rd == 0) {
        const unsigned ol0 = WS_LSE + (l * 16 + ((ROT * l) & 7)) * 128 + 4;
        unsigned t0, t1;
        for (unsigned spin = 0;; ++spin) {
          __builtin_amdgcn_s_sleep(32);
          asm volatile("global_load_dword %0, %2, %3 sc1\n\tglobal_load_dword %1, %2, %3 offset:1024 sc1" WAIT0
                       : "=&v"(t0), "=&v"(t1)
                       : "v"(ol0), "s"(ws)
                       : "memory");
          if (!__builtin_amdgcn_ballot_w64((t0 & t1 & T) != (t0 | t1 | T)) || spin > SPIN_CAP) break;
        }
        if (l == 0) __builtin_amdgcn_raw_buffer_store_b32(T, rs, WS_E, 0, AUX_SC1);
      }
      MARK(5);
      STAMP(9);
    }
    STAMP_FLUSH();
    __builtin_amdgcn_endpgm();
  }
  {
    const int ca = j >> 1, mb = j & 1;
    const int fl = tq | ((g4 & 1) << 2);
    if (__builtin_expect(w < 4, 1)) {
#pragma unroll 1
      for (int i = 0; i < 8; ++i, vq += 131072u, lq += 4096) dma16<NT_WQ>(wq, vq, lq);
      gld4_sc1(ep, ws, vzero);
      unsigned p = L_WQ + 256 * (8 * g4 + tq) + 16 * ((4 * w + (tp >> 1)) ^ (fl << 1)) + 8 * (tp & 1), p2 = p ^ 32;
      unsigned oqp = col < 4 ? WS_QP + ((h * 4 + j) * 8 + 2 * w) * 128 + col * 64 + g4 * 16 : NOWHERE;
      unsigned pxj = L_X + (col & 1) * XP + 16 * g4 + 256 * j;
      PIN(p); PIN(p2); PIN(oqp); PIN(pxj);
      unsigned o = WS_QP + (h * 4 + w) * 1024 + l * 16;
      unsigned pqw = smb + L_CC + (2 * w + ((l >> 2) & 1)) * SP + (l >> 3) * 32 + (l & 3) * 8, pqr = L_CC + (col & 7) * SP + 16 * g4;
      unsigned oqa = col < 4 ? WS_QA + ((col & 1) * NH + h) * 2048 + (64 * j + 16 * w + 8 * (col >> 1) + 2 * g4) * 8 : NOWHERE;
      unsigned pu = L_WUK + 256 * (32 * w + col) + 16 * (g4 ^ col);
      PIN(o); PIN(oqa); PIN(pu); PIN(pqw); PIN(pqr);
      VMWAIT(0);
      EPOCH();
      STAMP(1);
      BAR();
      STAMP(2);
      {
        v4u af[8], xf[4];
#pragma unroll
        for (int s = 0; s < 4; ++s) {
          xf[s] = *(LAS v4u*)(sm + pxj + 64 * s);
          af[s] = trrd2(sm + p + 256 * 32 * s, sm + p + 256 * (32 * s + 4));
          af[4 + s] = trrd2(sm + p2 + 256 * 32 * s, sm + p2 + 256 * (32 * s + 4));
        }
        __builtin_amdgcn_sched_barrier(0);
        f4 c0 = z, c1 = z;
#pragma unroll
        for (int s = 0; s < 4; ++s) {
          c0 = mfma32(af[s], xf[s], c0);
          c1 = mfma32(af[4 + s], xf[s], c1);
        }
        if (col & 2) c0 = c1;
        st_sc1(rs, oqp, v4u{pk(c0[0], c0[1]), T, pk(c0[2], c0[3]), T});
      }
      STAMP(3);
      v4u a, uk[8], qf[4];
      __builtin_amdgcn_s_sleep(H1_DELAY);
      asm volatile(LD16(0, 1, 2) " sc1" : "=&v"(a) : "v"(o), "s"(ws) : "memory");
      BAR();
#pragma unroll
      for (int s = 0; s < 4; ++s) {
        uk[s] = *(LAS v4u*)(sm + (pu ^ (64 * s)));
        uk[4 + s] = *(LAS v4u*)(sm + (pu ^ (64 * s)) + 4096);
      }
      VMWAIT(0);
      PIN(a);
      RETRY(TAG_BAD(a), asm volatile(LD16(0, 1, 2) " sc1" WAIT0 : "+v"(a) : "v"(o), "s"(ws) : "memory"));
      asm volatile("ds_write2_b32 %0, %1, %2 offset0:0 offset1:1" ::"v"(pqw), "v"(a.x), "v"(a.z) : "memory");
      STAMP(4);
      BAR();
#pragma unroll
      for (int s = 0; s < 4; ++s) qf[s] = *(LAS v4u*)(sm + pqr + 64 * s);
      __builtin_amdgcn_sched_barrier(0);
      f4 a0 = z, a1 = z;
#pragma unroll
      for (int s = 0; s < 4; ++s) {
        a0 = mfma32(uk[s], qf[s], a0);
        a1 = mfma32(uk[4 + s], qf[s], a1);
      }
      float q[4];
#pragma unroll
      for (int e = 0; e < 4; ++e) {
        const float mine = (col & 2) ? a1[e] : a0[e], other = (col & 2) ? a0[e] : a1[e];
        q[e] = mine + dpp<QP_2301>(other);
        q[e] += dpp<ROR4>(q[e]);
      }
      st_sc1(rs, oqa, v4u{pk(q[0], q[1]), T, pk(q[2], q[3]), T});
      STAMP(5);
      BAR();
    } else {
      unsigned sdummy = 0u;
      gld4_sc1(ep, ws, vzero);
      if (w < 6) {
        VMWAIT(1);
        LAYERNORM();
        STAMP(1);
        BAR();
#define STOUCH(base, off) asm volatile("s_load_dword %0, %1, %2" : "+s"(sdummy) : "s"(base), "s"((unsigned)(off)) : "memory")
        HOLD_UNTIL(TLB_LATE);
        {
          const int i = 2 * r + (w - 4);
          STOUCH(wuv, i * 131072);
          STOUCH(wo, h * 131072 + (w - 4) * 65536);
          STOUCH(wg, TLB_FINE ? min(i, 21) * 65536 : min(i, 10) * 131072);
          STOUCH(wu, TLB_FINE ? min(i, 21) * 65536 : min(i, 10) * 131072);
          STOUCH(wd, TLB_FINE ? min(i, 21) * 65536 : min(i, 10) * 131072);
          if (TLB_FINE) STOUCH(wuv, i * 131072 + 65536);
          STOUCH(out, 0);
        }
        MARK(0);
      } else {
        __builtin_amdgcn_s_sleep(UK_DELAY);
        const unsigned vu = (unsigned)((((128 * j + g4) * NH + h) * DH) * 2);
#pragma unroll 1
        for (int k = w - 6; k < 32; k += 2) {
          if (k >> 1 == UK_FIRST) asm volatile("s_barrier" ::: "memory");
          dma16<NT_WUK>(wuk, vu + (unsigned)k * 32768u + 16u * (col ^ ((4 * k + g4) & 15)), smb + L_WUK + k * 1024);
        }
        MARK(1);
      }
      if (CK2_START && w < 6) VMWAIT(2);
      else VMWAIT(0);
      EPOCH();
      asm volatile("s_barrier\n\ts_barrier\n\ts_barrier\n\ts_waitcnt lgkmcnt(0)" : "+s"(sdummy)::"memory");
    }
    PROBE_T(T);
    HOLD_UNTIL(T_START);
    {
      const unsigned vv = (unsigned)((((256 * ca + g4) * NH + h) * DH + 8 * (col ^ ((g4 | ((w & 2) << 1)) << 1))) * 2);
      const unsigned vo = (unsigned)(((h * DH + (l >> 5)) * DM + 256 * mb) * 2) + 16u * ((l & 31) ^ (((l >> 5) | ((w & 1) << 1) | (w & 4)) << 1));
      int kv = w + 32 * mb, ko = w + 32 * ca;
#pragma unroll 1
      for (int hf = 0; hf < 2; ++hf) {
#pragma unroll 1
        for (int i = 0; i < 4; ++i, kv = (kv + 8) & 63) dma16<NT_T>(wuv, vv + (unsigned)kv * 32768u, smb + L_WUV + kv * 1024);
#pragma unroll 1
        for (int i = 0; i < 4; ++i, ko = (ko + 8) & 63) dma16<NT_T>(wo, vo + (unsigned)ko * 2048u, smb + L_WO + ko * 1024);
        if (hf == 0) asm volatile("s_barrier" ::: "memory");
      }
    }
    const int tb = w >> 2, nq = w & 3;
    unsigned o = WS_PART + (((h * 2 + ca) * 2 + tb) * 32 + 8 * nq + g4) * 512 + 16 * col;
    unsigned pw = L_W + (tb * 32 + 8 * nq + g4) * 4;
    unsigned pv = L_WUV + 256 * (8 * g4 + tq) + 16 * ((2 * w + (tp >> 1)) ^ (fl << 1)) + 8 * (tp & 1);
    unsigned po = L_WO + 512 * (8 * g4 + tq) + 16 * ((4 * w + (tp >> 1)) ^ (fl << 1)) + 8 * (tp & 1);
    unsigned pcw = L_CC + (nq * 2 + tb) * SP2 + 16 * col + 8 * g4, pcr = L_CC + (col & 7) * SP2 + 16 * g4;
    unsigned ptw = L_CT + col * SP + (16 * w + 4 * g4) * 2, ptr = L_CT + (col & 1) * SP + 16 * g4;
    unsigned oo = col >= 4 ? NOWHERE : WS_OPT + ((16 * mb + 2 * w + (col >> 1)) * 64 + ((2 * h + ca + ROT * 4 * (2 * w + (col >> 1))) & 63)) * 128 + ((col & 1) * 4 + g4) * 16;
    v4u mk;
#pragma unroll
    for (int d = 0; d < 4; ++d) mk[d] = (col >> 1) == d ? ((col & 1) ? 0xffff0000u : 0xffffu) : 0u;
    PIN(o); PIN(pw); PIN(pv); PIN(po); PIN(pcw); PIN(pcr); PIN(ptw); PIN(ptr); PIN(oo); PIN(mk);
    STAMP(11);
        if (w == 7) {
      const unsigned ol = WS_LSE + (HMAP ? ((l * 2 + (r >> 3)) * 8 + ((xcd + ROT * l) & 7)) * 128 + ((r >> 2) & 1) * 8 : (l * 8 + xcd) * 128 + (r >> 2) * 8);
      v2u la;
      UNDEF(la);
      for (unsigned spin = SPIN_CAP; spin; --spin) {
        asm volatile("global_load_dwordx2 %0, %1, %2 sc1" WAIT0 : "+v"(la) : "v"(ol), "s"(ws) : "memory");
        if (!__builtin_amdgcn_ballot_w64(la.y != T)) break;
      }
      float M = asf(la.x);
      M = fmax2(M, dpp<ROR8>(M));
      M = fmax2(M, dpp<ROR4>(M));
      M = fmax2(M, dpp<ROR2>(M));
      M = fmax2(M, dpp<ROR1>(M));
      M = sw16_max(M);
      const float e = __builtin_amdgcn_exp2f(asf(la.x) - M);
      float L = e + dpp<ROR8>(e);
      L += dpp<ROR4>(L);
      L += dpp<ROR2>(L);
      L += dpp<ROR1>(L);
      L = sw16_add(L, L);
      const float wt = e * __builtin_amdgcn_rcpf(L);
      *(LAS unsigned*)(sm + L_W + 4 * ((l & 32) | ((l + ROT * 4 * (h & 7)) & 31))) = pk(wt, wt);
      STAMP(7);
    }
    v4u a[4];
    {
      if (LSE_PIPE && w == 7) VMWAIT(2);
      else VMWAIT(0);
      STAMP(6);
      BAR();
      SWEEP_4(a, o, 0, 256, 2048, 2304, "");
    }
    v4u A0 = mk & *(LAS unsigned*)(sm + pw), A1 = mk & *(LAS unsigned*)(sm + pw + 16);
    v4u av[8], ao[8];
#pragma unroll
    for (int s = 0; s < 8; ++s) av[s] = trrd2(sm + pv + 256 * 32 * s, sm + pv + 256 * (32 * s + 4));
#pragma unroll
    for (int t = 0; t < 2; ++t)
#pragma unroll
      for (int s = 0; s < 4; ++s) ao[4 * t + s] = trrd2(sm + (po ^ (32 * t)) + 512 * 32 * s, sm + (po ^ (32 * t)) + 512 * (32 * s + 4));
    VMWAIT(0);
    asm volatile("" : "+v"(a[0]), "+v"(a[1]), "+v"(a[2]), "+v"(a[3]), "+v"(A0), "+v"(A1)::"memory");
    RETRY(HOLES4(a), TRETRY());
    STAMP(8);
    {
      f4 c0 = mfma32(A0, a[0], z), c1 = mfma32(A0, a[1], z);
      c0 = mfma32(A1, a[2], c0);
      c1 = mfma32(A1, a[3], c1);
      if (g4 < 2) {
        *(LAS v2u*)(sm + pcw) = v2u{pk(c0[0], c0[1]), pk(c0[2], c0[3])};
        *(LAS v2u*)(sm + pcw + 256) = v2u{pk(c1[0], c1[1]), pk(c1[2], c1[3])};
      }
    }
    BAR();
    {
      f4 acc = z;
      v4u bf[8];
#pragma unroll
      for (int s = 0; s < 8; ++s) bf[s] = *(LAS v4u*)(sm + pcr + 64 * s);
      __builtin_amdgcn_sched_barrier(0);
#pragma unroll
      for (int s = 0; s < 8; ++s) acc = mfma32(av[s], bf[s], acc);
#pragma unroll
      for (int e = 0; e < 4; ++e) {
        acc[e] += dpp<QP_2301>(acc[e]);
        acc[e] += dpp<ROR4>(acc[e]);
      }
      if (col < 2) *(LAS v2u*)(sm + ptw) = v2u{pk(acc[0], acc[1]), pk(acc[2], acc[3])};
    }
    BAR();
    {
      f4 c0 = z, c1 = z;
      v4u bf[4];
#pragma unroll
      for (int s = 0; s < 4; ++s) bf[s] = *(LAS v4u*)(sm + ptr + 64 * s);
      __builtin_amdgcn_sched_barrier(0);
#pragma unroll
      for (int s = 0; s < 4; ++s) {
        c0 = mfma32(ao[s], bf[s], c0);
        c1 = mfma32(ao[4 + s], bf[s], c1);
      }
      if (col & 2) c0 = c1;
      st_sc1(rs, oo, v4u{pk(c0[0], c0[1]), T, pk(c0[2], c0[3]), T});
    }
    MARK(2);
    STAMP(9);
  }
  STAMP_FLUSH();
}

#define WS_END 0
#define WS_BASE (WS_END ? ((ws_size - (size_t)WS_END * 1048576) & ~(size_t)4095) : 0)
extern "C" void kernel_launch(void* const* d_in, const int* in_sizes, int n_in, void* d_out, int out_size, void* d_ws, size_t ws_size,
                              hipStream_t stream) {
  mla_glu_decode<<<256, 512, 0, stream>>>((const u16*)d_in[0], (const u16*)d_in[1], (const u16*)d_in[2], (const u16*)d_in[3],
                                          (const u16*)d_in[4], (const u16*)d_in[5], (const u16*)d_in[7], (const u16*)d_in[8],
                                          (const u16*)d_in[9], (u16*)d_out, (char*)d_ws + WS_BASE);
}
```

```cpp
#define PROBE_CLASS 0
#define PROBE_IDX 3
#define PROBE_WAVE 0
#define PROBE_K 10
#include <hip/hip_runtime.h>
#include <stdint.h>

#define GAS __attribute__((address_space(1)))
#define LAS __attribute__((address_space(3)))
typedef unsigned short u16;
typedef unsigned v4u __attribute__((ext_vector_type(4)));
typedef unsigned v2u __attribute__((ext_vector_type(2)));
typedef float f4 __attribute__((ext_vector_type(4)));
typedef float f2 __attribute__((ext_vector_type(2)));
typedef __bf16 bf8 __attribute__((ext_vector_type(8)));
typedef __bf16 bf2 __attribute__((ext_vector_type(2)));
typedef short s4 __attribute__((ext_vector_type(4)));

#define ROT 1
namespace {
constexpr unsigned NOWHERE = 0x7ff00000u;
constexpr int NH = 32, SK = 4096, DH = 128, DM = 512, DC = 512, FF = 1376;
constexpr int NSL = FF / 16;
constexpr int WS_E = 0;
constexpr int WS_QP = 4096;
constexpr int WS_QA = WS_QP + NH * 4 * 1024;
constexpr int WS_LSE = WS_QA + 2 * NH * 2048;
constexpr int WS_GO = WS_LSE + 2 * 32 * 16 * 128;
constexpr int WS_OPT = WS_GO + 2 * 32 * 16 * 128;
constexpr int WS_OPG = WS_OPT + 32 * 64 * 128;
constexpr int OGP = 16384 + ROT * 384;
constexpr int WS_PART = (WS_OPG + 32 * OGP + 4095) / 4096 * 4096;
constexpr int WS_BYTES = WS_PART + NH * 2 * 2 * 32 * 512;
constexpr int WS_STAMP = 7 << 19;
constexpr int CP = 1056;
constexpr int XP = 1088, SP = 288, SP2 = 544;
constexpr int L_C = 0, L_Q = 128 * CP, L_P = L_Q + 16 * CP, L_X = L_P + 4096, L_MX = L_X + 2 * XP, L_RR = L_MX + 256,
              L_JUNK = L_RR + 1024, L_FL = L_JUNK + 256, L_END = L_FL + 128;
static_assert(L_END <= 163840, "LDS");
constexpr int L_GG = 0, L_GD = 32768, L_GR = 65536;
constexpr int L_WUV = 0, L_WO = 65536, L_WQ = 0, L_WUK = 32768, L_CC = 131072, L_CT = L_CC + 8 * SP2, L_W = L_CT + 2 * SP;
#define UK_DELAY 20
#define H1_DELAY 12
#define T_START 340
#define CODE_TOUCH 1
#define NT_CKV 0
#define NT_WQ 0
#define NT_WUK 0
#define NT_T 0
#define NT_G 0
#define A_START 70
#define GLU_EARLY 0
#define G_START 0
#define C_START 0
#define LSE_LATE 0
#define LSE_WAVE 3
#define DATA_DELAY 0
#define NEWRED 1
#define R_START 0
#define G_TOUCH 0
#define LSE_PIPE 0
#define PV_MIX 4
#define LSE_QK 1
#define HMAP 1
#define GQ 1
#define DOWN_FIRST 1
#define EARLY_SWEEP 1
#define GO_DELAY 0
#define OUT_TOUCH 1
#define OUT_SC1 1
#define BUMP_EARLY 1
#define G2_DELAY 0
#define UK_FIRST 6
#define UK_TOUCH 1
#define TLB_LATE 150
#define U_START 0
#define TLB_FINE 0
#define CK_TLB 1
#define CK2_START 0
#define UK_WAVES 4
#define PIECEWISE 1
#define PF_GLU 1
constexpr unsigned SPIN_CAP = 1u << 20;
constexpr int AUX_SC1 = 16;

#define STAMPS
#define STAMP_W 0
#define STAMP_ON(i) ((i) == PROBE_IDX)
#define STAMP(i)                                                                          \
  do {                                                                                    \
    if (!STAMP_ON(i)) break;                                                              \
    unsigned long long t__;                                                               \
    asm volatile("s_memrealtime %0\n\ts_waitcnt lgkmcnt(0)" : "=s"(t__)::"memory");       \
    asm volatile("v_writelane_b32 %0, %1, " #i : "+v"(tsv) : "s"((unsigned)t__));         \
  } while (0)
#define STAMP_FLUSH()                                                                      \
  do {                                                                                     \
    const int cls_ = r < 16 ? 0 : (r < 28 ? 1 : 2);                                        \
    if ((cls_ == PROBE_CLASS || PROBE_CLASS == 3) && w == PROBE_WAVE && __builtin_amdgcn_readlane(tsv, PROBE_IDX) != 0) {   \
      v4u g_;                                                                              \
      for (unsigned sp_ = 0; sp_ < SPIN_CAP; ++sp_) { asm volatile("" ::: "memory"); g_ = ld_sc1(rs, 2048); if (g_.z == Tg) break; } \
      const unsigned long long t0_ = ((unsigned long long)__builtin_amdgcn_readfirstlane(g_.y) << 32) | (unsigned)__builtin_amdgcn_readfirstlane(g_.x); \
      const long long d_ = (int)(__builtin_amdgcn_readlane(tsv, PROBE_IDX) - (unsigned)t0_);                               \
      const unsigned long long until_ = t0_ + 4000ull + PROBE_K * d_;                      \
      while (__builtin_amdgcn_s_memrealtime() < until_) __builtin_amdgcn_s_sleep(1);       \
    }                                                                                      \
  } while (0)
#define PROBE_T(T_) do { Tg = T_; if (blockIdx.x == 0 && tid == 0) st_sc1(rs, 2048, v4u{(unsigned)tstart, (unsigned)(tstart >> 32), T_, 0u}); } while (0)

__device__ __forceinline__ f4 mfma32(v4u a, v4u b, f4 c) {
  return __builtin_amdgcn_mfma_f32_16x16x32_bf16(__builtin_bit_cast(bf8, a), __builtin_bit_cast(bf8, b), c, 0, 0, 0);
}
__device__ __forceinline__ f4 mfma16(v2u a, v2u b, f4 c) {
  return __builtin_amdgcn_mfma_f32_16x16x16bf16_1k(__builtin_bit_cast(s4, a), __builtin_bit_cast(s4, b), c, 0, 0, 0);
}
__device__ __forceinline__ unsigned pk(float lo, float hi) {
  f2 x = {lo, hi};
  return __builtin_bit_cast(unsigned, __builtin_convertvector(x, bf2));
}
__device__ __forceinline__ float asf(unsigned u) { return __builtin_bit_cast(float, u); }
__device__ __forceinline__ float blo(unsigned u) { return __builtin_bit_cast(float, u << 16); }
__device__ __forceinline__ float bhi(unsigned u) { return __builtin_bit_cast(float, u & 0xffff0000u); }
template <int CTRL> __device__ __forceinline__ float dpp(float v) {
  return __builtin_bit_cast(float, __builtin_amdgcn_update_dpp(0, __builtin_bit_cast(int, v), CTRL, 0xf, 0xf, true));
}
__device__ __forceinline__ float fmax2(float a, float b) {
#pragma float_control(precise, off)
  return __builtin_fmaxf(a, b);
}
typedef unsigned short h2 __attribute__((ext_vector_type(2)));
__device__ __forceinline__ unsigned scale_dn(unsigned p, unsigned kk) {
  return __builtin_bit_cast(unsigned, __builtin_elementwise_sub_sat(__builtin_bit_cast(h2, p), __builtin_bit_cast(h2, kk)));
}
#define PIN(x) asm volatile("" : "+v"(x))
constexpr int ROR1 = 0x121, ROR2 = 0x122, ROR4 = 0x124, ROR8 = 0x128, QP_2301 = 0x4e, QP_2222 = 0xaa;
__device__ __forceinline__ float sw32_add(float a, float b) {
  auto r = __builtin_amdgcn_permlane32_swap(__builtin_bit_cast(unsigned, a), __builtin_bit_cast(unsigned, b), false, false);
  return asf(r[0]) + asf(r[1]);
}
__device__ __forceinline__ float sw16_add(float a, float b) {
  auto r = __builtin_amdgcn_permlane16_swap(__builtin_bit_cast(unsigned, a), __builtin_bit_cast(unsigned, b), false, false);
  return asf(r[0]) + asf(r[1]);
}
__device__ __forceinline__ float vmax(unsigned a, unsigned b) {
  float r;
  asm("v_max_f32 %0, %1, %2" : "=v"(r) : "v"(a), "v"(b));
  return r;
}
__device__ __forceinline__ float vmax3(float a, float b, float c) {
  float r;
  asm("v_max3_f32 %0, %1, %2, %3" : "=v"(r) : "v"(a), "v"(b), "v"(c));
  return r;
}
__device__ __forceinline__ float sw32_max(float a) {
  auto r = __builtin_amdgcn_permlane32_swap(__builtin_bit_cast(unsigned, a), __builtin_bit_cast(unsigned, a), false, false);
  return vmax(r[0], r[1]);
}
__device__ __forceinline__ float sw16_max(float a) {
  auto r = __builtin_amdgcn_permlane16_swap(__builtin_bit_cast(unsigned, a), __builtin_bit_cast(unsigned, a), false, false);
  return vmax(r[0], r[1]);
}
__device__ __forceinline__ float wave_sum(float v) {
  v += dpp<ROR8>(v);
  v += dpp<ROR4>(v);
  v += dpp<ROR2>(v);
  v += dpp<ROR1>(v);
  v = sw16_add(v, v);
  return sw32_add(v, v);
}
__device__ __forceinline__ v2u trrd(LAS char* p) {
  return __builtin_bit_cast(v2u, __builtin_amdgcn_ds_read_tr16_b64_v4i16((LAS s4*)p));
}
__device__ __forceinline__ v4u trrd2(LAS char* p0, LAS char* p1) {
  v2u a = trrd(p0), b = trrd(p1);
  return v4u{a.x, a.y, b.x, b.y};
}
#define BAR() asm volatile("s_waitcnt lgkmcnt(0)\n\ts_barrier" ::: "memory")
#define VMWAIT(k) asm volatile("s_waitcnt vmcnt(%0)" ::"n"(k) : "memory")
__device__ __forceinline__ void gld16(v4u& d, const void* base, unsigned off) {
  asm volatile("global_load_dwordx4 %0, %1, %2" : "=v"(d) : "v"(off), "s"(base) : "memory");
}
__device__ __forceinline__ void gld4_sc1(unsigned& d, const void* base, unsigned off) {
  asm volatile("global_load_dword %0, %1, %2 sc1" : "=v"(d) : "v"(off), "s"(base) : "memory");
}
template <int NT = 0> __device__ __forceinline__ void touch(const void* base, unsigned off, unsigned lds) {
  if (NT) asm volatile("s_mov_b32 m0, %2\n\ts_nop 0\n\tglobal_load_lds_dword %0, %1 nt" ::"v"(off), "s"(base), "s"(lds) : "memory");
  else asm volatile("s_mov_b32 m0, %2\n\ts_nop 0\n\tglobal_load_lds_dword %0, %1" ::"v"(off), "s"(base), "s"(lds) : "memory");
}
__device__ __forceinline__ void touch_sc1(const void* base, unsigned off, unsigned lds) {
  asm volatile("s_mov_b32 m0, %2\n\ts_nop 0\n\tglobal_load_lds_dword %0, %1 sc1" ::"v"(off), "s"(base), "s"(lds) : "memory");
}
#define LD16(i, a, o) "global_load_dwordx4 %" #i ", %" #a ", %" #o
#define SWEEP_4(q, o, o0, o1, o2, o3, W)                                                                                     \
  asm volatile(LD16(0, 4, 5) " offset:" #o0 " sc1\n\t" LD16(1, 4, 5) " offset:" #o1 " sc1\n\t" LD16(2, 4, 5) " offset:" #o2         \
               " sc1\n\t" LD16(3, 4, 5) " offset:" #o3 " sc1" W                                                               \
               : "=&v"((q)[0]), "=&v"((q)[1]), "=&v"((q)[2]), "=&v"((q)[3])                                                  \
               : "v"(o), "s"(ws)                                                                                             \
               : "memory")
#define RESWEEP_4(q, o, o0, o1, o2, o3)                                                                                      \
  asm volatile(LD16(0, 4, 5) " offset:" #o0 " sc1\n\t" LD16(1, 4, 5) " offset:" #o1 " sc1\n\t" LD16(2, 4, 5) " offset:" #o2         \
               " sc1\n\t" LD16(3, 4, 5) " offset:" #o3 " sc1\n\ts_waitcnt vmcnt(0)"                                            \
               : "+v"((q)[0]), "+v"((q)[1]), "+v"((q)[2]), "+v"((q)[3])                                                      \
               : "v"(o), "s"(ws)                                                                                             \
               : "memory")
#define RELOAD(q, o, off) asm volatile(LD16(0, 1, 2) " offset:" #off " sc1" : "+v"(q) : "v"(o), "s"(ws) : "memory")
#define PIN4(q) asm volatile("s_waitcnt vmcnt(0)" : "+v"((q)[0]), "+v"((q)[1]), "+v"((q)[2]), "+v"((q)[3])::"memory")
#define TAG_BAD(q) __builtin_amdgcn_ballot_w64((T & (q).y & (q).w) != (T | (q).y | (q).w))
#define HOLE(q) __builtin_amdgcn_ballot_w64(max(max((q).x, (q).y), max((q).z, (q).w)) == ~0u)
#define MAX4(q) max(max((q).x, (q).y), max((q).z, (q).w))
#define HOLES4(q) __builtin_amdgcn_ballot_w64(max(max(MAX4((q)[0]), MAX4((q)[1])), max(MAX4((q)[2]), MAX4((q)[3]))) == ~0u)
#define TRETRY()                          \
  if (HOLE(a[0])) RELOAD(a[0], o, 0);     \
  if (HOLE(a[1])) RELOAD(a[1], o, 256);   \
  if (HOLE(a[2])) RELOAD(a[2], o, 2048);  \
  if (HOLE(a[3])) RELOAD(a[3], o, 2304);  \
  PIN4(a)
#define SWEEP_L(W)                                                                                                          \
  asm volatile("global_load_dwordx2 %0, %3, %5 sc1\n\tglobal_load_dwordx2 %1, %4, %5 sc1\n\t"                              \
               "global_load_dwordx2 %2, %4, %5 offset:512 sc1" W                                                            \
               : "=&v"(la), "=&v"(l0), "=&v"(l1)                                                                            \
               : "v"(ola), "v"(olo), "s"(ws)                                                                                \
               : "memory")
#define SWEEP_2(p, q, oa, ob, W) \
  asm volatile(LD16(0, 2, 4) " sc1\n\t" LD16(1, 3, 4) " sc1" W : "=&v"(p), "=&v"(q) : "v"(oa), "v"(ob), "s"(ws) : "memory")
#define TAGS_BAD4(q) \
  __builtin_amdgcn_ballot_w64((T & (q)[0].y & (q)[0].w & (q)[1].y & (q)[1].w & (q)[2].y & (q)[2].w & (q)[3].y & (q)[3].w) != \
                              (T | (q)[0].y | (q)[0].w | (q)[1].y | (q)[1].w | (q)[2].y | (q)[2].w | (q)[3].y | (q)[3].w))
#define FLAG_SET(i) asm volatile("ds_write_b32 %0, %1" ::"v"(smb + L_FL + 4 * (i)), "v"(1u) : "memory")
#define FLAG_WAIT4(i)                                                                                     \
  for (unsigned spin_ = 0; spin_ < SPIN_CAP; ++spin_) {                                                   \
    v4u f_;                                                                                               \
    asm volatile("ds_read_b128 %0, %1\n\ts_waitcnt lgkmcnt(0)" : "=v"(f_) : "v"(smb + L_FL + 16 * (i)) : "memory"); \
    if (__builtin_amdgcn_readfirstlane(f_.x & f_.y & f_.z & f_.w)) break;                                 \
  }
#define HOLD_UNTIL(ticks)              \
  asm volatile("" : "+s"(tstart));     \
  while ((unsigned)__builtin_amdgcn_s_memrealtime() - (unsigned)tstart < (ticks)) __builtin_amdgcn_s_sleep(1)
#define MARK(i) asm volatile(".Lmla_e" #i ":")
#define RETRY(bad, again)                                   \
  if (bad) {                                                \
    for (unsigned spin = SPIN_CAP; spin; --spin) {          \
      again;                                                \
      if (!(bad)) break;                                    \
    }                                                       \
  }
#define UNDEF(x) asm volatile("" : "=v"(x))
#define EPOCH() asm volatile("v_add_u32 %0, 1, %1" : "=v"(T) : "v"(ep) : "memory")
#define WAIT0 "\n\ts_waitcnt vmcnt(0)"
template <int NT = 0> __device__ __forceinline__ void dma16(const void* base, unsigned off, unsigned lds) {
  if (NT) asm volatile("s_mov_b32 m0, %2\n\ts_nop 0\n\tglobal_load_lds_dwordx4 %0, %1 nt" ::"v"(off), "s"(base), "s"(lds) : "memory");
  else asm volatile("s_mov_b32 m0, %2\n\ts_nop 0\n\tglobal_load_lds_dwordx4 %0, %1" ::"v"(off), "s"(base), "s"(lds) : "memory");
}
__device__ __forceinline__ v4u ld_sc1(__amdgpu_buffer_rsrc_t rs, int off) {
  return __builtin_amdgcn_raw_buffer_load_b128(rs, off, 0, AUX_SC1);
}
__device__ __forceinline__ v2u ld8_sc1(__amdgpu_buffer_rsrc_t rs, int off) {
  return __builtin_amdgcn_raw_buffer_load_b64(rs, off, 0, AUX_SC1);
}
__device__ __forceinline__ void st_sc1(__amdgpu_buffer_rsrc_t rs, int off, v4u v) {
  __builtin_amdgcn_raw_buffer_store_b128(v, rs, off, 0, AUX_SC1);
}
__device__ __forceinline__ void ln_stats(v4u r, float& mean, float& rstd) {
  float e[8] = {blo(r.x), bhi(r.x), blo(r.y), bhi(r.y), blo(r.z), bhi(r.z), blo(r.w), bhi(r.w)};
  float s = ((e[0] + e[1]) + (e[2] + e[3])) + ((e[4] + e[5]) + (e[6] + e[7]));
  mean = wave_sum(s) * (1.f / DM);
  float q = 0.f;
#pragma unroll
  for (int i = 0; i < 8; ++i) {
    float d = e[i] - mean;
    q = fmaf(d, d, q);
  }
  rstd = __builtin_amdgcn_rsqf(wave_sum(q) * (1.f / DM) + 1e-5f);
}
}

__global__ void __launch_bounds__(512, 2)
mla_glu_decode(const u16* __restrict__ x, const u16* __restrict__ ckv, const u16* __restrict__ wq, const u16* __restrict__ wuk,
               const u16* __restrict__ wuv, const u16* __restrict__ wo, const u16* __restrict__ wg, const u16* __restrict__ wu,
               const u16* __restrict__ wd, u16* __restrict__ out, char* __restrict__ ws) {
  unsigned long long tstart = __builtin_amdgcn_s_memrealtime();
  __builtin_amdgcn_sched_barrier(0);
  __shared__ __attribute__((aligned(16))) char smem_[L_END];
  LAS char* sm = (LAS char*)smem_;
  const unsigned smb = (unsigned)(size_t)sm;
  const int tid = threadIdx.x, l = tid & 63, w = __builtin_amdgcn_readfirstlane(tid >> 6);
  const int col = l & 15, g4 = l >> 4, tq = (l & 15) >> 2, tp = l & 3;
  const int xcd = blockIdx.x & 7, r = blockIdx.x >> 3;
  const __amdgpu_buffer_rsrc_t rs = __builtin_amdgcn_make_buffer_rsrc(ws, 0, WS_BYTES, 0x00020000);
  int bw = (blockIdx.x >> 7) * w;
  asm volatile("" : "+s"(bw));
  if (bw >= 5) {
    const char* pc0;
    unsigned len;
    asm volatile("s_getpc_b64 %0\n.Lmla_pc:\n\ts_mov_b32 %1, .Lmla_e0-.Lmla_pc\n\ts_max_i32 %1, %1, .Lmla_e1-.Lmla_pc\n\t"
                 "s_max_i32 %1, %1, .Lmla_e2-.Lmla_pc\n\ts_max_i32 %1, %1, .Lmla_e3-.Lmla_pc\n\ts_max_i32 %1, %1, .Lmla_e4-.Lmla_pc\n\t"
                 "s_max_i32 %1, %1, .Lmla_e5-.Lmla_pc"
                 : "=s"(pc0), "=s"(len)::"scc");
    touch(pc0, min((unsigned)(64 * (bw - 5) + l) * 128u, len), smb + L_JUNK);
  }
  __builtin_amdgcn_sched_barrier(0);
  const int h = HMAP ? xcd + 8 * ((r >> 2) & 3) : 4 * xcd + ((r >> 2) & 3), j = r & 3;
  unsigned vq = (unsigned)(((128 * j + g4 + 4 * w) * 4096 + h * DH + 8 * (col ^ ((g4 | ((w & 2) << 1)) << 1))) * 2), vx = (w & 1) * 1024 + l * 16;
  unsigned vzero = 0u;
  int lq = smb + L_WQ + w * 1024, lx = smb + L_X + (w & 1) * XP;
  asm volatile("" : "+v"(vq), "+v"(vx), "+v"(vzero), "+s"(lq), "+s"(lx));
  asm volatile("" ::"s"(x), "s"(ckv), "s"(wq), "s"(wuk), "s"(wuv), "s"(wo), "s"(wg), "s"(wu), "s"(wd), "s"(out), "s"(ws));
  unsigned tsv = 0u;
  STAMP(0);
  unsigned Tg = 0;

  unsigned ep, T;
  LAS char* const px = sm + L_X + (col & 1) * XP + 16 * g4;
  if ((w >> 1) == 2) dma16(x, vx, lx);
  const f4 z = {0.f, 0.f, 0.f, 0.f};
#define LAYERNORM()                                                                                                              \
  {                                                                                                                              \
    const v4u xr = *(LAS v4u*)(sm + L_X + (w & 1) * XP + l * 16);                                                                \
    float e[8] = {blo(xr.x), bhi(xr.x), blo(xr.y), bhi(xr.y), blo(xr.z), bhi(xr.z), blo(xr.w), bhi(xr.w)};                       \
    const float mean = wave_sum(((e[0] + e[1]) + (e[2] + e[3])) + ((e[4] + e[5]) + (e[6] + e[7]))) * (1.f / DM);                 \
    float q = 0.f;                                                                                                               \
    _Pragma("unroll") for (int i = 0; i < 8; ++i) {                                                                              \
      e[i] -= mean;                                                                                                              \
      q = fmaf(e[i], e[i], q);                                                                                                   \
    }                                                                                                                            \
    const float rstd = __builtin_amdgcn_rsqf(wave_sum(q) * (1.f / DM) + 1e-5f);                                                  \
    *(LAS v4u*)(sm + L_X + (w & 1) * XP + l * 16) =                                                                              \
        v4u{pk(e[0] * rstd, e[1] * rstd), pk(e[2] * rstd, e[3] * rstd), pk(e[4] * rstd, e[5] * rstd), pk(e[6] * rstd, e[7] * rstd)}; \
  }

  if (__builtin_expect(r >= 16, 0)) {
    const int a = r - 16, b = xcd >> 2, n = 8 * (xcd & 3) + (a >> 1), g = a & 1;
    const int sl = 12 * xcd + a, rd = (4 * xcd + a) & 31;
    const bool glu = a < 12 && sl < NSL;
    const int v = w & 3;
    unsigned op = WS_PART + ((((16 * g + (col & 7)) * 2 + (v >> 1)) * 2 + b) * 32 + ((n + ROT * 4 * (col & 7)) & 31)) * 512 + 256 * (v & 1) +
                  (col >> 3) * 64 + (g4 & 1) * 32 + (g4 >> 1) * 16;
    gld4_sc1(ep, ws, vzero);
    unsigned o = WS_QA + (b * NH + 16 * g) * 2048 + w * 4096 + l * 16, pqs = smb + L_Q + 2 * w * CP + l * 8;
    PIN(o); PIN(pqs);
    {
    {
      const unsigned vc = (unsigned)(((b * SK + 128 * n) * DC + 8 * l) * 2);
      STAMP(10);
      if (w == 4) touch(ckv, (unsigned)((b * SK + 128 * n) * DC * 2), smb + L_JUNK);
      HOLD_UNTIL(U_START);
      if (w < UK_WAVES) touch(wuk, (unsigned)((32 * a + 8 * w + (l >> 3)) * 8192 + (xcd + 8 * ((l >> 1) & 3)) * 256 + (l & 1) * 128), smb + L_JUNK);
      HOLD_UNTIL(C_START);
    if (w >= 4) {
#pragma unroll
      for (int q = 0; q < 4; ++q) st_sc1(rs, op + 128 * (q >> 1) + (q & 1) * 8 * 65536, v4u{~0u, ~0u, ~0u, ~0u});
      if (w == 7 && l < 32) *(LAS unsigned*)(sm + L_FL + 4 * l) = 0u;
    }
      HOLD_UNTIL(A_START);
#pragma unroll 1
      for (int q = 0; q < 4; ++q) {
        int row = w + ((64 * g + 32 * q) & 127);
        asm volatile("s_barrier" ::: "memory");
#pragma unroll 1
        for (int i = 0; i < 4; ++i, row += 8) dma16<NT_CKV>(ckv, vc + row * 1024u, smb + L_C + row * CP);
      }
    }
#define CHALF()                               \
  asm volatile("s_barrier" ::: "memory");     \
  SWEEP_4(qg, o, 0, 1024, 2048, 3072, "");    \
  VMWAIT(12);                                 \
  asm volatile("s_barrier" ::: "memory")
#define CLAND() \
  VMWAIT(4);    \
  EPOCH();      \
  PROBE_T(T);   \
  BAR();        \
  STAMP(1)
#define QRETRY()                              \
  if (TAG_BAD(qg[0])) RELOAD(qg[0], o, 0);    \
  if (TAG_BAD(qg[1])) RELOAD(qg[1], o, 1024); \
  if (TAG_BAD(qg[2])) RELOAD(qg[2], o, 2048); \
  if (TAG_BAD(qg[3])) RELOAD(qg[3], o, 3072); \
  PIN4(qg)
#define QGATHER()                                                                                                           \
  VMWAIT(0);                                                                                                      \
  asm volatile("" : "+v"(qg[0]), "+v"(qg[1]), "+v"(qg[2]), "+v"(qg[3])::"memory");                                          \
  RETRY(TAGS_BAD4(qg), QRETRY());                                                                                           \
  STAMP(3);                                                                                                                 \
  asm volatile("ds_write2_b32 %0, %2, %3 offset0:0 offset1:1\n\tds_write2_b32 %0, %4, %5 offset0:128 offset1:129\n\t"        \
               "ds_write2_b32 %1, %6, %7 offset0:0 offset1:1\n\tds_write2_b32 %1, %8, %9 offset0:128 offset1:129" ::"v"(pqs), \
               "v"(pqs + CP), "v"(qg[0].x), "v"(qg[0].z), "v"(qg[1].x), "v"(qg[1].z), "v"(qg[2].x), "v"(qg[2].z), "v"(qg[3].x), \
               "v"(qg[3].z)                                                                                                 \
               : "memory")
    if (__builtin_expect(w < 4, 1)) {
      v4u fr[32], qg[4];
      unsigned pc = L_C + (32 * w + col) * CP + 16 * g4;
      PIN(pc);
      CHALF();
      const bool early = (w >> 1) == g;
      if (!early) {
        CLAND();
      }
#pragma unroll
      for (int kt = 0; kt < 2; ++kt)
#pragma unroll
        for (int s = 0; s < 16; ++s) fr[16 * kt + s] = *(LAS v4u*)(sm + pc + 16 * kt * CP + 64 * s);
      if (early) {
        CLAND();
      }
      const int u = w ^ (2 * g);
      unsigned p = L_Q + col * CP + 16 * g4, pp = L_P + u * 1024 + l * 16, pm = L_MX + col * 16 + u * 4;
      unsigned ol = WS_LSE + (HMAP ? (((b * 32 + n) * 2 + g) * 8 + ((col + ROT * n) & 7)) * 128 + (col >> 3) * 8 : ((b * 32 + n) * 8 + 4 * g + (col >> 2)) * 128 + (col & 3) * 8);
      if (g4) ol = NOWHERE;
      PIN(p); PIN(pp); PIN(pm); PIN(ol);
      STAMP(2);
      QGATHER();
      BAR();
      STAMP(4);
      f4 s0 = z, s1 = z;
      v4u qf[16];
#pragma unroll
      for (int s = 0; s < 6; ++s) qf[s] = *(LAS v4u*)(sm + p + 64 * s);
      __builtin_amdgcn_sched_barrier(0);
#pragma unroll
      for (int s = 0; s < 16; ++s) {
        if (s + 6 < 16) qf[s + 6] = *(LAS v4u*)(sm + p + 64 * (s + 6));
        s0 = mfma32(fr[s], qf[s], s0);
        s1 = mfma32(fr[16 + s], qf[s], s1);
        __builtin_amdgcn_sched_barrier(0);
      }
      float mx = fmax2(fmax2(fmax2(s0[0], s0[1]), fmax2(s0[2], s0[3])), fmax2(fmax2(s1[0], s1[1]), fmax2(s1[2], s1[3])));
      mx = sw32_max(sw16_max(mx));
      constexpr float SC = 0.08838834764831845f * 1.4426950408889634f;
      const float mi = __builtin_ceilf(mx * SC);
#pragma unroll
      for (int e = 0; e < 4; ++e) {
        s0[e] = __builtin_amdgcn_exp2f(fmaf(s0[e], SC, -mi));
        s1[e] = __builtin_amdgcn_exp2f(fmaf(s1[e], SC, -mi));
      }
      *(LAS float*)(sm + pm) = mi;
      *(LAS v4u*)(sm + pp) = v4u{pk(s0[0], s0[1]), pk(s0[2], s0[3]), pk(s1[0], s1[1]), pk(s1[2], s1[3])};
      BAR();
      STAMP(5);
      if (w == LSE_QK - 1) {
        const f4 m4 = *(LAS f4*)(sm + pm - 4 * u);
        v4u pf[4];
#pragma unroll
        for (int k = 0; k < 4; ++k) pf[k] = *(LAS v4u*)(sm + pp + (k - u) * 1024);
        __builtin_amdgcn_sched_barrier(0);
        const float M = vmax3(vmax3(m4[0], m4[1], m4[2]), m4[3], m4[3]);
        const v4u one = {0x3f803f80u, 0x3f803f80u, 0x3f803f80u, 0x3f803f80u};
        f4 ls = z;
#pragma unroll
        for (int u = 0; u < 4; ++u) {
          const unsigned kk = min((unsigned)(M - m4[u]), 255u) * 0x00800080u;
#pragma unroll
          for (int e = 0; e < 4; ++e) pf[u][e] = scale_dn(pf[u][e], kk);
          ls = mfma32(one, pf[u], ls);
        }
        __builtin_amdgcn_raw_buffer_store_b64(v2u{__builtin_bit_cast(unsigned, M + __builtin_amdgcn_logf(ls[0])), T}, rs, ol, 0, AUX_SC1);
      }
    } else {
      v4u fr[32], qg[4];
      unsigned p0 = L_C + (64 * g + 4 * g4 + tq) * CP + 256 * v + 8 * tp, p1 = L_C + (64 * (g ^ 1) + 4 * g4 + tq) * CP + 256 * v + 8 * tp;
      PIN(p0); PIN(p1);
      CHALF();
#pragma unroll
      for (int ct = 0; ct < 8; ++ct)
#pragma unroll
        for (int u = 0; u < 2; ++u) fr[4 * ct + u] = trrd2(sm + p0 + 32 * u * CP + 32 * ct, sm + p0 + (32 * u + 16) * CP + 32 * ct);
      CLAND();
#pragma unroll
      for (int ct = 0; ct < 8; ++ct)
#pragma unroll
        for (int u = 0; u < 2; ++u) fr[4 * ct + 2 + u] = trrd2(sm + p1 + 32 * u * CP + 32 * ct, sm + p1 + (32 * u + 16) * CP + 32 * ct);
      unsigned ol = WS_LSE + (HMAP ? (((b * 32 + n) * 2 + g) * 8 + ((col + ROT * n) & 7)) * 128 + (col >> 3) * 8 : ((b * 32 + n) * 8 + 4 * g + (col >> 2)) * 128 + (col & 3) * 8), pp = L_P + l * 16, pm = L_MX + col * 16;
      v4u one = {0x3f803f80u, 0x3f803f80u, 0x3f803f80u, 0x3f803f80u};
      unsigned op2 = op + 8 * 65536;
      PIN(ol); PIN(pp); PIN(pm); PIN(one); PIN(op); PIN(op2);
      STAMP(2);
      QGATHER();
      BAR();
      STAMP(4);
      if (glu) {
        const int k = 8 * (v >> 1) + 2 * (a & 3);
        const unsigned og = (unsigned)(((32 * k + (l >> 1)) * FF + 16 * sl + 8 * (l & 1)) * 2);
        dma16((v & 1) ? wu : wg, og, smb + L_GG + (v & 1) * 16384 + k * 1024);
        dma16((v & 1) ? wu : wg, og + 32 * FF * 2, smb + L_GG + (v & 1) * 16384 + k * 1024 + 1024);
      } else if (v < G_TOUCH) {
        const int ln = 128 * a + 64 * (v >> 1) + l;
        touch((v & 1) ? wu : wg, (unsigned)((ln >> 2) * (FF * 2)) + (unsigned)min(384 * xcd + 128 * (ln & 3), FF * 2 - 4), smb + L_JUNK);
      }
      if (glu && w < 6) LAYERNORM();
      BAR();
      STAMP(5);
      const f4 m4 = *(LAS f4*)(sm + pm);
      v4u pf[4];
#pragma unroll
      for (int u = 0; u < 4; ++u) pf[u] = *(LAS v4u*)(sm + pp + u * 1024);
      __builtin_amdgcn_sched_barrier(0);
      const float M = vmax3(vmax3(m4[0], m4[1], m4[2]), m4[3], m4[3]);
      f4 ls = z, c[4] = {z, z, z, z};
#pragma unroll
      for (int u = 0; u < 4; ++u) {
        const unsigned kk = min((unsigned)(M - m4[u]), 255u) * 0x00800080u;
#pragma unroll
        for (int e = 0; e < 4; ++e) pf[u][e] = scale_dn(pf[u][e], kk);
        ls = mfma32(one, pf[u], ls);
        if (u < 3) {
#pragma unroll
          for (int k = 0; k < 4; ++k) c[k] = mfma32(fr[4 * k + u], pf[u], c[k]);
        }
      }
      __builtin_amdgcn_sched_barrier(0);
      c[0] = mfma32(fr[3], pf[3], c[0]);
      c[1] = mfma32(fr[7], pf[3], c[1]);
      __builtin_amdgcn_sched_barrier(0);
      float il = __builtin_amdgcn_rcpf(ls[0]);
      unsigned lse = __builtin_bit_cast(unsigned, M + __builtin_amdgcn_logf(ls[0]));
      c[2] = mfma32(fr[11], pf[3], c[2]);
      c[3] = mfma32(fr[15], pf[3], c[3]);
#define PV_OUT(c, q)                                                                                          \
  {                                                                                                           \
    v4u R[2];                                                                                                 \
    _Pragma("unroll") for (int k = 0; k < 2; ++k) {                                                           \
      const f4 c0 = c[2 * k] * il, c1 = c[2 * k + 1] * il;                                                    \
      const unsigned x0 = min(pk(c0[0], c0[1]), 0xfffffffeu), x1 = min(pk(c0[2], c0[3]), 0xfffffffeu);        \
      const unsigned y0 = min(pk(c1[0], c1[1]), 0xfffffffeu), y1 = min(pk(c1[2], c1[3]), 0xfffffffeu);        \
      auto r0 = __builtin_amdgcn_permlane16_swap(x0, y0, false, false);                                       \
      auto r1 = __builtin_amdgcn_permlane16_swap(x1, y1, false, false);                                       \
      R[k] = v4u{r0[0], r1[0], r0[1], r1[1]};                                                                 \
    }                                                                                                         \
    v4u A, B;                                                                                                 \
    _Pragma("unroll") for (int e = 0; e < 4; ++e) {                                                           \
      A[e] = (unsigned)__builtin_amdgcn_update_dpp((int)R[0][e], (int)R[1][e], ROR8, 0xf, 0xc, false);        \
      B[e] = (unsigned)__builtin_amdgcn_update_dpp((int)R[1][e], (int)R[0][e], ROR8, 0xf, 0x3, false);        \
    }                                                                                                         \
    st_sc1(rs, op + 128 * q, A);                                                                              \
    st_sc1(rs, op2 + 128 * q, B);                                                                             \
  }
      __builtin_amdgcn_sched_barrier(0);
      f4 d[4] = {z, z, z, z};
#pragma unroll
      for (int u = 0; u < 4; ++u)
#pragma unroll
        for (int k = 0; k < 4; ++k) d[k] = mfma32(fr[16 + 4 * k + u], pf[u], d[k]);
      PV_OUT(c, 0);
#pragma unroll
      for (int i = 0; i < 16; ++i) {
        __builtin_amdgcn_sched_group_barrier(0x8, 1, 0);
        __builtin_amdgcn_sched_group_barrier(0x2, PV_MIX, 0);
      }
      __builtin_amdgcn_sched_barrier(0);
      PV_OUT(d, 1);
      FLAG_SET(v);
      if (glu) {
        VMWAIT(4);
        FLAG_SET(12 + v);
      }
      MARK(3);
      STAMP(6);
    }
    }
      if (glu && w < 4) {
        v4u af[8], xf[8];
        unsigned oog = WS_OPG + (8 * w + (col >> 1)) * OGP + sl * 128 + ((col & 1) * 4 + g4) * 16;
        PIN(oog);
        FLAG_WAIT4(0);
#pragma unroll 1
        for (int i = 2; i < 8; ++i) {
          const int k = 8 * (w >> 1) + ((2 * a + i) & 7);
          dma16((w & 1) ? wu : wg, (unsigned)(((32 * k + (l >> 1)) * FF + 16 * sl + 8 * (l & 1)) * 2), smb + L_GG + (w & 1) * 16384 + k * 1024);
        }
#pragma unroll 1
        for (int i = 0; i < 4; ++i) dma16(wd, (unsigned)(((16 * sl + 4 * w + i) * DM + 8 * l) * 2), smb + L_GD + (4 * w + i) * CP);
        unsigned pg = L_GG + (w & 1) * 16384 + 32 * (256 * (w >> 1) + 8 * g4 + tq) + 8 * tp;
        unsigned pd = L_GD + (4 * g4 + tq) * CP + 256 * w + 8 * tp, pgw = L_GR + ((w & 1) * 2 + (w >> 1)) * 1024 + l * 16;
        PIN(pg); PIN(pd); PIN(pgw);
#pragma unroll
        for (int s = 0; s < 8; ++s) xf[s] = *(LAS v4u*)(px + 512 * (w >> 1) + 64 * s);
        FLAG_WAIT4(3);
        VMWAIT(4);
        STAMP(7);
        f4 acc = z;
#pragma unroll
        for (int s = 0; s < 8; ++s) af[s] = trrd2(sm + pg + 32 * 32 * s, sm + pg + 32 * (32 * s + 4));
        __builtin_amdgcn_sched_barrier(0);
#pragma unroll
        for (int s = 0; s < 8; ++s) acc = mfma32(af[s], xf[s], acc);
        *(LAS f4*)(sm + pgw) = acc;
        VMWAIT(0);
        FLAG_SET(8 + w);
        FLAG_WAIT4(2);
        v2u dn[8];
        LAS char* pr = sm + L_GR + l * 16;
        const f4 gx = *(LAS f4*)(pr) + *(LAS f4*)(pr + 1024), ux = *(LAS f4*)(pr + 2048) + *(LAS f4*)(pr + 3072);
#pragma unroll
        for (int t = 0; t < 8; ++t) dn[t] = trrd(sm + pd + 32 * t);
        float hh[4];
#pragma unroll
        for (int e = 0; e < 4; ++e) hh[e] = ux[e] * __builtin_amdgcn_rcpf(1.f + __builtin_amdgcn_exp2f(-1.4426950408889634f * gx[e]));
        const v2u hb = {pk(hh[0], hh[1]), pk(hh[2], hh[3])};
        f4 d = z;
#pragma unroll
        for (int t = 0; t < 8; ++t) d = mfma16(dn[t], (col >> 1) == t ? hb : v2u{0u, 0u}, d);
        v4u dv = {pk(d[0], d[1]), T, pk(d[2], d[3]), T};
        PIN(dv);
        st_sc1(rs, oog, dv);
        MARK(4);
        STAMP(9);
      }
    if (a >= 12) {
      const unsigned m = (col & 1) ? 0x3f800000u : 0x3f80u;
      v4u sel = {col < 2 ? m : 0u, (col >> 1) == 1 ? m : 0u, col < 2 ? m : 0u, (col >> 1) == 1 ? m : 0u};
      PIN(sel);
      if (w == 0) {
        unsigned ot = WS_OPT + rd * 8192 + 4096 + l * 16, oy = (l >> 2) * 1024 + rd * 32 + (l & 3) * 8, pr = smb + L_RR + (l & 7) * 16;
        PIN(ot); PIN(oy); PIN(pr);
        v4u p[8];
        touch(out, (unsigned)((l & 1) * 1024 + rd * 32), smb + L_JUNK);
        HOLD_UNTIL(R_START);
        SWEEP_4(p, ot, -4096, -3072, -2048, -1024, "");
        SWEEP_4(p + 4, ot, 0, 1024, 2048, 3072, "");
        PIN4(p);
        PIN4(p + 4);
#define RRETRY()                                \
  if (TAG_BAD(p[0])) RELOAD(p[0], ot, -4096);   \
  if (TAG_BAD(p[1])) RELOAD(p[1], ot, -3072);   \
  if (TAG_BAD(p[2])) RELOAD(p[2], ot, -2048);   \
  if (TAG_BAD(p[3])) RELOAD(p[3], ot, -1024);   \
  if (TAG_BAD(p[4])) RELOAD(p[4], ot, 0);       \
  if (TAG_BAD(p[5])) RELOAD(p[5], ot, 1024);    \
  if (TAG_BAD(p[6])) RELOAD(p[6], ot, 2048);    \
  if (TAG_BAD(p[7])) RELOAD(p[7], ot, 3072);    \
  PIN4(p);                                      \
  PIN4(p + 4)
        RETRY(TAGS_BAD4(p) | TAGS_BAD4(p + 4), RRETRY());
        STAMP(8);
        f4 acc = z;
#pragma unroll
        for (int i = 0; i < 4; ++i) acc = mfma32(sel, v4u{p[2 * i].x, p[2 * i].z, p[2 * i + 1].x, p[2 * i + 1].z}, acc);
        f4 rest[6];
        for (unsigned spin = 0; spin < SPIN_CAP; ++spin) {
          v4u f0;
          v2u f1;
          asm volatile("ds_read_b128 %0, %8\n\tds_read_b64 %1, %8 offset:16\n\tds_read_b128 %2, %9\n\tds_read_b128 %3, %9 offset:128\n\t"
                       "ds_read_b128 %4, %9 offset:256\n\tds_read_b128 %5, %9 offset:384\n\tds_read_b128 %6, %9 offset:512\n\t"
                       "ds_read_b128 %7, %9 offset:640\n\ts_waitcnt lgkmcnt(0)"
                       : "=&v"(f0), "=&v"(f1), "=&v"(rest[0]), "=&v"(rest[1]), "=&v"(rest[2]), "=&v"(rest[3]), "=&v"(rest[4]), "=&v"(rest[5])
                       : "v"(smb + L_FL + 64), "v"(pr)
                       : "memory");
          if (__builtin_amdgcn_readfirstlane(f0.x & f0.y & f0.z & f0.w & f1.x & f1.y)) break;
        }
        const f4 gl = ((rest[0] + rest[1]) + (rest[2] + rest[3])) + (rest[4] + rest[5]);
#pragma unroll
        for (int e = 0; e < 4; ++e) acc[e] = (acc[e] + dpp<ROR8>(acc[e])) + gl[e];
        if (l < 8) asm volatile("global_store_dwordx2 %0, %1, %2 sc1" ::"v"(oy), "v"(v2u{pk(acc[0], acc[1]), pk(acc[2], acc[3])}), "s"(out) : "memory");
      } else if (w < 7) {
        const bool last = w == 6;
        unsigned og = WS_OPG + rd * OGP + (w - 1) * 2048 + (last ? min(l, 47) : l) * 16, og2 = last ? og : og + 1024;
        const v2u xr = *(const v2u*)((const char*)x + ((l >> 2) & 1) * 1024 + rd * 32 + (l & 3) * 8);
        unsigned m0 = (last && l >= 48) ? 0u : ~0u, m1 = last ? 0u : ~0u;
        f4 xv = {blo(xr.x), bhi(xr.x), blo(xr.y), bhi(xr.y)};
        if (!last) xv = z;
        PIN(og); PIN(og2); PIN(m0); PIN(m1); PIN(xv);
        v4u p0, p1;
        HOLD_UNTIL(R_START);
        UNDEF(p0); UNDEF(p1);
        for (unsigned spin = SPIN_CAP; spin; --spin) {
          SWEEP_2(p0, p1, og, og2, WAIT0);
          if (!(TAG_BAD(p0) | TAG_BAD(p1))) break;
        }
        STAMP(8);
        f4 acc = mfma32(sel, v4u{p0.x & m0, p0.z & m0, p1.x & m1, p1.z & m1}, z);
#pragma unroll
        for (int e = 0; e < 4; ++e) acc[e] = (acc[e] + dpp<ROR8>(acc[e])) + xv[e];
        if (l < 8) *(LAS f4*)(sm + L_RR + (w - 1) * 128 + l * 16) = acc;
        FLAG_SET(15 + w);
      }
      else if (rd == 0) {
        const unsigned ol0 = WS_LSE + (l * 16 + ((ROT * l) & 7)) * 128 + 4;
        unsigned t0, t1;
        for (unsigned spin = 0;; ++spin) {
          __builtin_amdgcn_s_sleep(32);
          asm volatile("global_load_dword %0, %2, %3 sc1\n\tglobal_load_dword %1, %2, %3 offset:1024 sc1" WAIT0
                       : "=&v"(t0), "=&v"(t1)
                       : "v"(ol0), "s"(ws)
                       : "memory");
          if (!__builtin_amdgcn_ballot_w64((t0 & t1 & T) != (t0 | t1 | T)) || spin > SPIN_CAP) break;
        }
        if (l == 0) __builtin_amdgcn_raw_buffer_store_b32(T, rs, WS_E, 0, AUX_SC1);
      }
      MARK(5);
      STAMP(9);
    }
    STAMP_FLUSH();
    __builtin_amdgcn_endpgm();
  }
  {
    const int ca = j >> 1, mb = j & 1;
    const int fl = tq | ((g4 & 1) << 2);
    if (__builtin_expect(w < 4, 1)) {
#pragma unroll 1
      for (int i = 0; i < 8; ++i, vq += 131072u, lq += 4096) dma16<NT_WQ>(wq, vq, lq);
      gld4_sc1(ep, ws, vzero);
      unsigned p = L_WQ + 256 * (8 * g4 + tq) + 16 * ((4 * w + (tp >> 1)) ^ (fl << 1)) + 8 * (tp & 1), p2 = p ^ 32;
      unsigned oqp = col < 4 ? WS_QP + ((h * 4 + j) * 8 + 2 * w) * 128 + col * 64 + g4 * 16 : NOWHERE;
      unsigned pxj = L_X + (col & 1) * XP + 16 * g4 + 256 * j;
      PIN(p); PIN(p2); PIN(oqp); PIN(pxj);
      unsigned o = WS_QP + (h * 4 + w) * 1024 + l * 16;
      unsigned pqw = smb + L_CC + (2 * w + ((l >> 2) & 1)) * SP + (l >> 3) * 32 + (l & 3) * 8, pqr = L_CC + (col & 7) * SP + 16 * g4;
      unsigned oqa = col < 4 ? WS_QA + ((col & 1) * NH + h) * 2048 + (64 * j + 16 * w + 8 * (col >> 1) + 2 * g4) * 8 : NOWHERE;
      unsigned pu = L_WUK + 256 * (32 * w + col) + 16 * (g4 ^ col);
      PIN(o); PIN(oqa); PIN(pu); PIN(pqw); PIN(pqr);
      VMWAIT(0);
      EPOCH();
      STAMP(1);
      BAR();
      STAMP(2);
      {
        v4u af[8], xf[4];
#pragma unroll
        for (int s = 0; s < 4; ++s) {
          xf[s] = *(LAS v4u*)(sm + pxj + 64 * s);
          af[s] = trrd2(sm + p + 256 * 32 * s, sm + p + 256 * (32 * s + 4));
          af[4 + s] = trrd2(sm + p2 + 256 * 32 * s, sm + p2 + 256 * (32 * s + 4));
        }
        __builtin_amdgcn_sched_barrier(0);
        f4 c0 = z, c1 = z;
#pragma unroll
        for (int s = 0; s < 4; ++s) {
          c0 = mfma32(af[s], xf[s], c0);
          c1 = mfma32(af[4 + s], xf[s], c1);
        }
        if (col & 2) c0 = c1;
        st_sc1(rs, oqp, v4u{pk(c0[0], c0[1]), T, pk(c0[2], c0[3]), T});
      }
      STAMP(3);
      v4u a, uk[8], qf[4];
      __builtin_amdgcn_s_sleep(H1_DELAY);
      asm volatile(LD16(0, 1, 2) " sc1" : "=&v"(a) : "v"(o), "s"(ws) : "memory");
      BAR();
#pragma unroll
      for (int s = 0; s < 4; ++s) {
        uk[s] = *(LAS v4u*)(sm + (pu ^ (64 * s)));
        uk[4 + s] = *(LAS v4u*)(sm + (pu ^ (64 * s)) + 4096);
      }
      VMWAIT(0);
      PIN(a);
      RETRY(TAG_BAD(a), asm volatile(LD16(0, 1, 2) " sc1" WAIT0 : "+v"(a) : "v"(o), "s"(ws) : "memory"));
      asm volatile("ds_write2_b32 %0, %1, %2 offset0:0 offset1:1" ::"v"(pqw), "v"(a.x), "v"(a.z) : "memory");
      STAMP(4);
      BAR();
#pragma unroll
      for (int s = 0; s < 4; ++s) qf[s] = *(LAS v4u*)(sm + pqr + 64 * s);
      __builtin_amdgcn_sched_barrier(0);
      f4 a0 = z, a1 = z;
#pragma unroll
      for (int s = 0; s < 4; ++s) {
        a0 = mfma32(uk[s], qf[s], a0);
        a1 = mfma32(uk[4 + s], qf[s], a1);
      }
      float q[4];
#pragma unroll
      for (int e = 0; e < 4; ++e) {
        const float mine = (col & 2) ? a1[e] : a0[e], other = (col & 2) ? a0[e] : a1[e];
        q[e] = mine + dpp<QP_2301>(other);
        q[e] += dpp<ROR4>(q[e]);
      }
      st_sc1(rs, oqa, v4u{pk(q[0], q[1]), T, pk(q[2], q[3]), T});
      STAMP(5);
      BAR();
    } else {
      unsigned sdummy = 0u;
      gld4_sc1(ep, ws, vzero);
      if (w < 6) {
        VMWAIT(1);
        LAYERNORM();
        STAMP(1);
        BAR();
#define STOUCH(base, off) asm volatile("s_load_dword %0, %1, %2" : "+s"(sdummy) : "s"(base), "s"((unsigned)(off)) : "memory")
        HOLD_UNTIL(TLB_LATE);
        {
          const int i = 2 * r + (w - 4);
          STOUCH(wuv, i * 131072);
          STOUCH(wo, h * 131072 + (w - 4) * 65536);
          STOUCH(wg, TLB_FINE ? min(i, 21) * 65536 : min(i, 10) * 131072);
          STOUCH(wu, TLB_FINE ? min(i, 21) * 65536 : min(i, 10) * 131072);
          STOUCH(wd, TLB_FINE ? min(i, 21) * 65536 : min(i, 10) * 131072);
          if (TLB_FINE) STOUCH(wuv, i * 131072 + 65536);
          STOUCH(out, 0);
        }
        MARK(0);
      } else {
        __builtin_amdgcn_s_sleep(UK_DELAY);
        const unsigned vu = (unsigned)((((128 * j + g4) * NH + h) * DH) * 2);
#pragma unroll 1
        for (int k = w - 6; k < 32; k += 2) {
          if (k >> 1 == UK_FIRST) asm volatile("s_barrier" ::: "memory");
          dma16<NT_WUK>(wuk, vu + (unsigned)k * 32768u + 16u * (col ^ ((4 * k + g4) & 15)), smb + L_WUK + k * 1024);
        }
        MARK(1);
      }
      if (CK2_START && w < 6) VMWAIT(2);
      else VMWAIT(0);
      EPOCH();
      asm volatile("s_barrier\n\ts_barrier\n\ts_barrier\n\ts_waitcnt lgkmcnt(0)" : "+s"(sdummy)::"memory");
    }
    PROBE_T(T);
    HOLD_UNTIL(T_START);
    {
      const unsigned vv = (unsigned)((((256 * ca + g4) * NH + h) * DH + 8 * (col ^ ((g4 | ((w & 2) << 1)) << 1))) * 2);
      const unsigned vo = (unsigned)(((h * DH + (l >> 5)) * DM + 256 * mb) * 2) + 16u * ((l & 31) ^ (((l >> 5) | ((w & 1) << 1) | (w & 4)) << 1));
      int kv = w + 32 * mb, ko = w + 32 * ca;
#pragma unroll 1
      for (int hf = 0; hf < 2; ++hf) {
#pragma unroll 1
        for (int i = 0; i < 4; ++i, kv = (kv + 8) & 63) dma16<NT_T>(wuv, vv + (unsigned)kv * 32768u, smb + L_WUV + kv * 1024);
#pragma unroll 1
        for (int i = 0; i < 4; ++i, ko = (ko + 8) & 63) dma16<NT_T>(wo, vo + (unsigned)ko * 2048u, smb + L_WO + ko * 1024);
        if (hf == 0) asm volatile("s_barrier" ::: "memory");
      }
    }
    const int tb = w >> 2, nq = w & 3;
    unsigned o = WS_PART + (((h * 2 + ca) * 2 + tb) * 32 + 8 * nq + g4) * 512 + 16 * col;
    unsigned pw = L_W + (tb * 32 + 8 * nq + g4) * 4;
    unsigned pv = L_WUV + 256 * (8 * g4 + tq) + 16 * ((2 * w + (tp >> 1)) ^ (fl << 1)) + 8 * (tp & 1);
    unsigned po = L_WO + 512 * (8 * g4 + tq) + 16 * ((4 * w + (tp >> 1)) ^ (fl << 1)) + 8 * (tp & 1);
    unsigned pcw = L_CC + (nq * 2 + tb) * SP2 + 16 * col + 8 * g4, pcr = L_CC + (col & 7) * SP2 + 16 * g4;
    unsigned ptw = L_CT + col * SP + (16 * w + 4 * g4) * 2, ptr = L_CT + (col & 1) * SP + 16 * g4;
    unsigned oo = col >= 4 ? NOWHERE : WS_OPT + ((16 * mb + 2 * w + (col >> 1)) * 64 + ((2 * h + ca + ROT * 4 * (2 * w + (col >> 1))) & 63)) * 128 + ((col & 1) * 4 + g4) * 16;
    v4u mk;
#pragma unroll
    for (int d = 0; d < 4; ++d) mk[d] = (col >> 1) == d ? ((col & 1) ? 0xffff0000u : 0xffffu) : 0u;
    PIN(o); PIN(pw); PIN(pv); PIN(po); PIN(pcw); PIN(pcr); PIN(ptw); PIN(ptr); PIN(oo); PIN(mk);
    STAMP(11);
        if (w == 7) {
      const unsigned ol = WS_LSE + (HMAP ? ((l * 2 + (r >> 3)) * 8 + ((xcd + ROT * l) & 7)) * 128 + ((r >> 2) & 1) * 8 : (l * 8 + xcd) * 128 + (r >> 2) * 8);
      v2u la;
      UNDEF(la);
      for (unsigned spin = SPIN_CAP; spin; --spin) {
        asm volatile("global_load_dwordx2 %0, %1, %2 sc1" WAIT0 : "+v"(la) : "v"(ol), "s"(ws) : "memory");
        if (!__builtin_amdgcn_ballot_w64(la.y != T)) break;
      }
      float M = asf(la.x);
      M = fmax2(M, dpp<ROR8>(M));
      M = fmax2(M, dpp<ROR4>(M));
      M = fmax2(M, dpp<ROR2>(M));
      M = fmax2(M, dpp<ROR1>(M));
      M = sw16_max(M);
      const float e = __builtin_amdgcn_exp2f(asf(la.x) - M);
      float L = e + dpp<ROR8>(e);
      L += dpp<ROR4>(L);
      L += dpp<ROR2>(L);
      L += dpp<ROR1>(L);
      L = sw16_add(L, L);
      const float wt = e * __builtin_amdgcn_rcpf(L);
      *(LAS unsigned*)(sm + L_W + 4 * ((l & 32) | ((l + ROT * 4 * (h & 7)) & 31))) = pk(wt, wt);
      STAMP(7);
    }
    v4u a[4];
    {
      if (LSE_PIPE && w == 7) VMWAIT(2);
      else VMWAIT(0);
      STAMP(6);
      BAR();
      SWEEP_4(a, o, 0, 256, 2048, 2304, "");
    }
    v4u A0 = mk & *(LAS unsigned*)(sm + pw), A1 = mk & *(LAS unsigned*)(sm + pw + 16);
    v4u av[8], ao[8];
#pragma unroll
    for (int s = 0; s < 8; ++s) av[s] = trrd2(sm + pv + 256 * 32 * s, sm + pv + 256 * (32 * s + 4));
#pragma unroll
    for (int t = 0; t < 2; ++t)
#pragma unroll
      for (int s = 0; s < 4; ++s) ao[4 * t + s] = trrd2(sm + (po ^ (32 * t)) + 512 * 32 * s, sm + (po ^ (32 * t)) + 512 * (32 * s + 4));
    VMWAIT(0);
    asm volatile("" : "+v"(a[0]), "+v"(a[1]), "+v"(a[2]), "+v"(a[3]), "+v"(A0), "+v"(A1)::"memory");
    RETRY(HOLES4(a), TRETRY());
    STAMP(8);
    {
      f4 c0 = mfma32(A0, a[0], z), c1 = mfma32(A0, a[1], z);
      c0 = mfma32(A1, a[2], c0);
      c1 = mfma32(A1, a[3], c1);
      if (g4 < 2) {
        *(LAS v2u*)(sm + pcw) = v2u{pk(c0[0], c0[1]), pk(c0[2], c0[3])};
        *(LAS v2u*)(sm + pcw + 256) = v2u{pk(c1[0], c1[1]), pk(c1[2], c1[3])};
      }
    }
    BAR();
    {
      f4 acc = z;
      v4u bf[8];
#pragma unroll
      for (int s = 0; s < 8; ++s) bf[s] = *(LAS v4u*)(sm + pcr + 64 * s);
      __builtin_amdgcn_sched_barrier(0);
#pragma unroll
      for (int s = 0; s < 8; ++s) acc = mfma32(av[s], bf[s], acc);
#pragma unroll
      for (int e = 0; e < 4; ++e) {
        acc[e] += dpp<QP_2301>(acc[e]);
        acc[e] += dpp<ROR4>(acc[e]);
      }
      if (col < 2) *(LAS v2u*)(sm + ptw) = v2u{pk(acc[0], acc[1]), pk(acc[2], acc[3])};
    }
    BAR();
    {
      f4 c0 = z, c1 = z;
      v4u bf[4];
#pragma unroll
      for (int s = 0; s < 4; ++s) bf[s] = *(LAS v4u*)(sm + ptr + 64 * s);
      __builtin_amdgcn_sched_barrier(0);
#pragma unroll
      for (int s = 0; s < 4; ++s) {
        c0 = mfma32(ao[s], bf[s], c0);
        c1 = mfma32(ao[4 + s], bf[s], c1);
      }
      if (col & 2) c0 = c1;
      st_sc1(rs, oo, v4u{pk(c0[0], c0[1]), T, pk(c0[2], c0[3]), T});
    }
    MARK(2);
    STAMP(9);
  }
  STAMP_FLUSH();
}

#define WS_END 0
#define WS_BASE (WS_END ? ((ws_size - (size_t)WS_END * 1048576) & ~(size_t)4095) : 0)
extern "C" void kernel_launch(void* const* d_in, const int* in_sizes, int n_in, void* d_out, int out_size, void* d_ws, size_t ws_size,
                              hipStream_t stream) {
  mla_glu_decode<<<256, 512, 0, stream>>>((const u16*)d_in[0], (const u16*)d_in[1], (const u16*)d_in[2], (const u16*)d_in[3],
                                          (const u16*)d_in[4], (const u16*)d_in[5], (const u16*)d_in[7], (const u16*)d_in[8],
                                          (const u16*)d_in[9], (u16*)d_out, (char*)d_ws + WS_BASE);
}
```

```cpp
#define T_START 440
#include <hip/hip_runtime.h>
#include <stdint.h>

#define GAS __attribute__((address_space(1)))
#define LAS __attribute__((address_space(3)))
typedef unsigned short u16;
typedef unsigned v4u __attribute__((ext_vector_type(4)));
typedef unsigned v2u __attribute__((ext_vector_type(2)));
typedef float f4 __attribute__((ext_vector_type(4)));
typedef float f2 __attribute__((ext_vector_type(2)));
typedef __bf16 bf8 __attribute__((ext_vector_type(8)));
typedef __bf16 bf2 __attribute__((ext_vector_type(2)));
typedef short s4 __attribute__((ext_vector_type(4)));

#define ROT 1
namespace {
constexpr unsigned NOWHERE = 0x7ff00000u;
constexpr int NH = 32, SK = 4096, DH = 128, DM = 512, DC = 512, FF = 1376;
constexpr int NSL = FF / 16;
constexpr int WS_E = 0;
constexpr int WS_QP = 4096;
constexpr int WS_QA = WS_QP + NH * 4 * 1024;
constexpr int WS_LSE = WS_QA + 2 * NH * 2048;
constexpr int WS_GO = WS_LSE + 2 * 32 * 16 * 128;
constexpr int WS_OPT = WS_GO + 2 * 32 * 16 * 128;
constexpr int WS_OPG = WS_OPT + 32 * 64 * 128;
constexpr int OGP = 16384 + ROT * 384;
constexpr int WS_PART = (WS_OPG + 32 * OGP + 4095) / 4096 * 4096;
constexpr int WS_BYTES = WS_PART + NH * 2 * 2 * 32 * 512;
constexpr int WS_STAMP = 7 << 19;
constexpr int CP = 1056;
constexpr int XP = 1088, SP = 288, SP2 = 544;
constexpr int L_C = 0, L_Q = 128 * CP, L_P = L_Q + 16 * CP, L_X = L_P + 4096, L_MX = L_X + 2 * XP, L_RR = L_MX + 256,
              L_JUNK = L_RR + 1024, L_FL = L_JUNK + 256, L_END = L_FL + 128;
static_assert(L_END <= 163840, "LDS");
constexpr int L_GG = 0, L_GD = 32768, L_GR = 65536;
constexpr int L_WUV = 0, L_WO = 65536, L_WQ = 0, L_WUK = 32768, L_CC = 131072, L_CT = L_CC + 8 * SP2, L_W = L_CT + 2 * SP;
#define UK_DELAY 20
#define H1_DELAY 12
#define CODE_TOUCH 1
#define NT_CKV 0
#define NT_WQ 0
#define NT_WUK 0
#define NT_T 0
#define NT_G 0
#define A_START 70
#define GLU_EARLY 0
#define G_START 0
#define C_START 0
#define LSE_LATE 0
#define LSE_WAVE 3
#define DATA_DELAY 0
#define NEWRED 1
#define R_START 0
#define G_TOUCH 0
#define LSE_PIPE 0
#define PV_MIX 4
#define LSE_QK 1
#define HMAP 1
#define GQ 1
#define DOWN_FIRST 1
#define EARLY_SWEEP 1
#define GO_DELAY 0
#define OUT_TOUCH 1
#define OUT_SC1 1
#define BUMP_EARLY 1
#define G2_DELAY 0
#define UK_FIRST 6
#define UK_TOUCH 1
#define TLB_LATE 150
#define U_START 0
#define TLB_FINE 0
#define CK_TLB 1
#define CK2_START 0
#define UK_WAVES 4
#define PIECEWISE 1
#define PF_GLU 1
constexpr unsigned SPIN_CAP = 1u << 20;
constexpr int AUX_SC1 = 16;

#define STAMP_W 0
#define STAMP(i)
#define STAMP_FLUSH()
#define PROBE_T(T_)

__device__ __forceinline__ f4 mfma32(v4u a, v4u b, f4 c) {
  return __builtin_amdgcn_mfma_f32_16x16x32_bf16(__builtin_bit_cast(bf8, a), __builtin_bit_cast(bf8, b), c, 0, 0, 0);
}
__device__ __forceinline__ f4 mfma16(v2u a, v2u b, f4 c) {
  return __builtin_amdgcn_mfma_f32_16x16x16bf16_1k(__builtin_bit_cast(s4, a), __builtin_bit_cast(s4, b), c, 0, 0, 0);
}
__device__ __forceinline__ unsigned pk(float lo, float hi) {
  f2 x = {lo, hi};
  return __builtin_bit_cast(unsigned, __builtin_convertvector(x, bf2));
}
__device__ __forceinline__ float asf(unsigned u) { return __builtin_bit_cast(float, u); }
__device__ __forceinline__ float blo(unsigned u) { return __builtin_bit_cast(float, u << 16); }
__device__ __forceinline__ float bhi(unsigned u) { return __builtin_bit_cast(float, u & 0xffff0000u); }
template <int CTRL> __device__ __forceinline__ float dpp(float v) {
  return __builtin_bit_cast(float, __builtin_amdgcn_update_dpp(0, __builtin_bit_cast(int, v), CTRL, 0xf, 0xf, true));
}
__device__ __forceinline__ float fmax2(float a, float b) {
#pragma float_control(precise, off)
  return __builtin_fmaxf(a, b);
}
typedef unsigned short h2 __attribute__((ext_vector_type(2)));
__device__ __forceinline__ unsigned scale_dn(unsigned p, unsigned kk) {
  return __builtin_bit_cast(unsigned, __builtin_elementwise_sub_sat(__builtin_bit_cast(h2, p), __builtin_bit_cast(h2, kk)));
}
#define PIN(x) asm volatile("" : "+v"(x))
constexpr int ROR1 = 0x121, ROR2 = 0x122, ROR4 = 0x124, ROR8 = 0x128, QP_2301 = 0x4e, QP_2222 = 0xaa;
__device__ __forceinline__ float sw32_add(float a, float b) {
  auto r = __builtin_amdgcn_permlane32_swap(__builtin_bit_cast(unsigned, a), __builtin_bit_cast(unsigned, b), false, false);
  return asf(r[0]) + asf(r[1]);
}
__device__ __forceinline__ float sw16_add(float a, float b) {
  auto r = __builtin_amdgcn_permlane16_swap(__builtin_bit_cast(unsigned, a), __builtin_bit_cast(unsigned, b), false, false);
  return asf(r[0]) + asf(r[1]);
}
__device__ __forceinline__ float vmax(unsigned a, unsigned b) {
  float r;
  asm("v_max_f32 %0, %1, %2" : "=v"(r) : "v"(a), "v"(b));
  return r;
}
__device__ __forceinline__ float vmax3(float a, float b, float c) {
  float r;
  asm("v_max3_f32 %0, %1, %2, %3" : "=v"(r) : "v"(a), "v"(b), "v"(c));
  return r;
}
__device__ __forceinline__ float sw32_max(float a) {
  auto r = __builtin_amdgcn_permlane32_swap(__builtin_bit_cast(unsigned, a), __builtin_bit_cast(unsigned, a), false, false);
  return vmax(r[0], r[1]);
}
__device__ __forceinline__ float sw16_max(float a) {
  auto r = __builtin_amdgcn_permlane16_swap(__builtin_bit_cast(unsigned, a), __builtin_bit_cast(unsigned, a), false, false);
  return vmax(r[0], r[1]);
}
__device__ __forceinline__ float wave_sum(float v) {
  v += dpp<ROR8>(v);
  v += dpp<ROR4>(v);
  v += dpp<ROR2>(v);
  v += dpp<ROR1>(v);
  v = sw16_add(v, v);
  return sw32_add(v, v);
}
__device__ __forceinline__ v2u trrd(LAS char* p) {
  return __builtin_bit_cast(v2u, __builtin_amdgcn_ds_read_tr16_b64_v4i16((LAS s4*)p));
}
__device__ __forceinline__ v4u trrd2(LAS char* p0, LAS char* p1) {
  v2u a = trrd(p0), b = trrd(p1);
  return v4u{a.x, a.y, b.x, b.y};
}
#define BAR() asm volatile("s_waitcnt lgkmcnt(0)\n\ts_barrier" ::: "memory")
#define VMWAIT(k) asm volatile("s_waitcnt vmcnt(%0)" ::"n"(k) : "memory")
__device__ __forceinline__ void gld16(v4u& d, const void* base, unsigned off) {
  asm volatile("global_load_dwordx4 %0, %1, %2" : "=v"(d) : "v"(off), "s"(base) : "memory");
}
__device__ __forceinline__ void gld4_sc1(unsigned& d, const void* base, unsigned off) {
  asm volatile("global_load_dword %0, %1, %2 sc1" : "=v"(d) : "v"(off), "s"(base) : "memory");
}
template <int NT = 0> __device__ __forceinline__ void touch(const void* base, unsigned off, unsigned lds) {
  if (NT) asm volatile("s_mov_b32 m0, %2\n\ts_nop 0\n\tglobal_load_lds_dword %0, %1 nt" ::"v"(off), "s"(base), "s"(lds) : "memory");
  else asm volatile("s_mov_b32 m0, %2\n\ts_nop 0\n\tglobal_load_lds_dword %0, %1" ::"v"(off), "s"(base), "s"(lds) : "memory");
}
__device__ __forceinline__ void touch_sc1(const void* base, unsigned off, unsigned lds) {
  asm volatile("s_mov_b32 m0, %2\n\ts_nop 0\n\tglobal_load_lds_dword %0, %1 sc1" ::"v"(off), "s"(base), "s"(lds) : "memory");
}
#define LD16(i, a, o) "global_load_dwordx4 %" #i ", %" #a ", %" #o
#define SWEEP_4(q, o, o0, o1, o2, o3, W)                                                                                     \
  asm volatile(LD16(0, 4, 5) " offset:" #o0 " sc1\n\t" LD16(1, 4, 5) " offset:" #o1 " sc1\n\t" LD16(2, 4, 5) " offset:" #o2         \
               " sc1\n\t" LD16(3, 4, 5) " offset:" #o3 " sc1" W                                                               \
               : "=&v"((q)[0]), "=&v"((q)[1]), "=&v"((q)[2]), "=&v"((q)[3])                                                  \
               : "v"(o), "s"(ws)                                                                                             \
               : "memory")
#define RESWEEP_4(q, o, o0, o1, o2, o3)                                                                                      \
  asm volatile(LD16(0, 4, 5) " offset:" #o0 " sc1\n\t" LD16(1, 4, 5) " offset:" #o1 " sc1\n\t" LD16(2, 4, 5) " offset:" #o2         \
               " sc1\n\t" LD16(3, 4, 5) " offset:" #o3 " sc1\n\ts_waitcnt vmcnt(0)"                                            \
               : "+v"((q)[0]), "+v"((q)[1]), "+v"((q)[2]), "+v"((q)[3])                                                      \
               : "v"(o), "s"(ws)                                                                                             \
               : "memory")
#define RELOAD(q, o, off) asm volatile(LD16(0, 1, 2) " offset:" #off " sc1" : "+v"(q) : "v"(o), "s"(ws) : "memory")
#define PIN4(q) asm volatile("s_waitcnt vmcnt(0)" : "+v"((q)[0]), "+v"((q)[1]), "+v"((q)[2]), "+v"((q)[3])::"memory")
#define TAG_BAD(q) __builtin_amdgcn_ballot_w64((T & (q).y & (q).w) != (T | (q).y | (q).w))
#define HOLE(q) __builtin_amdgcn_ballot_w64(max(max((q).x, (q).y), max((q).z, (q).w)) == ~0u)
#define MAX4(q) max(max((q).x, (q).y), max((q).z, (q).w))
#define HOLES4(q) __builtin_amdgcn_ballot_w64(max(max(MAX4((q)[0]), MAX4((q)[1])), max(MAX4((q)[2]), MAX4((q)[3]))) == ~0u)
#define TRETRY()                          \
  if (HOLE(a[0])) RELOAD(a[0], o, 0);     \
  if (HOLE(a[1])) RELOAD(a[1], o, 256);   \
  if (HOLE(a[2])) RELOAD(a[2], o, 2048);  \
  if (HOLE(a[3])) RELOAD(a[3], o, 2304);  \
  PIN4(a)
#define SWEEP_L(W)                                                                                                          \
  asm volatile("global_load_dwordx2 %0, %3, %5 sc1\n\tglobal_load_dwordx2 %1, %4, %5 sc1\n\t"                              \
               "global_load_dwordx2 %2, %4, %5 offset:512 sc1" W                                                            \
               : "=&v"(la), "=&v"(l0), "=&v"(l1)                                                                            \
               : "v"(ola), "v"(olo), "s"(ws)                                                                                \
               : "memory")
#define SWEEP_2(p, q, oa, ob, W) \
  asm volatile(LD16(0, 2, 4) " sc1\n\t" LD16(1, 3, 4) " sc1" W : "=&v"(p), "=&v"(q) : "v"(oa), "v"(ob), "s"(ws) : "memory")
#define TAGS_BAD4(q) \
  __builtin_amdgcn_ballot_w64((T & (q)[0].y & (q)[0].w & (q)[1].y & (q)[1].w & (q)[2].y & (q)[2].w & (q)[3].y & (q)[3].w) != \
                              (T | (q)[0].y | (q)[0].w | (q)[1].y | (q)[1].w | (q)[2].y | (q)[2].w | (q)[3].y | (q)[3].w))
#define FLAG_SET(i) asm volatile("ds_write_b32 %0, %1" ::"v"(smb + L_FL + 4 * (i)), "v"(1u) : "memory")
#define FLAG_WAIT4(i)                                                                                     \
  for (unsigned spin_ = 0; spin_ < SPIN_CAP; ++spin_) {                                                   \
    v4u f_;                                                                                               \
    asm volatile("ds_read_b128 %0, %1\n\ts_waitcnt lgkmcnt(0)" : "=v"(f_) : "v"(smb + L_FL + 16 * (i)) : "memory"); \
    if (__builtin_amdgcn_readfirstlane(f_.x & f_.y & f_.z & f_.w)) break;                                 \
  }
#define HOLD_UNTIL(ticks)              \
  asm volatile("" : "+s"(tstart));     \
  while ((unsigned)__builtin_amdgcn_s_memrealtime() - (unsigned)tstart < (ticks)) __builtin_amdgcn_s_sleep(1)
#define MARK(i) asm volatile(".Lmla_e" #i ":")
#define RETRY(bad, again)                                   \
  if (bad) {                                                \
    for (unsigned spin = SPIN_CAP; spin; --spin) {          \
      again;                                                \
      if (!(bad)) break;                                    \
    }                                                       \
  }
#define UNDEF(x) asm volatile("" : "=v"(x))
#define EPOCH() asm volatile("v_add_u32 %0, 1, %1" : "=v"(T) : "v"(ep) : "memory")
#define WAIT0 "\n\ts_waitcnt vmcnt(0)"
template <int NT = 0> __device__ __forceinline__ void dma16(const void* base, unsigned off, unsigned lds) {
  if (NT) asm volatile("s_mov_b32 m0, %2\n\ts_nop 0\n\tglobal_load_lds_dwordx4 %0, %1 nt" ::"v"(off), "s"(base), "s"(lds) : "memory");
  else asm volatile("s_mov_b32 m0, %2\n\ts_nop 0\n\tglobal_load_lds_dwordx4 %0, %1" ::"v"(off), "s"(base), "s"(lds) : "memory");
}
__device__ __forceinline__ v4u ld_sc1(__amdgpu_buffer_rsrc_t rs, int off) {
  return __builtin_amdgcn_raw_buffer_load_b128(rs, off, 0, AUX_SC1);
}
__device__ __forceinline__ v2u ld8_sc1(__amdgpu_buffer_rsrc_t rs, int off) {
  return __builtin_amdgcn_raw_buffer_load_b64(rs, off, 0, AUX_SC1);
}
__device__ __forceinline__ void st_sc1(__amdgpu_buffer_rsrc_t rs, int off, v4u v) {
  __builtin_amdgcn_raw_buffer_store_b128(v, rs, off, 0, AUX_SC1);
}
__device__ __forceinline__ void ln_stats(v4u r, float& mean, float& rstd) {
  float e[8] = {blo(r.x), bhi(r.x), blo(r.y), bhi(r.y), blo(r.z), bhi(r.z), blo(r.w), bhi(r.w)};
  float s = ((e[0] + e[1]) + (e[2] + e[3])) + ((e[4] + e[5]) + (e[6] + e[7]));
  mean = wave_sum(s) * (1.f / DM);
  float q = 0.f;
#pragma unroll
  for (int i = 0; i < 8; ++i) {
    float d = e[i] - mean;
    q = fmaf(d, d, q);
  }
  rstd = __builtin_amdgcn_rsqf(wave_sum(q) * (1.f / DM) + 1e-5f);
}
}

__global__ void __launch_bounds__(512, 2)
mla_glu_decode(const u16* __restrict__ x, const u16* __restrict__ ckv, const u16* __restrict__ wq, const u16* __restrict__ wuk,
               const u16* __restrict__ wuv, const u16* __restrict__ wo, const u16* __restrict__ wg, const u16* __restrict__ wu,
               const u16* __restrict__ wd, u16* __restrict__ out, char* __restrict__ ws) {
  unsigned long long tstart = __builtin_amdgcn_s_memrealtime();
  __builtin_amdgcn_sched_barrier(0);
  __shared__ __attribute__((aligned(16))) char smem_[L_END];
  LAS char* sm = (LAS char*)smem_;
  const unsigned smb = (unsigned)(size_t)sm;
  const int tid = threadIdx.x, l = tid & 63, w = __builtin_amdgcn_readfirstlane(tid >> 6);
  const int col = l & 15, g4 = l >> 4, tq = (l & 15) >> 2, tp = l & 3;
  const int xcd = blockIdx.x & 7, r = blockIdx.x >> 3;
  const __amdgpu_buffer_rsrc_t rs = __builtin_amdgcn_make_buffer_rsrc(ws, 0, WS_BYTES, 0x00020000);
  int bw = (blockIdx.x >> 7) * w;
  asm volatile("" : "+s"(bw));
  if (bw >= 5) {
    const char* pc0;
    unsigned len;
    asm volatile("s_getpc_b64 %0\n.Lmla_pc:\n\ts_mov_b32 %1, .Lmla_e0-.Lmla_pc\n\ts_max_i32 %1, %1, .Lmla_e1-.Lmla_pc\n\t"
                 "s_max_i32 %1, %1, .Lmla_e2-.Lmla_pc\n\ts_max_i32 %1, %1, .Lmla_e3-.Lmla_pc\n\ts_max_i32 %1, %1, .Lmla_e4-.Lmla_pc\n\t"
                 "s_max_i32 %1, %1, .Lmla_e5-.Lmla_pc"
                 : "=s"(pc0), "=s"(len)::"scc");
    touch(pc0, min((unsigned)(64 * (bw - 5) + l) * 128u, len), smb + L_JUNK);
  }
  __builtin_amdgcn_sched_barrier(0);
  const int h = HMAP ? xcd + 8 * ((r >> 2) & 3) : 4 * xcd + ((r >> 2) & 3), j = r & 3;
  unsigned vq = (unsigned)(((128 * j + g4 + 4 * w) * 4096 + h * DH + 8 * (col ^ ((g4 | ((w & 2) << 1)) << 1))) * 2), vx = (w & 1) * 1024 + l * 16;
  unsigned vzero = 0u;
  int lq = smb + L_WQ + w * 1024, lx = smb + L_X + (w & 1) * XP;
  asm volatile("" : "+v"(vq), "+v"(vx), "+v"(vzero), "+s"(lq), "+s"(lx));
  asm volatile("" ::"s"(x), "s"(ckv), "s"(wq), "s"(wuk), "s"(wuv), "s"(wo), "s"(wg), "s"(wu), "s"(wd), "s"(out), "s"(ws));
  STAMP(0);

  unsigned ep, T;
  LAS char* const px = sm + L_X + (col & 1) * XP + 16 * g4;
  if ((w >> 1) == 2) dma16(x, vx, lx);
  const f4 z = {0.f, 0.f, 0.f, 0.f};
#define LAYERNORM()                                                                                                              \
  {                                                                                                                              \
    const v4u xr = *(LAS v4u*)(sm + L_X + (w & 1) * XP + l * 16);                                                                \
    float e[8] = {blo(xr.x), bhi(xr.x), blo(xr.y), bhi(xr.y), blo(xr.z), bhi(xr.z), blo(xr.w), bhi(xr.w)};                       \
    const float mean = wave_sum(((e[0] + e[1]) + (e[2] + e[3])) + ((e[4] + e[5]) + (e[6] + e[7]))) * (1.f / DM);                 \
    float q = 0.f;                                                                                                               \
    _Pragma("unroll") for (int i = 0; i < 8; ++i) {                                                                              \
      e[i] -= mean;                                                                                                              \
      q = fmaf(e[i], e[i], q);                                                                                                   \
    }                                                                                                                            \
    const float rstd = __builtin_amdgcn_rsqf(wave_sum(q) * (1.f / DM) + 1e-5f);                                                  \
    *(LAS v4u*)(sm + L_X + (w & 1) * XP + l * 16) =                                                                              \
        v4u{pk(e[0] * rstd, e[1] * rstd), pk(e[2] * rstd, e[3] * rstd), pk(e[4] * rstd, e[5] * rstd), pk(e[6] * rstd, e[7] * rstd)}; \
  }

  if (__builtin_expect(r >= 16, 0)) {
    const int a = r - 16, b = xcd >> 2, n = 8 * (xcd & 3) + (a >> 1), g = a & 1;
    const int sl = 12 * xcd + a, rd = (4 * xcd + a) & 31;
    const bool glu = a < 12 && sl < NSL;
    const int v = w & 3;
    unsigned op = WS_PART + ((((16 * g + (col & 7)) * 2 + (v >> 1)) * 2 + b) * 32 + ((n + ROT * 4 * (col & 7)) & 31)) * 512 + 256 * (v & 1) +
                  (col >> 3) * 64 + (g4 & 1) * 32 + (g4 >> 1) * 16;
    gld4_sc1(ep, ws, vzero);
    unsigned o = WS_QA + (b * NH + 16 * g) * 2048 + w * 4096 + l * 16, pqs = smb + L_Q + 2 * w * CP + l * 8;
    PIN(o); PIN(pqs);
    {
    {
      const unsigned vc = (unsigned)(((b * SK + 128 * n) * DC + 8 * l) * 2);
      STAMP(10);
      if (w == 4) touch(ckv, (unsigned)((b * SK + 128 * n) * DC * 2), smb + L_JUNK);
      HOLD_UNTIL(U_START);
      if (w < UK_WAVES) touch(wuk, (unsigned)((32 * a + 8 * w + (l >> 3)) * 8192 + (xcd + 8 * ((l >> 1) & 3)) * 256 + (l & 1) * 128), smb + L_JUNK);
      HOLD_UNTIL(C_START);
    if (w >= 4) {
#pragma unroll
      for (int q = 0; q < 4; ++q) st_sc1(rs, op + 128 * (q >> 1) + (q & 1) * 8 * 65536, v4u{~0u, ~0u, ~0u, ~0u});
      if (w == 7 && l < 32) *(LAS unsigned*)(sm + L_FL + 4 * l) = 0u;
    }
      HOLD_UNTIL(A_START);
#pragma unroll 1
      for (int q = 0; q < 4; ++q) {
        int row = w + ((64 * g + 32 * q) & 127);
        asm volatile("s_barrier" ::: "memory");
#pragma unroll 1
        for (int i = 0; i < 4; ++i, row += 8) dma16<NT_CKV>(ckv, vc + row * 1024u, smb + L_C + row * CP);
      }
    }
#define CHALF()                               \
  asm volatile("s_barrier" ::: "memory");     \
  SWEEP_4(qg, o, 0, 1024, 2048, 3072, "");    \
  VMWAIT(12);                                 \
  asm volatile("s_barrier" ::: "memory")
#define CLAND() \
  VMWAIT(4);    \
  EPOCH();      \
  PROBE_T(T);   \
  BAR();        \
  STAMP(1)
#define QRETRY()                              \
  if (TAG_BAD(qg[0])) RELOAD(qg[0], o, 0);    \
  if (TAG_BAD(qg[1])) RELOAD(qg[1], o, 1024); \
  if (TAG_BAD(qg[2])) RELOAD(qg[2], o, 2048); \
  if (TAG_BAD(qg[3])) RELOAD(qg[3], o, 3072); \
  PIN4(qg)
#define QGATHER()                                                                                                           \
  VMWAIT(0);                                                                                                      \
  asm volatile("" : "+v"(qg[0]), "+v"(qg[1]), "+v"(qg[2]), "+v"(qg[3])::"memory");                                          \
  RETRY(TAGS_BAD4(qg), QRETRY());                                                                                           \
  STAMP(3);                                                                                                                 \
  asm volatile("ds_write2_b32 %0, %2, %3 offset0:0 offset1:1\n\tds_write2_b32 %0, %4, %5 offset0:128 offset1:129\n\t"        \
               "ds_write2_b32 %1, %6, %7 offset0:0 offset1:1\n\tds_write2_b32 %1, %8, %9 offset0:128 offset1:129" ::"v"(pqs), \
               "v"(pqs + CP), "v"(qg[0].x), "v"(qg[0].z), "v"(qg[1].x), "v"(qg[1].z), "v"(qg[2].x), "v"(qg[2].z), "v"(qg[3].x), \
               "v"(qg[3].z)                                                                                                 \
               : "memory")
    if (__builtin_expect(w < 4, 1)) {
      v4u fr[32], qg[4];
      unsigned pc = L_C + (32 * w + col) * CP + 16 * g4;
      PIN(pc);
      CHALF();
      const bool early = (w >> 1) == g;
      if (!early) {
        CLAND();
      }
#pragma unroll
      for (int kt = 0; kt < 2; ++kt)
#pragma unroll
        for (int s = 0; s < 16; ++s) fr[16 * kt + s] = *(LAS v4u*)(sm + pc + 16 * kt * CP + 64 * s);
      if (early) {
        CLAND();
      }
      const int u = w ^ (2 * g);
      unsigned p = L_Q + col * CP + 16 * g4, pp = L_P + u * 1024 + l * 16, pm = L_MX + col * 16 + u * 4;
      unsigned ol = WS_LSE + (HMAP ? (((b * 32 + n) * 2 + g) * 8 + ((col + ROT * n) & 7)) * 128 + (col >> 3) * 8 : ((b * 32 + n) * 8 + 4 * g + (col >> 2)) * 128 + (col & 3) * 8);
      if (g4) ol = NOWHERE;
      PIN(p); PIN(pp); PIN(pm); PIN(ol);
      STAMP(2);
      QGATHER();
      BAR();
      STAMP(4);
      f4 s0 = z, s1 = z;
      v4u qf[16];
#pragma unroll
      for (int s = 0; s < 6; ++s) qf[s] = *(LAS v4u*)(sm + p + 64 * s);
      __builtin_amdgcn_sched_barrier(0);
#pragma unroll
      for (int s = 0; s < 16; ++s) {
        if (s + 6 < 16) qf[s + 6] = *(LAS v4u*)(sm + p + 64 * (s + 6));
        s0 = mfma32(fr[s], qf[s], s0);
        s1 = mfma32(fr[16 + s], qf[s], s1);
        __builtin_amdgcn_sched_barrier(0);
      }
      float mx = fmax2(fmax2(fmax2(s0[0], s0[1]), fmax2(s0[2], s0[3])), fmax2(fmax2(s1[0], s1[1]), fmax2(s1[2], s1[3])));
      mx = sw32_max(sw16_max(mx));
      constexpr float SC = 0.08838834764831845f * 1.4426950408889634f;
      const float mi = __builtin_ceilf(mx * SC);
#pragma unroll
      for (int e = 0; e < 4; ++e) {
        s0[e] = __builtin_amdgcn_exp2f(fmaf(s0[e], SC, -mi));
        s1[e] = __builtin_amdgcn_exp2f(fmaf(s1[e], SC, -mi));
      }
      *(LAS float*)(sm + pm) = mi;
      *(LAS v4u*)(sm + pp) = v4u{pk(s0[0], s0[1]), pk(s0[2], s0[3]), pk(s1[0], s1[1]), pk(s1[2], s1[3])};
      BAR();
      STAMP(5);
      if (w == LSE_QK - 1) {
        const f4 m4 = *(LAS f4*)(sm + pm - 4 * u);
        v4u pf[4];
#pragma unroll
        for (int k = 0; k < 4; ++k) pf[k] = *(LAS v4u*)(sm + pp + (k - u) * 1024);
        __builtin_amdgcn_sched_barrier(0);
        const float M = vmax3(vmax3(m4[0], m4[1], m4[2]), m4[3], m4[3]);
        const v4u one = {0x3f803f80u, 0x3f803f80u, 0x3f803f80u, 0x3f803f80u};
        f4 ls = z;
#pragma unroll
        for (int u = 0; u < 4; ++u) {
          const unsigned kk = min((unsigned)(M - m4[u]), 255u) * 0x00800080u;
#pragma unroll
          for (int e = 0; e < 4; ++e) pf[u][e] = scale_dn(pf[u][e], kk);
          ls = mfma32(one, pf[u], ls);
        }
        __builtin_amdgcn_raw_buffer_store_b64(v2u{__builtin_bit_cast(unsigned, M + __builtin_amdgcn_logf(ls[0])), T}, rs, ol, 0, AUX_SC1);
      }
    } else {
      v4u fr[32], qg[4];
      unsigned p0 = L_C + (64 * g + 4 * g4 + tq) * CP + 256 * v + 8 * tp, p1 = L_C + (64 * (g ^ 1) + 4 * g4 + tq) * CP + 256 * v + 8 * tp;
      PIN(p0); PIN(p1);
      CHALF();
#pragma unroll
      for (int ct = 0; ct < 8; ++ct)
#pragma unroll
        for (int u = 0; u < 2; ++u) fr[4 * ct + u] = trrd2(sm + p0 + 32 * u * CP + 32 * ct, sm + p0 + (32 * u + 16) * CP + 32 * ct);
      CLAND();
#pragma unroll
      for (int ct = 0; ct < 8; ++ct)
#pragma unroll
        for (int u = 0; u < 2; ++u) fr[4 * ct + 2 + u] = trrd2(sm + p1 + 32 * u * CP + 32 * ct, sm + p1 + (32 * u + 16) * CP + 32 * ct);
      unsigned ol = WS_LSE + (HMAP ? (((b * 32 + n) * 2 + g) * 8 + ((col + ROT * n) & 7)) * 128 + (col >> 3) * 8 : ((b * 32 + n) * 8 + 4 * g + (col >> 2)) * 128 + (col & 3) * 8), pp = L_P + l * 16, pm = L_MX + col * 16;
      v4u one = {0x3f803f80u, 0x3f803f80u, 0x3f803f80u, 0x3f803f80u};
      unsigned op2 = op + 8 * 65536;
      PIN(ol); PIN(pp); PIN(pm); PIN(one); PIN(op); PIN(op2);
      STAMP(2);
      QGATHER();
      BAR();
      STAMP(4);
      if (glu) {
        const int k = 8 * (v >> 1) + 2 * (a & 3);
        const unsigned og = (unsigned)(((32 * k + (l >> 1)) * FF + 16 * sl + 8 * (l & 1)) * 2);
        dma16((v & 1) ? wu : wg, og, smb + L_GG + (v & 1) * 16384 + k * 1024);
        dma16((v & 1) ? wu : wg, og + 32 * FF * 2, smb + L_GG + (v & 1) * 16384 + k * 1024 + 1024);
      } else if (v < G_TOUCH) {
        const int ln = 128 * a + 64 * (v >> 1) + l;
        touch((v & 1) ? wu : wg, (unsigned)((ln >> 2) * (FF * 2)) + (unsigned)min(384 * xcd + 128 * (ln & 3), FF * 2 - 4), smb + L_JUNK);
      }
      if (glu && w < 6) LAYERNORM();
      BAR();
      STAMP(5);
      const f4 m4 = *(LAS f4*)(sm + pm);
      v4u pf[4];
#pragma unroll
      for (int u = 0; u < 4; ++u) pf[u] = *(LAS v4u*)(sm + pp + u * 1024);
      __builtin_amdgcn_sched_barrier(0);
      const float M = vmax3(vmax3(m4[0], m4[1], m4[2]), m4[3], m4[3]);
      f4 ls = z, c[4] = {z, z, z, z};
#pragma unroll
      for (int u = 0; u < 4; ++u) {
        const unsigned kk = min((unsigned)(M - m4[u]), 255u) * 0x00800080u;
#pragma unroll
        for (int e = 0; e < 4; ++e) pf[u][e] = scale_dn(pf[u][e], kk);
        ls = mfma32(one, pf[u], ls);
        if (u < 3) {
#pragma unroll
          for (int k = 0; k < 4; ++k) c[k] = mfma32(fr[4 * k + u], pf[u], c[k]);
        }
      }
      __builtin_amdgcn_sched_barrier(0);
      c[0] = mfma32(fr[3], pf[3], c[0]);
      c[1] = mfma32(fr[7], pf[3], c[1]);
      __builtin_amdgcn_sched_barrier(0);
      float il = __builtin_amdgcn_rcpf(ls[0]);
      unsigned lse = __builtin_bit_cast(unsigned, M + __builtin_amdgcn_logf(ls[0]));
      c[2] = mfma32(fr[11], pf[3], c[2]);
      c[3] = mfma32(fr[15], pf[3], c[3]);
#define PV_OUT(c, q)                                                                                          \
  {                                                                                                           \
    v4u R[2];                                                                                                 \
    _Pragma("unroll") for (int k = 0; k < 2; ++k) {                                                           \
      const f4 c0 = c[2 * k] * il, c1 = c[2 * k + 1] * il;                                                    \
      const unsigned x0 = min(pk(c0[0], c0[1]), 0xfffffffeu), x1 = min(pk(c0[2], c0[3]), 0xfffffffeu);        \
      const unsigned y0 = min(pk(c1[0], c1[1]), 0xfffffffeu), y1 = min(pk(c1[2], c1[3]), 0xfffffffeu);        \
      auto r0 = __builtin_amdgcn_permlane16_swap(x0, y0, false, false);                                       \
      auto r1 = __builtin_amdgcn_permlane16_swap(x1, y1, false, false);                                       \
      R[k] = v4u{r0[0], r1[0], r0[1], r1[1]};                                                                 \
    }                                                                                                         \
    v4u A, B;                                                                                                 \
    _Pragma("unroll") for (int e = 0; e < 4; ++e) {                                                           \
      A[e] = (unsigned)__builtin_amdgcn_update_dpp((int)R[0][e], (int)R[1][e], ROR8, 0xf, 0xc, false);        \
      B[e] = (unsigned)__builtin_amdgcn_update_dpp((int)R[1][e], (int)R[0][e], ROR8, 0xf, 0x3, false);        \
    }                                                                                                         \
    st_sc1(rs, op + 128 * q, A);                                                                              \
    st_sc1(rs, op2 + 128 * q, B);                                                                             \
  }
      __builtin_amdgcn_sched_barrier(0);
      f4 d[4] = {z, z, z, z};
#pragma unroll
      for (int u = 0; u < 4; ++u)
#pragma unroll
        for (int k = 0; k < 4; ++k) d[k] = mfma32(fr[16 + 4 * k + u], pf[u], d[k]);
      PV_OUT(c, 0);
#pragma unroll
      for (int i = 0; i < 16; ++i) {
        __builtin_amdgcn_sched_group_barrier(0x8, 1, 0);
        __builtin_amdgcn_sched_group_barrier(0x2, PV_MIX, 0);
      }
      __builtin_amdgcn_sched_barrier(0);
      PV_OUT(d, 1);
      FLAG_SET(v);
      if (glu) {
        VMWAIT(4);
        FLAG_SET(12 + v);
      }
      MARK(3);
      STAMP(6);
    }
    }
      if (glu && w < 4) {
        v4u af[8], xf[8];
        unsigned oog = WS_OPG + (8 * w + (col >> 1)) * OGP + sl * 128 + ((col & 1) * 4 + g4) * 16;
        PIN(oog);
        FLAG_WAIT4(0);
#pragma unroll 1
        for (int i = 2; i < 8; ++i) {
          const int k = 8 * (w >> 1) + ((2 * a + i) & 7);
          dma16((w & 1) ? wu : wg, (unsigned)(((32 * k + (l >> 1)) * FF + 16 * sl + 8 * (l & 1)) * 2), smb + L_GG + (w & 1) * 16384 + k * 1024);
        }
#pragma unroll 1
        for (int i = 0; i < 4; ++i) dma16(wd, (unsigned)(((16 * sl + 4 * w + i) * DM + 8 * l) * 2), smb + L_GD + (4 * w + i) * CP);
        unsigned pg = L_GG + (w & 1) * 16384 + 32 * (256 * (w >> 1) + 8 * g4 + tq) + 8 * tp;
        unsigned pd = L_GD + (4 * g4 + tq) * CP + 256 * w + 8 * tp, pgw = L_GR + ((w & 1) * 2 + (w >> 1)) * 1024 + l * 16;
        PIN(pg); PIN(pd); PIN(pgw);
#pragma unroll
        for (int s = 0; s < 8; ++s) xf[s] = *(LAS v4u*)(px + 512 * (w >> 1) + 64 * s);
        FLAG_WAIT4(3);
        VMWAIT(4);
        STAMP(7);
        f4 acc = z;
#pragma unroll
        for (int s = 0; s < 8; ++s) af[s] = trrd2(sm + pg + 32 * 32 * s, sm + pg + 32 * (32 * s + 4));
        __builtin_amdgcn_sched_barrier(0);
#pragma unroll
        for (int s = 0; s < 8; ++s) acc = mfma32(af[s], xf[s], acc);
        *(LAS f4*)(sm + pgw) = acc;
        VMWAIT(0);
        FLAG_SET(8 + w);
        FLAG_WAIT4(2);
        v2u dn[8];
        LAS char* pr = sm + L_GR + l * 16;
        const f4 gx = *(LAS f4*)(pr) + *(LAS f4*)(pr + 1024), ux = *(LAS f4*)(pr + 2048) + *(LAS f4*)(pr + 3072);
#pragma unroll
        for (int t = 0; t < 8; ++t) dn[t] = trrd(sm + pd + 32 * t);
        float hh[4];
#pragma unroll
        for (int e = 0; e < 4; ++e) hh[e] = ux[e] * __builtin_amdgcn_rcpf(1.f + __builtin_amdgcn_exp2f(-1.4426950408889634f * gx[e]));
        const v2u hb = {pk(hh[0], hh[1]), pk(hh[2], hh[3])};
        f4 d = z;
#pragma unroll
        for (int t = 0; t < 8; ++t) d = mfma16(dn[t], (col >> 1) == t ? hb : v2u{0u, 0u}, d);
        v4u dv = {pk(d[0], d[1]), T, pk(d[2], d[3]), T};
        PIN(dv);
        st_sc1(rs, oog, dv);
        MARK(4);
        STAMP(9);
      }
    if (a >= 12) {
      const unsigned m = (col & 1) ? 0x3f800000u : 0x3f80u;
      v4u sel = {col < 2 ? m : 0u, (col >> 1) == 1 ? m : 0u, col < 2 ? m : 0u, (col >> 1) == 1 ? m : 0u};
      PIN(sel);
      if (w == 0) {
        unsigned ot = WS_OPT + rd * 8192 + 4096 + l * 16, oy = (l >> 2) * 1024 + rd * 32 + (l & 3) * 8, pr = smb + L_RR + (l & 7) * 16;
        PIN(ot); PIN(oy); PIN(pr);
        v4u p[8];
        touch(out, (unsigned)((l & 1) * 1024 + rd * 32), smb + L_JUNK);
        HOLD_UNTIL(R_START);
        SWEEP_4(p, ot, -4096, -3072, -2048, -1024, "");
        SWEEP_4(p + 4, ot, 0, 1024, 2048, 3072, "");
        PIN4(p);
        PIN4(p + 4);
#define RRETRY()                                \
  if (TAG_BAD(p[0])) RELOAD(p[0], ot, -4096);   \
  if (TAG_BAD(p[1])) RELOAD(p[1], ot, -3072);   \
  if (TAG_BAD(p[2])) RELOAD(p[2], ot, -2048);   \
  if (TAG_BAD(p[3])) RELOAD(p[3], ot, -1024);   \
  if (TAG_BAD(p[4])) RELOAD(p[4], ot, 0);       \
  if (TAG_BAD(p[5])) RELOAD(p[5], ot, 1024);    \
  if (TAG_BAD(p[6])) RELOAD(p[6], ot, 2048);    \
  if (TAG_BAD(p[7])) RELOAD(p[7], ot, 3072);    \
  PIN4(p);                                      \
  PIN4(p + 4)
        RETRY(TAGS_BAD4(p) | TAGS_BAD4(p + 4), RRETRY());
        STAMP(8);
        f4 acc = z;
#pragma unroll
        for (int i = 0; i < 4; ++i) acc = mfma32(sel, v4u{p[2 * i].x, p[2 * i].z, p[2 * i + 1].x, p[2 * i + 1].z}, acc);
        f4 rest[6];
        for (unsigned spin = 0; spin < SPIN_CAP; ++spin) {
          v4u f0;
          v2u f1;
          asm volatile("ds_read_b128 %0, %8\n\tds_read_b64 %1, %8 offset:16\n\tds_read_b128 %2, %9\n\tds_read_b128 %3, %9 offset:128\n\t"
                       "ds_read_b128 %4, %9 offset:256\n\tds_read_b128 %5, %9 offset:384\n\tds_read_b128 %6, %9 offset:512\n\t"
                       "ds_read_b128 %7, %9 offset:640\n\ts_waitcnt lgkmcnt(0)"
                       : "=&v"(f0), "=&v"(f1), "=&v"(rest[0]), "=&v"(rest[1]), "=&v"(rest[2]), "=&v"(rest[3]), "=&v"(rest[4]), "=&v"(rest[5])
                       : "v"(smb + L_FL + 64), "v"(pr)
                       : "memory");
          if (__builtin_amdgcn_readfirstlane(f0.x & f0.y & f0.z & f0.w & f1.x & f1.y)) break;
        }
        const f4 gl = ((rest[0] + rest[1]) + (rest[2] + rest[3])) + (rest[4] + rest[5]);
#pragma unroll
        for (int e = 0; e < 4; ++e) acc[e] = (acc[e] + dpp<ROR8>(acc[e])) + gl[e];
        if (l < 8) asm volatile("global_store_dwordx2 %0, %1, %2 sc1" ::"v"(oy), "v"(v2u{pk(acc[0], acc[1]), pk(acc[2], acc[3])}), "s"(out) : "memory");
      } else if (w < 7) {
        const bool last = w == 6;
        unsigned og = WS_OPG + rd * OGP + (w - 1) * 2048 + (last ? min(l, 47) : l) * 16, og2 = last ? og : og + 1024;
        const v2u xr = *(const v2u*)((const char*)x + ((l >> 2) & 1) * 1024 + rd * 32 + (l & 3) * 8);
        unsigned m0 = (last && l >= 48) ? 0u : ~0u, m1 = last ? 0u : ~0u;
        f4 xv = {blo(xr.x), bhi(xr.x), blo(xr.y), bhi(xr.y)};
        if (!last) xv = z;
        PIN(og); PIN(og2); PIN(m0); PIN(m1); PIN(xv);
        v4u p0, p1;
        HOLD_UNTIL(R_START);
        UNDEF(p0); UNDEF(p1);
        for (unsigned spin = SPIN_CAP; spin; --spin) {
          SWEEP_2(p0, p1, og, og2, WAIT0);
          if (!(TAG_BAD(p0) | TAG_BAD(p1))) break;
        }
        STAMP(8);
        f4 acc = mfma32(sel, v4u{p0.x & m0, p0.z & m0, p1.x & m1, p1.z & m1}, z);
#pragma unroll
        for (int e = 0; e < 4; ++e) acc[e] = (acc[e] + dpp<ROR8>(acc[e])) + xv[e];
        if (l < 8) *(LAS f4*)(sm + L_RR + (w - 1) * 128 + l * 16) = acc;
        FLAG_SET(15 + w);
      }
      else if (rd == 0) {
        const unsigned ol0 = WS_LSE + (l * 16 + ((ROT * l) & 7)) * 128 + 4;
        unsigned t0, t1;
        for (unsigned spin = 0;; ++spin) {
          __builtin_amdgcn_s_sleep(32);
          asm volatile("global_load_dword %0, %2, %3 sc1\n\tglobal_load_dword %1, %2, %3 offset:1024 sc1" WAIT0
                       : "=&v"(t0), "=&v"(t1)
                       : "v"(ol0), "s"(ws)
                       : "memory");
          if (!__builtin_amdgcn_ballot_w64((t0 & t1 & T) != (t0 | t1 | T)) || spin > SPIN_CAP) break;
        }
        if (l == 0) __builtin_amdgcn_raw_buffer_store_b32(T, rs, WS_E, 0, AUX_SC1);
      }
      MARK(5);
      STAMP(9);
    }
    STAMP_FLUSH();
    __builtin_amdgcn_endpgm();
  }
  {
    const int ca = j >> 1, mb = j & 1;
    const int fl = tq | ((g4 & 1) << 2);
    if (__builtin_expect(w < 4, 1)) {
#pragma unroll 1
      for (int i = 0; i < 8; ++i, vq += 131072u, lq += 4096) dma16<NT_WQ>(wq, vq, lq);
      gld4_sc1(ep, ws, vzero);
      unsigned p = L_WQ + 256 * (8 * g4 + tq) + 16 * ((4 * w + (tp >> 1)) ^ (fl << 1)) + 8 * (tp & 1), p2 = p ^ 32;
      unsigned oqp = col < 4 ? WS_QP + ((h * 4 + j) * 8 + 2 * w) * 128 + col * 64 + g4 * 16 : NOWHERE;
      unsigned pxj = L_X + (col & 1) * XP + 16 * g4 + 256 * j;
      PIN(p); PIN(p2); PIN(oqp); PIN(pxj);
      unsigned o = WS_QP + (h * 4 + w) * 1024 + l * 16;
      unsigned pqw = smb + L_CC + (2 * w + ((l >> 2) & 1)) * SP + (l >> 3) * 32 + (l & 3) * 8, pqr = L_CC + (col & 7) * SP + 16 * g4;
      unsigned oqa = col < 4 ? WS_QA + ((col & 1) * NH + h) * 2048 + (64 * j + 16 * w + 8 * (col >> 1) + 2 * g4) * 8 : NOWHERE;
      unsigned pu = L_WUK + 256 * (32 * w + col) + 16 * (g4 ^ col);
      PIN(o); PIN(oqa); PIN(pu); PIN(pqw); PIN(pqr);
      VMWAIT(0);
      EPOCH();
      STAMP(1);
      BAR();
      STAMP(2);
      {
        v4u af[8], xf[4];
#pragma unroll
        for (int s = 0; s < 4; ++s) {
          xf[s] = *(LAS v4u*)(sm + pxj + 64 * s);
          af[s] = trrd2(sm + p + 256 * 32 * s, sm + p + 256 * (32 * s + 4));
          af[4 + s] = trrd2(sm + p2 + 256 * 32 * s, sm + p2 + 256 * (32 * s + 4));
        }
        __builtin_amdgcn_sched_barrier(0);
        f4 c0 = z, c1 = z;
#pragma unroll
        for (int s = 0; s < 4; ++s) {
          c0 = mfma32(af[s], xf[s], c0);
          c1 = mfma32(af[4 + s], xf[s], c1);
        }
        if (col & 2) c0 = c1;
        st_sc1(rs, oqp, v4u{pk(c0[0], c0[1]), T, pk(c0[2], c0[3]), T});
      }
      STAMP(3);
      v4u a, uk[8], qf[4];
      __builtin_amdgcn_s_sleep(H1_DELAY);
      asm volatile(LD16(0, 1, 2) " sc1" : "=&v"(a) : "v"(o), "s"(ws) : "memory");
      BAR();
#pragma unroll
      for (int s = 0; s < 4; ++s) {
        uk[s] = *(LAS v4u*)(sm + (pu ^ (64 * s)));
        uk[4 + s] = *(LAS v4u*)(sm + (pu ^ (64 * s)) + 4096);
      }
      VMWAIT(0);
      PIN(a);
      RETRY(TAG_BAD(a), asm volatile(LD16(0, 1, 2) " sc1" WAIT0 : "+v"(a) : "v"(o), "s"(ws) : "memory"));
      asm volatile("ds_write2_b32 %0, %1, %2 offset0:0 offset1:1" ::"v"(pqw), "v"(a.x), "v"(a.z) : "memory");
      STAMP(4);
      BAR();
#pragma unroll
      for (int s = 0; s < 4; ++s) qf[s] = *(LAS v4u*)(sm + pqr + 64 * s);
      __builtin_amdgcn_sched_barrier(0);
      f4 a0 = z, a1 = z;
#pragma unroll
      for (int s = 0; s < 4; ++s) {
        a0 = mfma32(uk[s], qf[s], a0);
        a1 = mfma32(uk[4 + s], qf[s], a1);
      }
      float q[4];
#pragma unroll
      for (int e = 0; e < 4; ++e) {
        const float mine = (col & 2) ? a1[e] : a0[e], other = (col & 2) ? a0[e] : a1[e];
        q[e] = mine + dpp<QP_2301>(other);
        q[e] += dpp<ROR4>(q[e]);
      }
      st_sc1(rs, oqa, v4u{pk(q[0], q[1]), T, pk(q[2], q[3]), T});
      STAMP(5);
      BAR();
    } else {
      unsigned sdummy = 0u;
      gld4_sc1(ep, ws, vzero);
      if (w < 6) {
        VMWAIT(1);
        LAYERNORM();
        STAMP(1);
        BAR();
#define STOUCH(base, off) asm volatile("s_load_dword %0, %1, %2" : "+s"(sdummy) : "s"(base), "s"((unsigned)(off)) : "memory")
        HOLD_UNTIL(TLB_LATE);
        {
          const int i = 2 * r + (w - 4);
          STOUCH(wuv, i * 131072);
          STOUCH(wo, h * 131072 + (w - 4) * 65536);
          STOUCH(wg, TLB_FINE ? min(i, 21) * 65536 : min(i, 10) * 131072);
          STOUCH(wu, TLB_FINE ? min(i, 21) * 65536 : min(i, 10) * 131072);
          STOUCH(wd, TLB_FINE ? min(i, 21) * 65536 : min(i, 10) * 131072);
          if (TLB_FINE) STOUCH(wuv, i * 131072 + 65536);
          STOUCH(out, 0);
        }
        MARK(0);
      } else {
        __builtin_amdgcn_s_sleep(UK_DELAY);
        const unsigned vu = (unsigned)((((128 * j + g4) * NH + h) * DH) * 2);
#pragma unroll 1
        for (int k = w - 6; k < 32; k += 2) {
          if (k >> 1 == UK_FIRST) asm volatile("s_barrier" ::: "memory");
          dma16<NT_WUK>(wuk, vu + (unsigned)k * 32768u + 16u * (col ^ ((4 * k + g4) & 15)), smb + L_WUK + k * 1024);
        }
        MARK(1);
      }
      if (CK2_START && w < 6) VMWAIT(2);
      else VMWAIT(0);
      EPOCH();
      asm volatile("s_barrier\n\ts_barrier\n\ts_barrier\n\ts_waitcnt lgkmcnt(0)" : "+s"(sdummy)::"memory");
    }
    PROBE_T(T);
    HOLD_UNTIL(T_START);
    {
      const unsigned vv = (unsigned)((((256 * ca + g4) * NH + h) * DH + 8 * (col ^ ((g4 | ((w & 2) << 1)) << 1))) * 2);
      const unsigned vo = (unsigned)(((h * DH + (l >> 5)) * DM + 256 * mb) * 2) + 16u * ((l & 31) ^ (((l >> 5) | ((w & 1) << 1) | (w & 4)) << 1));
      int kv = w + 32 * mb, ko = w + 32 * ca;
#pragma unroll 1
      for (int hf = 0; hf < 2; ++hf) {
#pragma unroll 1
        for (int i = 0; i < 4; ++i, kv = (kv + 8) & 63) dma16<NT_T>(wuv, vv + (unsigned)kv * 32768u, smb + L_WUV + kv * 1024);
#pragma unroll 1
        for (int i = 0; i < 4; ++i, ko = (ko + 8) & 63) dma16<NT_T>(wo, vo + (unsigned)ko * 2048u, smb + L_WO + ko * 1024);
        if (hf == 0) asm volatile("s_barrier" ::: "memory");
      }
    }
    const int tb = w >> 2, nq = w & 3;
    unsigned o = WS_PART + (((h * 2 + ca) * 2 + tb) * 32 + 8 * nq + g4) * 512 + 16 * col;
    unsigned pw = L_W + (tb * 32 + 8 * nq + g4) * 4;
    unsigned pv = L_WUV + 256 * (8 * g4 + tq) + 16 * ((2 * w + (tp >> 1)) ^ (fl << 1)) + 8 * (tp & 1);
    unsigned po = L_WO + 512 * (8 * g4 + tq) + 16 * ((4 * w + (tp >> 1)) ^ (fl << 1)) + 8 * (tp & 1);
    unsigned pcw = L_CC + (nq * 2 + tb) * SP2 + 16 * col + 8 * g4, pcr = L_CC + (col & 7) * SP2 + 16 * g4;
    unsigned ptw = L_CT + col * SP + (16 * w + 4 * g4) * 2, ptr = L_CT + (col & 1) * SP + 16 * g4;
    unsigned oo = col >= 4 ? NOWHERE : WS_OPT + ((16 * mb + 2 * w + (col >> 1)) * 64 + ((2 * h + ca + ROT * 4 * (2 * w + (col >> 1))) & 63)) * 128 + ((col & 1) * 4 + g4) * 16;
    v4u mk;
#pragma unroll
    for (int d = 0; d < 4; ++d) mk[d] = (col >> 1) == d ? ((col & 1) ? 0xffff0000u : 0xffffu) : 0u;
    PIN(o); PIN(pw); PIN(pv); PIN(po); PIN(pcw); PIN(pcr); PIN(ptw); PIN(ptr); PIN(oo); PIN(mk);
    STAMP(11);
        if (w == 7) {
      const unsigned ol = WS_LSE + (HMAP ? ((l * 2 + (r >> 3)) * 8 + ((xcd + ROT * l) & 7)) * 128 + ((r >> 2) & 1) * 8 : (l * 8 + xcd) * 128 + (r >> 2) * 8);
      v2u la;
      UNDEF(la);
      for (unsigned spin = SPIN_CAP; spin; --spin) {
        asm volatile("global_load_dwordx2 %0, %1, %2 sc1" WAIT0 : "+v"(la) : "v"(ol), "s"(ws) : "memory");
        if (!__builtin_amdgcn_ballot_w64(la.y != T)) break;
      }
      float M = asf(la.x);
      M = fmax2(M, dpp<ROR8>(M));
      M = fmax2(M, dpp<ROR4>(M));
      M = fmax2(M, dpp<ROR2>(M));
      M = fmax2(M, dpp<ROR1>(M));
      M = sw16_max(M);
      const float e = __builtin_amdgcn_exp2f(asf(la.x) - M);
      float L = e + dpp<ROR8>(e);
      L += dpp<ROR4>(L);
      L += dpp<ROR2>(L);
      L += dpp<ROR1>(L);
      L = sw16_add(L, L);
      const float wt = e * __builtin_amdgcn_rcpf(L);
      *(LAS unsigned*)(sm + L_W + 4 * ((l & 32) | ((l + ROT * 4 * (h & 7)) & 31))) = pk(wt, wt);
      STAMP(7);
    }
    v4u a[4];
    {
      if (LSE_PIPE && w == 7) VMWAIT(2);
      else VMWAIT(0);
      STAMP(6);
      BAR();
      SWEEP_4(a, o, 0, 256, 2048, 2304, "");
    }
    v4u A0 = mk & *(LAS unsigned*)(sm + pw), A1 = mk & *(LAS unsigned*)(sm + pw + 16);
    v4u av[8], ao[8];
#pragma unroll
    for (int s = 0; s < 8; ++s) av[s] = trrd2(sm + pv + 256 * 32 * s, sm + pv + 256 * (32 * s + 4));
#pragma unroll
    for (int t = 0; t < 2; ++t)
#pragma unroll
      for (int s = 0; s < 4; ++s) ao[4 * t + s] = trrd2(sm + (po ^ (32 * t)) + 512 * 32 * s, sm + (po ^ (32 * t)) + 512 * (32 * s + 4));
    VMWAIT(0);
    asm volatile("" : "+v"(a[0]), "+v"(a[1]), "+v"(a[2]), "+v"(a[3]), "+v"(A0), "+v"(A1)::"memory");
    RETRY(HOLES4(a), TRETRY());
    STAMP(8);
    {
      f4 c0 = mfma32(A0, a[0], z), c1 = mfma32(A0, a[1], z);
      c0 = mfma32(A1, a[2], c0);
      c1 = mfma32(A1, a[3], c1);
      if (g4 < 2) {
        *(LAS v2u*)(sm + pcw) = v2u{pk(c0[0], c0[1]), pk(c0[2], c0[3])};
        *(LAS v2u*)(sm + pcw + 256) = v2u{pk(c1[0], c1[1]), pk(c1[2], c1[3])};
      }
    }
    BAR();
    {
      f4 acc = z;
      v4u bf[8];
#pragma unroll
      for (int s = 0; s < 8; ++s) bf[s] = *(LAS v4u*)(sm + pcr + 64 * s);
      __builtin_amdgcn_sched_barrier(0);
#pragma unroll
      for (int s = 0; s < 8; ++s) acc = mfma32(av[s], bf[s], acc);
#pragma unroll
      for (int e = 0; e < 4; ++e) {
        acc[e] += dpp<QP_2301>(acc[e]);
        acc[e] += dpp<ROR4>(acc[e]);
      }
      if (col < 2) *(LAS v2u*)(sm + ptw) = v2u{pk(acc[0], acc[1]), pk(acc[2], acc[3])};
    }
    BAR();
    {
      f4 c0 = z, c1 = z;
      v4u bf[4];
#pragma unroll
      for (int s = 0; s < 4; ++s) bf[s] = *(LAS v4u*)(sm + ptr + 64 * s);
      __builtin_amdgcn_sched_barrier(0);
#pragma unroll
      for (int s = 0; s < 4; ++s) {
        c0 = mfma32(ao[s], bf[s], c0);
        c1 = mfma32(ao[4 + s], bf[s], c1);
      }
      if (col & 2) c0 = c1;
      st_sc1(rs, oo, v4u{pk(c0[0], c0[1]), T, pk(c0[2], c0[3]), T});
    }
    MARK(2);
    STAMP(9);
  }
  STAMP_FLUSH();
}

#define WS_END 0
#define WS_BASE (WS_END ? ((ws_size - (size_t)WS_END * 1048576) & ~(size_t)4095) : 0)
extern "C" void kernel_launch(void* const* d_in, const int* in_sizes, int n_in, void* d_out, int out_size, void* d_ws, size_t ws_size,
                              hipStream_t stream) {
  mla_glu_decode<<<256, 512, 0, stream>>>((const u16*)d_in[0], (const u16*)d_in[1], (const u16*)d_in[2], (const u16*)d_in[3],
                                          (const u16*)d_in[4], (const u16*)d_in[5], (const u16*)d_in[7], (const u16*)d_in[8],
                                          (const u16*)d_in[9], (u16*)d_out, (char*)d_ws + WS_BASE);
}
```

```cpp
#define PROBE_CLASS 0
#define PROBE_IDX 5
#define PROBE_WAVE 0
#define PROBE_K 10
#include <hip/hip_runtime.h>
#include <stdint.h>

#define GAS __attribute__((address_space(1)))
#define LAS __attribute__((address_space(3)))
typedef unsigned short u16;
typedef unsigned v4u __attribute__((ext_vector_type(4)));
typedef unsigned v2u __attribute__((ext_vector_type(2)));
typedef float f4 __attribute__((ext_vector_type(4)));
typedef float f2 __attribute__((ext_vector_type(2)));
typedef __bf16 bf8 __attribute__((ext_vector_type(8)));
typedef __bf16 bf2 __attribute__((ext_vector_type(2)));
typedef short s4 __attribute__((ext_vector_type(4)));

#define ROT 1
namespace {
constexpr unsigned NOWHERE = 0x7ff00000u;
constexpr int NH = 32, SK = 4096, DH = 128, DM = 512, DC = 512, FF = 1376;
constexpr int NSL = FF / 16;
constexpr int WS_E = 0;
constexpr int WS_QP = 4096;
constexpr int WS_QA = WS_QP + NH * 4 * 1024;
constexpr int WS_LSE = WS_QA + 2 * NH * 2048;
constexpr int WS_GO = WS_LSE + 2 * 32 * 16 * 128;
constexpr int WS_OPT = WS_GO + 2 * 32 * 16 * 128;
constexpr int WS_OPG = WS_OPT + 32 * 64 * 128;
constexpr int OGP = 16384 + ROT * 384;
constexpr int WS_PART = (WS_OPG + 32 * OGP + 4095) / 4096 * 4096;
constexpr int WS_BYTES = WS_PART + NH * 2 * 2 * 32 * 512;
constexpr int WS_STAMP = 7 << 19;
constexpr int CP = 1056;
constexpr int XP = 1088, SP = 288, SP2 = 544;
constexpr int L_C = 0, L_Q = 128 * CP, L_P = L_Q + 16 * CP, L_X = L_P + 4096, L_MX = L_X + 2 * XP, L_RR = L_MX + 256,
              L_JUNK = L_RR + 1024, L_FL = L_JUNK + 256, L_END = L_FL + 128;
static_assert(L_END <= 163840, "LDS");
constexpr int L_GG = 0, L_GD = 32768, L_GR = 65536;
constexpr int L_WUV = 0, L_WO = 65536, L_WQ = 0, L_WUK = 32768, L_CC = 131072, L_CT = L_CC + 8 * SP2, L_W = L_CT + 2 * SP;
#define UK_DELAY 20
#define H1_DELAY 12
#define T_START 430
#define CODE_TOUCH 1
#define NT_CKV 0
#define NT_WQ 0
#define NT_WUK 0
#define NT_T 0
#define NT_G 0
#define A_START 70
#define GLU_EARLY 0
#define G_START 0
#define C_START 0
#define LSE_LATE 0
#define LSE_WAVE 3
#define DATA_DELAY 0
#define NEWRED 1
#define R_START 0
#define G_TOUCH 0
#define LSE_PIPE 0
#define PV_MIX 4
#define LSE_QK 1
#define HMAP 1
#define GQ 1
#define DOWN_FIRST 1
#define EARLY_SWEEP 1
#define GO_DELAY 0
#define OUT_TOUCH 1
#define OUT_SC1 1
#define BUMP_EARLY 1
#define G2_DELAY 0
#define UK_FIRST 6
#define UK_TOUCH 1
#define TLB_LATE 150
#define U_START 0
#define TLB_FINE 0
#define CK_TLB 1
#define CK2_START 0
#define UK_WAVES 4
#define CK3 1
#define G3_START 360
#define ORD 0
#define PIECEWISE 1
#define PF_GLU 1
constexpr unsigned SPIN_CAP = 1u << 20;
constexpr int AUX_SC1 = 16;

#define STAMPS
#define STAMP_W 0
#define STAMP_ON(i) ((i) == PROBE_IDX)
#define STAMP(i)                                                                          \
  do {                                                                                    \
    if (!STAMP_ON(i)) break;                                                              \
    unsigned long long t__;                                                               \
    asm volatile("s_memrealtime %0\n\ts_waitcnt lgkmcnt(0)" : "=s"(t__)::"memory");       \
    asm volatile("v_writelane_b32 %0, %1, " #i : "+v"(tsv) : "s"((unsigned)t__));         \
  } while (0)
#define STAMP_FLUSH()                                                                      \
  do {                                                                                     \
    const int cls_ = r < 16 ? 0 : (r < 28 ? 1 : 2);                                        \
    if ((cls_ == PROBE_CLASS || PROBE_CLASS == 3) && w == PROBE_WAVE && __builtin_amdgcn_readlane(tsv, PROBE_IDX) != 0) {   \
      v4u g_;                                                                              \
      for (unsigned sp_ = 0; sp_ < SPIN_CAP; ++sp_) { asm volatile("" ::: "memory"); g_ = ld_sc1(rs, 2048); if (g_.z == Tg) break; } \
      const unsigned long long t0_ = ((unsigned long long)__builtin_amdgcn_readfirstlane(g_.y) << 32) | (unsigned)__builtin_amdgcn_readfirstlane(g_.x); \
      const long long d_ = (int)(__builtin_amdgcn_readlane(tsv, PROBE_IDX) - (unsigned)t0_);                               \
      const unsigned long long until_ = t0_ + 4000ull + PROBE_K * d_;                      \
      while (__builtin_amdgcn_s_memrealtime() < until_) __builtin_amdgcn_s_sleep(1);       \
    }                                                                                      \
  } while (0)
#define PROBE_T(T_) do { Tg = T_; if (blockIdx.x == 0 && tid == 0) st_sc1(rs, 2048, v4u{(unsigned)tstart, (unsigned)(tstart >> 32), T_, 0u}); } while (0)

__device__ __forceinline__ f4 mfma32(v4u a, v4u b, f4 c) {
  return __builtin_amdgcn_mfma_f32_16x16x32_bf16(__builtin_bit_cast(bf8, a), __builtin_bit_cast(bf8, b), c, 0, 0, 0);
}
__device__ __forceinline__ f4 mfma16(v2u a, v2u b, f4 c) {
  return __builtin_amdgcn_mfma_f32_16x16x16bf16_1k(__builtin_bit_cast(s4, a), __builtin_bit_cast(s4, b), c, 0, 0, 0);
}
__device__ __forceinline__ unsigned pk(float lo, float hi) {
  f2 x = {lo, hi};
  return __builtin_bit_cast(unsigned, __builtin_convertvector(x, bf2));
}
__device__ __forceinline__ float asf(unsigned u) { return __builtin_bit_cast(float, u); }
__device__ __forceinline__ float blo(unsigned u) { return __builtin_bit_cast(float, u << 16); }
__device__ __forceinline__ float bhi(unsigned u) { return __builtin_bit_cast(float, u & 0xffff0000u); }
template <int CTRL> __device__ __forceinline__ float dpp(float v) {
  return __builtin_bit_cast(float, __builtin_amdgcn_update_dpp(0, __builtin_bit_cast(int, v), CTRL, 0xf, 0xf, true));
}
__device__ __forceinline__ float fmax2(float a, float b) {
#pragma float_control(precise, off)
  return __builtin_fmaxf(a, b);
}
typedef unsigned short h2 __attribute__((ext_vector_type(2)));
__device__ __forceinline__ unsigned scale_dn(unsigned p, unsigned kk) {
  return __builtin_bit_cast(unsigned, __builtin_elementwise_sub_sat(__builtin_bit_cast(h2, p), __builtin_bit_cast(h2, kk)));
}
#define PIN(x) asm volatile("" : "+v"(x))
constexpr int ROR1 = 0x121, ROR2 = 0x122, ROR4 = 0x124, ROR8 = 0x128, QP_2301 = 0x4e, QP_2222 = 0xaa;
__device__ __forceinline__ float sw32_add(float a, float b) {
  auto r = __builtin_amdgcn_permlane32_swap(__builtin_bit_cast(unsigned, a), __builtin_bit_cast(unsigned, b), false, false);
  return asf(r[0]) + asf(r[1]);
}
__device__ __forceinline__ float sw16_add(float a, float b) {
  auto r = __builtin_amdgcn_permlane16_swap(__builtin_bit_cast(unsigned, a), __builtin_bit_cast(unsigned, b), false, false);
  return asf(r[0]) + asf(r[1]);
}
__device__ __forceinline__ float vmax(unsigned a, unsigned b) {
  float r;
  asm("v_max_f32 %0, %1, %2" : "=v"(r) : "v"(a), "v"(b));
  return r;
}
__device__ __forceinline__ float vmax3(float a, float b, float c) {
  float r;
  asm("v_max3_f32 %0, %1, %2, %3" : "=v"(r) : "v"(a), "v"(b), "v"(c));
  return r;
}
__device__ __forceinline__ float sw32_max(float a) {
  auto r = __builtin_amdgcn_permlane32_swap(__builtin_bit_cast(unsigned, a), __builtin_bit_cast(unsigned, a), false, false);
  return vmax(r[0], r[1]);
}
__device__ __forceinline__ float sw16_max(float a) {
  auto r = __builtin_amdgcn_permlane16_swap(__builtin_bit_cast(unsigned, a), __builtin_bit_cast(unsigned, a), false, false);
  return vmax(r[0], r[1]);
}
__device__ __forceinline__ float wave_sum(float v) {
  v += dpp<ROR8>(v);
  v += dpp<ROR4>(v);
  v += dpp<ROR2>(v);
  v += dpp<ROR1>(v);
  v = sw16_add(v, v);
  return sw32_add(v, v);
}
__device__ __forceinline__ v2u trrd(LAS char* p) {
  return __builtin_bit_cast(v2u, __builtin_amdgcn_ds_read_tr16_b64_v4i16((LAS s4*)p));
}
__device__ __forceinline__ v4u trrd2(LAS char* p0, LAS char* p1) {
  v2u a = trrd(p0), b = trrd(p1);
  return v4u{a.x, a.y, b.x, b.y};
}
#define BAR() asm volatile("s_waitcnt lgkmcnt(0)\n\ts_barrier" ::: "memory")
#define VMWAIT(k) asm volatile("s_waitcnt vmcnt(%0)" ::"n"(k) : "memory")
__device__ __forceinline__ void gld16(v4u& d, const void* base, unsigned off) {
  asm volatile("global_load_dwordx4 %0, %1, %2" : "=v"(d) : "v"(off), "s"(base) : "memory");
}
__device__ __forceinline__ void gld4_sc1(unsigned& d, const void* base, unsigned off) {
  asm volatile("global_load_dword %0, %1, %2 sc1" : "=v"(d) : "v"(off), "s"(base) : "memory");
}
template <int NT = 0> __device__ __forceinline__ void touch(const void* base, unsigned off, unsigned lds) {
  if (NT) asm volatile("s_mov_b32 m0, %2\n\ts_nop 0\n\tglobal_load_lds_dword %0, %1 nt" ::"v"(off), "s"(base), "s"(lds) : "memory");
  else asm volatile("s_mov_b32 m0, %2\n\ts_nop 0\n\tglobal_load_lds_dword %0, %1" ::"v"(off), "s"(base), "s"(lds) : "memory");
}
__device__ __forceinline__ void touch_sc1(const void* base, unsigned off, unsigned lds) {
  asm volatile("s_mov_b32 m0, %2\n\ts_nop 0\n\tglobal_load_lds_dword %0, %1 sc1" ::"v"(off), "s"(base), "s"(lds) : "memory");
}
#define LD16(i, a, o) "global_load_dwordx4 %" #i ", %" #a ", %" #o
#define SWEEP_4(q, o, o0, o1, o2, o3, W)                                                                                     \
  asm volatile(LD16(0, 4, 5) " offset:" #o0 " sc1\n\t" LD16(1, 4, 5) " offset:" #o1 " sc1\n\t" LD16(2, 4, 5) " offset:" #o2         \
               " sc1\n\t" LD16(3, 4, 5) " offset:" #o3 " sc1" W                                                               \
               : "=&v"((q)[0]), "=&v"((q)[1]), "=&v"((q)[2]), "=&v"((q)[3])                                                  \
               : "v"(o), "s"(ws)                                                                                             \
               : "memory")
#define RESWEEP_4(q, o, o0, o1, o2, o3)                                                                                      \
  asm volatile(LD16(0, 4, 5) " offset:" #o0 " sc1\n\t" LD16(1, 4, 5) " offset:" #o1 " sc1\n\t" LD16(2, 4, 5) " offset:" #o2         \
               " sc1\n\t" LD16(3, 4, 5) " offset:" #o3 " sc1\n\ts_waitcnt vmcnt(0)"                                            \
               : "+v"((q)[0]), "+v"((q)[1]), "+v"((q)[2]), "+v"((q)[3])                                                      \
               : "v"(o), "s"(ws)                                                                                             \
               : "memory")
#define RELOAD(q, o, off) asm volatile(LD16(0, 1, 2) " offset:" #off " sc1" : "+v"(q) : "v"(o), "s"(ws) : "memory")
#define PIN4(q) asm volatile("s_waitcnt vmcnt(0)" : "+v"((q)[0]), "+v"((q)[1]), "+v"((q)[2]), "+v"((q)[3])::"memory")
#define TAG_BAD(q) __builtin_amdgcn_ballot_w64((T & (q).y & (q).w) != (T | (q).y | (q).w))
#define HOLE(q) __builtin_amdgcn_ballot_w64(max(max((q).x, (q).y), max((q).z, (q).w)) == ~0u)
#define MAX4(q) max(max((q).x, (q).y), max((q).z, (q).w))
#define HOLES4(q) __builtin_amdgcn_ballot_w64(max(max(MAX4((q)[0]), MAX4((q)[1])), max(MAX4((q)[2]), MAX4((q)[3]))) == ~0u)
#define TRETRY()                          \
  if (HOLE(a[0])) RELOAD(a[0], o, 0);     \
  if (HOLE(a[1])) RELOAD(a[1], o, 256);   \
  if (HOLE(a[2])) RELOAD(a[2], o, 2048);  \
  if (HOLE(a[3])) RELOAD(a[3], o, 2304);  \
  PIN4(a)
#define SWEEP_L(W)                                                                                                          \
  asm volatile("global_load_dwordx2 %0, %3, %5 sc1\n\tglobal_load_dwordx2 %1, %4, %5 sc1\n\t"                              \
               "global_load_dwordx2 %2, %4, %5 offset:512 sc1" W                                                            \
               : "=&v"(la), "=&v"(l0), "=&v"(l1)                                                                            \
               : "v"(ola), "v"(olo), "s"(ws)                                                                                \
               : "memory")
#define SWEEP_2(p, q, oa, ob, W) \
  asm volatile(LD16(0, 2, 4) " sc1\n\t" LD16(1, 3, 4) " sc1" W : "=&v"(p), "=&v"(q) : "v"(oa), "v"(ob), "s"(ws) : "memory")
#define TAGS_BAD4(q) \
  __builtin_amdgcn_ballot_w64((T & (q)[0].y & (q)[0].w & (q)[1].y & (q)[1].w & (q)[2].y & (q)[2].w & (q)[3].y & (q)[3].w) != \
                              (T | (q)[0].y | (q)[0].w | (q)[1].y | (q)[1].w | (q)[2].y | (q)[2].w | (q)[3].y | (q)[3].w))
#define FLAG_SET(i) asm volatile("ds_write_b32 %0, %1" ::"v"(smb + L_FL + 4 * (i)), "v"(1u) : "memory")
#define FLAG_WAIT4(i)                                                                                     \
  for (unsigned spin_ = 0; spin_ < SPIN_CAP; ++spin_) {                                                   \
    v4u f_;                                                                                               \
    asm volatile("ds_read_b128 %0, %1\n\ts_waitcnt lgkmcnt(0)" : "=v"(f_) : "v"(smb + L_FL + 16 * (i)) : "memory"); \
    if (__builtin_amdgcn_readfirstlane(f_.x & f_.y & f_.z & f_.w)) break;                                 \
  }
#define HOLD_UNTIL(ticks)              \
  asm volatile("" : "+s"(tstart));     \
  while ((unsigned)__builtin_amdgcn_s_memrealtime() - (unsigned)tstart < (ticks)) __builtin_amdgcn_s_sleep(1)
#define MARK(i) asm volatile(".Lmla_e" #i ":")
#define RETRY(bad, again)                                   \
  if (bad) {                                                \
    for (unsigned spin = SPIN_CAP; spin; --spin) {          \
      again;                                                \
      if (!(bad)) break;                                    \
    }                                                       \
  }
#define UNDEF(x) asm volatile("" : "=v"(x))
#define EPOCH() asm volatile("v_add_u32 %0, 1, %1" : "=v"(T) : "v"(ep) : "memory")
#define WAIT0 "\n\ts_waitcnt vmcnt(0)"
template <int NT = 0> __device__ __forceinline__ void dma16(const void* base, unsigned off, unsigned lds) {
  if (NT) asm volatile("s_mov_b32 m0, %2\n\ts_nop 0\n\tglobal_load_lds_dwordx4 %0, %1 nt" ::"v"(off), "s"(base), "s"(lds) : "memory");
  else asm volatile("s_mov_b32 m0, %2\n\ts_nop 0\n\tglobal_load_lds_dwordx4 %0, %1" ::"v"(off), "s"(base), "s"(lds) : "memory");
}
__device__ __forceinline__ v4u ld_sc1(__amdgpu_buffer_rsrc_t rs, int off) {
  return __builtin_amdgcn_raw_buffer_load_b128(rs, off, 0, AUX_SC1);
}
__device__ __forceinline__ v2u ld8_sc1(__amdgpu_buffer_rsrc_t rs, int off) {
  return __builtin_amdgcn_raw_buffer_load_b64(rs, off, 0, AUX_SC1);
}
__device__ __forceinline__ void st_sc1(__amdgpu_buffer_rsrc_t rs, int off, v4u v) {
  __builtin_amdgcn_raw_buffer_store_b128(v, rs, off, 0, AUX_SC1);
}
__device__ __forceinline__ void ln_stats(v4u r, float& mean, float& rstd) {
  float e[8] = {blo(r.x), bhi(r.x), blo(r.y), bhi(r.y), blo(r.z), bhi(r.z), blo(r.w), bhi(r.w)};
  float s = ((e[0] + e[1]) + (e[2] + e[3])) + ((e[4] + e[5]) + (e[6] + e[7]));
  mean = wave_sum(s) * (1.f / DM);
  float q = 0.f;
#pragma unroll
  for (int i = 0; i < 8; ++i) {
    float d = e[i] - mean;
    q = fmaf(d, d, q);
  }
  rstd = __builtin_amdgcn_rsqf(wave_sum(q) * (1.f / DM) + 1e-5f);
}
}

__global__ void __launch_bounds__(512, 2)
mla_glu_decode(const u16* __restrict__ x, const u16* __restrict__ ckv, const u16* __restrict__ wq, const u16* __restrict__ wuk,
               const u16* __restrict__ wuv, const u16* __restrict__ wo, const u16* __restrict__ wg, const u16* __restrict__ wu,
               const u16* __restrict__ wd, u16* __restrict__ out, char* __restrict__ ws) {
  unsigned long long tstart = __builtin_amdgcn_s_memrealtime();
  __builtin_amdgcn_sched_barrier(0);
  __shared__ __attribute__((aligned(16))) char smem_[L_END];
  LAS char* sm = (LAS char*)smem_;
  const unsigned smb = (unsigned)(size_t)sm;
  const int tid = threadIdx.x, l = tid & 63, w = __builtin_amdgcn_readfirstlane(tid >> 6);
  const int col = l & 15, g4 = l >> 4, tq = (l & 15) >> 2, tp = l & 3;
  const int xcd = blockIdx.x & 7, r = blockIdx.x >> 3;
  const __amdgpu_buffer_rsrc_t rs = __builtin_amdgcn_make_buffer_rsrc(ws, 0, WS_BYTES, 0x00020000);
  int bw = (blockIdx.x >> 7) * w;
  asm volatile("" : "+s"(bw));
  if (bw >= 5) {
    const char* pc0;
    unsigned len;
    asm volatile("s_getpc_b64 %0\n.Lmla_pc:\n\ts_mov_b32 %1, .Lmla_e0-.Lmla_pc\n\ts_max_i32 %1, %1, .Lmla_e1-.Lmla_pc\n\t"
                 "s_max_i32 %1, %1, .Lmla_e2-.Lmla_pc\n\ts_max_i32 %1, %1, .Lmla_e3-.Lmla_pc\n\ts_max_i32 %1, %1, .Lmla_e4-.Lmla_pc\n\t"
                 "s_max_i32 %1, %1, .Lmla_e5-.Lmla_pc"
                 : "=s"(pc0), "=s"(len)::"scc");
    touch(pc0, min((unsigned)(64 * (bw - 5) + l) * 128u, len), smb + L_JUNK);
  }
  __builtin_amdgcn_sched_barrier(0);
  const int h = HMAP ? xcd + 8 * ((r >> 2) & 3) : 4 * xcd + ((r >> 2) & 3), j = r & 3;
  unsigned vq = (unsigned)(((128 * j + g4 + 4 * w) * 4096 + h * DH + 8 * (col ^ ((g4 | ((w & 2) << 1)) << 1))) * 2), vx = (w & 1) * 1024 + l * 16;
  unsigned vzero = 0u;
  int lq = smb + L_WQ + w * 1024, lx = smb + L_X + (w & 1) * XP;
  asm volatile("" : "+v"(vq), "+v"(vx), "+v"(vzero), "+s"(lq), "+s"(lx));
  asm volatile("" ::"s"(x), "s"(ckv), "s"(wq), "s"(wuk), "s"(wuv), "s"(wo), "s"(wg), "s"(wu), "s"(wd), "s"(out), "s"(ws));
  unsigned tsv = 0u;
  STAMP(0);
  unsigned Tg = 0;

  unsigned ep, T;
  LAS char* const px = sm + L_X + (col & 1) * XP + 16 * g4;
  if ((w >> 1) == 2) dma16(x, vx, lx);
  const f4 z = {0.f, 0.f, 0.f, 0.f};
#define LAYERNORM()                                                                                                              \
  {                                                                                                                              \
    const v4u xr = *(LAS v4u*)(sm + L_X + (w & 1) * XP + l * 16);                                                                \
    float e[8] = {blo(xr.x), bhi(xr.x), blo(xr.y), bhi(xr.y), blo(xr.z), bhi(xr.z), blo(xr.w), bhi(xr.w)};                       \
    const float mean = wave_sum(((e[0] + e[1]) + (e[2] + e[3])) + ((e[4] + e[5]) + (e[6] + e[7]))) * (1.f / DM);                 \
    float q = 0.f;                                                                                                               \
    _Pragma("unroll") for (int i = 0; i < 8; ++i) {                                                                              \
      e[i] -= mean;                                                                                                              \
      q = fmaf(e[i], e[i], q);                                                                                                   \
    }                                                                                                                            \
    const float rstd = __builtin_amdgcn_rsqf(wave_sum(q) * (1.f / DM) + 1e-5f);                                                  \
    *(LAS v4u*)(sm + L_X + (w & 1) * XP + l * 16) =                                                                              \
        v4u{pk(e[0] * rstd, e[1] * rstd), pk(e[2] * rstd, e[3] * rstd), pk(e[4] * rstd, e[5] * rstd), pk(e[6] * rstd, e[7] * rstd)}; \
  }

  if (__builtin_expect(r >= 16, 0)) {
    const int a = r - 16, b = xcd >> 2, n = 8 * (xcd & 3) + (a >> 1), g = a & 1;
    const int sl = 12 * xcd + a, rd = (4 * xcd + a) & 31;
    const bool glu = a < 12 && sl < NSL;
    const int v = w & 3;
    unsigned op = WS_PART + ((((16 * g + (col & 7)) * 2 + (v >> 1)) * 2 + b) * 32 + ((n + ROT * 4 * (col & 7)) & 31)) * 512 + 256 * (v & 1) +
                  (col >> 3) * 64 + (g4 & 1) * 32 + (g4 >> 1) * 16;
    gld4_sc1(ep, ws, vzero);
    unsigned o = WS_QA + (b * NH + 16 * g) * 2048 + w * 4096 + l * 16, pqs = smb + L_Q + 2 * w * CP + l * 8;
    PIN(o); PIN(pqs);
    {
    {
      const unsigned vc = (unsigned)(((b * SK + 128 * n) * DC + 8 * l) * 2);
      STAMP(10);
      if (w == 4) touch(ckv, (unsigned)((b * SK + 128 * n) * DC * 2), smb + L_JUNK);
      HOLD_UNTIL(U_START);
      if (w < UK_WAVES) touch(wuk, (unsigned)((32 * a + 8 * w + (l >> 3)) * 8192 + (xcd + 8 * ((l >> 1) & 3)) * 256 + (l & 1) * 128), smb + L_JUNK);
      HOLD_UNTIL(C_START);
    if (w >= 4) {
#pragma unroll
      for (int q = 0; q < 4; ++q) st_sc1(rs, op + 128 * (q >> 1) + (q & 1) * 8 * 65536, v4u{~0u, ~0u, ~0u, ~0u});
      if (w == 7 && l < 32) *(LAS unsigned*)(sm + L_FL + 4 * l) = 0u;
    }
      if (glu && (w >> 1) == 2) {
        VMWAIT(5);
        LAYERNORM();
      }
      HOLD_UNTIL(A_START);
#pragma unroll 1
      for (int q = 0; q < 4; ++q) {
        int row = w + (ORD ? ((64 * (g + q) + 32 * (q >> 1)) & 127) : ((64 * g + 32 * q) & 127));
        asm volatile("s_barrier" ::: "memory");
#pragma unroll 1
        for (int i = 0; i < 4; ++i, row += 8) dma16<NT_CKV>(ckv, vc + row * 1024u, smb + L_C + row * CP);
      }
    }
#define CHALF()                               \
  asm volatile("s_barrier" ::: "memory");     \
  SWEEP_4(qg, o, 0, 1024, 2048, 3072, "");    \
  VMWAIT(12);                                 \
  asm volatile("s_barrier" ::: "memory")
#define CLAND() \
  VMWAIT(4);    \
  EPOCH();      \
  PROBE_T(T);   \
  BAR();        \
  STAMP(1)
#define QRETRY()                              \
  if (TAG_BAD(qg[0])) RELOAD(qg[0], o, 0);    \
  if (TAG_BAD(qg[1])) RELOAD(qg[1], o, 1024); \
  if (TAG_BAD(qg[2])) RELOAD(qg[2], o, 2048); \
  if (TAG_BAD(qg[3])) RELOAD(qg[3], o, 3072); \
  PIN4(qg)
#define QGATHER()                                                                                                           \
  VMWAIT(0);                                                                                                      \
  asm volatile("" : "+v"(qg[0]), "+v"(qg[1]), "+v"(qg[2]), "+v"(qg[3])::"memory");                                          \
  RETRY(TAGS_BAD4(qg), QRETRY());                                                                                           \
  STAMP(3);                                                                                                                 \
  asm volatile("ds_write2_b32 %0, %2, %3 offset0:0 offset1:1\n\tds_write2_b32 %0, %4, %5 offset0:128 offset1:129\n\t"        \
               "ds_write2_b32 %1, %6, %7 offset0:0 offset1:1\n\tds_write2_b32 %1, %8, %9 offset0:128 offset1:129" ::"v"(pqs), \
               "v"(pqs + CP), "v"(qg[0].x), "v"(qg[0].z), "v"(qg[1].x), "v"(qg[1].z), "v"(qg[2].x), "v"(qg[2].z), "v"(qg[3].x), \
               "v"(qg[3].z)                                                                                                 \
               : "memory")
    if (__builtin_expect(w < 4, 1)) {
      v4u fr[32], qg[4];
      unsigned pc = L_C + (32 * w + col) * CP + 16 * g4;
      PIN(pc);
      CHALF();
      const bool early = ORD ? !(w & 1) : (w >> 1) == g;
      if (!early) {
        CLAND();
      }
#pragma unroll
      for (int kt = 0; kt < 2; ++kt)
#pragma unroll
        for (int s = 0; s < 16; ++s) fr[16 * kt + s] = *(LAS v4u*)(sm + pc + 16 * kt * CP + 64 * s);
      if (early) {
        CLAND();
      }
      const int u = ORD ? (w >> 1) | ((w & 1) << 1) : w ^ (2 * g);
      unsigned p = L_Q + col * CP + 16 * g4, pp = L_P + u * 1024 + l * 16, pm = L_MX + col * 16 + u * 4;
      unsigned ol = WS_LSE + (HMAP ? (((b * 32 + n) * 2 + g) * 8 + ((col + ROT * n) & 7)) * 128 + (col >> 3) * 8 : ((b * 32 + n) * 8 + 4 * g + (col >> 2)) * 128 + (col & 3) * 8);
      if (g4) ol = NOWHERE;
      PIN(p); PIN(pp); PIN(pm); PIN(ol);
      STAMP(2);
      QGATHER();
      BAR();
      STAMP(4);
      f4 s0 = z, s1 = z;
      v4u qf[16];
#pragma unroll
      for (int s = 0; s < 6; ++s) qf[s] = *(LAS v4u*)(sm + p + 64 * s);
      __builtin_amdgcn_sched_barrier(0);
#pragma unroll
      for (int s = 0; s < 16; ++s) {
        if (s + 6 < 16) qf[s + 6] = *(LAS v4u*)(sm + p + 64 * (s + 6));
        s0 = mfma32(fr[s], qf[s], s0);
        s1 = mfma32(fr[16 + s], qf[s], s1);
        __builtin_amdgcn_sched_barrier(0);
      }
      float mx = fmax2(fmax2(fmax2(s0[0], s0[1]), fmax2(s0[2], s0[3])), fmax2(fmax2(s1[0], s1[1]), fmax2(s1[2], s1[3])));
      mx = sw32_max(sw16_max(mx));
      constexpr float SC = 0.08838834764831845f * 1.4426950408889634f;
      const float mi = __builtin_ceilf(mx * SC);
#pragma unroll
      for (int e = 0; e < 4; ++e) {
        s0[e] = __builtin_amdgcn_exp2f(fmaf(s0[e], SC, -mi));
        s1[e] = __builtin_amdgcn_exp2f(fmaf(s1[e], SC, -mi));
      }
      *(LAS float*)(sm + pm) = mi;
      *(LAS v4u*)(sm + pp) = v4u{pk(s0[0], s0[1]), pk(s0[2], s0[3]), pk(s1[0], s1[1]), pk(s1[2], s1[3])};
      BAR();
      STAMP(5);
      if (w == LSE_QK - 1) {
        const f4 m4 = *(LAS f4*)(sm + pm - 4 * u);
        v4u pf[4];
#pragma unroll
        for (int k = 0; k < 4; ++k) pf[k] = *(LAS v4u*)(sm + pp + (k - u) * 1024);
        __builtin_amdgcn_sched_barrier(0);
        const float M = vmax3(vmax3(m4[0], m4[1], m4[2]), m4[3], m4[3]);
        const v4u one = {0x3f803f80u, 0x3f803f80u, 0x3f803f80u, 0x3f803f80u};
        f4 ls = z;
#pragma unroll
        for (int u = 0; u < 4; ++u) {
          const unsigned kk = min((unsigned)(M - m4[u]), 255u) * 0x00800080u;
#pragma unroll
          for (int e = 0; e < 4; ++e) pf[u][e] = scale_dn(pf[u][e], kk);
          ls = mfma32(one, pf[u], ls);
        }
        __builtin_amdgcn_raw_buffer_store_b64(v2u{__builtin_bit_cast(unsigned, M + __builtin_amdgcn_logf(ls[0])), T}, rs, ol, 0, AUX_SC1);
      }
    } else {
      v4u fr[32], qg[4];
      unsigned p0 = L_C + ((ORD ? 0 : 64 * g) + 4 * g4 + tq) * CP + 256 * v + 8 * tp, p1 = L_C + ((ORD ? 32 : 64 * (g ^ 1)) + 4 * g4 + tq) * CP + 256 * v + 8 * tp;
      unsigned p0b = p0 + (ORD ? 64 : 32) * CP, p1b = p1 + (ORD ? 64 : 32) * CP;
      PIN(p0b); PIN(p1b);
      PIN(p0); PIN(p1);
      CHALF();
#pragma unroll
      for (int ct = 0; ct < 8; ++ct)
#pragma unroll
        for (int u = 0; u < 2; ++u) fr[4 * ct + u] = trrd2(sm + (u ? p0b : p0) + 32 * ct, sm + (u ? p0b : p0) + 16 * CP + 32 * ct);
      CLAND();
#pragma unroll
      for (int ct = 0; ct < 8; ++ct)
#pragma unroll
        for (int u = 0; u < 2; ++u) fr[4 * ct + 2 + u] = trrd2(sm + (u ? p1b : p1) + 32 * ct, sm + (u ? p1b : p1) + 16 * CP + 32 * ct);
      unsigned ol = WS_LSE + (HMAP ? (((b * 32 + n) * 2 + g) * 8 + ((col + ROT * n) & 7)) * 128 + (col >> 3) * 8 : ((b * 32 + n) * 8 + 4 * g + (col >> 2)) * 128 + (col & 3) * 8), pp = L_P + l * 16, pm = L_MX + col * 16;
      v4u one = {0x3f803f80u, 0x3f803f80u, 0x3f803f80u, 0x3f803f80u};
      unsigned op2 = op + 8 * 65536;
      PIN(ol); PIN(pp); PIN(pm); PIN(one); PIN(op); PIN(op2);
      STAMP(2);
      QGATHER();
      BAR();
      STAMP(4);
      if (glu) {
        const int k = 8 * (v >> 1) + 2 * (a & 3);
        const unsigned og = (unsigned)(((32 * k + (l >> 1)) * FF + 16 * sl + 8 * (l & 1)) * 2);
        dma16((v & 1) ? wu : wg, og, smb + L_GG + (v & 1) * 16384 + k * 1024);
        dma16((v & 1) ? wu : wg, og + 32 * FF * 2, smb + L_GG + (v & 1) * 16384 + k * 1024 + 1024);
      } else if (v < G_TOUCH) {
        const int ln = 128 * a + 64 * (v >> 1) + l;
        touch((v & 1) ? wu : wg, (unsigned)((ln >> 2) * (FF * 2)) + (unsigned)min(384 * xcd + 128 * (ln & 3), FF * 2 - 4), smb + L_JUNK);
      }
      BAR();
      STAMP(5);
      const f4 m4 = *(LAS f4*)(sm + pm);
      v4u pf[4];
#pragma unroll
      for (int u = 0; u < 4; ++u) pf[u] = *(LAS v4u*)(sm + pp + u * 1024);
      __builtin_amdgcn_sched_barrier(0);
      const float M = vmax3(vmax3(m4[0], m4[1], m4[2]), m4[3], m4[3]);
      f4 ls = z, c[4] = {z, z, z, z};
#pragma unroll
      for (int u = 0; u < 4; ++u) {
        const unsigned kk = min((unsigned)(M - m4[u]), 255u) * 0x00800080u;
#pragma unroll
        for (int e = 0; e < 4; ++e) pf[u][e] = scale_dn(pf[u][e], kk);
        ls = mfma32(one, pf[u], ls);
        if (u < 3) {
#pragma unroll
          for (int k = 0; k < 4; ++k) c[k] = mfma32(fr[4 * k + u], pf[u], c[k]);
        }
      }
      __builtin_amdgcn_sched_barrier(0);
      c[0] = mfma32(fr[3], pf[3], c[0]);
      c[1] = mfma32(fr[7], pf[3], c[1]);
      __builtin_amdgcn_sched_barrier(0);
      float il = __builtin_amdgcn_rcpf(ls[0]);
      unsigned lse = __builtin_bit_cast(unsigned, M + __builtin_amdgcn_logf(ls[0]));
      c[2] = mfma32(fr[11], pf[3], c[2]);
      c[3] = mfma32(fr[15], pf[3], c[3]);
#define PV_OUT(c, q)                                                                                          \
  {                                                                                                           \
    v4u R[2];                                                                                                 \
    _Pragma("unroll") for (int k = 0; k < 2; ++k) {                                                           \
      const f4 c0 = c[2 * k] * il, c1 = c[2 * k + 1] * il;                                                    \
      const unsigned x0 = min(pk(c0[0], c0[1]), 0xfffffffeu), x1 = min(pk(c0[2], c0[3]), 0xfffffffeu);        \
      const unsigned y0 = min(pk(c1[0], c1[1]), 0xfffffffeu), y1 = min(pk(c1[2], c1[3]), 0xfffffffeu);        \
      auto r0 = __builtin_amdgcn_permlane16_swap(x0, y0, false, false);                                       \
      auto r1 = __builtin_amdgcn_permlane16_swap(x1, y1, false, false);                                       \
      R[k] = v4u{r0[0], r1[0], r0[1], r1[1]};                                                                 \
    }                                                                                                         \
    v4u A, B;                                                                                                 \
    _Pragma("unroll") for (int e = 0; e < 4; ++e) {                                                           \
      A[e] = (unsigned)__builtin_amdgcn_update_dpp((int)R[0][e], (int)R[1][e], ROR8, 0xf, 0xc, false);        \
      B[e] = (unsigned)__builtin_amdgcn_update_dpp((int)R[1][e], (int)R[0][e], ROR8, 0xf, 0x3, false);        \
    }                                                                                                         \
    st_sc1(rs, op + 128 * q, A);                                                                              \
    st_sc1(rs, op2 + 128 * q, B);                                                                             \
  }
      __builtin_amdgcn_sched_barrier(0);
      f4 d[4] = {z, z, z, z};
#pragma unroll
      for (int u = 0; u < 4; ++u)
#pragma unroll
        for (int k = 0; k < 4; ++k) d[k] = mfma32(fr[16 + 4 * k + u], pf[u], d[k]);
      PV_OUT(c, 0);
#pragma unroll
      for (int i = 0; i < 16; ++i) {
        __builtin_amdgcn_sched_group_barrier(0x8, 1, 0);
        __builtin_amdgcn_sched_group_barrier(0x2, PV_MIX, 0);
      }
      __builtin_amdgcn_sched_barrier(0);
      PV_OUT(d, 1);
      FLAG_SET(v);
      if (glu) {
        VMWAIT(4);
        FLAG_SET(12 + v);
      }
      MARK(3);
      STAMP(6);
    }
    }
      if (glu && w < 4) {
        v4u af[8], xf[8];
        unsigned oog = WS_OPG + (8 * w + (col >> 1)) * OGP + sl * 128 + ((col & 1) * 4 + g4) * 16;
        PIN(oog);
        FLAG_WAIT4(0);
#pragma unroll 1
        for (int i = 2; i < 8; ++i) {
          const int k = 8 * (w >> 1) + ((2 * a + i) & 7);
          dma16((w & 1) ? wu : wg, (unsigned)(((32 * k + (l >> 1)) * FF + 16 * sl + 8 * (l & 1)) * 2), smb + L_GG + (w & 1) * 16384 + k * 1024);
        }
#pragma unroll 1
        for (int i = 0; i < 4; ++i) dma16(wd, (unsigned)(((16 * sl + 4 * w + i) * DM + 8 * l) * 2), smb + L_GD + (4 * w + i) * CP);
        unsigned pg = L_GG + (w & 1) * 16384 + 32 * (256 * (w >> 1) + 8 * g4 + tq) + 8 * tp;
        unsigned pd = L_GD + (4 * g4 + tq) * CP + 256 * w + 8 * tp, pgw = L_GR + ((w & 1) * 2 + (w >> 1)) * 1024 + l * 16;
        PIN(pg); PIN(pd); PIN(pgw);
#pragma unroll
        for (int s = 0; s < 8; ++s) xf[s] = *(LAS v4u*)(px + 512 * (w >> 1) + 64 * s);
        FLAG_WAIT4(3);
        VMWAIT(4);
        STAMP(7);
        f4 acc = z;
#pragma unroll
        for (int s = 0; s < 8; ++s) af[s] = trrd2(sm + pg + 32 * 32 * s, sm + pg + 32 * (32 * s + 4));
        __builtin_amdgcn_sched_barrier(0);
#pragma unroll
        for (int s = 0; s < 8; ++s) acc = mfma32(af[s], xf[s], acc);
        *(LAS f4*)(sm + pgw) = acc;
        VMWAIT(0);
        FLAG_SET(8 + w);
        FLAG_WAIT4(2);
        v2u dn[8];
        LAS char* pr = sm + L_GR + l * 16;
        const f4 gx = *(LAS f4*)(pr) + *(LAS f4*)(pr + 1024), ux = *(LAS f4*)(pr + 2048) + *(LAS f4*)(pr + 3072);
#pragma unroll
        for (int t = 0; t < 8; ++t) dn[t] = trrd(sm + pd + 32 * t);
        float hh[4];
#pragma unroll
        for (int e = 0; e < 4; ++e) hh[e] = ux[e] * __builtin_amdgcn_rcpf(1.f + __builtin_amdgcn_exp2f(-1.4426950408889634f * gx[e]));
        const v2u hb = {pk(hh[0], hh[1]), pk(hh[2], hh[3])};
        f4 d = z;
#pragma unroll
        for (int t = 0; t < 8; ++t) d = mfma16(dn[t], (col >> 1) == t ? hb : v2u{0u, 0u}, d);
        v4u dv = {pk(d[0], d[1]), T, pk(d[2], d[3]), T};
        PIN(dv);
        st_sc1(rs, oog, dv);
        MARK(4);
        STAMP(9);
      }
    if (a >= 12) {
      const unsigned m = (col & 1) ? 0x3f800000u : 0x3f80u;
      v4u sel = {col < 2 ? m : 0u, (col >> 1) == 1 ? m : 0u, col < 2 ? m : 0u, (col >> 1) == 1 ? m : 0u};
      PIN(sel);
      if (w == 0) {
        unsigned ot = WS_OPT + rd * 8192 + 4096 + l * 16, oy = (l >> 2) * 1024 + rd * 32 + (l & 3) * 8, pr = smb + L_RR + (l & 7) * 16;
        PIN(ot); PIN(oy); PIN(pr);
        v4u p[8];
        touch(out, (unsigned)((l & 1) * 1024 + rd * 32), smb + L_JUNK);
        HOLD_UNTIL(R_START);
        SWEEP_4(p, ot, -4096, -3072, -2048, -1024, "");
        SWEEP_4(p + 4, ot, 0, 1024, 2048, 3072, "");
        PIN4(p);
        PIN4(p + 4);
#define RRETRY()                                \
  if (TAG_BAD(p[0])) RELOAD(p[0], ot, -4096);   \
  if (TAG_BAD(p[1])) RELOAD(p[1], ot, -3072);   \
  if (TAG_BAD(p[2])) RELOAD(p[2], ot, -2048);   \
  if (TAG_BAD(p[3])) RELOAD(p[3], ot, -1024);   \
  if (TAG_BAD(p[4])) RELOAD(p[4], ot, 0);       \
  if (TAG_BAD(p[5])) RELOAD(p[5], ot, 1024);    \
  if (TAG_BAD(p[6])) RELOAD(p[6], ot, 2048);    \
  if (TAG_BAD(p[7])) RELOAD(p[7], ot, 3072);    \
  PIN4(p);                                      \
  PIN4(p + 4)
        RETRY(TAGS_BAD4(p) | TAGS_BAD4(p + 4), RRETRY());
        STAMP(8);
        f4 acc = z;
#pragma unroll
        for (int i = 0; i < 4; ++i) acc = mfma32(sel, v4u{p[2 * i].x, p[2 * i].z, p[2 * i + 1].x, p[2 * i + 1].z}, acc);
        f4 rest[6];
        for (unsigned spin = 0; spin < SPIN_CAP; ++spin) {
          v4u f0;
          v2u f1;
          asm volatile("ds_read_b128 %0, %8\n\tds_read_b64 %1, %8 offset:16\n\tds_read_b128 %2, %9\n\tds_read_b128 %3, %9 offset:128\n\t"
                       "ds_read_b128 %4, %9 offset:256\n\tds_read_b128 %5, %9 offset:384\n\tds_read_b128 %6, %9 offset:512\n\t"
                       "ds_read_b128 %7, %9 offset:640\n\ts_waitcnt lgkmcnt(0)"
                       : "=&v"(f0), "=&v"(f1), "=&v"(rest[0]), "=&v"(rest[1]), "=&v"(rest[2]), "=&v"(rest[3]), "=&v"(rest[4]), "=&v"(rest[5])
                       : "v"(smb + L_FL + 64), "v"(pr)
                       : "memory");
          if (__builtin_amdgcn_readfirstlane(f0.x & f0.y & f0.z & f0.w & f1.x & f1.y)) break;
        }
        const f4 gl = ((rest[0] + rest[1]) + (rest[2] + rest[3])) + (rest[4] + rest[5]);
#pragma unroll
        for (int e = 0; e < 4; ++e) acc[e] = (acc[e] + dpp<ROR8>(acc[e])) + gl[e];
        if (l < 8) asm volatile("global_store_dwordx2 %0, %1, %2 sc1" ::"v"(oy), "v"(v2u{pk(acc[0], acc[1]), pk(acc[2], acc[3])}), "s"(out) : "memory");
      } else if (w < 7) {
        const bool last = w == 6;
        unsigned og = WS_OPG + rd * OGP + (w - 1) * 2048 + (last ? min(l, 47) : l) * 16, og2 = last ? og : og + 1024;
        const v2u xr = *(const v2u*)((const char*)x + ((l >> 2) & 1) * 1024 + rd * 32 + (l & 3) * 8);
        unsigned m0 = (last && l >= 48) ? 0u : ~0u, m1 = last ? 0u : ~0u;
        f4 xv = {blo(xr.x), bhi(xr.x), blo(xr.y), bhi(xr.y)};
        if (!last) xv = z;
        PIN(og); PIN(og2); PIN(m0); PIN(m1); PIN(xv);
        v4u p0, p1;
        HOLD_UNTIL(R_START);
        UNDEF(p0); UNDEF(p1);
        for (unsigned spin = SPIN_CAP; spin; --spin) {
          SWEEP_2(p0, p1, og, og2, WAIT0);
          if (!(TAG_BAD(p0) | TAG_BAD(p1))) break;
        }
        STAMP(8);
        f4 acc = mfma32(sel, v4u{p0.x & m0, p0.z & m0, p1.x & m1, p1.z & m1}, z);
#pragma unroll
        for (int e = 0; e < 4; ++e) acc[e] = (acc[e] + dpp<ROR8>(acc[e])) + xv[e];
        if (l < 8) *(LAS f4*)(sm + L_RR + (w - 1) * 128 + l * 16) = acc;
        FLAG_SET(15 + w);
      }
      else if (rd == 0) {
        const unsigned ol0 = WS_LSE + (l * 16 + ((ROT * l) & 7)) * 128 + 4;
        unsigned t0, t1;
        for (unsigned spin = 0;; ++spin) {
          __builtin_amdgcn_s_sleep(32);
          asm volatile("global_load_dword %0, %2, %3 sc1\n\tglobal_load_dword %1, %2, %3 offset:1024 sc1" WAIT0
                       : "=&v"(t0), "=&v"(t1)
                       : "v"(ol0), "s"(ws)
                       : "memory");
          if (!__builtin_amdgcn_ballot_w64((t0 & t1 & T) != (t0 | t1 | T)) || spin > SPIN_CAP) break;
        }
        if (l == 0) __builtin_amdgcn_raw_buffer_store_b32(T, rs, WS_E, 0, AUX_SC1);
      }
      MARK(5);
      STAMP(9);
    }
    STAMP_FLUSH();
    __builtin_amdgcn_endpgm();
  }
  {
    const int ca = j >> 1, mb = j & 1;
    const int fl = tq | ((g4 & 1) << 2);
    if (__builtin_expect(w < 4, 1)) {
#pragma unroll 1
      for (int i = 0; i < 8; ++i, vq += 131072u, lq += 4096) dma16<NT_WQ>(wq, vq, lq);
      gld4_sc1(ep, ws, vzero);
      unsigned p = L_WQ + 256 * (8 * g4 + tq) + 16 * ((4 * w + (tp >> 1)) ^ (fl << 1)) + 8 * (tp & 1), p2 = p ^ 32;
      unsigned oqp = col < 4 ? WS_QP + ((h * 4 + j) * 8 + 2 * w) * 128 + col * 64 + g4 * 16 : NOWHERE;
      unsigned pxj = L_X + (col & 1) * XP + 16 * g4 + 256 * j;
      PIN(p); PIN(p2); PIN(oqp); PIN(pxj);
      unsigned o = WS_QP + (h * 4 + w) * 1024 + l * 16;
      unsigned pqw = smb + L_CC + (2 * w + ((l >> 2) & 1)) * SP + (l >> 3) * 32 + (l & 3) * 8, pqr = L_CC + (col & 7) * SP + 16 * g4;
      unsigned oqa = col < 4 ? WS_QA + ((col & 1) * NH + h) * 2048 + (64 * j + 16 * w + 8 * (col >> 1) + 2 * g4) * 8 : NOWHERE;
      unsigned pu = L_WUK + 256 * (32 * w + col) + 16 * (g4 ^ col);
      PIN(o); PIN(oqa); PIN(pu); PIN(pqw); PIN(pqr);
      VMWAIT(0);
      EPOCH();
      STAMP(1);
      BAR();
      STAMP(2);
      {
        v4u af[8], xf[4];
#pragma unroll
        for (int s = 0; s < 4; ++s) {
          xf[s] = *(LAS v4u*)(sm + pxj + 64 * s);
          af[s] = trrd2(sm + p + 256 * 32 * s, sm + p + 256 * (32 * s + 4));
          af[4 + s] = trrd2(sm + p2 + 256 * 32 * s, sm + p2 + 256 * (32 * s + 4));
        }
        __builtin_amdgcn_sched_barrier(0);
        f4 c0 = z, c1 = z;
#pragma unroll
        for (int s = 0; s < 4; ++s) {
          c0 = mfma32(af[s], xf[s], c0);
          c1 = mfma32(af[4 + s], xf[s], c1);
        }
        if (col & 2) c0 = c1;
        st_sc1(rs, oqp, v4u{pk(c0[0], c0[1]), T, pk(c0[2], c0[3]), T});
      }
      STAMP(3);
      v4u a, uk[8], qf[4];
      __builtin_amdgcn_s_sleep(H1_DELAY);
      asm volatile(LD16(0, 1, 2) " sc1" : "=&v"(a) : "v"(o), "s"(ws) : "memory");
      BAR();
#pragma unroll
      for (int s = 0; s < 4; ++s) {
        uk[s] = *(LAS v4u*)(sm + (pu ^ (64 * s)));
        uk[4 + s] = *(LAS v4u*)(sm + (pu ^ (64 * s)) + 4096);
      }
      VMWAIT(0);
      PIN(a);
      RETRY(TAG_BAD(a), asm volatile(LD16(0, 1, 2) " sc1" WAIT0 : "+v"(a) : "v"(o), "s"(ws) : "memory"));
      asm volatile("ds_write2_b32 %0, %1, %2 offset0:0 offset1:1" ::"v"(pqw), "v"(a.x), "v"(a.z) : "memory");
      STAMP(4);
      BAR();
#pragma unroll
      for (int s = 0; s < 4; ++s) qf[s] = *(LAS v4u*)(sm + pqr + 64 * s);
      __builtin_amdgcn_sched_barrier(0);
      f4 a0 = z, a1 = z;
#pragma unroll
      for (int s = 0; s < 4; ++s) {
        a0 = mfma32(uk[s], qf[s], a0);
        a1 = mfma32(uk[4 + s], qf[s], a1);
      }
      float q[4];
#pragma unroll
      for (int e = 0; e < 4; ++e) {
        const float mine = (col & 2) ? a1[e] : a0[e], other = (col & 2) ? a0[e] : a1[e];
        q[e] = mine + dpp<QP_2301>(other);
        q[e] += dpp<ROR4>(q[e]);
      }
      st_sc1(rs, oqa, v4u{pk(q[0], q[1]), T, pk(q[2], q[3]), T});
      STAMP(5);
      BAR();
      touch(ckv, (unsigned)(((xcd >> 2) * SK + 1024 * (xcd & 3) + 64 * r + 32 + 8 * w) * 1024 + l * 128), smb + L_JUNK);
    } else {
      unsigned sdummy = 0u;
      gld4_sc1(ep, ws, vzero);
      if (w < 6) {
        VMWAIT(1);
        LAYERNORM();
        STAMP(1);
        BAR();
#define STOUCH(base, off) asm volatile("s_load_dword %0, %1, %2" : "+s"(sdummy) : "s"(base), "s"((unsigned)(off)) : "memory")
        HOLD_UNTIL(TLB_LATE);
        {
          const int i = 2 * r + (w - 4);
          STOUCH(wuv, i * 131072);
          STOUCH(wo, h * 131072 + (w - 4) * 65536);
          STOUCH(wg, TLB_FINE ? min(i, 21) * 65536 : min(i, 10) * 131072);
          STOUCH(wu, TLB_FINE ? min(i, 21) * 65536 : min(i, 10) * 131072);
          STOUCH(wd, TLB_FINE ? min(i, 21) * 65536 : min(i, 10) * 131072);
          if (TLB_FINE) STOUCH(wuv, i * 131072 + 65536);
          STOUCH(out, 0);
        }
        MARK(0);
      } else {
        __builtin_amdgcn_s_sleep(UK_DELAY);
        const unsigned vu = (unsigned)((((128 * j + g4) * NH + h) * DH) * 2);
#pragma unroll 1
        for (int k = w - 6; k < 32; k += 2) {
          if (k >> 1 == UK_FIRST) asm volatile("s_barrier" ::: "memory");
          dma16<NT_WUK>(wuk, vu + (unsigned)k * 32768u + 16u * (col ^ ((4 * k + g4) & 15)), smb + L_WUK + k * 1024);
        }
        MARK(1);
      }
      if (CK2_START && w < 6) VMWAIT(2);
      else VMWAIT(0);
      EPOCH();
      asm volatile("s_barrier\n\ts_barrier\n\ts_barrier\n\ts_waitcnt lgkmcnt(0)" : "+s"(sdummy)::"memory");
      HOLD_UNTIL(G3_START);
      const int ln = 128 * r + 64 * ((w >> 1) & 1) + l;
      touch((w & 1) ? wu : wg, (unsigned)((ln >> 2) * (FF * 2)) + (unsigned)min(384 * xcd + 128 * (ln & 3), FF * 2 - 4), smb + L_JUNK);
    }
    PROBE_T(T);
    HOLD_UNTIL(T_START);
    {
      const unsigned vv = (unsigned)((((256 * ca + g4) * NH + h) * DH + 8 * (col ^ ((g4 | ((w & 2) << 1)) << 1))) * 2);
      const unsigned vo = (unsigned)(((h * DH + (l >> 5)) * DM + 256 * mb) * 2) + 16u * ((l & 31) ^ (((l >> 5) | ((w & 1) << 1) | (w & 4)) << 1));
      int kv = w + 32 * mb, ko = w + 32 * ca;
#pragma unroll 1
      for (int hf = 0; hf < 2; ++hf) {
#pragma unroll 1
        for (int i = 0; i < 4; ++i, kv = (kv + 8) & 63) dma16<NT_T>(wuv, vv + (unsigned)kv * 32768u, smb + L_WUV + kv * 1024);
#pragma unroll 1
        for (int i = 0; i < 4; ++i, ko = (ko + 8) & 63) dma16<NT_T>(wo, vo + (unsigned)ko * 2048u, smb + L_WO + ko * 1024);
        if (hf == 0) asm volatile("s_barrier" ::: "memory");
      }
    }
    const int tb = w >> 2, nq = w & 3;
    unsigned o = WS_PART + (((h * 2 + ca) * 2 + tb) * 32 + 8 * nq + g4) * 512 + 16 * col;
    unsigned pw = L_W + (tb * 32 + 8 * nq + g4) * 4;
    unsigned pv = L_WUV + 256 * (8 * g4 + tq) + 16 * ((2 * w + (tp >> 1)) ^ (fl << 1)) + 8 * (tp & 1);
    unsigned po = L_WO + 512 * (8 * g4 + tq) + 16 * ((4 * w + (tp >> 1)) ^ (fl << 1)) + 8 * (tp & 1);
    unsigned pcw = L_CC + (nq * 2 + tb) * SP2 + 16 * col + 8 * g4, pcr = L_CC + (col & 7) * SP2 + 16 * g4;
    unsigned ptw = L_CT + col * SP + (16 * w + 4 * g4) * 2, ptr = L_CT + (col & 1) * SP + 16 * g4;
    unsigned oo = col >= 4 ? NOWHERE : WS_OPT + ((16 * mb + 2 * w + (col >> 1)) * 64 + ((2 * h + ca + ROT * 4 * (2 * w + (col >> 1))) & 63)) * 128 + ((col & 1) * 4 + g4) * 16;
    v4u mk;
#pragma unroll
    for (int d = 0; d < 4; ++d) mk[d] = (col >> 1) == d ? ((col & 1) ? 0xffff0000u : 0xffffu) : 0u;
    PIN(o); PIN(pw); PIN(pv); PIN(po); PIN(pcw); PIN(pcr); PIN(ptw); PIN(ptr); PIN(oo); PIN(mk);
    STAMP(11);
        if (w == 7) {
      const unsigned ol = WS_LSE + (HMAP ? ((l * 2 + (r >> 3)) * 8 + ((xcd + ROT * l) & 7)) * 128 + ((r >> 2) & 1) * 8 : (l * 8 + xcd) * 128 + (r >> 2) * 8);
      v2u la;
      UNDEF(la);
      for (unsigned spin = SPIN_CAP; spin; --spin) {
        asm volatile("global_load_dwordx2 %0, %1, %2 sc1" WAIT0 : "+v"(la) : "v"(ol), "s"(ws) : "memory");
        if (!__builtin_amdgcn_ballot_w64(la.y != T)) break;
      }
      float M = asf(la.x);
      M = fmax2(M, dpp<ROR8>(M));
      M = fmax2(M, dpp<ROR4>(M));
      M = fmax2(M, dpp<ROR2>(M));
      M = fmax2(M, dpp<ROR1>(M));
      M = sw16_max(M);
      const float e = __builtin_amdgcn_exp2f(asf(la.x) - M);
      float L = e + dpp<ROR8>(e);
      L += dpp<ROR4>(L);
      L += dpp<ROR2>(L);
      L += dpp<ROR1>(L);
      L = sw16_add(L, L);
      const float wt = e * __builtin_amdgcn_rcpf(L);
      *(LAS unsigned*)(sm + L_W + 4 * ((l & 32) | ((l + ROT * 4 * (h & 7)) & 31))) = pk(wt, wt);
      STAMP(7);
    }
    v4u a[4];
    {
      if (LSE_PIPE && w == 7) VMWAIT(2);
      else VMWAIT(0);
      STAMP(6);
      BAR();
      SWEEP_4(a, o, 0, 256, 2048, 2304, "");
    }
    v4u A0 = mk & *(LAS unsigned*)(sm + pw), A1 = mk & *(LAS unsigned*)(sm + pw + 16);
    v4u av[8], ao[8];
#pragma unroll
    for (int s = 0; s < 8; ++s) av[s] = trrd2(sm + pv + 256 * 32 * s, sm + pv + 256 * (32 * s + 4));
#pragma unroll
    for (int t = 0; t < 2; ++t)
#pragma unroll
      for (int s = 0; s < 4; ++s) ao[4 * t + s] = trrd2(sm + (po ^ (32 * t)) + 512 * 32 * s, sm + (po ^ (32 * t)) + 512 * (32 * s + 4));
    VMWAIT(0);
    asm volatile("" : "+v"(a[0]), "+v"(a[1]), "+v"(a[2]), "+v"(a[3]), "+v"(A0), "+v"(A1)::"memory");
    RETRY(HOLES4(a), TRETRY());
    STAMP(8);
    {
      f4 c0 = mfma32(A0, a[0], z), c1 = mfma32(A0, a[1], z);
      c0 = mfma32(A1, a[2], c0);
      c1 = mfma32(A1, a[3], c1);
      if (g4 < 2) {
        *(LAS v2u*)(sm + pcw) = v2u{pk(c0[0], c0[1]), pk(c0[2], c0[3])};
        *(LAS v2u*)(sm + pcw + 256) = v2u{pk(c1[0], c1[1]), pk(c1[2], c1[3])};
      }
    }
    BAR();
    {
      f4 acc = z;
      v4u bf[8];
#pragma unroll
      for (int s = 0; s < 8; ++s) bf[s] = *(LAS v4u*)(sm + pcr + 64 * s);
      __builtin_amdgcn_sched_barrier(0);
#pragma unroll
      for (int s = 0; s < 8; ++s) acc = mfma32(av[s], bf[s], acc);
#pragma unroll
      for (int e = 0; e < 4; ++e) {
        acc[e] += dpp<QP_2301>(acc[e]);
        acc[e] += dpp<ROR4>(acc[e]);
      }
      if (col < 2) *(LAS v2u*)(sm + ptw) = v2u{pk(acc[0], acc[1]), pk(acc[2], acc[3])};
    }
    BAR();
    {
      f4 c0 = z, c1 = z;
      v4u bf[4];
#pragma unroll
      for (int s = 0; s < 4; ++s) bf[s] = *(LAS v4u*)(sm + ptr + 64 * s);
      __builtin_amdgcn_sched_barrier(0);
#pragma unroll
      for (int s = 0; s < 4; ++s) {
        c0 = mfma32(ao[s], bf[s], c0);
        c1 = mfma32(ao[4 + s], bf[s], c1);
      }
      if (col & 2) c0 = c1;
      st_sc1(rs, oo, v4u{pk(c0[0], c0[1]), T, pk(c0[2], c0[3]), T});
    }
    MARK(2);
    STAMP(9);
  }
  STAMP_FLUSH();
}

#define WS_END 0
#define WS_BASE (WS_END ? ((ws_size - (size_t)WS_END * 1048576) & ~(size_t)4095) : 0)
extern "C" void kernel_launch(void* const* d_in, const int* in_sizes, int n_in, void* d_out, int out_size, void* d_ws, size_t ws_size,
                              hipStream_t stream) {
  mla_glu_decode<<<256, 512, 0, stream>>>((const u16*)d_in[0], (const u16*)d_in[1], (const u16*)d_in[2], (const u16*)d_in[3],
                                          (const u16*)d_in[4], (const u16*)d_in[5], (const u16*)d_in[7], (const u16*)d_in[8],
                                          (const u16*)d_in[9], (u16*)d_out, (char*)d_ws + WS_BASE);
}
```

```cpp
#include <hip/hip_runtime.h>
#include <stdint.h>

#define LAS __attribute__((address_space(3)))
typedef unsigned short u16;
typedef unsigned v4u __attribute__((ext_vector_type(4)));
typedef unsigned v2u __attribute__((ext_vector_type(2)));
typedef float f4 __attribute__((ext_vector_type(4)));
typedef float f2 __attribute__((ext_vector_type(2)));
typedef __bf16 bf8 __attribute__((ext_vector_type(8)));
typedef __bf16 bf2 __attribute__((ext_vector_type(2)));
typedef short s4 __attribute__((ext_vector_type(4)));

namespace {
constexpr int ROT = 1;
constexpr int UK_DELAY = 20;
constexpr int H1_DELAY = 12;
constexpr int T_START = 430;
constexpr int A_START = 70;
constexpr int PV_MIX = 4;
constexpr int UK_FIRST = 6;
constexpr int TLB_LATE = 150;
constexpr unsigned NOWHERE = 0x7ff00000u;
constexpr int NH = 32, SK = 4096, DH = 128, DM = 512, DC = 512, FF = 1376;
constexpr int NSL = FF / 16;
constexpr int WS_E = 0;
constexpr int WS_QP = 4096;
constexpr int WS_QA = WS_QP + NH * 4 * 1024;
constexpr int WS_LSE = WS_QA + 2 * NH * 2048;
constexpr int WS_OPT = WS_LSE + 2 * 32 * 16 * 128;
constexpr int WS_OPG = WS_OPT + 32 * 64 * 128;
constexpr int OGP = 16384 + ROT * 384;
constexpr int WS_PART = (WS_OPG + 32 * OGP + 4095) / 4096 * 4096;
constexpr int WS_BYTES = WS_PART + NH * 2 * 2 * 32 * 512;
constexpr int CP = 1056;
constexpr int XP = 1088, SP = 288, SP2 = 544;
constexpr int L_C = 0, L_Q = 128 * CP, L_P = L_Q + 16 * CP, L_X = L_P + 4096, L_MX = L_X + 2 * XP, L_RR = L_MX + 256,
              L_JUNK = L_RR + 1024, L_FL = L_JUNK + 256, L_END = L_FL + 128;
static_assert(L_END <= 163840, "LDS");
constexpr int L_GG = 0, L_GD = 32768, L_GR = 65536;
constexpr int L_WUV = 0, L_WO = 65536, L_WQ = 0, L_WUK = 32768, L_CC = 131072, L_CT = L_CC + 8 * SP2, L_W = L_CT + 2 * SP;
constexpr unsigned SPIN_CAP = 1u << 20;
constexpr int AUX_SC1 = 16;

__device__ __forceinline__ f4 mfma32(v4u a, v4u b, f4 c) {
  return __builtin_amdgcn_mfma_f32_16x16x32_bf16(__builtin_bit_cast(bf8, a), __builtin_bit_cast(bf8, b), c, 0, 0, 0);
}
__device__ __forceinline__ f4 mfma16(v2u a, v2u b, f4 c) {
  return __builtin_amdgcn_mfma_f32_16x16x16bf16_1k(__builtin_bit_cast(s4, a), __builtin_bit_cast(s4, b), c, 0, 0, 0);
}
__device__ __forceinline__ unsigned pk(float lo, float hi) {
  f2 x = {lo, hi};
  return __builtin_bit_cast(unsigned, __builtin_convertvector(x, bf2));
}
__device__ __forceinline__ float asf(unsigned u) { return __builtin_bit_cast(float, u); }
__device__ __forceinline__ float blo(unsigned u) { return __builtin_bit_cast(float, u << 16); }
__device__ __forceinline__ float bhi(unsigned u) { return __builtin_bit_cast(float, u & 0xffff0000u); }
template <int CTRL> __device__ __forceinline__ float dpp(float v) {
  return __builtin_bit_cast(float, __builtin_amdgcn_update_dpp(0, __builtin_bit_cast(int, v), CTRL, 0xf, 0xf, true));
}
__device__ __forceinline__ float fmax2(float a, float b) {
#pragma float_control(precise, off)
  return __builtin_fmaxf(a, b);
}
typedef unsigned short h2 __attribute__((ext_vector_type(2)));
__device__ __forceinline__ unsigned scale_dn(unsigned p, unsigned kk) {
  return __builtin_bit_cast(unsigned, __builtin_elementwise_sub_sat(__builtin_bit_cast(h2, p), __builtin_bit_cast(h2, kk)));
}
#define PIN(x) asm volatile("" : "+v"(x))
constexpr int ROR1 = 0x121, ROR2 = 0x122, ROR4 = 0x124, ROR8 = 0x128, QP_2301 = 0x4e;
__device__ __forceinline__ float sw32_add(float a, float b) {
  auto r = __builtin_amdgcn_permlane32_swap(__builtin_bit_cast(unsigned, a), __builtin_bit_cast(unsigned, b), false, false);
  return asf(r[0]) + asf(r[1]);
}
__device__ __forceinline__ float sw16_add(float a, float b) {
  auto r = __builtin_amdgcn_permlane16_swap(__builtin_bit_cast(unsigned, a), __builtin_bit_cast(unsigned, b), false, false);
  return asf(r[0]) + asf(r[1]);
}
__device__ __forceinline__ float vmax(unsigned a, unsigned b) {
  float r;
  asm("v_max_f32 %0, %1, %2" : "=v"(r) : "v"(a), "v"(b));
  return r;
}
__device__ __forceinline__ float vmax3(float a, float b, float c) {
  float r;
  asm("v_max3_f32 %0, %1, %2, %3" : "=v"(r) : "v"(a), "v"(b), "v"(c));
  return r;
}
__device__ __forceinline__ float sw32_max(float a) {
  auto r = __builtin_amdgcn_permlane32_swap(__builtin_bit_cast(unsigned, a), __builtin_bit_cast(unsigned, a), false, false);
  return vmax(r[0], r[1]);
}
__device__ __forceinline__ float sw16_max(float a) {
  auto r = __builtin_amdgcn_permlane16_swap(__builtin_bit_cast(unsigned, a), __builtin_bit_cast(unsigned, a), false, false);
  return vmax(r[0], r[1]);
}
__device__ __forceinline__ float wave_sum(float v) {
  v += dpp<ROR8>(v);
  v += dpp<ROR4>(v);
  v += dpp<ROR2>(v);
  v += dpp<ROR1>(v);
  v = sw16_add(v, v);
  return sw32_add(v, v);
}
__device__ __forceinline__ v2u trrd(LAS char* p) {
  return __builtin_bit_cast(v2u, __builtin_amdgcn_ds_read_tr16_b64_v4i16((LAS s4*)p));
}
__device__ __forceinline__ v4u trrd2(LAS char* p0, LAS char* p1) {
  v2u a = trrd(p0), b = trrd(p1);
  return v4u{a.x, a.y, b.x, b.y};
}
#define BAR() asm volatile("s_waitcnt lgkmcnt(0)\n\ts_barrier" ::: "memory")
#define VMWAIT(k) asm volatile("s_waitcnt vmcnt(%0)" ::"n"(k) : "memory")
__device__ __forceinline__ void gld4_sc1(unsigned& d, const void* base, unsigned off) {
  asm volatile("global_load_dword %0, %1, %2 sc1" : "=v"(d) : "v"(off), "s"(base) : "memory");
}
__device__ __forceinline__ void touch(const void* base, unsigned off, unsigned lds) {
  asm volatile("s_mov_b32 m0, %2\n\ts_nop 0\n\tglobal_load_lds_dword %0, %1" ::"v"(off), "s"(base), "s"(lds) : "memory");
}
#define LD16(i, a, o) "global_load_dwordx4 %" #i ", %" #a ", %" #o
#define SWEEP_4(q, o, o0, o1, o2, o3, W)                                                                                     \
  asm volatile(LD16(0, 4, 5) " offset:" #o0 " sc1\n\t" LD16(1, 4, 5) " offset:" #o1 " sc1\n\t" LD16(2, 4, 5) " offset:" #o2         \
               " sc1\n\t" LD16(3, 4, 5) " offset:" #o3 " sc1" W                                                               \
               : "=&v"((q)[0]), "=&v"((q)[1]), "=&v"((q)[2]), "=&v"((q)[3])                                                  \
               : "v"(o), "s"(ws)                                                                                             \
               : "memory")
#define RELOAD(q, o, off) asm volatile(LD16(0, 1, 2) " offset:" #off " sc1" : "+v"(q) : "v"(o), "s"(ws) : "memory")
#define PIN4(q) asm volatile("s_waitcnt vmcnt(0)" : "+v"((q)[0]), "+v"((q)[1]), "+v"((q)[2]), "+v"((q)[3])::"memory")
#define TAG_BAD(q) __builtin_amdgcn_ballot_w64((T & (q).y & (q).w) != (T | (q).y | (q).w))
#define HOLE(q) __builtin_amdgcn_ballot_w64(max(max((q).x, (q).y), max((q).z, (q).w)) == ~0u)
#define MAX4(q) max(max((q).x, (q).y), max((q).z, (q).w))
#define HOLES4(q) __builtin_amdgcn_ballot_w64(max(max(MAX4((q)[0]), MAX4((q)[1])), max(MAX4((q)[2]), MAX4((q)[3]))) == ~0u)
#define TRETRY()                          \
  if (HOLE(a[0])) RELOAD(a[0], o, 0);     \
  if (HOLE(a[1])) RELOAD(a[1], o, 256);   \
  if (HOLE(a[2])) RELOAD(a[2], o, 2048);  \
  if (HOLE(a[3])) RELOAD(a[3], o, 2304);  \
  PIN4(a)
#define SWEEP_2(p, q, oa, ob, W) \
  asm volatile(LD16(0, 2, 4) " sc1\n\t" LD16(1, 3, 4) " sc1" W : "=&v"(p), "=&v"(q) : "v"(oa), "v"(ob), "s"(ws) : "memory")
#define TAGS_BAD4(q) \
  __builtin_amdgcn_ballot_w64((T & (q)[0].y & (q)[0].w & (q)[1].y & (q)[1].w & (q)[2].y & (q)[2].w & (q)[3].y & (q)[3].w) != \
                              (T | (q)[0].y | (q)[0].w | (q)[1].y | (q)[1].w | (q)[2].y | (q)[2].w | (q)[3].y | (q)[3].w))
#define FLAG_SET(i) asm volatile("ds_write_b32 %0, %1" ::"v"(smb + L_FL + 4 * (i)), "v"(1u) : "memory")
#define FLAG_WAIT4(i)                                                                                     \
  for (unsigned spin_ = 0; spin_ < SPIN_CAP; ++spin_) {                                                   \
    v4u f_;                                                                                               \
    asm volatile("ds_read_b128 %0, %1\n\ts_waitcnt lgkmcnt(0)" : "=v"(f_) : "v"(smb + L_FL + 16 * (i)) : "memory"); \
    if (__builtin_amdgcn_readfirstlane(f_.x & f_.y & f_.z & f_.w)) break;                                 \
  }
#define HOLD_UNTIL(ticks)              \
  asm volatile("" : "+s"(tstart));     \
  while ((unsigned)__builtin_amdgcn_s_memrealtime() - (unsigned)tstart < (ticks)) __builtin_amdgcn_s_sleep(1)
#define MARK(i) asm volatile(".Lmla_e" #i ":")
#define RETRY(bad, again)                                   \
  if (bad) {                                                \
    for (unsigned spin = SPIN_CAP; spin; --spin) {          \
      again;                                                \
      if (!(bad)) break;                                    \
    }                                                       \
  }
#define UNDEF(x) asm volatile("" : "=v"(x))
#define EPOCH() asm volatile("v_add_u32 %0, 1, %1" : "=v"(T) : "v"(ep) : "memory")
#define WAIT0 "\n\ts_waitcnt vmcnt(0)"
__device__ __forceinline__ void dma16(const void* base, unsigned off, unsigned lds) {
  asm volatile("s_mov_b32 m0, %2\n\ts_nop 0\n\tglobal_load_lds_dwordx4 %0, %1" ::"v"(off), "s"(base), "s"(lds) : "memory");
}
__device__ __forceinline__ void st_sc1(__amdgpu_buffer_rsrc_t rs, int off, v4u v) {
  __builtin_amdgcn_raw_buffer_store_b128(v, rs, off, 0, AUX_SC1);
}
}

__global__ void __launch_bounds__(512, 2)
mla_glu_decode(const u16* __restrict__ x, const u16* __restrict__ ckv, const u16* __restrict__ wq, const u16* __restrict__ wuk,
               const u16* __restrict__ wuv, const u16* __restrict__ wo, const u16* __restrict__ wg, const u16* __restrict__ wu,
               const u16* __restrict__ wd, u16* __restrict__ out, char* __restrict__ ws) {
  unsigned long long tstart = __builtin_amdgcn_s_memrealtime();
  __builtin_amdgcn_sched_barrier(0);
  __shared__ __attribute__((aligned(16))) char smem_[L_END];
  LAS char* sm = (LAS char*)smem_;
  const unsigned smb = (unsigned)(size_t)sm;
  const int tid = threadIdx.x, l = tid & 63, w = __builtin_amdgcn_readfirstlane(tid >> 6);
  const int col = l & 15, g4 = l >> 4, tq = (l & 15) >> 2, tp = l & 3;
  const int xcd = blockIdx.x & 7, r = blockIdx.x >> 3;
  const __amdgpu_buffer_rsrc_t rs = __builtin_amdgcn_make_buffer_rsrc(ws, 0, WS_BYTES, 0x00020000);
  int bw = (blockIdx.x >> 7) * w;
  asm volatile("" : "+s"(bw));
  if (bw >= 5) {
    const char* pc0;
    unsigned len;
    asm volatile("s_getpc_b64 %0\n.Lmla_pc:\n\ts_mov_b32 %1, .Lmla_e0-.Lmla_pc\n\ts_max_i32 %1, %1, .Lmla_e1-.Lmla_pc\n\t"
                 "s_max_i32 %1, %1, .Lmla_e2-.Lmla_pc\n\ts_max_i32 %1, %1, .Lmla_e3-.Lmla_pc\n\ts_max_i32 %1, %1, .Lmla_e4-.Lmla_pc\n\t"
                 "s_max_i32 %1, %1, .Lmla_e5-.Lmla_pc"
                 : "=s"(pc0), "=s"(len)::"scc");
    touch(pc0, min((unsigned)(64 * (bw - 5) + l) * 128u, len), smb + L_JUNK);
  }
  __builtin_amdgcn_sched_barrier(0);
  const int h = xcd + 8 * ((r >> 2) & 3), j = r & 3;
  unsigned vq = (unsigned)(((128 * j + g4 + 4 * w) * 4096 + h * DH + 8 * (col ^ ((g4 | ((w & 2) << 1)) << 1))) * 2), vx = (w & 1) * 1024 + l * 16;
  unsigned vzero = 0u;
  int lq = smb + L_WQ + w * 1024, lx = smb + L_X + (w & 1) * XP;
  asm volatile("" : "+v"(vq), "+v"(vx), "+v"(vzero), "+s"(lq), "+s"(lx));
  asm volatile("" ::"s"(x), "s"(ckv), "s"(wq), "s"(wuk), "s"(wuv), "s"(wo), "s"(wg), "s"(wu), "s"(wd), "s"(out), "s"(ws));


  unsigned ep, T;
  LAS char* const px = sm + L_X + (col & 1) * XP + 16 * g4;
  if ((w >> 1) == 2) dma16(x, vx, lx);
  const f4 z = {0.f, 0.f, 0.f, 0.f};
#define LAYERNORM()                                                                                                              \
  {                                                                                                                              \
    const v4u xr = *(LAS v4u*)(sm + L_X + (w & 1) * XP + l * 16);                                                                \
    float e[8] = {blo(xr.x), bhi(xr.x), blo(xr.y), bhi(xr.y), blo(xr.z), bhi(xr.z), blo(xr.w), bhi(xr.w)};                       \
    const float mean = wave_sum(((e[0] + e[1]) + (e[2] + e[3])) + ((e[4] + e[5]) + (e[6] + e[7]))) * (1.f / DM);                 \
    float q = 0.f;                                                                                                               \
    _Pragma("unroll") for (int i = 0; i < 8; ++i) {                                                                              \
      e[i] -= mean;                                                                                                              \
      q = fmaf(e[i], e[i], q);                                                                                                   \
    }                                                                                                                            \
    const float rstd = __builtin_amdgcn_rsqf(wave_sum(q) * (1.f / DM) + 1e-5f);                                                  \
    *(LAS v4u*)(sm + L_X + (w & 1) * XP + l * 16) =                                                                              \
        v4u{pk(e[0] * rstd, e[1] * rstd), pk(e[2] * rstd, e[3] * rstd), pk(e[4] * rstd, e[5] * rstd), pk(e[6] * rstd, e[7] * rstd)}; \
  }

  if (__builtin_expect(r >= 16, 0)) {
    const int a = r - 16, b = xcd >> 2, n = 8 * (xcd & 3) + (a >> 1), g = a & 1;
    const int sl = 12 * xcd + a, rd = (4 * xcd + a) & 31;
    const bool glu = a < 12 && sl < NSL;
    const int v = w & 3;
    unsigned op = WS_PART + ((((16 * g + (col & 7)) * 2 + (v >> 1)) * 2 + b) * 32 + ((n + ROT * 4 * (col & 7)) & 31)) * 512 + 256 * (v & 1) +
                  (col >> 3) * 64 + (g4 & 1) * 32 + (g4 >> 1) * 16;
    gld4_sc1(ep, ws, vzero);
    unsigned o = WS_QA + (b * NH + 16 * g) * 2048 + w * 4096 + l * 16, pqs = smb + L_Q + 2 * w * CP + l * 8, pqs2 = pqs + CP;
    PIN(o); PIN(pqs); PIN(pqs2);
    {
    {
      const unsigned vc = (unsigned)(((b * SK + 128 * n) * DC + 8 * l) * 2);
      if (w == 4) touch(ckv, (unsigned)((b * SK + 128 * n) * DC * 2), smb + L_JUNK);
      if (w < 4) touch(wuk, (unsigned)((32 * a + 8 * w + (l >> 3)) * 8192 + (xcd + 8 * ((l >> 1) & 3)) * 256 + (l & 1) * 128), smb + L_JUNK);
    if (w >= 4) {
#pragma unroll
      for (int q = 0; q < 4; ++q) st_sc1(rs, op + 128 * (q >> 1) + (q & 1) * 8 * 65536, v4u{~0u, ~0u, ~0u, ~0u});
      if (w == 7 && l < 32) *(LAS unsigned*)(sm + L_FL + 4 * l) = 0u;
    }
      HOLD_UNTIL(A_START);
#pragma unroll 1
      for (int q = 0; q < 4; ++q) {
        int row = w + ((64 * g + 32 * q) & 127);
        asm volatile("s_barrier" ::: "memory");
#pragma unroll 1
        for (int i = 0; i < 4; ++i, row += 8) dma16(ckv, vc + row * 1024u, smb + L_C + row * CP);
      }
    }
#define CHALF()                               \
  asm volatile("s_barrier" ::: "memory");     \
  SWEEP_4(qg, o, 0, 1024, 2048, 3072, "");    \
  VMWAIT(12);                                 \
  asm volatile("s_barrier" ::: "memory")
#define CLAND() \
  VMWAIT(4);    \
  EPOCH();      \
  BAR()
#define QRETRY()                              \
  if (TAG_BAD(qg[0])) RELOAD(qg[0], o, 0);    \
  if (TAG_BAD(qg[1])) RELOAD(qg[1], o, 1024); \
  if (TAG_BAD(qg[2])) RELOAD(qg[2], o, 2048); \
  if (TAG_BAD(qg[3])) RELOAD(qg[3], o, 3072); \
  PIN4(qg)
#define QGATHER()                                                                                                           \
  VMWAIT(0);                                                                                                      \
  asm volatile("" : "+v"(qg[0]), "+v"(qg[1]), "+v"(qg[2]), "+v"(qg[3])::"memory");                                          \
  RETRY(TAGS_BAD4(qg), QRETRY());                                                                                           \
  asm volatile("ds_write2_b32 %0, %2, %3 offset0:0 offset1:1\n\tds_write2_b32 %0, %4, %5 offset0:128 offset1:129\n\t"        \
               "ds_write2_b32 %1, %6, %7 offset0:0 offset1:1\n\tds_write2_b32 %1, %8, %9 offset0:128 offset1:129" ::"v"(pqs), \
               "v"(pqs2), "v"(qg[0].x), "v"(qg[0].z), "v"(qg[1].x), "v"(qg[1].z), "v"(qg[2].x), "v"(qg[2].z), "v"(qg[3].x), \
               "v"(qg[3].z)                                                                                                 \
               : "memory")
    if (__builtin_expect(w < 4, 1)) {
      v4u fr[32], qg[4];
      unsigned pc = L_C + (32 * w + col) * CP + 16 * g4;
      PIN(pc);
      const int u = w ^ (2 * g);
      unsigned p = L_Q + col * CP + 16 * g4, pp = L_P + u * 1024 + l * 16, pm = L_MX + col * 16 + u * 4;
      unsigned ol = WS_LSE + (((b * 32 + n) * 2 + g) * 8 + ((col + ROT * n) & 7)) * 128 + (col >> 3) * 8;
      if (g4) ol = NOWHERE;
      PIN(p); PIN(pp); PIN(pm); PIN(ol);
      CHALF();
      const bool early = (w >> 1) == g;
      if (!early) {
        CLAND();
      }
#pragma unroll
      for (int kt = 0; kt < 2; ++kt)
#pragma unroll
        for (int s = 0; s < 16; ++s) fr[16 * kt + s] = *(LAS v4u*)(sm + pc + 16 * kt * CP + 64 * s);
      if (early) {
        CLAND();
      }
      QGATHER();
      BAR();
      f4 s0 = z, s1 = z;
      v4u qf[16];
#pragma unroll
      for (int s = 0; s < 6; ++s) qf[s] = *(LAS v4u*)(sm + p + 64 * s);
      __builtin_amdgcn_sched_barrier(0);
#pragma unroll
      for (int s = 0; s < 16; ++s) {
        if (s + 6 < 16) qf[s + 6] = *(LAS v4u*)(sm + p + 64 * (s + 6));
        s0 = mfma32(fr[s], qf[s], s0);
        s1 = mfma32(fr[16 + s], qf[s], s1);
        __builtin_amdgcn_sched_barrier(0);
      }
      float mx = fmax2(fmax2(fmax2(s0[0], s0[1]), fmax2(s0[2], s0[3])), fmax2(fmax2(s1[0], s1[1]), fmax2(s1[2], s1[3])));
      mx = sw32_max(sw16_max(mx));
      constexpr float SC = 0.08838834764831845f * 1.4426950408889634f;
      const float mi = __builtin_ceilf(mx * SC);
#pragma unroll
      for (int e = 0; e < 4; ++e) {
        s0[e] = __builtin_amdgcn_exp2f(fmaf(s0[e], SC, -mi));
        s1[e] = __builtin_amdgcn_exp2f(fmaf(s1[e], SC, -mi));
      }
      *(LAS float*)(sm + pm) = mi;
      *(LAS v4u*)(sm + pp) = v4u{pk(s0[0], s0[1]), pk(s0[2], s0[3]), pk(s1[0], s1[1]), pk(s1[2], s1[3])};
      BAR();
      if (w == 0) {
        const f4 m4 = *(LAS f4*)(sm + pm - 4 * u);
        v4u pf[4];
#pragma unroll
        for (int k = 0; k < 4; ++k) pf[k] = *(LAS v4u*)(sm + pp + (k - u) * 1024);
        __builtin_amdgcn_sched_barrier(0);
        const float M = vmax3(vmax3(m4[0], m4[1], m4[2]), m4[3], m4[3]);
        const v4u one = {0x3f803f80u, 0x3f803f80u, 0x3f803f80u, 0x3f803f80u};
        f4 ls = z;
#pragma unroll
        for (int u = 0; u < 4; ++u) {
          const unsigned kk = min((unsigned)(M - m4[u]), 255u) * 0x00800080u;
#pragma unroll
          for (int e = 0; e < 4; ++e) pf[u][e] = scale_dn(pf[u][e], kk);
          ls = mfma32(one, pf[u], ls);
        }
        __builtin_amdgcn_raw_buffer_store_b64(v2u{__builtin_bit_cast(unsigned, M + __builtin_amdgcn_logf(ls[0])), T}, rs, ol, 0, AUX_SC1);
      }
    } else {
      v4u fr[32], qg[4];
      unsigned p0 = L_C + (64 * g + 4 * g4 + tq) * CP + 256 * v + 8 * tp, p1 = L_C + (64 * (g ^ 1) + 4 * g4 + tq) * CP + 256 * v + 8 * tp;
      PIN(p0); PIN(p1);
      unsigned pp = L_P + l * 16, pm = L_MX + col * 16;
      v4u one = {0x3f803f80u, 0x3f803f80u, 0x3f803f80u, 0x3f803f80u};
      unsigned op2 = op + 8 * 65536;
      PIN(pp); PIN(pm); PIN(one); PIN(op); PIN(op2);
      CHALF();
#pragma unroll
      for (int ct = 0; ct < 8; ++ct)
#pragma unroll
        for (int u = 0; u < 2; ++u) fr[4 * ct + u] = trrd2(sm + p0 + 32 * u * CP + 32 * ct, sm + p0 + (32 * u + 16) * CP + 32 * ct);
      CLAND();
#pragma unroll
      for (int ct = 0; ct < 8; ++ct)
#pragma unroll
        for (int u = 0; u < 2; ++u) fr[4 * ct + 2 + u] = trrd2(sm + p1 + 32 * u * CP + 32 * ct, sm + p1 + (32 * u + 16) * CP + 32 * ct);
      QGATHER();
      BAR();
      if (glu) {
        const int k = 8 * (v >> 1) + 2 * (a & 3);
        const unsigned og = (unsigned)(((32 * k + (l >> 1)) * FF + 16 * sl + 8 * (l & 1)) * 2);
        dma16((v & 1) ? wu : wg, og, smb + L_GG + (v & 1) * 16384 + k * 1024);
        dma16((v & 1) ? wu : wg, og + 32 * FF * 2, smb + L_GG + (v & 1) * 16384 + k * 1024 + 1024);
      }
      if (glu && w < 6) LAYERNORM();
      BAR();
      const f4 m4 = *(LAS f4*)(sm + pm);
      v4u pf[4];
#pragma unroll
      for (int u = 0; u < 4; ++u) pf[u] = *(LAS v4u*)(sm + pp + u * 1024);
      __builtin_amdgcn_sched_barrier(0);
      const float M = vmax3(vmax3(m4[0], m4[1], m4[2]), m4[3], m4[3]);
      f4 ls = z, c[4] = {z, z, z, z};
#pragma unroll
      for (int u = 0; u < 4; ++u) {
        const unsigned kk = min((unsigned)(M - m4[u]), 255u) * 0x00800080u;
#pragma unroll
        for (int e = 0; e < 4; ++e) pf[u][e] = scale_dn(pf[u][e], kk);
        ls = mfma32(one, pf[u], ls);
        if (u < 3) {
#pragma unroll
          for (int k = 0; k < 4; ++k) c[k] = mfma32(fr[4 * k + u], pf[u], c[k]);
        }
      }
      __builtin_amdgcn_sched_barrier(0);
      c[0] = mfma32(fr[3], pf[3], c[0]);
      c[1] = mfma32(fr[7], pf[3], c[1]);
      __builtin_amdgcn_sched_barrier(0);
      const float il = __builtin_amdgcn_rcpf(ls[0]);
      c[2] = mfma32(fr[11], pf[3], c[2]);
      c[3] = mfma32(fr[15], pf[3], c[3]);
#define PV_OUT(c, q)                                                                                          \
  {                                                                                                           \
    v4u R[2];                                                                                                 \
    _Pragma("unroll") for (int k = 0; k < 2; ++k) {                                                           \
      const f4 c0 = c[2 * k] * il, c1 = c[2 * k + 1] * il;                                                    \
      const unsigned x0 = min(pk(c0[0], c0[1]), 0xfffffffeu), x1 = min(pk(c0[2], c0[3]), 0xfffffffeu);        \
      const unsigned y0 = min(pk(c1[0], c1[1]), 0xfffffffeu), y1 = min(pk(c1[2], c1[3]), 0xfffffffeu);        \
      auto r0 = __builtin_amdgcn_permlane16_swap(x0, y0, false, false);                                       \
      auto r1 = __builtin_amdgcn_permlane16_swap(x1, y1, false, false);                                       \
      R[k] = v4u{r0[0], r1[0], r0[1], r1[1]};                                                                 \
    }                                                                                                         \
    v4u A, B;                                                                                                 \
    _Pragma("unroll") for (int e = 0; e < 4; ++e) {                                                           \
      A[e] = (unsigned)__builtin_amdgcn_update_dpp((int)R[0][e], (int)R[1][e], ROR8, 0xf, 0xc, false);        \
      B[e] = (unsigned)__builtin_amdgcn_update_dpp((int)R[1][e], (int)R[0][e], ROR8, 0xf, 0x3, false);        \
    }                                                                                                         \
    st_sc1(rs, op + 128 * q, A);                                                                              \
    st_sc1(rs, op2 + 128 * q, B);                                                                             \
  }
      __builtin_amdgcn_sched_barrier(0);
      f4 d[4] = {z, z, z, z};
#pragma unroll
      for (int u = 0; u < 4; ++u)
#pragma unroll
        for (int k = 0; k < 4; ++k) d[k] = mfma32(fr[16 + 4 * k + u], pf[u], d[k]);
      PV_OUT(c, 0);
#pragma unroll
      for (int i = 0; i < 16; ++i) {
        __builtin_amdgcn_sched_group_barrier(0x8, 1, 0);
        __builtin_amdgcn_sched_group_barrier(0x2, PV_MIX, 0);
      }
      __builtin_amdgcn_sched_barrier(0);
      PV_OUT(d, 1);
      FLAG_SET(v);
      if (glu) {
        VMWAIT(4);
        FLAG_SET(12 + v);
      }
      MARK(3);
    }
    }
      if (glu && w < 4) {
        v4u af[8], xf[8];
        unsigned oog = WS_OPG + (8 * w + (col >> 1)) * OGP + sl * 128 + ((col & 1) * 4 + g4) * 16;
        PIN(oog);
        FLAG_WAIT4(0);
#pragma unroll 1
        for (int i = 2; i < 8; ++i) {
          const int k = 8 * (w >> 1) + ((2 * a + i) & 7);
          dma16((w & 1) ? wu : wg, (unsigned)(((32 * k + (l >> 1)) * FF + 16 * sl + 8 * (l & 1)) * 2), smb + L_GG + (w & 1) * 16384 + k * 1024);
        }
#pragma unroll 1
        for (int i = 0; i < 4; ++i) dma16(wd, (unsigned)(((16 * sl + 4 * w + i) * DM + 8 * l) * 2), smb + L_GD + (4 * w + i) * CP);
        unsigned pg = L_GG + (w & 1) * 16384 + 32 * (256 * (w >> 1) + 8 * g4 + tq) + 8 * tp;
        unsigned pd = L_GD + (4 * g4 + tq) * CP + 256 * w + 8 * tp, pgw = L_GR + ((w & 1) * 2 + (w >> 1)) * 1024 + l * 16;
        PIN(pg); PIN(pd); PIN(pgw);
#pragma unroll
        for (int s = 0; s < 8; ++s) xf[s] = *(LAS v4u*)(px + 512 * (w >> 1) + 64 * s);
        FLAG_WAIT4(3);
        VMWAIT(4);
        f4 acc = z;
#pragma unroll
        for (int s = 0; s < 8; ++s) af[s] = trrd2(sm + pg + 32 * 32 * s, sm + pg + 32 * (32 * s + 4));
        __builtin_amdgcn_sched_barrier(0);
#pragma unroll
        for (int s = 0; s < 8; ++s) acc = mfma32(af[s], xf[s], acc);
        *(LAS f4*)(sm + pgw) = acc;
        VMWAIT(0);
        FLAG_SET(8 + w);
        FLAG_WAIT4(2);
        v2u dn[8];
        LAS char* pr = sm + L_GR + l * 16;
        const f4 gx = *(LAS f4*)(pr) + *(LAS f4*)(pr + 1024), ux = *(LAS f4*)(pr + 2048) + *(LAS f4*)(pr + 3072);
#pragma unroll
        for (int t = 0; t < 8; ++t) dn[t] = trrd(sm + pd + 32 * t);
        float hh[4];
#pragma unroll
        for (int e = 0; e < 4; ++e) hh[e] = ux[e] * __builtin_amdgcn_rcpf(1.f + __builtin_amdgcn_exp2f(-1.4426950408889634f * gx[e]));
        const v2u hb = {pk(hh[0], hh[1]), pk(hh[2], hh[3])};
        f4 d = z;
#pragma unroll
        for (int t = 0; t < 8; ++t) d = mfma16(dn[t], (col >> 1) == t ? hb : v2u{0u, 0u}, d);
        v4u dv = {pk(d[0], d[1]), T, pk(d[2], d[3]), T};
        PIN(dv);
        st_sc1(rs, oog, dv);
        MARK(4);
      }
    if (a >= 12) {
      const unsigned m = (col & 1) ? 0x3f800000u : 0x3f80u;
      v4u sel = {col < 2 ? m : 0u, (col >> 1) == 1 ? m : 0u, col < 2 ? m : 0u, (col >> 1) == 1 ? m : 0u};
      PIN(sel);
      if (w == 0) {
        unsigned ot = WS_OPT + rd * 8192 + 4096 + l * 16, oy = (l >> 2) * 1024 + rd * 32 + (l & 3) * 8, pr = smb + L_RR + (l & 7) * 16;
        PIN(ot); PIN(oy); PIN(pr);
        v4u p[8];
        touch(out, (unsigned)((l & 1) * 1024 + rd * 32), smb + L_JUNK);
        SWEEP_4(p, ot, -4096, -3072, -2048, -1024, "");
        SWEEP_4(p + 4, ot, 0, 1024, 2048, 3072, "");
        PIN4(p);
        PIN4(p + 4);
#define RRETRY()                                \
  if (TAG_BAD(p[0])) RELOAD(p[0], ot, -4096);   \
  if (TAG_BAD(p[1])) RELOAD(p[1], ot, -3072);   \
  if (TAG_BAD(p[2])) RELOAD(p[2], ot, -2048);   \
  if (TAG_BAD(p[3])) RELOAD(p[3], ot, -1024);   \
  if (TAG_BAD(p[4])) RELOAD(p[4], ot, 0);       \
  if (TAG_BAD(p[5])) RELOAD(p[5], ot, 1024);    \
  if (TAG_BAD(p[6])) RELOAD(p[6], ot, 2048);    \
  if (TAG_BAD(p[7])) RELOAD(p[7], ot, 3072);    \
  PIN4(p);                                      \
  PIN4(p + 4)
        RETRY(TAGS_BAD4(p) | TAGS_BAD4(p + 4), RRETRY());
        f4 acc = z;
#pragma unroll
        for (int i = 0; i < 4; ++i) acc = mfma32(sel, v4u{p[2 * i].x, p[2 * i].z, p[2 * i + 1].x, p[2 * i + 1].z}, acc);
        f4 rest[6];
        for (unsigned spin = 0; spin < SPIN_CAP; ++spin) {
          v4u f0;
          v2u f1;
          asm volatile("ds_read_b128 %0, %8\n\tds_read_b64 %1, %8 offset:16\n\tds_read_b128 %2, %9\n\tds_read_b128 %3, %9 offset:128\n\t"
                       "ds_read_b128 %4, %9 offset:256\n\tds_read_b128 %5, %9 offset:384\n\tds_read_b128 %6, %9 offset:512\n\t"
                       "ds_read_b128 %7, %9 offset:640\n\ts_waitcnt lgkmcnt(0)"
                       : "=&v"(f0), "=&v"(f1), "=&v"(rest[0]), "=&v"(rest[1]), "=&v"(rest[2]), "=&v"(rest[3]), "=&v"(rest[4]), "=&v"(rest[5])
                       : "v"(smb + L_FL + 64), "v"(pr)
                       : "memory");
          if (__builtin_amdgcn_readfirstlane(f0.x & f0.y & f0.z & f0.w & f1.x & f1.y)) break;
        }
        const f4 gl = ((rest[0] + rest[1]) + (rest[2] + rest[3])) + (rest[4] + rest[5]);
#pragma unroll
        for (int e = 0; e < 4; ++e) acc[e] = (acc[e] + dpp<ROR8>(acc[e])) + gl[e];
        if (l < 8) asm volatile("global_store_dwordx2 %0, %1, %2 sc1" ::"v"(oy), "v"(v2u{pk(acc[0], acc[1]), pk(acc[2], acc[3])}), "s"(out) : "memory");
      } else if (w < 7) {
        const bool last = w == 6;
        unsigned og = WS_OPG + rd * OGP + (w - 1) * 2048 + (last ? min(l, 47) : l) * 16, og2 = last ? og : og + 1024;
        const v2u xr = *(const v2u*)((const char*)x + ((l >> 2) & 1) * 1024 + rd * 32 + (l & 3) * 8);
        unsigned m0 = (last && l >= 48) ? 0u : ~0u, m1 = last ? 0u : ~0u;
        f4 xv = {blo(xr.x), bhi(xr.x), blo(xr.y), bhi(xr.y)};
        if (!last) xv = z;
        PIN(og); PIN(og2); PIN(m0); PIN(m1); PIN(xv);
        v4u p0, p1;
        UNDEF(p0); UNDEF(p1);
        for (unsigned spin = SPIN_CAP; spin; --spin) {
          SWEEP_2(p0, p1, og, og2, WAIT0);
          if (!(TAG_BAD(p0) | TAG_BAD(p1))) break;
        }
        f4 acc = mfma32(sel, v4u{p0.x & m0, p0.z & m0, p1.x & m1, p1.z & m1}, z);
#pragma unroll
        for (int e = 0; e < 4; ++e) acc[e] = (acc[e] + dpp<ROR8>(acc[e])) + xv[e];
        if (l < 8) *(LAS f4*)(sm + L_RR + (w - 1) * 128 + l * 16) = acc;
        FLAG_SET(15 + w);
      }
      else if (rd == 0) {
        const unsigned ol0 = WS_LSE + (l * 16 + ((ROT * l) & 7)) * 128 + 4;
        unsigned t0, t1;
        for (unsigned spin = 0;; ++spin) {
          __builtin_amdgcn_s_sleep(32);
          asm volatile("global_load_dword %0, %2, %3 sc1\n\tglobal_load_dword %1, %2, %3 offset:1024 sc1" WAIT0
                       : "=&v"(t0), "=&v"(t1)
                       : "v"(ol0), "s"(ws)
                       : "memory");
          if (!__builtin_amdgcn_ballot_w64((t0 & t1 & T) != (t0 | t1 | T)) || spin > SPIN_CAP) break;
        }
        if (l == 0) __builtin_amdgcn_raw_buffer_store_b32(T, rs, WS_E, 0, AUX_SC1);
      }
      MARK(5);
    }
    __builtin_amdgcn_endpgm();
  }
  {
    const int ca = j >> 1, mb = j & 1;
    const int fl = tq | ((g4 & 1) << 2);
    if (__builtin_expect(w < 4, 1)) {
#pragma unroll 1
      for (int i = 0; i < 8; ++i, vq += 131072u, lq += 4096) dma16(wq, vq, lq);
      gld4_sc1(ep, ws, vzero);
      unsigned p = L_WQ + 256 * (8 * g4 + tq) + 16 * ((4 * w + (tp >> 1)) ^ (fl << 1)) + 8 * (tp & 1), p2 = p ^ 32;
      unsigned oqp = col < 4 ? WS_QP + ((h * 4 + j) * 8 + 2 * w) * 128 + col * 64 + g4 * 16 : NOWHERE;
      unsigned pxj = L_X + (col & 1) * XP + 16 * g4 + 256 * j;
      PIN(p); PIN(p2); PIN(oqp); PIN(pxj);
      unsigned o = WS_QP + (h * 4 + w) * 1024 + l * 16;
      unsigned pqw = smb + L_CC + (2 * w + ((l >> 2) & 1)) * SP + (l >> 3) * 32 + (l & 3) * 8, pqr = L_CC + (col & 7) * SP + 16 * g4;
      unsigned oqa = col < 4 ? WS_QA + ((col & 1) * NH + h) * 2048 + (64 * j + 16 * w + 8 * (col >> 1) + 2 * g4) * 8 : NOWHERE;
      unsigned pu = L_WUK + 256 * (32 * w + col) + 16 * (g4 ^ col);
      PIN(o); PIN(oqa); PIN(pu); PIN(pqw); PIN(pqr);
      VMWAIT(0);
      EPOCH();
      BAR();
      {
        v4u af[8], xf[4];
#pragma unroll
        for (int s = 0; s < 4; ++s) {
          xf[s] = *(LAS v4u*)(sm + pxj + 64 * s);
          af[s] = trrd2(sm + p + 256 * 32 * s, sm + p + 256 * (32 * s + 4));
          af[4 + s] = trrd2(sm + p2 + 256 * 32 * s, sm + p2 + 256 * (32 * s + 4));
        }
        __builtin_amdgcn_sched_barrier(0);
        f4 c0 = z, c1 = z;
#pragma unroll
        for (int s = 0; s < 4; ++s) {
          c0 = mfma32(af[s], xf[s], c0);
          c1 = mfma32(af[4 + s], xf[s], c1);
        }
        if (col & 2) c0 = c1;
        st_sc1(rs, oqp, v4u{pk(c0[0], c0[1]), T, pk(c0[2], c0[3]), T});
      }
      v4u a, uk[8], qf[4];
      __builtin_amdgcn_s_sleep(H1_DELAY);
      asm volatile(LD16(0, 1, 2) " sc1" : "=&v"(a) : "v"(o), "s"(ws) : "memory");
      BAR();
#pragma unroll
      for (int s = 0; s < 4; ++s) {
        uk[s] = *(LAS v4u*)(sm + (pu ^ (64 * s)));
        uk[4 + s] = *(LAS v4u*)(sm + (pu ^ (64 * s)) + 4096);
      }
      VMWAIT(0);
      PIN(a);
      RETRY(TAG_BAD(a), asm volatile(LD16(0, 1, 2) " sc1" WAIT0 : "+v"(a) : "v"(o), "s"(ws) : "memory"));
      asm volatile("ds_write2_b32 %0, %1, %2 offset0:0 offset1:1" ::"v"(pqw), "v"(a.x), "v"(a.z) : "memory");
      BAR();
#pragma unroll
      for (int s = 0; s < 4; ++s) qf[s] = *(LAS v4u*)(sm + pqr + 64 * s);
      __builtin_amdgcn_sched_barrier(0);
      f4 a0 = z, a1 = z;
#pragma unroll
      for (int s = 0; s < 4; ++s) {
        a0 = mfma32(uk[s], qf[s], a0);
        a1 = mfma32(uk[4 + s], qf[s], a1);
      }
      float q[4];
#pragma unroll
      for (int e = 0; e < 4; ++e) {
        const float mine = (col & 2) ? a1[e] : a0[e], other = (col & 2) ? a0[e] : a1[e];
        q[e] = mine + dpp<QP_2301>(other);
        q[e] += dpp<ROR4>(q[e]);
      }
      st_sc1(rs, oqa, v4u{pk(q[0], q[1]), T, pk(q[2], q[3]), T});
      BAR();
      touch(ckv, (unsigned)(((xcd >> 2) * SK + 1024 * (xcd & 3) + 64 * r + 32 + 8 * w) * 1024 + l * 128), smb + L_JUNK);
    } else {
      unsigned sdummy = 0u;
      gld4_sc1(ep, ws, vzero);
      if (w < 6) {
        VMWAIT(1);
        LAYERNORM();
        BAR();
#define STOUCH(base, off) asm volatile("s_load_dword %0, %1, %2" : "+s"(sdummy) : "s"(base), "s"((unsigned)(off)) : "memory")
        HOLD_UNTIL(TLB_LATE);
        {
          const int i = 2 * r + (w - 4);
          STOUCH(wuv, i * 131072);
          STOUCH(wo, h * 131072 + (w - 4) * 65536);
          STOUCH(wg, min(i, 10) * 131072);
          STOUCH(wu, min(i, 10) * 131072);
          STOUCH(wd, min(i, 10) * 131072);
          STOUCH(out, 0);
        }
        MARK(0);
      } else {
        __builtin_amdgcn_s_sleep(UK_DELAY);
        const unsigned vu = (unsigned)((((128 * j + g4) * NH + h) * DH) * 2);
#pragma unroll 1
        for (int k = w - 6; k < 32; k += 2) {
          if (k >> 1 == UK_FIRST) asm volatile("s_barrier" ::: "memory");
          dma16(wuk, vu + (unsigned)k * 32768u + 16u * (col ^ ((4 * k + g4) & 15)), smb + L_WUK + k * 1024);
        }
        MARK(1);
      }
      VMWAIT(0);
      EPOCH();
      asm volatile("s_barrier\n\ts_barrier\n\ts_barrier\n\ts_waitcnt lgkmcnt(0)" : "+s"(sdummy)::"memory");
      HOLD_UNTIL(360);
      const int ln = 128 * r + 64 * ((w >> 1) & 1) + l;
      touch((w & 1) ? wu : wg, (unsigned)((ln >> 2) * (FF * 2)) + (unsigned)min(384 * xcd + 128 * (ln & 3), FF * 2 - 4), smb + L_JUNK);
    }
    HOLD_UNTIL(T_START);
    {
      const unsigned vv = (unsigned)((((256 * ca + g4) * NH + h) * DH + 8 * (col ^ ((g4 | ((w & 2) << 1)) << 1))) * 2);
      const unsigned vo = (unsigned)(((h * DH + (l >> 5)) * DM + 256 * mb) * 2) + 16u * ((l & 31) ^ (((l >> 5) | ((w & 1) << 1) | (w & 4)) << 1));
      int kv = w + 32 * mb, ko = w + 32 * ca;
#pragma unroll 1
      for (int hf = 0; hf < 2; ++hf) {
#pragma unroll 1
        for (int i = 0; i < 4; ++i, kv = (kv + 8) & 63) dma16(wuv, vv + (unsigned)kv * 32768u, smb + L_WUV + kv * 1024);
#pragma unroll 1
        for (int i = 0; i < 4; ++i, ko = (ko + 8) & 63) dma16(wo, vo + (unsigned)ko * 2048u, smb + L_WO + ko * 1024);
        if (hf == 0) asm volatile("s_barrier" ::: "memory");
      }
    }
    const int tb = w >> 2, nq = w & 3;
    unsigned o = WS_PART + (((h * 2 + ca) * 2 + tb) * 32 + 8 * nq + g4) * 512 + 16 * col;
    unsigned pw = L_W + (tb * 32 + 8 * nq + g4) * 4;
    unsigned pv = L_WUV + 256 * (8 * g4 + tq) + 16 * ((2 * w + (tp >> 1)) ^ (fl << 1)) + 8 * (tp & 1);
    unsigned po = L_WO + 512 * (8 * g4 + tq) + 16 * ((4 * w + (tp >> 1)) ^ (fl << 1)) + 8 * (tp & 1);
    unsigned pcw = L_CC + (nq * 2 + tb) * SP2 + 16 * col + 8 * g4, pcr = L_CC + (col & 7) * SP2 + 16 * g4;
    unsigned ptw = L_CT + col * SP + (16 * w + 4 * g4) * 2, ptr = L_CT + (col & 1) * SP + 16 * g4;
    unsigned oo = col >= 4 ? NOWHERE : WS_OPT + ((16 * mb + 2 * w + (col >> 1)) * 64 + ((2 * h + ca + ROT * 4 * (2 * w + (col >> 1))) & 63)) * 128 + ((col & 1) * 4 + g4) * 16;
    v4u mk;
#pragma unroll
    for (int d = 0; d < 4; ++d) mk[d] = (col >> 1) == d ? ((col & 1) ? 0xffff0000u : 0xffffu) : 0u;
    PIN(o); PIN(pw); PIN(pv); PIN(po); PIN(pcw); PIN(pcr); PIN(ptw); PIN(ptr); PIN(oo); PIN(mk);
        if (w == 7) {
      const unsigned ol = WS_LSE + ((l * 2 + (r >> 3)) * 8 + ((xcd + ROT * l) & 7)) * 128 + ((r >> 2) & 1) * 8;
      v2u la;
      UNDEF(la);
      for (unsigned spin = SPIN_CAP; spin; --spin) {
        asm volatile("global_load_dwordx2 %0, %1, %2 sc1" WAIT0 : "+v"(la) : "v"(ol), "s"(ws) : "memory");
        if (!__builtin_amdgcn_ballot_w64(la.y != T)) break;
      }
      float M = asf(la.x);
      M = fmax2(M, dpp<ROR8>(M));
      M = fmax2(M, dpp<ROR4>(M));
      M = fmax2(M, dpp<ROR2>(M));
      M = fmax2(M, dpp<ROR1>(M));
      M = sw16_max(M);
      const float e = __builtin_amdgcn_exp2f(asf(la.x) - M);
      float L = e + dpp<ROR8>(e);
      L += dpp<ROR4>(L);
      L += dpp<ROR2>(L);
      L += dpp<ROR1>(L);
      L = sw16_add(L, L);
      const float wt = e * __builtin_amdgcn_rcpf(L);
      *(LAS unsigned*)(sm + L_W + 4 * ((l & 32) | ((l + ROT * 4 * (h & 7)) & 31))) = pk(wt, wt);
    }
    v4u a[4];
    {
      VMWAIT(0);
      BAR();
      SWEEP_4(a, o, 0, 256, 2048, 2304, "");
    }
    v4u A0 = mk & *(LAS unsigned*)(sm + pw), A1 = mk & *(LAS unsigned*)(sm + pw + 16);
    v4u av[8], ao[8];
#pragma unroll
    for (int s = 0; s < 8; ++s) av[s] = trrd2(sm + pv + 256 * 32 * s, sm + pv + 256 * (32 * s + 4));
#pragma unroll
    for (int t = 0; t < 2; ++t)
#pragma unroll
      for (int s = 0; s < 4; ++s) ao[4 * t + s] = trrd2(sm + (po ^ (32 * t)) + 512 * 32 * s, sm + (po ^ (32 * t)) + 512 * (32 * s + 4));
    VMWAIT(0);
    asm volatile("" : "+v"(a[0]), "+v"(a[1]), "+v"(a[2]), "+v"(a[3]), "+v"(A0), "+v"(A1)::"memory");
    RETRY(HOLES4(a), TRETRY());
    {
      f4 c0 = mfma32(A0, a[0], z), c1 = mfma32(A0, a[1], z);
      c0 = mfma32(A1, a[2], c0);
      c1 = mfma32(A1, a[3], c1);
      if (g4 < 2) {
        *(LAS v2u*)(sm + pcw) = v2u{pk(c0[0], c0[1]), pk(c0[2], c0[3])};
        *(LAS v2u*)(sm + pcw + 256) = v2u{pk(c1[0], c1[1]), pk(c1[2], c1[3])};
      }
    }
    BAR();
    {
      f4 acc = z;
      v4u bf[8];
#pragma unroll
      for (int s = 0; s < 8; ++s) bf[s] = *(LAS v4u*)(sm + pcr + 64 * s);
      __builtin_amdgcn_sched_barrier(0);
#pragma unroll
      for (int s = 0; s < 8; ++s) acc = mfma32(av[s], bf[s], acc);
#pragma unroll
      for (int e = 0; e < 4; ++e) {
        acc[e] += dpp<QP_2301>(acc[e]);
        acc[e] += dpp<ROR4>(acc[e]);
      }
      if (col < 2) *(LAS v2u*)(sm + ptw) = v2u{pk(acc[0], acc[1]), pk(acc[2], acc[3])};
    }
    BAR();
    {
      f4 c0 = z, c1 = z;
      v4u bf[4];
#pragma unroll
      for (int s = 0; s < 4; ++s) bf[s] = *(LAS v4u*)(sm + ptr + 64 * s);
      __builtin_amdgcn_sched_barrier(0);
#pragma unroll
      for (int s = 0; s < 4; ++s) {
        c0 = mfma32(ao[s], bf[s], c0);
        c1 = mfma32(ao[4 + s], bf[s], c1);
      }
      if (col & 2) c0 = c1;
      st_sc1(rs, oo, v4u{pk(c0[0], c0[1]), T, pk(c0[2], c0[3]), T});
    }
    MARK(2);
  }
}

extern "C" void kernel_launch(void* const* d_in, const int* in_sizes, int n_in, void* d_out, int out_size, void* d_ws, size_t ws_size,
                              hipStream_t stream) {
  mla_glu_decode<<<256, 512, 0, stream>>>((const u16*)d_in[0], (const u16*)d_in[1], (const u16*)d_in[2], (const u16*)d_in[3],
                                          (const u16*)d_in[4], (const u16*)d_in[5], (const u16*)d_in[7], (const u16*)d_in[8],
                                          (const u16*)d_in[9], (u16*)d_out, (char*)d_ws);
}
```
